```python
import torch

def _softmax(x, axis=-1):
    return torch.softmax(x, dim=axis)
def _gelu(x):
    return torch.nn.functional.gelu(x, approximate="tanh")
def _silu(x):
    return torch.nn.functional.silu(x)
def _sigmoid(x):
    return torch.sigmoid(x)
def _layernorm(x, axis=-1, eps=1e-5):
    m = x.mean(dim=axis, keepdim=True)
    v = x.var(dim=axis, keepdim=True, unbiased=False)
    return (x - m) / torch.sqrt(v + eps)
def _scatter_add(tgt, idx, src, axis=0):
    return tgt.clone().index_add_(axis, idx, src)
def _segment_sum(src, seg, n):
    out = torch.zeros((n,) + src.shape[1:], dtype=src.dtype)
    return out.index_add_(0, seg, src)
def _pad(x, pw):
    flat = tuple(v for pair in reversed(pw) for v in pair)
    return torch.nn.functional.pad(x, flat)

kernel_name = 'recurrent_scan__ssm_conv_h3__dense_activation_silu__prompt'

def setup_inputs():

    _w = (lambda a: (lambda t: t.to(torch.bfloat16) if t.is_floating_point() else t)(torch.as_tensor(a)))
    def _A_setup():
        B, S, D = 1, 2048, 640
        ks = 4
        return {
            "q": _w((lambda *s: torch.randn(*s, dtype=torch.bfloat16) * 0.05)(B, S, D)),
            "k": _w((lambda *s: torch.randn(*s, dtype=torch.bfloat16) * 0.8)(B, S, D)),
            "v": _w((lambda *s: torch.randn(*s, dtype=torch.bfloat16) * 0.8)(B, S, D)),
            "shift_k": _w((lambda *s: torch.randn(*s, dtype=torch.bfloat16) * 0.8)(D, ks) * 0.5),
            "ssm_k": _w((lambda *s: torch.randn(*s, dtype=torch.bfloat16) * 0.8)(D, S) * (1.0 / S ** 0.5)),
            "ssm_D": _w((lambda *s: torch.randn(*s, dtype=torch.bfloat16) * 0.8)(D)),
        }
    _A_in = _A_setup()

    def _B_setup():
        B, S, D, F = 1, 2048, 640, 1728
        return {
            "x": _w((lambda *s: torch.randn(*s, dtype=torch.bfloat16) * 0.05)(B, S, D)),
            "w1": _w((lambda *s: torch.randn(*s, dtype=torch.bfloat16) * 0.8)(D, F) * (D ** -0.5)),
            "w2": _w((lambda *s: torch.randn(*s, dtype=torch.bfloat16) * 0.2)(F, D) * (F ** -0.5)),
        }
    _B_in = _B_setup()

    return {
        "A_q": _A_in["q"],
        "A_k": _A_in["k"],
        "A_v": _A_in["v"],
        "A_shift_k": _A_in["shift_k"],
        "A_ssm_k": _A_in["ssm_k"],
        "A_ssm_D": _A_in["ssm_D"],
        "B_x": _B_in["x"],
        "B_w1": _B_in["w1"],
        "B_w2": _B_in["w2"],
    }

def reference(A_q, A_k, A_v, A_shift_k, A_ssm_k, A_ssm_D, B_x, B_w1, B_w2):
    def _A_ref(q, k, v, shift_k, ssm_k, ssm_D):
        B, S, D = q.shape
        ks = shift_k.shape[-1]
        k_pad = _pad(k, ((0, 0), (ks - 1, 0), (0, 0)))
        k_shift = torch.zeros_like(k)
        for t in range(ks):
            k_shift = k_shift + k_pad[:, t:t + S, :] * shift_k[:, ks - 1 - t][None, None, :]
        kv = k_shift * v
        L = 2 * S
        kv_pad = _pad(kv, ((0, 0), (0, S), (0, 0)))
        kf_pad = _pad(ssm_k, ((0, 0), (0, S)))
        kv_f = (lambda x, axes=(-2,-1): torch.fft.rfft2(x.float(), dim=axes))(kv_pad[:, None, :, :], axes=(1, 2))
        kf_f = (lambda x, axes=(-2,-1): torch.fft.rfft2(x.float(), dim=axes))(kf_pad[:, None, :], axes=(1, 2))
        kf_f = (lambda x, *d: x.permute(*d))(kf_f, 1, 2, 0)
        y_f = kv_f * kf_f[None, :, :, :]
        y = (lambda x, s, axes=(-2,-1): torch.fft.irfft2(x, s=s, dim=axes).to(torch.bfloat16))(y_f, (1, L), axes=(1, 2))[:, 0, :S, :]
        y = y + kv * ssm_D[None, None, :]
        return q * y

    def _B_ref(x, w1, w2):
        h = torch.einsum("bsd,df->bsf", x, w1)
        h = _silu(h)
        out = torch.einsum("bsf,fd->bsd", h, w2)
        return out

    x = A_q
    xn = _layernorm(x, axis=-1)
    s = _A_ref(xn, A_k, A_v, A_shift_k, A_ssm_k, A_ssm_D)
    g = _silu(_B_ref(xn, B_w1, B_w2))
    return x + s * g
```

```cpp
#include <hip/hip_runtime.h>
#include <hip/hip_bf16.h>
#include <stdint.h>

typedef unsigned short u16;
constexpr int S = 2048, D = 640, F = 1728;

__device__ __forceinline__ float bf2f(u16 b) { return __uint_as_float(((unsigned)b) << 16); }
__device__ __forceinline__ u16 f2bf(float f) {
  __hip_bfloat16 h = __float2bfloat16(f);
  return *reinterpret_cast<u16*>(&h);
}
__device__ __forceinline__ float rbf(float f) { return bf2f(f2bf(f)); }

__global__ void ln_kernel(const u16* x, u16* xn) {
  int row = blockIdx.x;
  int lane = threadIdx.x;
  float v[10];
  float s = 0.f;
  for (int i = 0; i < 10; ++i) { v[i] = bf2f(x[row * D + lane + 64 * i]); s += v[i]; }
  for (int o = 32; o > 0; o >>= 1) s += __shfl_xor(s, o);
  float mean = s / D;
  float q = 0.f;
  for (int i = 0; i < 10; ++i) { float d = v[i] - mean; q += d * d; }
  for (int o = 32; o > 0; o >>= 1) q += __shfl_xor(q, o);
  float var = q / D;
  float mb = rbf(mean), vb = rbf(var);
  float den = rbf(sqrtf(rbf(vb + 1e-5f)));
  for (int i = 0; i < 10; ++i) xn[row * D + lane + 64 * i] = f2bf(rbf(v[i] - mb) / den);
}

__global__ void kv_kernel(const u16* k, const u16* v, const u16* sk, u16* uT) {
  int idx = blockIdx.x * blockDim.x + threadIdx.x;
  if (idx >= S * D) return;
  int t = idx / D, d = idx % D;
  float acc = 0.f;
  for (int tt = 0; tt < 4; ++tt) {
    int s = t + tt - 3;
    float kk = s >= 0 ? bf2f(k[s * D + d]) : 0.f;
    float p = rbf(kk * bf2f(sk[d * 4 + 3 - tt]));
    acc = rbf(acc + p);
  }
  uT[d * S + t] = f2bf(acc * bf2f(v[idx]));
}

__global__ void conv_kernel(const u16* uT, const u16* hk, const u16* Dv, u16* yT) {
  __shared__ float su[S], sh[S];
  int d = blockIdx.x;
  for (int i = threadIdx.x; i < S; i += blockDim.x) { su[i] = bf2f(uT[d * S + i]); sh[i] = bf2f(hk[d * S + i]); }
  __syncthreads();
  for (int t = threadIdx.x; t < S; t += blockDim.x) {
    float acc = 0.f;
    for (int s = 0; s <= t; ++s) acc += sh[t - s] * su[s];
    float y = rbf(acc);
    y = rbf(y + rbf(su[t] * bf2f(Dv[d])));
    yT[d * S + t] = f2bf(y);
  }
}

__device__ __forceinline__ float silu(float x) { return x / (1.f + __expf(-x)); }

__global__ void gemm1_kernel(const u16* xn, const u16* w1, u16* h) {
  int n = blockIdx.x * 64 + threadIdx.x;
  int m = blockIdx.y;
  float acc = 0.f;
  for (int k = 0; k < D; ++k) acc += bf2f(xn[m * D + k]) * bf2f(w1[k * F + n]);
  h[m * F + n] = f2bf(silu(rbf(acc)));
}

__global__ void gemm2_kernel(const u16* h, const u16* w2, const u16* x, const u16* xn, const u16* yT, u16* out) {
  int n = blockIdx.x * 64 + threadIdx.x;
  int m = blockIdx.y;
  float acc = 0.f;
  for (int k = 0; k < F; ++k) acc += bf2f(h[m * F + k]) * bf2f(w2[k * D + n]);
  float g = rbf(silu(rbf(acc)));
  float s = rbf(bf2f(xn[m * D + n]) * bf2f(yT[n * S + m]));
  out[m * D + n] = f2bf(bf2f(x[m * D + n]) + rbf(s * g));
}

extern "C" void kernel_launch(void* const* d_in, const int* in_sizes, int n_in,
                              void* d_out, int out_size, void* d_ws, size_t ws_size,
                              hipStream_t stream) {
  const u16* q = (const u16*)d_in[0];
  const u16* k = (const u16*)d_in[1];
  const u16* v = (const u16*)d_in[2];
  const u16* sk = (const u16*)d_in[3];
  const u16* hk = (const u16*)d_in[4];
  const u16* Dv = (const u16*)d_in[5];
  const u16* w1 = (const u16*)d_in[7];
  const u16* w2 = (const u16*)d_in[8];
  u16* out = (u16*)d_out;
  u16* ws = (u16*)d_ws;
  u16* xn = ws;
  u16* uT = xn + S * D;
  u16* yT = uT + S * D;
  u16* h = yT + S * D;
  ln_kernel<<<S, 64, 0, stream>>>(q, xn);
  kv_kernel<<<(S * D + 255) / 256, 256, 0, stream>>>(k, v, sk, uT);
  conv_kernel<<<D, 256, 0, stream>>>(uT, hk, Dv, yT);
  gemm1_kernel<<<dim3(F / 64, S), 64, 0, stream>>>(xn, w1, h);
  gemm2_kernel<<<dim3(D / 64, S), 64, 0, stream>>>(h, w2, q, xn, yT, out);
}
```

```cpp
#include <hip/hip_runtime.h>
#include <stdint.h>

typedef unsigned short u16;
typedef unsigned v4u __attribute__((ext_vector_type(4)));
typedef unsigned v2u __attribute__((ext_vector_type(2)));
typedef float f32x2 __attribute__((ext_vector_type(2)));
typedef float f32x4 __attribute__((ext_vector_type(4)));
typedef float f32x16 __attribute__((ext_vector_type(16)));
typedef short bf16x8 __attribute__((ext_vector_type(8)));
typedef short s16x4 __attribute__((ext_vector_type(4)));
typedef __bf16 bf16x2 __attribute__((ext_vector_type(2)));
#define LAS __attribute__((address_space(3)))
#define DI __device__ __forceinline__

constexpr int S = 2048, D = 640, F = 1728;
constexpr int KS2 = 54;
constexpr int HP_OFF = 0;
constexpr int HP_MB = KS2 * 4096;
constexpr int W2P_OFF = HP_OFF + 32 * HP_MB;
constexpr int W2P_NB = KS2 * 5120;
constexpr int R_OFF = W2P_OFF + 8 * W2P_NB;
constexpr int Y_OFF = R_OFF + D * S * 2;
constexpr int ST_OFF = Y_OFF + D * S * 2;
constexpr int WS_BYTES = ST_OFF + S * 8;
constexpr unsigned RSRC3 = 0x00020000u;

DI float bflo(unsigned u) { return __uint_as_float(u << 16); }
DI float bfhi(unsigned u) { return __uint_as_float(u & 0xffff0000u); }
DI unsigned pack2(float a, float b) {
  bf16x2 r = __builtin_convertvector(f32x2{a, b}, bf16x2);
  return __builtin_bit_cast(unsigned, r);
}
DI float rbf(float a) { return bflo(pack2(a, a)); }
DI float silu_f(float x) { return x * __builtin_amdgcn_rcpf(1.f + __builtin_amdgcn_exp2f(-1.44269504088896f * x)); }
DI f32x4 mfma16(v4u a, v4u b, f32x4 c) {
  return __builtin_amdgcn_mfma_f32_16x16x32_bf16(__builtin_bit_cast(bf16x8, a), __builtin_bit_cast(bf16x8, b), c, 0, 0, 0);
}
DI f32x16 mfma32(v4u a, v4u b, f32x16 c) {
  return __builtin_amdgcn_mfma_f32_32x32x16_bf16(__builtin_bit_cast(bf16x8, a), __builtin_bit_cast(bf16x8, b), c, 0, 0, 0);
}
DI float dot2(unsigned a, unsigned b, float c) {
  return __builtin_amdgcn_fdot2_f32_bf16(__builtin_bit_cast(bf16x2, a), __builtin_bit_cast(bf16x2, b), c, false);
}
DI v2u trread(LAS char* p) { return __builtin_bit_cast(v2u, __builtin_amdgcn_ds_read_tr16_b64_v4i16((LAS s16x4*)p)); }
DI v4u ldsr128(LAS char* p) { return *(LAS v4u*)p; }
DI __amdgpu_buffer_rsrc_t mkrsrc(const void* p, int bytes) { return __builtin_amdgcn_make_buffer_rsrc((void*)p, 0, bytes, RSRC3); }
DI void glds_asm(__amdgpu_buffer_rsrc_t rs, unsigned ldsa, int voff, int soff) {
  asm volatile("s_nop 4\n\ts_mov_b32 m0, %0\n\ts_nop 0\n\tbuffer_load_dwordx4 %1, %2, %3 offen lds"
               :: "s"(ldsa), "v"(voff), "s"(rs), "s"(soff) : "m0", "memory");
}
#define GLDS(rs, ldsp, voff, soff) glds_asm(rs, (unsigned)(uintptr_t)(ldsp), voff, soff)
#define WAITV(n) asm volatile("s_waitcnt vmcnt(" #n ")" ::: "memory")
#define BAR() do { asm volatile("s_waitcnt lgkmcnt(0)" ::: "memory"); __builtin_amdgcn_s_barrier(); asm volatile("" ::: "memory"); } while (0)

#define STAMP_DECL
#define STAMP(i)
#define STAMP_OUT(kid)
constexpr int PK = 98304, PV = PK + 11264, PW = PV + 10240, PS = PW + 10240, PO = 131072;
constexpr int XST = PO + 10240;
constexpr int XCS = XST + 8192;
constexpr int XFS = XCS + 2048;
constexpr int XFC = XFS + 1024;
constexpr int LDS_A = XFC + 512;

struct Frags { v4u x0[4], x1[4], wf[4]; };
DI void g1_read(LAS char* L, int bufoff, const int (&xa)[4], const int (&wa)[4][2], Frags& f) {
#pragma unroll
  for (int i = 0; i < 4; ++i) {
    f.x0[i] = ldsr128(L + bufoff + xa[i]);
    f.x1[i] = ldsr128(L + bufoff + xa[i] + 4096);
    v2u wl = trread(L + bufoff + wa[i][0]);
    v2u wh = trread(L + bufoff + wa[i][1]);
    f.wf[i] = v4u{wl.x, wl.y, wh.x, wh.y};
  }
}
DI void g1_mma(const Frags& f, f32x16& acc0, f32x16& acc1, float& s0, float& q0, float& s1, float& q1, float& cs) {
  const unsigned ONES = 0x3f803f80u;
#pragma unroll
  for (int i = 0; i < 4; ++i) {
    acc0 = mfma32(f.wf[i], f.x0[i], acc0);
    acc1 = mfma32(f.wf[i], f.x1[i], acc1);
    if (i == 0) {
#pragma unroll
      for (int e = 0; e < 4; ++e) {
        s0 = dot2(f.x0[0][e], ONES, s0); q0 = dot2(f.x0[0][e], f.x0[0][e], q0);
        s1 = dot2(f.x1[0][e], ONES, s1); q1 = dot2(f.x1[0][e], f.x1[0][e], q1);
      }
    }
    if (i < 2) {
#pragma unroll
      for (int e = 0; e < 4; ++e) cs = dot2(f.wf[i][e], ONES, cs);
    }
  }
}
DI void g1_sched() {
#pragma unroll
  for (int i = 0; i < 8; ++i) {
    __builtin_amdgcn_sched_group_barrier(0x8, 1, 0);
    __builtin_amdgcn_sched_group_barrier(0x100, 2, 0);
    __builtin_amdgcn_sched_group_barrier(0x2, 3, 0);
  }
}

__global__ __launch_bounds__(512, 2) void ka(const u16* __restrict__ x, const u16* __restrict__ kk, const u16* __restrict__ vv,
                                             const u16* __restrict__ sk, const u16* __restrict__ w1, const u16* __restrict__ w2,
                                             char* __restrict__ ws) {
  __shared__ __attribute__((aligned(16))) char smem[LDS_A];
  LAS char* L = (LAS char*)smem;
  const int b = blockIdx.x, xcd = b & 7, r = b >> 3;
  const int p = 2 * xcd + (r >> 4), j = r & 15, mb = 2 * p + (j >> 3), nb = j & 7;
  const int lane = threadIdx.x & 63;
  const int w = __builtin_amdgcn_readfirstlane(threadIdx.x >> 6);
  const bool g1 = j < 14;
  STAMP_DECL; STAMP(0);
  const __amdgpu_buffer_rsrc_t rws = mkrsrc(ws, WS_BYTES);

  {
    const int t0 = 64 * mb, d0b = 160 * nb;
#pragma unroll
    for (int i = 0; i < 4; ++i) {
      const int s = 4 * w + i;
      const void* base; int bytes, n0, rb;
      if (s < 11) { base = kk; bytes = S * D * 2; n0 = s * 64; rb = t0 - 4; }
      else if (s < 21) { base = vv; bytes = S * D * 2; n0 = (s - 11) * 64; rb = t0; }
      else if (s < 31) { base = w2; bytes = F * D * 2; n0 = (s - 21) * 64; rb = 32 * mb; }
      else { base = sk; bytes = D * 8; n0 = 0; rb = 0; }
      const int n = n0 + lane;
      int row = n / 10; const int c = n - row * 10;
      if (s >= 21 && row >= 32) row += 992;
      int voff = (rb + row) * 1280 + d0b + c * 16;
      if (s == 31) voff = nb * 640 + lane * 16;
      GLDS(mkrsrc(base, bytes), L + PK + s * 1024, voff, 0);
    }
  }
  const __amdgpu_buffer_rsrc_t rx = mkrsrc(x + (size_t)p * 128 * D, 128 * D * 2);
  const __amdgpu_buffer_rsrc_t rw = mkrsrc(w1, D * F * 2);
  int vx[2], vw[2];
#pragma unroll
  for (int ii = 0; ii < 2; ++ii) {
    const int row = 16 * w + 8 * ii + (lane >> 3);
    vx[ii] = row * 1280 + (((lane & 7) ^ ((row >> 1) & 7)) << 4);
    const int kr = 8 * w + 4 * ii + (lane >> 4);
    vw[ii] = kr * (F * 2) + j * 256 + (((lane & 15) ^ (((kr & 3) << 2) | ((kr >> 2) & 3))) << 4);
  }
#define STAGE(s) do { \
    LAS char* bufp = L + ((s) & 3) * 32768; \
    GLDS(rx, bufp + (2 * w) * 1024, vx[0], (s) * 128); \
    GLDS(rx, bufp + (2 * w + 1) * 1024, vx[1], (s) * 128); \
    GLDS(rw, bufp + 16384 + (2 * w) * 1024, vw[0], (s) * 64 * F * 2); \
    GLDS(rw, bufp + 16384 + (2 * w + 1) * 1024, vw[1], (s) * 64 * F * 2); \
  } while (0)
  if (g1) { STAGE(0); STAGE(1); STAGE(2); STAMP(1); WAITV(12); } else { WAITV(0); }
  STAMP(2);
  BAR();
  STAMP(3);
  v4u w2frag[2];
  {
    const int g = lane >> 4, q = (lane & 15) >> 2, pp = lane & 3;
#pragma unroll
    for (int i = 0; i < 2; ++i) {
      int fi = w + 8 * i; if (fi > 9) fi = 9;
      const int half = fi / 5, nt = fi - half * 5;
      const int r0 = 16 * (g & 1) + 4 * (g >> 1);
      LAS char* a = L + PW + half * 5120 + (r0 + q) * 160 + nt * 32 + pp * 8;
      v2u lo = trread(a), hi = trread(a + 8 * 160);
      w2frag[i] = v4u{lo.x, lo.y, hi.x, hi.y};
    }
#pragma unroll
    for (int i = 0; i < 3; ++i) {
      const int it = w + 8 * i;
      if (it < 20) {
        const int tg = it / 5, cg = it - tg * 5;
        const int tl = 16 * tg + 4 * g;
        LAS char* ka_ = L + PK + (tl + q) * 160 + cg * 32 + pp * 8;
        v2u k0 = trread(ka_), k1 = trread(ka_ + 4 * 160);
        v2u v0 = trread(L + PV + (tl + q) * 160 + cg * 32 + pp * 8);
        v2u sw = *(LAS v2u*)(L + PS + (cg * 16 + (lane & 15)) * 8);
        float kf[8] = {bflo(k0.x), bfhi(k0.x), bflo(k0.y), bfhi(k0.y), bflo(k1.x), bfhi(k1.x), bflo(k1.y), bfhi(k1.y)};
        float vf[4] = {bflo(v0.x), bfhi(v0.x), bflo(v0.y), bfhi(v0.y)};
        const float c0 = bflo(sw.x), c1 = bfhi(sw.x), c2 = bflo(sw.y), c3 = bfhi(sw.y);
        float u[4];
#pragma unroll
        for (int e = 0; e < 4; ++e) {
          float a = rbf(kf[e + 1] * c3);
          a = rbf(a + rbf(kf[e + 2] * c2));
          a = rbf(a + rbf(kf[e + 3] * c1));
          a = rbf(a + rbf(kf[e + 4] * c0));
          u[e] = a * vf[e];
        }
        v2u o = v2u{pack2(u[3], u[2]), pack2(u[1], u[0])};
        const int ch = cg * 16 + (lane & 15);
        const int pos8 = (60 - tl) >> 2;
        *(LAS v2u*)(L + PO + ch * 128 + ((((pos8 >> 1) ^ (ch & 7)) << 4) | ((pos8 & 1) << 3))) = o;
      }
    }
  }
  STAMP(4);
  BAR();
  if (g1) STAGE(3);
  {
#pragma unroll
    for (int i = 0; i < 3; ++i) {
      v4u val; int off;
      if (i == 0) {
        val = w2frag[0];
        const int half = w / 5, nt = w - half * 5; const int ks = mb + 32 * half;
        off = ks < KS2 ? W2P_OFF + nb * W2P_NB + ks * 5120 + nt * 1024 + lane * 16 : -16;
      } else if (i == 1 || w >= 2) {
        const int ri = (i == 1) ? w : (w + 6);
        const int ch = 8 * ri + (lane >> 3), cc = lane & 7;
        val = ldsr128(L + PO + (ri < 10 ? ch : 0) * 128 + ((cc ^ (ch & 7)) << 4));
        off = ri < 10 ? R_OFF + (80 * nb + ch) * 4096 + (31 - mb) * 128 + cc * 16 : -16;
      } else {
        val = w2frag[1];
        const int nt = w + 3; const int ks = mb + 32;
        off = ks < KS2 ? W2P_OFF + nb * W2P_NB + ks * 5120 + nt * 1024 + lane * 16 : -16;
      }
      __builtin_amdgcn_raw_buffer_store_b128(val, rws, off, 0, 0);
    }
  }
  STAMP(5);
  if (!g1) { STAMP_OUT(0); return; }
  const int fg = w & 3, th = w >> 2, hi = lane >> 5;
  int xa[4], wa[4][2];
  {
    const int row = 64 * th + (lane & 31);
    const int c0 = hi ^ ((row >> 1) & 7);
    const int blk = (lane >> 4) & 1, q = (lane & 15) >> 2, pp = lane & 3;
#pragma unroll
    for (int i = 0; i < 4; ++i) {
      const int s = (fg + 2 * th + i) & 3;
      xa[i] = row * 128 + (((2 * s) ^ c0) << 4);
#pragma unroll
      for (int t = 0; t < 2; ++t) {
        const int kr = 16 * s + 8 * hi + 4 * t + q;
        const int ch = 4 * fg + 2 * blk + (pp >> 1);
        wa[i][t] = 16384 + 256 * kr + ((ch ^ ((q << 2) | ((2 * hi + t) & 3))) << 4) + 8 * (pp & 1);
      }
    }
  }
  f32x16 acc0 = {}, acc1 = {};
  float s0 = 0.f, q0 = 0.f, s1 = 0.f, q1 = 0.f, cs = 0.f;
  Frags fA, fB;
#define ITER(s, vm, cur, nxt) do { WAITV(vm); BAR(); if ((s) + 4 < 10) STAGE((s) + 4); \
    __builtin_amdgcn_sched_barrier(0); \
    g1_read(L, (((s) + 1) & 3) * 32768, xa, wa, nxt); g1_mma(cur, acc0, acc1, s0, q0, s1, q1, cs); g1_sched(); \
    __builtin_amdgcn_sched_barrier(0); } while (0)
  WAITV(15); BAR();
  g1_read(L, 0, xa, wa, fA);
  ITER(0, 11, fA, fB);
  STAMP(6);
  ITER(1, 11, fB, fA);
  ITER(2, 11, fA, fB);
  STAMP(7);
  ITER(3, 8, fB, fA);
  ITER(4, 8, fA, fB);
  STAMP(8);
  ITER(5, 8, fB, fA);
  ITER(6, 8, fA, fB);
  STAMP(9);
  ITER(7, 4, fB, fA);
  ITER(8, 0, fA, fB);
  g1_mma(fB, acc0, acc1, s0, q0, s1, q1, cs);
  STAMP(10);
  {
    const int tok = 64 * th + (lane & 31);
    *(LAS f32x2*)(L + XST + ((fg * 2 + hi) * 128 + tok) * 8) = f32x2{s0, q0};
    *(LAS f32x2*)(L + XST + ((fg * 2 + hi) * 128 + tok + 32) * 8) = f32x2{s1, q1};
    *(LAS float*)(L + XCS + ((th * 2 + hi) * 128 + 32 * fg + (lane & 31)) * 4) = cs;
  }
  BAR();
  if (w < 2) {
    const int tok = threadIdx.x;
    float s = 0.f, q = 0.f;
#pragma unroll
    for (int i = 0; i < 8; ++i) { f32x2 t = *(LAS f32x2*)(L + XST + (i * 128 + tok) * 8); s += t.x; q += t.y; }
    const float mean = s * (1.f / D);
    const float var = fmaxf(q * (1.f / D) - mean * mean, 0.f);
    const float mbf = rbf(mean);
    const float den = rbf(__builtin_sqrtf(rbf(rbf(var) + 1e-5f)));
    const f32x2 st = f32x2{mbf, 1.f / den};
    *(LAS f32x2*)(L + XFS + tok * 8) = st;
    if (j == 0) __builtin_amdgcn_raw_buffer_store_b64(__builtin_bit_cast(v2u, st), rws, ST_OFF + (p * 128 + tok) * 8, 0, 0);
  } else if (w < 4) {
    const int f = threadIdx.x - 128;
    float s = 0.f;
#pragma unroll
    for (int i = 0; i < 4; ++i) s += *(LAS float*)(L + XCS + (i * 128 + f) * 4);
    *(LAS float*)(L + XFC + f * 4) = s;
  }
  BAR();
  STAMP(11);
  {
    const int ks = 4 * j + fg;
    if (ks < KS2) {
      f32x4 c4[4];
#pragma unroll
      for (int g = 0; g < 4; ++g) c4[g] = *(LAS f32x4*)(L + XFC + (32 * fg + 8 * g + 4 * hi) * 4);
#pragma unroll
      for (int tt = 0; tt < 2; ++tt) {
        const int tok = 64 * th + 32 * tt + (lane & 31);
        const f32x2 st = *(LAS f32x2*)(L + XFS + tok * 8);
        const f32x16& a = tt ? acc1 : acc0;
        unsigned pk[8];
#pragma unroll
        for (int e = 0; e < 8; ++e) {
          float v0 = (a[2 * e] - st.x * c4[e >> 1][(2 * e) & 3]) * st.y;
          float v1 = (a[2 * e + 1] - st.x * c4[e >> 1][(2 * e + 1) & 3]) * st.y;
          unsigned hb = pack2(v0, v1);
          pk[e] = pack2(silu_f(bflo(hb)), silu_f(bfhi(hb)));
        }
        const int mb2 = 2 * p + th, mt = 2 * tt + ((lane >> 4) & 1);
        const int off = HP_OFF + mb2 * HP_MB + ks * 4096 + mt * 1024 + ((2 * hi) * 16 + (lane & 15)) * 16;
        __builtin_amdgcn_raw_buffer_store_b128(v4u{pk[0], pk[1], pk[2], pk[3]}, rws, off, 0, 0);
        __builtin_amdgcn_raw_buffer_store_b128(v4u{pk[4], pk[5], pk[6], pk[7]}, rws, off + 256, 0, 0);
      }
    }
  }
  STAMP(12);
  WAITV(0);
  STAMP(13);
  STAMP_OUT(0);
}

constexpr int CH_HA = 0, CH_HB = 4224, CH_R = 8448, CH_RS = 4640, CH_BYTES = CH_R + 4 * CH_RS;

template <int TS>
DI void conv_body(LAS char* pa, LAS char* pb, f32x4 (&acc)[4]) {
  constexpr int NA = TS == 0 ? 8 : 6;
#pragma unroll 1
  for (int r = 0; r < 8; ++r) {
    v4u A[NA], B[NA];
#pragma unroll
    for (int a = 0; a < NA; ++a) { A[a] = ldsr128(pa + a * 512); B[a] = ldsr128(pb + (7 - a) * 512); }
#pragma unroll
    for (int ti = 0; ti < 4; ++ti) {
      const int n = TS == 0 ? (ti == 0 ? 0 : ti == 1 ? 1 : ti == 2 ? 6 : 7) : 2 + ti;
#pragma unroll
      for (int a = 0; a <= n; ++a) acc[ti] = mfma16(A[a], B[n - a], acc[ti]);
    }
    pa += 64; pb += 64;
  }
}

DI void conv_stage(LAS char* Lc, const v4u (&hv)[2], const v4u (&rv)[2], int half, int lane) {
  if (half == 0) {
    if (lane < 16) *(LAS v4u*)(Lc + (lane >> 3) * CH_HB + (lane & 7) * 16) = v4u{0, 0, 0, 0};
  } else {
#pragma unroll
    for (int i = 0; i < 2; ++i) {
      const int n = lane + 64 * i;
      if (n < 100) { const int cp = n / 25, sl = n - cp * 25; *(LAS v4u*)(Lc + CH_R + cp * CH_RS + 4080 + sl * 16) = v4u{0, 0, 0, 0}; }
    }
  }
#pragma unroll
  for (int i = 0; i < 2; ++i) {
    const int g = half * 128 + i * 64 + lane;
    const v4u h = hv[i], u = rv[i];
    *(LAS v4u*)(Lc + CH_HB + 112 + g * 16) = h;
    LAS char* a = Lc + CH_HA + 114 + g * 16;
    *(LAS u16*)(a) = (u16)(h.x & 0xffff);
    *(LAS unsigned*)(a + 2) = __builtin_amdgcn_alignbit(h.y, h.x, 16);
    *(LAS v2u*)(a + 6) = v2u{__builtin_amdgcn_alignbit(h.z, h.y, 16), __builtin_amdgcn_alignbit(h.w, h.z, 16)};
    *(LAS u16*)(a + 14) = (u16)(h.w >> 16);
    LAS char* rb = Lc + CH_R + g * 16;
    *(LAS v4u*)(rb) = u;
    { LAS char* q = rb + CH_RS - 4; *(LAS unsigned*)(q) = u.x; *(LAS v2u*)(q + 4) = v2u{u.y, u.z}; *(LAS unsigned*)(q + 12) = u.w; }
    { LAS char* q = rb + 2 * CH_RS - 8; *(LAS v2u*)(q) = v2u{u.x, u.y}; *(LAS v2u*)(q + 8) = v2u{u.z, u.w}; }
    { LAS char* q = rb + 3 * CH_RS - 12; *(LAS unsigned*)(q) = u.x; *(LAS v2u*)(q + 4) = v2u{u.y, u.z}; *(LAS unsigned*)(q + 12) = u.w; }
  }
}

__global__ __launch_bounds__(256, 2) void kb(const u16* __restrict__ hk, const u16* __restrict__ Dv, char* __restrict__ ws) {
  __shared__ __attribute__((aligned(16))) char smem[2 * CH_BYTES];
  LAS char* L = (LAS char*)smem;
  const int lane = threadIdx.x & 63;
  const int cw = __builtin_amdgcn_readfirstlane(threadIdx.x >> 6);
  const int d = blockIdx.x * 2 + (cw >> 1), ts = cw & 1;
  const __amdgpu_buffer_rsrc_t rws = mkrsrc(ws, WS_BYTES);
  const __amdgpu_buffer_rsrc_t rh = mkrsrc(hk, D * S * 2);
  LAS char* Lc = L + (cw >> 1) * CH_BYTES;
  STAMP_DECL; STAMP(0);
  v4u hv[2], rv[2];
#pragma unroll
  for (int i = 0; i < 2; ++i) {
    hv[i] = __builtin_amdgcn_raw_buffer_load_b128(rh, d * 4096 + ts * 2048 + i * 1024 + lane * 16, 0, 0);
    rv[i] = __builtin_amdgcn_raw_buffer_load_b128(rws, R_OFF + d * 4096 + ts * 2048 + i * 1024 + lane * 16, 0, 0);
  }
  const float dv = bflo(Dv[d]);
  WAITV(0); STAMP(1);
  conv_stage(Lc, hv, rv, ts, lane);
  STAMP(2);
  BAR();
  STAMP(3);
  const int e = lane & 15, kq = lane >> 4;
  LAS char* pa = Lc + ((e & 1) ? CH_HB : CH_HA) + (kq + (e >> 1)) * 16;
  LAS char* pb = Lc + CH_R + (3 - (e & 3)) * CH_RS + (248 - 32 * 7 - 8 * (e >> 2) + kq) * 16;
  f32x4 acc[4] = {};
  if (ts == 0) conv_body<0>(pa, pb, acc); else conv_body<1>(pa, pb, acc);
  asm volatile("" :: "v"(acc[0]), "v"(acc[1]), "v"(acc[2]), "v"(acc[3]));
  STAMP(4);
#pragma unroll
  for (int ti = 0; ti < 4; ++ti) {
    const int n = ts == 0 ? (ti < 2 ? ti : ti + 4) : 2 + ti;
    const int t = 256 * n + 64 * (e >> 2) + 2 * (e & 3) + 16 * kq;
    const unsigned ua = *(LAS unsigned*)(Lc + CH_R + 2 * (2046 - t));
    const unsigned ub = *(LAS unsigned*)(Lc + CH_R + 2 * (2038 - t));
    const unsigned c01 = pack2(acc[ti][0], acc[ti][1]), c23 = pack2(acc[ti][2], acc[ti][3]);
    const unsigned d01 = pack2(bfhi(ua) * dv, bflo(ua) * dv), d23 = pack2(bfhi(ub) * dv, bflo(ub) * dv);
    v2u o;
    o.x = pack2(bflo(c01) + bflo(d01), bfhi(c01) + bfhi(d01));
    o.y = pack2(bflo(c23) + bflo(d23), bfhi(c23) + bfhi(d23));
    const int off = Y_OFF + d * 4096 + (256 * n + 64 * (e >> 2) + 16 * kq + 4 * (e & 3)) * 2;
    __builtin_amdgcn_raw_buffer_store_b64(o, rws, off, 0, 0);
  }
  STAMP(5);
  WAITV(0);
  STAMP(6);
  STAMP_OUT(1);
}

constexpr int C_RED = 0, C_Y = 81920, C_LDS = C_Y + 80 * 144;

__global__ __launch_bounds__(512, 2) void kc(const u16* __restrict__ x, char* __restrict__ ws, u16* __restrict__ out) {
  __shared__ __attribute__((aligned(16))) char smem[C_LDS];
  LAS char* L = (LAS char*)smem;
  const int b = blockIdx.x, xcd = b & 7, r = b >> 3;
  const int p = 2 * xcd + (r >> 4), j = r & 15, mb = 2 * p + (j >> 3), nb = j & 7;
  const int lane = threadIdx.x & 63;
  const int w = __builtin_amdgcn_readfirstlane(threadIdx.x >> 6);
  const __amdgpu_buffer_rsrc_t rws = mkrsrc(ws, WS_BYTES);
  STAMP_DECL; STAMP(0);
  if (w < 4) {
    const __amdgpu_buffer_rsrc_t ra = mkrsrc(ws + HP_OFF + mb * HP_MB, HP_MB);
    const __amdgpu_buffer_rsrc_t rb = mkrsrc(ws + W2P_OFF + nb * W2P_NB, W2P_NB);
    constexpr int WIN = 4, NKS = 14;
    v4u fa[WIN][4], fb[WIN][5];
    f32x4 acc[4][5] = {};
    const int va = lane * 16 + w * 4096, vb = lane * 16 + w * 5120;
#define LOADK(slot, i) do { \
    _Pragma("unroll") for (int m = 0; m < 4; ++m) fa[slot][m] = __builtin_amdgcn_raw_buffer_load_b128(ra, va + m * 1024, (i) * 4 * 4096, 0); \
    _Pragma("unroll") for (int n = 0; n < 5; ++n) fb[slot][n] = __builtin_amdgcn_raw_buffer_load_b128(rb, vb + (n & 3) * 1024, (i) * 4 * 5120 + (n >> 2) * 4096, 0); \
  } while (0)
#pragma unroll
    for (int i = 0; i < WIN; ++i) LOADK(i, i);
    __builtin_amdgcn_sched_barrier(0);
#pragma unroll
    for (int i = 0; i < NKS; ++i) {
#pragma unroll
      for (int m = 0; m < 4; ++m)
#pragma unroll
        for (int n = 0; n < 5; ++n) acc[m][n] = mfma16(fb[i % WIN][n], fa[i % WIN][m], acc[m][n]);
      __builtin_amdgcn_sched_barrier(0);
      if (i + WIN < NKS) LOADK(i % WIN, i + WIN);
      __builtin_amdgcn_sched_barrier(0);
    }
    STAMP(1);
#pragma unroll
    for (int m = 0; m < 4; ++m)
#pragma unroll
      for (int n = 0; n < 5; ++n) *(LAS f32x4*)(L + C_RED + ((w * 4 + m) * 5 + n) * 1024 + lane * 16) = acc[m][n];
  } else {
    const int aw = w - 4;
#pragma unroll
    for (int i = 0; i < 3; ++i) {
      const int ri = aw + 4 * i;
      if (ri < 10) {
        const int ch = 8 * ri + (lane >> 3), cc = lane & 7;
        v4u yv = __builtin_amdgcn_raw_buffer_load_b128(rws, Y_OFF + (80 * nb + ch) * 4096 + mb * 128 + cc * 16, 0, 0);
        *(LAS v4u*)(L + C_Y + ch * 144 + cc * 16) = yv;
      }
    }
  }
  STAMP(2);
  BAR();
  STAMP(3);
  {
    const int mt = w >> 1, tokl = lane & 15, q = lane >> 4;
    const int t = 64 * mb + 16 * mt + tokl;
    const f32x2 st = __builtin_bit_cast(f32x2, __builtin_amdgcn_raw_buffer_load_b64(rws, ST_OFF + t * 8, 0, 0));
    const int pos = 16 * mt + 4 * ((tokl & 7) >> 1) + (tokl & 1) + 2 * (tokl >> 3);
#pragma unroll
    for (int i = 0; i < 3; ++i) {
      const int nt = (w & 1) * 3 + i;
      if (nt < 5) {
        f32x4 o = {};
#pragma unroll
        for (int ww = 0; ww < 4; ++ww) o += *(LAS f32x4*)(L + C_RED + ((ww * 4 + mt) * 5 + nt) * 1024 + lane * 16);
        const int dl = 16 * nt + 4 * q;
        const v2u xv = *(const v2u*)(x + (size_t)t * D + 80 * nb + dl);
        const float xf[4] = {bflo(xv.x), bfhi(xv.x), bflo(xv.y), bfhi(xv.y)};
        float res[4];
#pragma unroll
        for (int e = 0; e < 4; ++e) {
          const float y = bflo(*(LAS u16*)(L + C_Y + (dl + e) * 144 + pos * 2));
          const float xn = rbf(rbf(xf[e] - st.x) * st.y);
          const float s = rbf(xn * y);
          const float g = rbf(silu_f(rbf(o[e])));
          res[e] = xf[e] + rbf(s * g);
        }
        *(v2u*)(out + (size_t)t * D + 80 * nb + dl) = v2u{pack2(res[0], res[1]), pack2(res[2], res[3])};
      }
    }
  }
  STAMP(4);
  WAITV(0);
  STAMP(5);
  STAMP_OUT(2);
}

extern "C" void kernel_launch(void* const* d_in, const int* in_sizes, int n_in,
                              void* d_out, int out_size, void* d_ws, size_t ws_size,
                              hipStream_t stream) {
  const u16* q = (const u16*)d_in[0];
  const u16* k = (const u16*)d_in[1];
  const u16* v = (const u16*)d_in[2];
  const u16* sk = (const u16*)d_in[3];
  const u16* hk = (const u16*)d_in[4];
  const u16* Dv = (const u16*)d_in[5];
  const u16* w1 = (const u16*)d_in[7];
  const u16* w2 = (const u16*)d_in[8];
  char* ws = (char*)d_ws;
  ka<<<256, 512, 0, stream>>>(q, k, v, sk, w1, w2, ws);
  kb<<<320, 256, 0, stream>>>(hk, Dv, ws);
  kc<<<256, 512, 0, stream>>>(q, ws, (u16*)d_out);
}
```

```cpp
#include <hip/hip_runtime.h>
#include <stdint.h>

typedef unsigned short u16;
typedef unsigned v4u __attribute__((ext_vector_type(4)));
typedef unsigned v2u __attribute__((ext_vector_type(2)));
typedef float f32x2 __attribute__((ext_vector_type(2)));
typedef float f32x4 __attribute__((ext_vector_type(4)));
typedef float f32x16 __attribute__((ext_vector_type(16)));
typedef short bf16x8 __attribute__((ext_vector_type(8)));
typedef short s16x4 __attribute__((ext_vector_type(4)));
typedef __bf16 bf16x2 __attribute__((ext_vector_type(2)));
#define LAS __attribute__((address_space(3)))
#define GAS __attribute__((address_space(1)))
#define DI __device__ __forceinline__
#define RLX_AGENT __ATOMIC_RELAXED, __HIP_MEMORY_SCOPE_AGENT
#define RLX_WG __ATOMIC_RELAXED, __HIP_MEMORY_SCOPE_WORKGROUP

constexpr int S = 2048, D = 640, F = 1728;
constexpr int KS2 = 54;
constexpr int FL_GEN = 0, FL_P = 4096, FL_H = 12288, FL_Y = 16384;
constexpr int ST_OFF = 32768;
constexpr int CS_OFF = ST_OFF + S * 8;
constexpr int HP_OFF = 65536;
constexpr int HP_MB = KS2 * 4096;
constexpr int W2P_OFF = HP_OFF + 32 * HP_MB;
constexpr int W2P_NB = KS2 * 5120;
constexpr int R_OFF = W2P_OFF + 8 * W2P_NB;
constexpr int Y_OFF = R_OFF + D * S * 2;
constexpr int WS_BYTES = Y_OFF + D * S * 2;
constexpr unsigned RSRC3 = 0x00020000u;
constexpr int SC1 = 16;
constexpr unsigned SPIN_CAP = 1u << 18;

DI float bflo(unsigned u) { return __uint_as_float(u << 16); }
DI float bfhi(unsigned u) { return __uint_as_float(u & 0xffff0000u); }
DI unsigned pack2(float a, float b) {
  bf16x2 r = __builtin_convertvector(f32x2{a, b}, bf16x2);
  return __builtin_bit_cast(unsigned, r);
}
DI float rbf(float a) { return bflo(pack2(a, a)); }
DI float silu_f(float x) { return x * __builtin_amdgcn_rcpf(1.f + __builtin_amdgcn_exp2f(-1.44269504088896f * x)); }
DI f32x4 mfma16(v4u a, v4u b, f32x4 c) {
  return __builtin_amdgcn_mfma_f32_16x16x32_bf16(__builtin_bit_cast(bf16x8, a), __builtin_bit_cast(bf16x8, b), c, 0, 0, 0);
}
DI f32x16 mfma32(v4u a, v4u b, f32x16 c) {
  return __builtin_amdgcn_mfma_f32_32x32x16_bf16(__builtin_bit_cast(bf16x8, a), __builtin_bit_cast(bf16x8, b), c, 0, 0, 0);
}
DI float dot2(unsigned a, unsigned b, float c) {
  return __builtin_amdgcn_fdot2_f32_bf16(__builtin_bit_cast(bf16x2, a), __builtin_bit_cast(bf16x2, b), c, false);
}
DI v2u trread(LAS char* p) { return __builtin_bit_cast(v2u, __builtin_amdgcn_ds_read_tr16_b64_v4i16((LAS s16x4*)p)); }
DI v4u ldsr128(LAS char* p) { return *(LAS v4u*)p; }
DI __amdgpu_buffer_rsrc_t mkrsrc(const void* p, int bytes) { return __builtin_amdgcn_make_buffer_rsrc((void*)p, 0, bytes, RSRC3); }
DI void glds_asm(__amdgpu_buffer_rsrc_t rs, unsigned ldsa, int voff, int soff) {
  asm volatile("s_nop 4\n\ts_mov_b32 m0, %0\n\ts_nop 0\n\tbuffer_load_dwordx4 %1, %2, %3 offen lds"
               :: "s"(ldsa), "v"(voff), "s"(rs), "s"(soff) : "m0", "memory");
}
#define GLDS(rs, ldsp, voff, soff) glds_asm(rs, (unsigned)(uintptr_t)(ldsp), voff, soff)
#define WAITV(n) asm volatile("s_waitcnt vmcnt(" #n ")" ::: "memory")
#define WAITL() asm volatile("s_waitcnt lgkmcnt(0)" ::: "memory")
#define BAR() do { WAITL(); __builtin_amdgcn_s_barrier(); asm volatile("" ::: "memory"); } while (0)
#define SB() __builtin_amdgcn_sched_barrier(0)

#define STAMP_DECL
#define STAMP(i)
#define STAMP_OUT(kid)

#define ROT_EXPR 0
#define WA_N 3
#define POLL_SLEEP 16
#define AUX_PRIO 0
#define MMA_PRIO 2
#define CONV_START 44
constexpr int NBUF = 3;
constexpr int CH_HA = 0, CH_HB = 4224, CH_R = 8448, CH_RS = 4512, CH_BYTES = CH_R + 4 * CH_RS;
constexpr int CV = 98304;
constexpr int CV3 = 32768;
constexpr int SC3 = 65536;
constexpr int PK = CV, PV = PK + 11264, PW = PV + 10240, PS = PW + 10240, PO = PS + 1024;
constexpr int EY = CV, EX = EY + 80 * 144, ES = EX + 10240;
constexpr int XFS = CV + 2 * CH_BYTES;
constexpr int XFC = XFS + 1024;
constexpr int CNT = XFC + 512;
constexpr int C_AUX = CNT, C_G2 = CNT + 4, C_Y = CNT + 8, C_H = CNT + 12, C_MMA = CNT + 16, C_S = CNT + 20, C_P = CNT + 24,
              C_ST = CNT + 28, C_HR = CNT + 32, C_YR = CNT + 36;
constexpr int LDS_BYTES = CNT + 64;
static_assert(PO + 10240 <= XFS && ES + 20480 <= XFS && LDS_BYTES <= 163840, "lds");
constexpr int PSTR = 336, PART = 64 * PSTR;

DI void lds_add(LAS char* c, unsigned v, int lane) {
  if (lane == 0) __hip_atomic_fetch_add((LAS unsigned*)c, v, RLX_WG);
}
DI void lds_wait(LAS char* c, unsigned target) {
  while ((unsigned)__builtin_amdgcn_readfirstlane(__hip_atomic_load((LAS unsigned*)c, RLX_WG)) < target) {}
  asm volatile("" ::: "memory");
}
DI void lds_wait_sleep(LAS char* c, unsigned target) {
  while ((unsigned)__builtin_amdgcn_readfirstlane(__hip_atomic_load((LAS unsigned*)c, RLX_WG)) < target) __builtin_amdgcn_s_sleep(1);
  asm volatile("" ::: "memory");
}
DI unsigned flag_load(char* ws, int off, int n, int lane) {
  return __hip_atomic_load((GAS unsigned*)(ws + off) + (lane < n ? lane : 0), RLX_AGENT);
}
DI void poll_flags(char* ws, int off, int n, unsigned tag, int lane) {
#pragma unroll 1
  for (unsigned spins = 0; spins < SPIN_CAP; ++spins) {
    const unsigned v = flag_load(ws, off, n, lane);
    if (__all(v == tag)) break;
    __builtin_amdgcn_s_sleep(POLL_SLEEP);
  }
  __builtin_amdgcn_fence(__ATOMIC_ACQUIRE, "wavefront");
}
DI void set_flag(char* ws, int off, unsigned tag, int lane) {
  if (lane == 0) __hip_atomic_store((GAS unsigned*)(ws + off), tag, RLX_AGENT);
}

DI unsigned opaque0() { unsigned z; asm volatile("v_mov_b32 %0, 0" : "=v"(z)); return z; }
struct Frags { v4u x[4][2], w[4][2]; };

template <int TS> DI void conv_ld(LAS char* pa, LAS char* pb, v4u (&A)[8], v4u (&B)[8]) {
  constexpr int NA = TS == 0 ? 8 : 6;
#pragma unroll
  for (int a = 0; a < NA; ++a) { A[a] = ldsr128(pa + a * 512); B[a] = ldsr128(pb + (7 - a) * 512); }
}
template <int TS> DI void conv_mm(const v4u (&A)[8], const v4u (&B)[8], f32x4 (&acc)[4]) {
#pragma unroll
  for (int ti = 0; ti < 4; ++ti) {
    const int n = TS == 0 ? (ti == 0 ? 0 : ti == 1 ? 1 : ti == 2 ? 6 : 7) : 2 + ti;
#pragma unroll
    for (int a = 0; a <= n; ++a) acc[ti] = mfma16(A[a], B[n - a], acc[ti]);
  }
}
DI void conv_sched() {
#pragma unroll
  for (int i = 0; i < 16; ++i) { __builtin_amdgcn_sched_group_barrier(0x8, 1, 0); __builtin_amdgcn_sched_group_barrier(0x100, 1, 0); }
  __builtin_amdgcn_sched_group_barrier(0x8, 2, 0);
}
template <int TS> DI void conv8(LAS char* pa, LAS char* pb, f32x4 (&acc)[4]) {
  v4u A0[8], B0[8], A1[8], B1[8];
  conv_ld<TS>(pa, pb, A0, B0);
  SB();
#pragma unroll 1
  for (int r = 0; r < 8; r += 2) {
    conv_ld<TS>(pa + 64, pb + 64, A1, B1); conv_mm<TS>(A0, B0, acc); conv_sched(); SB();
    pa += 128; pb += 128;
    conv_ld<TS>(pa, pb, A0, B0); conv_mm<TS>(A1, B1, acc); conv_sched(); SB();
  }
}
template <int TS> DI void conv2(LAS char* pa, LAS char* pb, f32x4 (&acc)[4]) {
#pragma unroll 1
  for (int r = 0; r < 2; ++r) {
    v4u A[8], B[8];
    conv_ld<TS>(pa, pb, A, B); conv_mm<TS>(A, B, acc);
    pa += 64; pb += 64;
  }
}
DI void stage_h(LAS char* Lc, v4u h, int g) {
  *(LAS v4u*)(Lc + CH_HB + 112 + g * 16) = h;
  LAS char* a = Lc + CH_HA + 114 + g * 16;
  *(LAS u16*)(a) = (u16)(h.x & 0xffff);
  *(LAS unsigned*)(a + 2) = __builtin_amdgcn_alignbit(h.y, h.x, 16);
  *(LAS v2u*)(a + 6) = v2u{__builtin_amdgcn_alignbit(h.z, h.y, 16), __builtin_amdgcn_alignbit(h.w, h.z, 16)};
  *(LAS u16*)(a + 14) = (u16)(h.w >> 16);
}
DI void stage_r(LAS char* Lc, v4u u, int g) {
  LAS char* rb = Lc + CH_R + g * 16;
  *(LAS v4u*)(rb) = u;
  { LAS char* q = rb + CH_RS - 4; *(LAS unsigned*)(q) = u.x; *(LAS v2u*)(q + 4) = v2u{u.y, u.z}; *(LAS unsigned*)(q + 12) = u.w; }
  { LAS char* q = rb + 2 * CH_RS - 8; *(LAS v2u*)(q) = v2u{u.x, u.y}; *(LAS v2u*)(q + 8) = v2u{u.z, u.w}; }
  { LAS char* q = rb + 3 * CH_RS - 12; *(LAS unsigned*)(q) = u.x; *(LAS v2u*)(q + 4) = v2u{u.y, u.z}; *(LAS unsigned*)(q + 12) = u.w; }
}
DI void conv_out(LAS char* Lc, f32x4 a, int n, int d, float dv, int e, int kq, __amdgpu_buffer_rsrc_t rws) {
  const int t = 256 * n + 64 * (e >> 2) + 2 * (e & 3) + 16 * kq;
  const unsigned ua = *(LAS unsigned*)(Lc + CH_R + 2 * (2046 - t));
  const unsigned ub = *(LAS unsigned*)(Lc + CH_R + 2 * (2038 - t));
  const unsigned c01 = pack2(a[0], a[1]), c23 = pack2(a[2], a[3]);
  const unsigned d01 = pack2(bfhi(ua) * dv, bflo(ua) * dv), d23 = pack2(bfhi(ub) * dv, bflo(ub) * dv);
  v2u o;
  o.x = pack2(bflo(c01) + bflo(d01), bfhi(c01) + bfhi(d01));
  o.y = pack2(bflo(c23) + bflo(d23), bfhi(c23) + bfhi(d23));
  const int off = Y_OFF + d * 4096 + (256 * n + 64 * (e >> 2) + 16 * kq + 4 * (e & 3)) * 2;
  __builtin_amdgcn_raw_buffer_store_b64(o, rws, off, 0, SC1);
}

__global__ __launch_bounds__(512, 2) void fused(const u16* __restrict__ x, const u16* __restrict__ kk, const u16* __restrict__ vv,
                                                const u16* __restrict__ sk, const u16* __restrict__ hk, const u16* __restrict__ Dv,
                                                const u16* __restrict__ w1, const u16* __restrict__ w2,
                                                char* ws, u16* out) {
  __shared__ __attribute__((aligned(16))) char smem[LDS_BYTES];
  LAS char* L = (LAS char*)smem;
  const int b = blockIdx.x, xcd = b & 7, r = b >> 3;
  const int rg = xcd >> 1, hf = xcd & 1, pi = r >> 3, jj = r & 7;
  const int p = 4 * rg + pi, j = 7 * hf + jj;
  const bool g1 = jj < 7;
  const int mbl = 2 * pi + ((r >> 2) & 1), nbl = r & 3;
  const int mb = 8 * rg + mbl, nb = 4 * hf + nbl;
  const int lane = threadIdx.x & 63;
  const int w = __builtin_amdgcn_readfirstlane(threadIdx.x >> 6);
  const int hi = lane >> 5, aw = w & 3;
  STAMP_DECL; STAMP(0);
  const __amdgpu_buffer_rsrc_t rws = mkrsrc(ws, WS_BYTES);
  const unsigned tag = *(const __attribute__((address_space(4))) unsigned*)(ws + FL_GEN) * 0x9E3779B1u + 0x85EBCA6Bu;
  if (threadIdx.x < 16) *(LAS unsigned*)(L + CNT + threadIdx.x * 4) = 0u;
  const int dpair = 80 * nb + 2 * mb, d3 = 80 * nb + 64 + (mb >> 1), ts3 = mb & 1;
  const int e = lane & 15, kq = lane >> 4;
  const int aoff = ((e & 1) ? CH_HB : CH_HA) + (kq + (e >> 1)) * 16;
  const int boff = CH_R + (3 - (e & 3)) * CH_RS + (248 - 32 * 7 - 8 * (e >> 2) + kq) * 16;

  if (w >= 4) {
    __builtin_amdgcn_s_setprio(AUX_PRIO);
    {
      const int t0 = 64 * mb, d0b = 160 * nb;
#pragma unroll
      for (int i = 0; i < 8; ++i) {
        const int s = 4 * i + aw;
        const void* base; int bytes, n0, rb;
        if (s < 11) { base = kk; bytes = S * D * 2; n0 = s * 64; rb = t0 - 4; }
        else if (s < 21) { base = vv; bytes = S * D * 2; n0 = (s - 11) * 64; rb = t0; }
        else if (s < 31) { base = w2; bytes = F * D * 2; n0 = (s - 21) * 64; rb = 32 * mb; }
        else { base = sk; bytes = D * 8; n0 = 0; rb = 0; }
        const int n = n0 + lane;
        int row = n / 10; const int c = n - row * 10;
        if (s >= 21 && row >= 32) row += 992;
        int voff = (rb + row) * 1280 + d0b + c * 16;
        if (s == 31) voff = nb * 640 + lane * 16;
        GLDS(mkrsrc(base, bytes), L + PK + s * 1024, voff, 0);
      }
    }
    BAR();
    STAMP(1);
    WAITV(0);
    unsigned ab = 0;
#define AUXBAR() do { WAITL(); lds_add(L + C_AUX, 1u, lane); ab += 4; lds_wait(L + C_AUX, ab); } while (0)
    AUXBAR();
    STAMP(2);
    const __amdgpu_buffer_rsrc_t rh = mkrsrc(hk, D * S * 2);
    v4u cu[3];
    cu[0] = __builtin_amdgcn_raw_buffer_load_b128(rh, dpair * 4096 + aw * 1024 + lane * 16, 0, 0);
    cu[1] = __builtin_amdgcn_raw_buffer_load_b128(rh, dpair * 4096 + 4096 + aw * 1024 + lane * 16, 0, 0);
    cu[2] = __builtin_amdgcn_raw_buffer_load_b128(rh, d3 * 4096 + aw * 1024 + lane * 16, 0, 0);
    {
      const int g = lane >> 4, q = (lane & 15) >> 2, pp = lane & 3;
      const int tl = 16 * aw + 4 * g;
#pragma unroll
      for (int cg = 0; cg < 5; ++cg) {
        LAS char* ka_ = L + PK + (tl + q) * 160 + cg * 32 + pp * 8;
        v2u k0 = trread(ka_), k1 = trread(ka_ + 4 * 160);
        v2u v0 = trread(L + PV + (tl + q) * 160 + cg * 32 + pp * 8);
        v2u sw = *(LAS v2u*)(L + PS + (cg * 16 + (lane & 15)) * 8);
        float kf[8] = {bflo(k0.x), bfhi(k0.x), bflo(k0.y), bfhi(k0.y), bflo(k1.x), bfhi(k1.x), bflo(k1.y), bfhi(k1.y)};
        float vf[4] = {bflo(v0.x), bfhi(v0.x), bflo(v0.y), bfhi(v0.y)};
        const float c0 = bflo(sw.x), c1 = bfhi(sw.x), c2 = bflo(sw.y), c3 = bfhi(sw.y);
        float u[4];
#pragma unroll
        for (int e2 = 0; e2 < 4; e2 += 2) {
          unsigned a = pack2(kf[e2 + 1] * c3, kf[e2 + 2] * c3);
          unsigned t = pack2(kf[e2 + 2] * c2, kf[e2 + 3] * c2);
          a = pack2(bflo(a) + bflo(t), bfhi(a) + bfhi(t));
          t = pack2(kf[e2 + 3] * c1, kf[e2 + 4] * c1);
          a = pack2(bflo(a) + bflo(t), bfhi(a) + bfhi(t));
          t = pack2(kf[e2 + 4] * c0, kf[e2 + 5] * c0);
          a = pack2(bflo(a) + bflo(t), bfhi(a) + bfhi(t));
          u[e2] = bflo(a) * vf[e2]; u[e2 + 1] = bfhi(a) * vf[e2 + 1];
        }
        v2u o = v2u{pack2(u[3], u[2]), pack2(u[1], u[0])};
        const int ch = cg * 16 + (lane & 15);
        const int pos8 = (60 - tl) >> 2;
        *(LAS v2u*)(L + PO + ch * 128 + ((((pos8 >> 1) ^ (ch & 7)) << 4) | ((pos8 & 1) << 3))) = o;
      }
      STAMP(3);
      v4u wf[3];
#pragma unroll
      for (int i = 0; i < 3; ++i) {
        int fi = aw + 4 * i; if (fi > 9) fi = 9;
        const int half = fi / 5, nt = fi - half * 5;
        const int r0 = 16 * (g & 1) + 4 * (g >> 1);
        LAS char* a = L + PW + half * 5120 + (r0 + q) * 160 + nt * 32 + pp * 8;
        v2u lo = trread(a), hi2 = trread(a + 8 * 160);
        wf[i] = v4u{lo.x, lo.y, hi2.x, hi2.y};
      }
      AUXBAR();
#pragma unroll
      for (int i = 0; i < 3; ++i) {
        const int ri = aw + 4 * i;
        if (ri < 10) {
          const int ch = 8 * ri + (lane >> 3), cc = lane & 7;
          v4u val = ldsr128(L + PO + ch * 128 + ((cc ^ (ch & 7)) << 4));
          __builtin_amdgcn_raw_buffer_store_b128(val, rws, R_OFF + (80 * nb + ch) * 4096 + (31 - mb) * 128 + cc * 16, 0, SC1);
          const int half = ri / 5, nt = ri - half * 5, ks = mb + 32 * half;
          __builtin_amdgcn_raw_buffer_store_b128(wf[i], rws, ks < KS2 ? W2P_OFF + nb * W2P_NB + ks * 5120 + nt * 1024 + lane * 16 : -16, 0, SC1);
        }
      }
    }
    STAMP(4);
    WAITV(0);
    AUXBAR();
    if (aw == 0) set_flag(ws, FL_P + (nb * 32 + mb) * 4, tag, lane);
    STAMP(5);
    const int g = aw * 64 + lane;
    if (aw == 0 && lane < 32) *(LAS v4u*)(L + CV + (lane >> 4) * CH_BYTES + ((lane >> 3) & 1) * CH_HB + (lane & 7) * 16) = v4u{0, 0, 0, 0};
    if (aw == 3) {
#pragma unroll
      for (int i = 0; i < 4; ++i) {
        const int n = lane + 64 * i;
        if (n < 200) { const int cp = n / 25, sl = n - cp * 25; *(LAS v4u*)(L + CV + (cp >> 2) * CH_BYTES + CH_R + (cp & 3) * CH_RS + 4080 + sl * 16) = v4u{0, 0, 0, 0}; }
      }
    }
    stage_h(L + CV, cu[0], g);
    stage_h(L + CV + CH_BYTES, cu[1], g);
    if (aw == 1) {
      const int jc = g1 ? j : 0;
      v2u gr[4];
#define GRL(off) __builtin_bit_cast(v2u, __hip_atomic_load((GAS unsigned long long*)(ws + (off)), RLX_AGENT))
#pragma unroll 1
      for (unsigned sp = 0; sp < SPIN_CAP; ++sp) {
        gr[0] = GRL(ST_OFF + (p * 128 + lane) * 8);
        gr[1] = GRL(ST_OFF + (p * 128 + 64 + lane) * 8);
        gr[2] = GRL(CS_OFF + (jc * 128 + lane) * 8);
        gr[3] = GRL(CS_OFF + (jc * 128 + (j == 13 ? 0 : 64) + lane) * 8);
        if (__all(gr[0].y == tag && gr[1].y == tag && gr[2].y == tag && gr[3].y == tag)) break;
        __builtin_amdgcn_s_sleep(POLL_SLEEP);
      }
      *(LAS f32x2*)(L + XFS + lane * 8) = f32x2{bflo(gr[0].x), 1.f / bfhi(gr[0].x)};
      *(LAS f32x2*)(L + XFS + (64 + lane) * 8) = f32x2{bflo(gr[1].x), 1.f / bfhi(gr[1].x)};
      *(LAS unsigned*)(L + XFC + lane * 4) = gr[2].x;
      *(LAS unsigned*)(L + XFC + (64 + lane) * 4) = gr[3].x;
      WAITL();
      lds_add(L + C_S, 1u, lane);
    }
    if (aw == 2) { poll_flags(ws, FL_P + nb * 128, 32, tag, lane); lds_add(L + C_P, 1u, lane); }
    lds_wait_sleep(L + C_P, 1);
    STAMP(6);
    v4u rv[3];
    rv[0] = __builtin_amdgcn_raw_buffer_load_b128(rws, R_OFF + dpair * 4096 + aw * 1024 + lane * 16, 0, SC1);
    rv[1] = __builtin_amdgcn_raw_buffer_load_b128(rws, R_OFF + dpair * 4096 + 4096 + aw * 1024 + lane * 16, 0, SC1);
    rv[2] = __builtin_amdgcn_raw_buffer_load_b128(rws, R_OFF + d3 * 4096 + aw * 1024 + lane * 16, 0, SC1);
    const float dv = bflo(Dv[dpair + (aw >> 1)]);
    stage_r(L + CV, rv[0], g);
    stage_r(L + CV + CH_BYTES, rv[1], g);
    STAMP(7);
    lds_wait_sleep(L + C_MMA, 36);
    if (aw == 0 && lane < 16) *(LAS v4u*)(L + CV3 + (lane >> 3) * CH_HB + (lane & 7) * 16) = v4u{0, 0, 0, 0};
    if (aw == 3) {
#pragma unroll
      for (int i = 0; i < 2; ++i) {
        const int n = lane + 64 * i;
        if (n < 100) { const int cp = n / 25, sl = n - cp * 25; *(LAS v4u*)(L + CV3 + CH_R + cp * CH_RS + 4080 + sl * 16) = v4u{0, 0, 0, 0}; }
      }
    }
    stage_h(L + CV3, cu[2], g);
    stage_r(L + CV3, rv[2], g);
    AUXBAR();
    if (aw == 0) lds_add(L + C_ST, 1u, lane);
    lds_wait_sleep(L + C_MMA, CONV_START);
    STAMP(8);
    {
      LAS char* Lc = L + CV + (aw >> 1) * CH_BYTES;
      const int cts = aw & 1;
      f32x4 acc[4] = {};
      if (cts == 0) conv8<0>(Lc + aoff, Lc + boff, acc); else conv8<1>(Lc + aoff, Lc + boff, acc);
      STAMP(9);
#pragma unroll
      for (int ti = 0; ti < 4; ++ti) conv_out(Lc, acc[ti], cts == 0 ? (ti < 2 ? ti : ti + 4) : 2 + ti, dpair + (aw >> 1), dv, e, kq, rws);
      WAITV(0); WAITL();
      unsigned old = 0;
      if (lane == 0) old = __hip_atomic_fetch_add((LAS unsigned*)(L + C_Y), 1u, RLX_WG);
      if (__builtin_amdgcn_readfirstlane(old) == 7) set_flag(ws, FL_Y + (nb * 32 + mb) * 4, tag, lane);
    }
    STAMP(10);
    AUXBAR();
    {
      const __amdgpu_buffer_rsrc_t rxx = mkrsrc(x, S * D * 2);
#pragma unroll
      for (int i = 0; i < 3; ++i) {
        const int s = aw + 4 * i;
        if (s < 10) {
          const int n = s * 64 + lane; const int row = n / 10, c = n - row * 10;
          GLDS(rxx, L + EX + s * 1024, (64 * mb + row) * 1280 + 160 * nb + c * 16, 0);
        }
      }
    }
    if (aw == 0) { poll_flags(ws, FL_Y + nb * 128, 32, tag, lane); lds_add(L + C_YR, 1u, lane); }
    lds_wait_sleep(L + C_YR, 1);
    STAMP(11);
#pragma unroll
    for (int i = 0; i < 3; ++i) {
      const int ri = aw + 4 * i;
      if (ri < 10) {
        const int ch = 8 * ri + (lane >> 3), cc = lane & 7;
        v4u yv = __builtin_amdgcn_raw_buffer_load_b128(rws, Y_OFF + (80 * nb + ch) * 4096 + mb * 128 + cc * 16, 0, SC1);
        *(LAS v4u*)(L + EY + ch * 144 + cc * 16) = yv;
      }
    }
    WAITV(0);
    AUXBAR();
    STAMP(12);
    {
      const int q = (lane & 15) >> 2, pp = lane & 3, pos = lane & 15;
      const int tok = 16 * aw + 2 * (pos >> 2) + (pos & 1) + 8 * ((pos >> 1) & 1);
      const f32x2 st = *(LAS f32x2*)(L + XFS + ((mb & 1) * 64 + tok) * 8);
#pragma unroll
      for (int i = 0; i < 5; ++i) {
        const int dq = 4 * i + kq;
        const v2u yv = trread(L + EY + (4 * dq + q) * 144 + aw * 32 + pp * 8);
        const v2u xv2 = *(LAS v2u*)(L + EX + tok * 160 + dq * 8);
        const unsigned d01 = pack2(bflo(xv2.x) - st.x, bfhi(xv2.x) - st.x), d23 = pack2(bflo(xv2.y) - st.x, bfhi(xv2.y) - st.x);
        const unsigned n01 = pack2(bflo(d01) * st.y, bfhi(d01) * st.y), n23 = pack2(bflo(d23) * st.y, bfhi(d23) * st.y);
        const unsigned s01 = pack2(bflo(n01) * bflo(yv.x), bfhi(n01) * bfhi(yv.x)), s23 = pack2(bflo(n23) * bflo(yv.y), bfhi(n23) * bfhi(yv.y));
        *(LAS v4u*)(L + ES + (tok * 20 + dq) * 16) = v4u{s01, s23, xv2.x, xv2.y};
      }
    }
    if (b == 0 && aw == 0) {
#pragma unroll 1
      for (int i = 0; i < 4; ++i) poll_flags(ws, FL_P + i * 256, 64, tag, lane);
      set_flag(ws, FL_GEN, tag, lane);
    }
  } else {
    __builtin_amdgcn_s_setprio(MMA_PRIO);
    const int rot2 = ((7 * mbl) / 4 + (7 * nbl) / 2) % 14;
    const __amdgpu_buffer_rsrc_t ra = mkrsrc(ws + HP_OFF + mb * HP_MB, HP_MB);
    const __amdgpu_buffer_rsrc_t rb = mkrsrc(ws + W2P_OFF + nb * W2P_NB, W2P_NB);
    const int va = lane * 16 + w * 4096, vb = lane * 16 + w * 5120;
    constexpr int WA = WA_N, WB = 5, NKS = 14;
    v4u fa[WA][4], fb[WB][5];
#define KIDX(i) ((i) + rot2 >= NKS ? (i) + rot2 - NKS : (i) + rot2)
#define LOADA(i) do { const int so_ = KIDX(i) * 16384; \
    _Pragma("unroll") for (int m = 0; m < 4; ++m) fa[(i) % WA][m] = __builtin_amdgcn_raw_buffer_load_b128(ra, va + m * 1024, so_, SC1); } while (0)
#define LOADB(i) do { const int so_ = KIDX(i) * 20480; \
    _Pragma("unroll") for (int n = 0; n < 5; ++n) fb[(i) % WB][n] = __builtin_amdgcn_raw_buffer_load_b128(rb, vb + (n & 3) * 1024, so_ + (n >> 2) * 4096, SC1); } while (0)
    const __amdgpu_buffer_rsrc_t rw = mkrsrc(w1, D * F * 2);
    if (g1) {
      const __amdgpu_buffer_rsrc_t rx = mkrsrc(x + (size_t)p * 128 * D, 128 * D * 2);
      int vx[2], vw[4];
      {
        const int row = 32 * w + (lane >> 3);
        vx[0] = row * 1280 + (((lane & 7) ^ ((row >> 1) & 7)) << 4);
        vx[1] = (row + 8) * 1280 + (((lane & 7) ^ (((row + 8) >> 1) & 7)) << 4);
#pragma unroll
        for (int ii = 0; ii < 4; ++ii) {
          const int kr = 16 * w + 4 * ii + (lane >> 4);
          vw[ii] = kr * (F * 2) + j * 256 + (((lane & 15) ^ (((kr & 3) << 2) | ((kr >> 2) & 3))) << 4);
        }
      }
      const int rot = ROT_EXPR;
      const unsigned lbase = (unsigned)(uintptr_t)L + w * 4096;
#define KC(s) ((s) + rot >= 10 ? (s) + rot - 10 : (s) + rot)
#define G1LD(s, i) do { \
    if ((i) < 4) glds_asm(rx, lbase + ((s) % NBUF) * 32768 + (i) * 1024, vx[(i) & 1], KC(s) * 128 + ((i) >> 1) * 16 * 1280); \
    else glds_asm(rw, lbase + ((s) % NBUF) * 32768 + 16384 + ((i) - 4) * 1024, vw[((i) - 4) & 3], KC(s) * (64 * F * 2)); \
  } while (0)
#pragma unroll
      for (int i = 0; i < 8; ++i) G1LD(0, i);
      BAR();
#pragma unroll
      for (int i = 0; i < 8; ++i) G1LD(1, i);
      STAMP(1);
      const int fh = w & 1, th = w >> 1;
      int xa[4], wa[2][2];
      {
        const int row = 64 * th + (lane & 31);
        const int c0 = hi ^ ((row >> 1) & 7);
        const int blk = (lane >> 4) & 1, q = (lane & 15) >> 2, pp = lane & 3;
#pragma unroll
        for (int s = 0; s < 4; ++s) xa[s] = row * 128 + (((2 * s) ^ c0) << 4);
#pragma unroll
        for (int ft = 0; ft < 2; ++ft)
#pragma unroll
          for (int t = 0; t < 2; ++t) {
            const int kr = 8 * hi + 4 * t + q;
            const int ch = 4 * (2 * fh + ft) + 2 * blk + (pp >> 1);
            wa[ft][t] = 16384 + 256 * kr + ((ch ^ ((q << 2) | ((2 * hi + t) & 3))) << 4) + 8 * (pp & 1);
          }
      }
      f32x16 acc[2][2] = {};
      Frags fA, fB;
#define RDX(f, bo, s, tt) f.x[s][tt] = ldsr128(L + (bo) + xa[s] + (tt) * 4096)
#define RDW(f, bo, s, ft) do { v2u wl_ = trread(L + (bo) + wa[ft][0] + (s) * 4096), wh_ = trread(L + (bo) + wa[ft][1] + (s) * 4096); f.w[s][ft] = v4u{wl_.x, wl_.y, wh_.x, wh_.y}; } while (0)
#define MM(f, s, ft, tt) acc[ft][tt] = mfma32(f.w[s][ft], f.x[s][tt], acc[ft][tt])
#define ITER(s, vm, cur, nxt) do { \
    asm volatile("s_waitcnt vmcnt(" #vm ") lgkmcnt(0)" ::: "memory"); \
    lds_add(L + C_MMA, 1u, lane); SB(); \
    MM(cur, 0, 0, 0); MM(cur, 0, 0, 1); MM(cur, 0, 1, 0); SB(); \
    lds_wait(L + C_MMA, 4 * ((s) + 2)); SB(); \
    constexpr int bo_ = (((s) + 1) % NBUF) * 32768; constexpr bool ld_ = (s) + NBUF < 10; \
    MM(cur, 0, 1, 1); RDX(nxt, bo_, 0, 0); RDX(nxt, bo_, 0, 1); SB(); if (ld_) G1LD((s) + NBUF, 0); SB(); \
    MM(cur, 1, 0, 0); RDW(nxt, bo_, 0, 0); SB(); \
    MM(cur, 1, 0, 1); RDW(nxt, bo_, 0, 1); SB(); if (ld_) G1LD((s) + NBUF, 4); SB(); \
    MM(cur, 1, 1, 0); RDX(nxt, bo_, 1, 0); RDX(nxt, bo_, 1, 1); SB(); if (ld_) G1LD((s) + NBUF, 1); SB(); \
    MM(cur, 1, 1, 1); RDW(nxt, bo_, 1, 0); SB(); \
    MM(cur, 2, 0, 0); RDW(nxt, bo_, 1, 1); SB(); if (ld_) G1LD((s) + NBUF, 5); SB(); \
    MM(cur, 2, 0, 1); RDX(nxt, bo_, 2, 0); RDX(nxt, bo_, 2, 1); SB(); if (ld_) G1LD((s) + NBUF, 2); SB(); \
    MM(cur, 2, 1, 0); RDW(nxt, bo_, 2, 0); SB(); \
    MM(cur, 2, 1, 1); RDW(nxt, bo_, 2, 1); SB(); if (ld_) G1LD((s) + NBUF, 6); SB(); \
    MM(cur, 3, 0, 0); RDX(nxt, bo_, 3, 0); RDX(nxt, bo_, 3, 1); SB(); if (ld_) G1LD((s) + NBUF, 3); SB(); \
    MM(cur, 3, 0, 1); RDW(nxt, bo_, 3, 0); RDW(nxt, bo_, 3, 1); SB(); \
    MM(cur, 3, 1, 0); SB(); if (ld_) G1LD((s) + NBUF, 7); SB(); \
    MM(cur, 3, 1, 1); SB(); \
  } while (0)
      WAITV(8);
      lds_add(L + C_MMA, 1u, lane); lds_wait(L + C_MMA, 4);
      STAMP(2);
#pragma unroll
      for (int s = 0; s < 4; ++s) { RDX(fA, 0, s, 0); RDX(fA, 0, s, 1); RDW(fA, 0, s, 0); RDW(fA, 0, s, 1); }
      SB();
#pragma unroll
      for (int i = 0; i < 8; ++i) G1LD(2, i);
      SB();
      ITER(0, 8, fA, fB);
      ITER(1, 8, fB, fA);
      ITER(2, 8, fA, fB);
      STAMP(3);
      ITER(3, 8, fB, fA);
      ITER(4, 8, fA, fB);
      ITER(5, 8, fB, fA);
      STAMP(4);
      ITER(6, 8, fA, fB);
      ITER(7, 8, fB, fA);
      ITER(8, 0, fA, fB);
#pragma unroll
      for (int s = 0; s < 4; ++s) { MM(fB, s, 0, 0); MM(fB, s, 0, 1); MM(fB, s, 1, 0); MM(fB, s, 1, 1); }
      SB();
      lds_add(L + C_MMA, 1u, lane);
      STAMP(5);
      const bool pready = (unsigned)__builtin_amdgcn_readfirstlane(__hip_atomic_load((LAS unsigned*)(L + C_P), RLX_WG)) >= 1u;
      lds_wait_sleep(L + C_S, 1);
      STAMP(6);
#pragma unroll
      for (int ft = 0; ft < 2; ++ft) {
        const int fg = 2 * fh + ft;
        const int ks = 4 * j + fg;
        f32x4 c4[4];
#pragma unroll
        for (int g = 0; g < 4; ++g) c4[g] = *(LAS f32x4*)(L + XFC + (32 * fg + 8 * g + 4 * hi) * 4);
#pragma unroll
        for (int tt = 0; tt < 2; ++tt) {
          const int tok = 64 * th + 32 * tt + (lane & 31);
          const f32x2 st = *(LAS f32x2*)(L + XFS + tok * 8);
          const f32x16& a = acc[ft][tt];
          const int mb2 = 2 * p + th, mt = 2 * tt + ((lane >> 4) & 1);
          const int off = ks < KS2 ? HP_OFF + mb2 * HP_MB + ks * 4096 + mt * 1024 + ((2 * hi) * 16 + (lane & 15)) * 16 : -1024;
#pragma unroll
          for (int hh = 0; hh < 2; ++hh) {
            const int slot = ft * 4 + tt * 2 + hh;
            if (slot < WB) { if (pready) LOADB(slot); }
            SB();
            unsigned pk[4];
#pragma unroll
            for (int e2 = 0; e2 < 4; ++e2) {
              const int ee = hh * 4 + e2;
              float v0 = (a[2 * ee] - st.x * c4[ee >> 1][(2 * ee) & 3]) * st.y;
              float v1 = (a[2 * ee + 1] - st.x * c4[ee >> 1][(2 * ee + 1) & 3]) * st.y;
              unsigned hb = pack2(v0, v1);
              pk[e2] = pack2(silu_f(bflo(hb)), silu_f(bfhi(hb)));
            }
            __builtin_amdgcn_raw_buffer_store_b128(v4u{pk[0], pk[1], pk[2], pk[3]}, rws, off + hh * 256, 0, SC1);
            SB();
          }
        }
      }
      STAMP(7);
      WAITV(0);
      {
        unsigned old = 0;
        if (lane == 0) old = __hip_atomic_fetch_add((LAS unsigned*)(L + C_H), 1u, RLX_WG);
        if (__builtin_amdgcn_readfirstlane(old) == 3) set_flag(ws, FL_H + (p * 16 + j) * 4, tag, lane);
      }
      if (!pready) {
        lds_wait_sleep(L + C_P, 1);
#pragma unroll
        for (int i = 0; i < WB; ++i) LOADB(i);
      }
      SB();
      STAMP(8);
    } else {
      const int idx16 = 4 * rg + pi;
      const int srow = 512 * rg + 64 * (4 * hf + pi) + 16 * w + (lane >> 3);
      const int cg = 14 * hf + idx16;
      const bool csv = idx16 < 14 - hf;
      const __amdgpu_buffer_rsrc_t rx = mkrsrc(x, S * D * 2);
      v4u xv[2][10], wv[20];
#pragma unroll
      for (int h2 = 0; h2 < 2; ++h2)
#pragma unroll
        for (int i = 0; i < 10; ++i) xv[h2][i] = __builtin_amdgcn_raw_buffer_load_b128(rx, (srow + 8 * h2) * 1280 + (lane & 7) * 16 + i * 128, 0, 0);
      BAR();
      const int wvo = csv ? (160 * w + (lane >> 3)) * (F * 2) + cg * 128 + (lane & 7) * 16 : -16;
#pragma unroll
      for (int i = 0; i < 20; ++i) wv[i] = __builtin_amdgcn_raw_buffer_load_b128(rw, wvo, csv ? i * 8 * F * 2 : 0, 0);
#pragma unroll
      for (int h2 = 0; h2 < 2; ++h2) {
        float s = 0.f;
#pragma unroll
        for (int i = 0; i < 10; ++i)
#pragma unroll
          for (int e2 = 0; e2 < 4; ++e2) s = dot2(xv[h2][i][e2], 0x3f803f80u, s);
        s += __shfl_xor(s, 1); s += __shfl_xor(s, 2); s += __shfl_xor(s, 4);
        const float mean = s * (1.f / D);
        float q0 = 0.f, q1 = 0.f;
#pragma unroll
        for (int i = 0; i < 10; ++i)
#pragma unroll
          for (int e2 = 0; e2 < 4; ++e2) { const float a0 = bflo(xv[h2][i][e2]) - mean, a1 = bfhi(xv[h2][i][e2]) - mean; q0 = fmaf(a0, a0, q0); q1 = fmaf(a1, a1, q1); }
        float q = q0 + q1;
        q += __shfl_xor(q, 1); q += __shfl_xor(q, 2); q += __shfl_xor(q, 4);
        const float den = __builtin_sqrtf(rbf(rbf(q * (1.f / D)) + 1e-5f));
        __builtin_amdgcn_raw_buffer_store_b64(v2u{pack2(mean, den), tag}, rws, (lane & 7) == 0 ? ST_OFF + (srow + 8 * h2) * 8 : -16, 0, SC1);
      }
      float c[8] = {};
#pragma unroll
      for (int i = 0; i < 20; ++i)
#pragma unroll
        for (int e2 = 0; e2 < 4; ++e2) { c[2 * e2] += bflo(wv[i][e2]); c[2 * e2 + 1] += bfhi(wv[i][e2]); }
      *(LAS f32x4*)(L + ((w * 8 + (lane >> 3)) * 64 + (lane & 7) * 8) * 4) = f32x4{c[0], c[1], c[2], c[3]};
      *(LAS f32x4*)(L + ((w * 8 + (lane >> 3)) * 64 + (lane & 7) * 8 + 4) * 4) = f32x4{c[4], c[5], c[6], c[7]};
      WAITL();
      unsigned old = 0;
      if (lane == 0) old = __hip_atomic_fetch_add((LAS unsigned*)(L + C_H), 1u, RLX_WG);
      if (__builtin_amdgcn_readfirstlane(old) == 3) {
        asm volatile("" ::: "memory");
        float t4[4] = {};
#pragma unroll
        for (int i = 0; i < 32; ++i) t4[i & 3] += *(LAS float*)(L + (i * 64 + lane) * 4);
        __builtin_amdgcn_raw_buffer_store_b64(v2u{__float_as_uint((t4[0] + t4[1]) + (t4[2] + t4[3])), tag}, rws, csv ? CS_OFF + (cg * 64 + lane) * 8 : -16, 0, SC1);
      }
      lds_add(L + C_MMA, 11u, lane);
      STAMP(5);
      lds_wait_sleep(L + C_P, 1);
#pragma unroll
      for (int i = 0; i < WB; ++i) LOADB(i);
      SB();
    }
    {
      const float dv = bflo(Dv[d3]);
      lds_wait_sleep(L + C_ST, 1);
      f32x4 acc[4] = {};
      if (ts3 == 0) conv2<0>(L + CV3 + aoff + w * 128, L + CV3 + boff + w * 128, acc); else conv2<1>(L + CV3 + aoff + w * 128, L + CV3 + boff + w * 128, acc);
#pragma unroll
      for (int ti = 0; ti < 4; ++ti) *(LAS f32x4*)(L + SC3 + (w * 4 + ti) * 1024 + lane * 16) = acc[ti];
      WAITL(); lds_add(L + C_G2, 1u, lane); lds_wait(L + C_G2, 4);
      f32x4 a = {};
#pragma unroll
      for (int ww = 0; ww < 4; ++ww) a += *(LAS f32x4*)(L + SC3 + (ww * 4 + w) * 1024 + lane * 16);
      conv_out(L + CV3, a, ts3 == 0 ? (w < 2 ? w : w + 4) : 2 + w, d3, dv, e, kq, rws);
      STAMP(9);
      WAITV(0); WAITL();
      unsigned old = 0;
      if (lane == 0) old = __hip_atomic_fetch_add((LAS unsigned*)(L + C_Y), 1u, RLX_WG);
      if (__builtin_amdgcn_readfirstlane(old) == 7) set_flag(ws, FL_Y + (nb * 32 + mb) * 4, tag, lane);
      lds_add(L + C_G2, 1u, lane);
    }
    STAMP(10);
    if (w == 0) { poll_flags(ws, FL_H + p * 64, 14, tag, lane); lds_add(L + C_HR, 1u, lane); }
    lds_wait_sleep(L + C_HR, 1);
    STAMP(11);
#pragma unroll
    for (int i = 0; i < WA; ++i) LOADA(i);
    SB();
    f32x4 acc2[4][5] = {};
#pragma unroll
    for (int i = 0; i < NKS; ++i) {
#pragma unroll
      for (int m = 0; m < 4; ++m)
#pragma unroll
        for (int n = 0; n < 5; ++n) acc2[m][n] = mfma16(fb[i % WB][n], fa[i % WA][m], acc2[m][n]);
      SB();
      if (i + WA < NKS) LOADA(i + WA);
      if (i + WB < NKS) LOADB(i + WB);
      SB();
      if (i == 0) STAMP(12);
    }
    STAMP(13);
    lds_wait(L + C_G2, 8);
    const int ln = __builtin_amdgcn_mbcnt_hi(~0u, __builtin_amdgcn_mbcnt_lo(~0u, opaque0()));
#pragma unroll
    for (int m = 0; m < 4; ++m)
#pragma unroll
      for (int n = 0; n < 5; ++n)
        *(LAS f32x4*)(L + w * PART + (16 * m + (ln & 15)) * PSTR + n * 64 + (ln >> 4) * 16) = acc2[m][n];
  }
  STAMP(14);
  BAR();
  const int lane2 = __builtin_amdgcn_mbcnt_hi(~0u, __builtin_amdgcn_mbcnt_lo(~0u, opaque0()));
#pragma unroll
  for (int i = 0; i < 3; ++i) {
    const int it = w + 8 * i;
    if (it < 20) {
      const int n = it * 64 + lane2;
      const int tok = n / 20, dq = n - tok * 20;
      f32x4 o = *(LAS f32x4*)(L + tok * PSTR + dq * 16);
#pragma unroll
      for (int ww = 1; ww < 4; ++ww) o += *(LAS f32x4*)(L + ww * PART + tok * PSTR + dq * 16);
      const v4u sx = *(LAS v4u*)(L + ES + n * 16);
      const unsigned o01 = pack2(o[0], o[1]), o23 = pack2(o[2], o[3]);
      const unsigned g01 = pack2(silu_f(bflo(o01)), silu_f(bfhi(o01))), g23 = pack2(silu_f(bflo(o23)), silu_f(bfhi(o23)));
      const unsigned m01 = pack2(bflo(sx.x) * bflo(g01), bfhi(sx.x) * bfhi(g01)), m23 = pack2(bflo(sx.y) * bflo(g23), bfhi(sx.y) * bfhi(g23));
      v2u res;
      res.x = pack2(bflo(sx.z) + bflo(m01), bfhi(sx.z) + bfhi(m01));
      res.y = pack2(bflo(sx.w) + bflo(m23), bfhi(sx.w) + bfhi(m23));
      *(v2u*)(out + (size_t)(64 * mb + tok) * D + 80 * nb + dq * 4) = res;
    }
  }
  STAMP(15);
  STAMP_OUT(0);
}

extern "C" void kernel_launch(void* const* d_in, const int* in_sizes, int n_in,
                              void* d_out, int out_size, void* d_ws, size_t ws_size,
                              hipStream_t stream) {
  fused<<<256, 512, 0, stream>>>((const u16*)d_in[0], (const u16*)d_in[1], (const u16*)d_in[2], (const u16*)d_in[3],
                                 (const u16*)d_in[4], (const u16*)d_in[5], (const u16*)d_in[7], (const u16*)d_in[8],
                                 (char*)d_ws, (u16*)d_out);
}
```

```cpp
#include <hip/hip_runtime.h>
#include <stdint.h>

typedef unsigned short u16;
typedef unsigned v4u __attribute__((ext_vector_type(4)));
typedef unsigned v2u __attribute__((ext_vector_type(2)));
typedef float f32x2 __attribute__((ext_vector_type(2)));
typedef float f32x4 __attribute__((ext_vector_type(4)));
typedef float f32x16 __attribute__((ext_vector_type(16)));
typedef short bf16x8 __attribute__((ext_vector_type(8)));
typedef short s16x4 __attribute__((ext_vector_type(4)));
typedef __bf16 bf16x2 __attribute__((ext_vector_type(2)));
#define LAS __attribute__((address_space(3)))
#define GAS __attribute__((address_space(1)))
#define DI __device__ __forceinline__
#define RLX_AGENT __ATOMIC_RELAXED, __HIP_MEMORY_SCOPE_AGENT
#define RLX_WG __ATOMIC_RELAXED, __HIP_MEMORY_SCOPE_WORKGROUP

constexpr int S = 2048, D = 640, F = 1728;
constexpr int KS2 = 54;
constexpr int FL_GEN = 0, FL_P = 4096, FL_H = 12288, FL_Y = 16384;
constexpr int ST_OFF = 32768;
constexpr int CS_OFF = ST_OFF + S * 8;
constexpr int HP_OFF = 65536;
constexpr int HP_MB = KS2 * 4096;
constexpr int W2P_OFF = HP_OFF + 32 * HP_MB;
constexpr int W2P_NB = KS2 * 5120;
constexpr int R_OFF = W2P_OFF + 8 * W2P_NB;
constexpr int Y_OFF = R_OFF + D * S * 2;
constexpr int WS_BYTES = Y_OFF + D * S * 2;
constexpr unsigned RSRC3 = 0x00020000u;
constexpr int SC1 = 16;
constexpr unsigned SPIN_CAP = 1u << 18;

DI float bflo(unsigned u) { return __uint_as_float(u << 16); }
DI float bfhi(unsigned u) { return __uint_as_float(u & 0xffff0000u); }
DI unsigned pack2(float a, float b) {
  bf16x2 r = __builtin_convertvector(f32x2{a, b}, bf16x2);
  return __builtin_bit_cast(unsigned, r);
}
DI float rbf(float a) { return bflo(pack2(a, a)); }
DI float silu_f(float x) { return x * __builtin_amdgcn_rcpf(1.f + __builtin_amdgcn_exp2f(-1.44269504088896f * x)); }
DI f32x4 mfma16(v4u a, v4u b, f32x4 c) {
  return __builtin_amdgcn_mfma_f32_16x16x32_bf16(__builtin_bit_cast(bf16x8, a), __builtin_bit_cast(bf16x8, b), c, 0, 0, 0);
}
DI f32x16 mfma32(v4u a, v4u b, f32x16 c) {
  return __builtin_amdgcn_mfma_f32_32x32x16_bf16(__builtin_bit_cast(bf16x8, a), __builtin_bit_cast(bf16x8, b), c, 0, 0, 0);
}
DI float dot2(unsigned a, unsigned b, float c) {
  return __builtin_amdgcn_fdot2_f32_bf16(__builtin_bit_cast(bf16x2, a), __builtin_bit_cast(bf16x2, b), c, false);
}
DI v2u trread(LAS char* p) { return __builtin_bit_cast(v2u, __builtin_amdgcn_ds_read_tr16_b64_v4i16((LAS s16x4*)p)); }
DI v4u ldsr128(LAS char* p) { return *(LAS v4u*)p; }
DI __amdgpu_buffer_rsrc_t mkrsrc(const void* p, int bytes) { return __builtin_amdgcn_make_buffer_rsrc((void*)p, 0, bytes, RSRC3); }
DI void glds_asm(__amdgpu_buffer_rsrc_t rs, unsigned ldsa, int voff, int soff) {
  asm volatile("s_nop 4\n\ts_mov_b32 m0, %0\n\ts_nop 0\n\tbuffer_load_dwordx4 %1, %2, %3 offen lds"
               :: "s"(ldsa), "v"(voff), "s"(rs), "s"(soff) : "m0", "memory");
}
DI void glds_fast(__amdgpu_buffer_rsrc_t rs, unsigned ldsa, int voff, int soff) {
  asm volatile("s_mov_b32 m0, %0\n\ts_nop 0\n\tbuffer_load_dwordx4 %1, %2, %3 offen lds"
               :: "s"(ldsa), "v"(voff), "s"(rs), "s"(soff) : "m0", "memory");
}
#define GLDS(rs, ldsp, voff, soff) glds_asm(rs, (unsigned)(uintptr_t)(ldsp), voff, soff)
#define WAITV(n) asm volatile("s_waitcnt vmcnt(" #n ")" ::: "memory")
#define WAITL() asm volatile("s_waitcnt lgkmcnt(0)" ::: "memory")
#define BAR() do { WAITL(); __builtin_amdgcn_s_barrier(); asm volatile("" ::: "memory"); } while (0)
#define SB() __builtin_amdgcn_sched_barrier(0)

#define STAMP_DECL
#define STAMP(i)
#define STAMP_OUT(kid)

#define ROT_EXPR (5 * ((pi + jj) & 1))
#define WA_N 4
#define POLL_SLEEP 8
#define CODE_PF 1
#define CODE_BYTES 41000u
#define EXACT_PREP 1
#define PREP_PRIO 3
#define CONV_PRIO 1
#define WB_N 5
#define ROT2_EXPR 0
#define DELAY_POLL 24
#define ABL 0
#define AUX_PRIO 0
#define MMA_PRIO 2
#define CONV_START 44
constexpr int NBUF = 3;
constexpr int CH_HA = 0, CH_HB = 4224, CH_R = 8448, CH_RS = 4512, CH_BYTES = CH_R + 4 * CH_RS;
constexpr int CV = 98304;
constexpr int CV3 = 32768;
constexpr int SC3 = 65536;
constexpr int PK = CV, PV = PK + 11264, PW = PV + 10240, PS = PW + 10240, PO = PS + 1024;
constexpr int EY = CV, EX = EY + 80 * 144, ES = EX + 10240;
constexpr int XFS = CV + 2 * CH_BYTES;
constexpr int XFC = XFS + 1024;
constexpr int CNT = XFC + 512;
constexpr int C_AUX = CNT, C_G2 = CNT + 4, C_Y = CNT + 8, C_H = CNT + 12, C_MMA = CNT + 16, C_S = CNT + 20, C_P = CNT + 24,
              C_TAG = CNT + 28, C_ST = CNT + 40, C_HR = CNT + 32, C_YR = CNT + 36;
constexpr int LDS_BYTES = CNT + 64;
static_assert(PO + 10240 <= XFS && ES + 20480 <= XFS && LDS_BYTES <= 163840, "lds");
constexpr int PSTR = 336, PART = 64 * PSTR;

DI void lds_add(LAS char* c, unsigned v, int lane) {
  if (lane == 0) __hip_atomic_fetch_add((LAS unsigned*)c, v, RLX_WG);
}
DI void lds_wait(LAS char* c, unsigned target) {
  while ((unsigned)__builtin_amdgcn_readfirstlane(__hip_atomic_load((LAS unsigned*)c, RLX_WG)) < target) {}
  asm volatile("" ::: "memory");
}
DI void lds_wait_sleep(LAS char* c, unsigned target) {
  while ((unsigned)__builtin_amdgcn_readfirstlane(__hip_atomic_load((LAS unsigned*)c, RLX_WG)) < target) __builtin_amdgcn_s_sleep(1);
  asm volatile("" ::: "memory");
}
DI unsigned flag_load(char* ws, int off, int n, int lane) {
  return __hip_atomic_load((GAS unsigned*)(ws + off) + (lane < n ? lane : 0), RLX_AGENT);
}
DI void poll_flags(char* ws, int off, int n, unsigned tag, int lane) {
#pragma unroll 1
  for (unsigned spins = 0; spins < SPIN_CAP; ++spins) {
    const unsigned v = flag_load(ws, off, n, lane);
    if (__all(v == tag)) break;
    __builtin_amdgcn_s_sleep(POLL_SLEEP);
  }
  __builtin_amdgcn_fence(__ATOMIC_ACQUIRE, "wavefront");
}
DI void set_flag(char* ws, int off, unsigned tag, int lane) {
  if (lane == 0) __hip_atomic_store((GAS unsigned*)(ws + off), tag, RLX_AGENT);
}

DI unsigned opaque0() { unsigned z; asm volatile("v_mov_b32 %0, 0" : "=v"(z)); return z; }
struct Frags { v4u x[4][2], w[4][2]; };

template <int TS> DI void conv_ld(LAS char* pa, LAS char* pb, v4u (&A)[8], v4u (&B)[8]) {
  constexpr int NA = TS == 0 ? 8 : 6;
#pragma unroll
  for (int a = 0; a < NA; ++a) { A[a] = ldsr128(pa + a * 512); B[a] = ldsr128(pb + (7 - a) * 512); }
}
template <int TS> DI void conv_mm(const v4u (&A)[8], const v4u (&B)[8], f32x4 (&acc)[4]) {
#pragma unroll
  for (int ti = 0; ti < 4; ++ti) {
    const int n = TS == 0 ? (ti == 0 ? 0 : ti == 1 ? 1 : ti == 2 ? 6 : 7) : 2 + ti;
#pragma unroll
    for (int a = 0; a <= n; ++a) acc[ti] = mfma16(A[a], B[n - a], acc[ti]);
  }
}
DI void conv_sched() {
#pragma unroll
  for (int i = 0; i < 16; ++i) { __builtin_amdgcn_sched_group_barrier(0x8, 1, 0); __builtin_amdgcn_sched_group_barrier(0x100, 1, 0); }
  __builtin_amdgcn_sched_group_barrier(0x8, 2, 0);
}
template <int TS> DI void conv8(LAS char* pa, LAS char* pb, f32x4 (&acc)[4]) {
  v4u A0[8], B0[8], A1[8], B1[8];
  conv_ld<TS>(pa, pb, A0, B0);
  SB();
#pragma unroll 1
  for (int r = 0; r < 8; r += 2) {
    conv_ld<TS>(pa + 64, pb + 64, A1, B1); conv_mm<TS>(A0, B0, acc); conv_sched(); SB();
    pa += 128; pb += 128;
    conv_ld<TS>(pa, pb, A0, B0); conv_mm<TS>(A1, B1, acc); conv_sched(); SB();
  }
}
template <int TS> DI void conv2(LAS char* pa, LAS char* pb, f32x4 (&acc)[4]) {
#pragma unroll 1
  for (int r = 0; r < 2; ++r) {
    v4u A[8], B[8];
    conv_ld<TS>(pa, pb, A, B); conv_mm<TS>(A, B, acc);
    pa += 64; pb += 64;
  }
}
DI unsigned kshift2(float k1, float k2, float k3, float k4, float k5, float c0, float c1, float c2, float c3) {
  unsigned a = pack2(k1 * c3, k2 * c3);
  unsigned t = pack2(k2 * c2, k3 * c2);
  a = pack2(bflo(a) + bflo(t), bfhi(a) + bfhi(t));
  t = pack2(k3 * c1, k4 * c1);
  a = pack2(bflo(a) + bflo(t), bfhi(a) + bfhi(t));
  t = pack2(k4 * c0, k5 * c0);
  return pack2(bflo(a) + bflo(t), bfhi(a) + bfhi(t));
}
DI void stage_h(LAS char* Lc, v4u h, int g) {
  *(LAS v4u*)(Lc + CH_HB + 112 + g * 16) = h;
  LAS char* a = Lc + CH_HA + 114 + g * 16;
  *(LAS u16*)(a) = (u16)(h.x & 0xffff);
  *(LAS unsigned*)(a + 2) = __builtin_amdgcn_alignbit(h.y, h.x, 16);
  *(LAS v2u*)(a + 6) = v2u{__builtin_amdgcn_alignbit(h.z, h.y, 16), __builtin_amdgcn_alignbit(h.w, h.z, 16)};
  *(LAS u16*)(a + 14) = (u16)(h.w >> 16);
}
DI void stage_r(LAS char* Lc, v4u u, int g) {
  LAS char* rb = Lc + CH_R + g * 16;
  *(LAS v4u*)(rb) = u;
  { LAS char* q = rb + CH_RS - 4; *(LAS unsigned*)(q) = u.x; *(LAS v2u*)(q + 4) = v2u{u.y, u.z}; *(LAS unsigned*)(q + 12) = u.w; }
  { LAS char* q = rb + 2 * CH_RS - 8; *(LAS v2u*)(q) = v2u{u.x, u.y}; *(LAS v2u*)(q + 8) = v2u{u.z, u.w}; }
  { LAS char* q = rb + 3 * CH_RS - 12; *(LAS unsigned*)(q) = u.x; *(LAS v2u*)(q + 4) = v2u{u.y, u.z}; *(LAS unsigned*)(q + 12) = u.w; }
}
DI void conv_out(LAS char* Lc, f32x4 a, int n, int d, float dv, int e, int kq, __amdgpu_buffer_rsrc_t rws) {
  const int t = 256 * n + 64 * (e >> 2) + 2 * (e & 3) + 16 * kq;
  const unsigned ua = *(LAS unsigned*)(Lc + CH_R + 2 * (2046 - t));
  const unsigned ub = *(LAS unsigned*)(Lc + CH_R + 2 * (2038 - t));
  const unsigned c01 = pack2(a[0], a[1]), c23 = pack2(a[2], a[3]);
  const unsigned d01 = pack2(bfhi(ua) * dv, bflo(ua) * dv), d23 = pack2(bfhi(ub) * dv, bflo(ub) * dv);
  v2u o;
  o.x = pack2(bflo(c01) + bflo(d01), bfhi(c01) + bfhi(d01));
  o.y = pack2(bflo(c23) + bflo(d23), bfhi(c23) + bfhi(d23));
  const int off = Y_OFF + d * 4096 + (256 * n + 64 * (e >> 2) + 16 * kq + 4 * (e & 3)) * 2;
  __builtin_amdgcn_raw_buffer_store_b64(o, rws, off, 0, SC1);
}

__global__ __launch_bounds__(512, 2) void fused(const u16* __restrict__ x, const u16* __restrict__ kk, const u16* __restrict__ vv,
                                                const u16* __restrict__ sk, const u16* __restrict__ hk, const u16* __restrict__ Dv,
                                                const u16* __restrict__ w1, const u16* __restrict__ w2,
                                                char* ws, u16* out) {
  __shared__ __attribute__((aligned(16))) char smem[LDS_BYTES];
  LAS char* L = (LAS char*)smem;
  if (__builtin_amdgcn_readfirstlane(threadIdx.x >> 6) == 7) {
    unsigned long long pc;
    asm volatile("s_getpc_b64 %0" : "=s"(pc));
    const unsigned co = min(((blockIdx.x >> 3) & 7) * 8192u + (threadIdx.x & 63) * 128u, CODE_BYTES);
    asm volatile("s_mov_b32 m0, %0\n\ts_nop 0\n\tglobal_load_lds_dword %1, %2" :: "s"((unsigned)(uintptr_t)(L + XFS)), "v"(co), "s"(pc) : "m0");
  }
  const int b = blockIdx.x, xcd = b & 7, r = b >> 3;
  const int rg = xcd >> 1, hf = xcd & 1, pi = r >> 3, jj = r & 7;
  const int p = 4 * rg + pi, j = 7 * hf + jj;
  const bool g1 = jj < 7;
  const int mbl = 2 * pi + ((r >> 2) & 1), nbl = r & 3;
  const int mb = 8 * rg + mbl, nb = 4 * hf + nbl;
  const int lane = threadIdx.x & 63;
  const int w = __builtin_amdgcn_readfirstlane(threadIdx.x >> 6);
  const int hi = lane >> 5, aw = w & 3;
  STAMP_DECL; STAMP(0);
  const __amdgpu_buffer_rsrc_t rws = mkrsrc(ws, WS_BYTES);
  if (threadIdx.x < 16) *(LAS unsigned*)(L + CNT + threadIdx.x * 4) = 0u;
  const int dpair = 80 * nb + 2 * mb, d3 = 80 * nb + 64 + (mb >> 1), ts3 = mb & 1;
  const int e = lane & 15, kq = lane >> 4;
  const int aoff = ((e & 1) ? CH_HB : CH_HA) + (kq + (e >> 1)) * 16;
  const int boff = CH_R + (3 - (e & 3)) * CH_RS + (248 - 32 * 7 - 8 * (e >> 2) + kq) * 16;

  {
    const __amdgpu_buffer_rsrc_t rk = mkrsrc(kk, S * D * 2), rv_ = mkrsrc(vv, S * D * 2), rsk = mkrsrc(sk, D * 8);
    const int sb = 64 * mb * 1280 + 160 * nb;
    int vo[2];
#pragma unroll
    for (int i = 0; i < 2; ++i) { const int n = (8 * i + w) * 64 + lane; const int row = n / 10; vo[i] = row * 1280 + (n - row * 10) * 16; }
    GLDS(rk, L + PK + w * 1024, vo[0] + sb - 5120, 0);
    GLDS(rv_, L + PV + w * 1024, vo[0], sb);
    {
      const int n = (5 + w) * 64 + lane; const int row = n / 10; const int vo3 = row * 1280 + (n - row * 10) * 16;
      if (w < 3) GLDS(rk, L + PK + (8 + w) * 1024, vo[1] + sb - 5120, 0);
      else if (w < 5) GLDS(rv_, L + PV + (5 + w) * 1024, vo3, sb);
      else if (w == 5) GLDS(rsk, L + PS, lane * 16, nb * 640);
    }
  }
#define PREP_ITEM(tg, cg) do { \
        const int q_ = (lane & 15) >> 2, pp_ = lane & 3, tl = 16 * (tg) + 4 * kq; \
        LAS char* ka_ = L + PK + (tl + q_) * 160 + (cg) * 32 + pp_ * 8; \
        v2u k0 = trread(ka_), k1 = trread(ka_ + 4 * 160); \
        v2u v0 = trread(ka_ + (PV - PK)); \
        v2u sw = *(LAS v2u*)(L + PS + ((cg) * 16 + e) * 8); \
        float kf[8] = {bflo(k0.x), bfhi(k0.x), bflo(k0.y), bfhi(k0.y), bflo(k1.x), bfhi(k1.x), bflo(k1.y), bfhi(k1.y)}; \
        float vf[4] = {bflo(v0.x), bfhi(v0.x), bflo(v0.y), bfhi(v0.y)}; \
        const float c0 = bflo(sw.x), c1 = bfhi(sw.x), c2 = bflo(sw.y), c3 = bfhi(sw.y); \
        float u[4]; \
        _Pragma("unroll") for (int e2 = 0; e2 < 4; e2 += 2) { \
          const unsigned a = kshift2(kf[e2 + 1], kf[e2 + 2], kf[e2 + 3], kf[e2 + 4], kf[e2 + 5], c0, c1, c2, c3); \
          u[e2] = bflo(a) * vf[e2]; u[e2 + 1] = bfhi(a) * vf[e2 + 1]; } \
        v2u o = v2u{pack2(u[3], u[2]), pack2(u[1], u[0])}; \
        const int ch = (cg) * 16 + e; \
        const int pos8 = (60 - tl) >> 2; \
        *(LAS v2u*)(L + PO + ch * 128 + ((((pos8 >> 1) ^ (ch & 7)) << 4) | ((pos8 & 1) << 3))) = o; \
      } while (0)

  if (w >= 4) {
    unsigned gen;
    asm volatile("s_load_dword %0, %1, 0x0\n\ts_waitcnt lgkmcnt(0)" : "=s"(gen) : "s"(ws) : "memory");
    const unsigned tag = gen * 0x9E3779B1u + 0x85EBCA6Bu;
    STAMP(1);
    WAITV(0);
    BAR();
    if (aw == 0 && lane == 0) *(LAS unsigned*)(L + C_TAG) = tag;
    unsigned ab = 4;
#define AUXBAR() do { WAITL(); lds_add(L + C_AUX, 1u, lane); ab += 4; lds_wait(L + C_AUX, ab); } while (0)
    STAMP(2);
    if (ABL & 4) lds_wait_sleep(L + C_MMA, 44);
    {
      const __amdgpu_buffer_rsrc_t rw2 = mkrsrc(w2, F * D * 2);
#pragma unroll
      for (int i = 0; i < 3; ++i) if (4 * i + aw < 10) {
        const int n = (4 * i + aw) * 64 + lane; const int row = n / 10;
        GLDS(rw2, L + PW + (4 * i + aw) * 1024, (row + (row >= 32 ? 992 : 0)) * 1280 + (n - row * 10) * 16, 32 * mb * 1280 + 160 * nb);
      }
    }
    {
      const int g = lane >> 4, q = (lane & 15) >> 2, pp = lane & 3;
      PREP_ITEM(aw, 0); PREP_ITEM(aw, 1); PREP_ITEM(aw, 2);
      AUXBAR();
#pragma unroll
      for (int i = 0; i < 3; ++i) {
        const int ri = aw + 4 * i;
        if (ri < 10) {
          const int ch = 8 * ri + (lane >> 3), cc = lane & 7;
          v4u val = ldsr128(L + PO + ch * 128 + ((cc ^ (ch & 7)) << 4));
          __builtin_amdgcn_raw_buffer_store_b128(val, rws, (ABL & 1) ? -16 : R_OFF + (80 * nb + ch) * 4096 + (31 - mb) * 128 + cc * 16, 0, SC1);
        }
      }
      __builtin_amdgcn_s_setprio(AUX_PRIO);
      if (aw < 2) WAITV(3); else WAITV(2);
      AUXBAR();
      STAMP(3);
#pragma unroll
      for (int i = 0; i < 3; ++i) {
        const int fi = aw + 4 * i;
        if (fi < 10) {
          const int half = fi / 5, nt = fi - half * 5, ks = mb + 32 * half;
          const int r0 = 16 * (g & 1) + 4 * (g >> 1);
          LAS char* a = L + PW + half * 5120 + (r0 + q) * 160 + nt * 32 + pp * 8;
          v2u lo = trread(a), hi2 = trread(a + 8 * 160);
          __builtin_amdgcn_raw_buffer_store_b128(v4u{lo.x, lo.y, hi2.x, hi2.y}, rws, (ks < KS2 && !(ABL & 1)) ? W2P_OFF + nb * W2P_NB + ks * 5120 + nt * 1024 + lane * 16 : -16, 0, SC1);
        }
      }
    }
    STAMP(4);
    WAITV(0);
    AUXBAR();
    if (aw == 0) set_flag(ws, FL_P + (nb * 32 + mb) * 4, tag, lane);
    STAMP(5);
    if (ABL & 2) lds_wait_sleep(L + C_MMA, 44);
    const __amdgpu_buffer_rsrc_t rh = mkrsrc(hk, D * S * 2);
    v4u cu[3];
    cu[0] = __builtin_amdgcn_raw_buffer_load_b128(rh, dpair * 4096 + aw * 1024 + lane * 16, 0, 0);
    cu[1] = __builtin_amdgcn_raw_buffer_load_b128(rh, dpair * 4096 + 4096 + aw * 1024 + lane * 16, 0, 0);
    cu[2] = __builtin_amdgcn_raw_buffer_load_b128(rh, d3 * 4096 + aw * 1024 + lane * 16, 0, 0);
    const int g = aw * 64 + lane;
    if (aw == 0 && lane < 32) *(LAS v4u*)(L + CV + (lane >> 4) * CH_BYTES + ((lane >> 3) & 1) * CH_HB + (lane & 7) * 16) = v4u{0, 0, 0, 0};
    if (aw == 3) {
#pragma unroll
      for (int i = 0; i < 4; ++i) {
        const int n = lane + 64 * i;
        if (n < 200) { const int cp = n / 25, sl = n - cp * 25; *(LAS v4u*)(L + CV + (cp >> 2) * CH_BYTES + CH_R + (cp & 3) * CH_RS + 4080 + sl * 16) = v4u{0, 0, 0, 0}; }
      }
    }
    stage_h(L + CV, cu[0], g);
    stage_h(L + CV + CH_BYTES, cu[1], g);
    lds_wait_sleep(L + C_MMA, DELAY_POLL);
    if (aw == 1) {
      const int jc = g1 ? j : 0;
      v2u gr[4];
#define GRL(off) __builtin_bit_cast(v2u, __hip_atomic_load((GAS unsigned long long*)(ws + (off)), RLX_AGENT))
#pragma unroll 1
      for (unsigned sp = 0; sp < SPIN_CAP; ++sp) {
        gr[0] = GRL(ST_OFF + (p * 128 + lane) * 8);
        gr[1] = GRL(ST_OFF + (p * 128 + 64 + lane) * 8);
        gr[2] = GRL(CS_OFF + (jc * 128 + lane) * 8);
        gr[3] = GRL(CS_OFF + (jc * 128 + (j == 13 ? 0 : 64) + lane) * 8);
        if (__all(gr[0].y == tag && gr[1].y == tag && gr[2].y == tag && gr[3].y == tag)) break;
        __builtin_amdgcn_s_sleep(POLL_SLEEP);
      }
      *(LAS f32x2*)(L + XFS + lane * 8) = f32x2{bflo(gr[0].x), 1.f / bfhi(gr[0].x)};
      *(LAS f32x2*)(L + XFS + (64 + lane) * 8) = f32x2{bflo(gr[1].x), 1.f / bfhi(gr[1].x)};
      *(LAS unsigned*)(L + XFC + lane * 4) = gr[2].x;
      *(LAS unsigned*)(L + XFC + (64 + lane) * 4) = gr[3].x;
      WAITL();
      lds_add(L + C_S, 1u, lane);
    }
    if (aw == 2) { poll_flags(ws, FL_P + nb * 128, 32, tag, lane); lds_add(L + C_P, 1u, lane); }
    lds_wait_sleep(L + C_P, 1);
    STAMP(6);
    v4u rv[3];
    rv[0] = __builtin_amdgcn_raw_buffer_load_b128(rws, R_OFF + dpair * 4096 + aw * 1024 + lane * 16, 0, SC1);
    rv[1] = __builtin_amdgcn_raw_buffer_load_b128(rws, R_OFF + dpair * 4096 + 4096 + aw * 1024 + lane * 16, 0, SC1);
    rv[2] = __builtin_amdgcn_raw_buffer_load_b128(rws, R_OFF + d3 * 4096 + aw * 1024 + lane * 16, 0, SC1);
    const int rot2 = ROT2_EXPR;
    const __amdgpu_buffer_rsrc_t ra = mkrsrc(ws + HP_OFF + mb * HP_MB, HP_MB);
    const __amdgpu_buffer_rsrc_t rb = mkrsrc(ws + W2P_OFF + nb * W2P_NB, W2P_NB);
    const int va = lane * 16 + aw * 4096, vb = lane * 16 + aw * 5120;
    constexpr int WA = WA_N, WB = WB_N, NKS = 14;
    v4u fa[WA][4], fb[WB][5];
#define KIDX(i) ((i) + rot2 >= NKS ? (i) + rot2 - NKS : (i) + rot2)
#define LOADA(i) do { const int so_ = KIDX(i) * 16384; \
    _Pragma("unroll") for (int m = 0; m < 4; ++m) fa[(i) % WA][m] = __builtin_amdgcn_raw_buffer_load_b128(ra, va + m * 1024, so_, SC1); } while (0)
#define LOADB(i) do { const int so_ = KIDX(i) * 20480; \
    _Pragma("unroll") for (int n = 0; n < 5; ++n) fb[(i) % WB][n] = __builtin_amdgcn_raw_buffer_load_b128(rb, vb + (n & 3) * 1024, so_ + (n >> 2) * 4096, SC1); } while (0)
    LOADB(0); LOADB(1);
    SB();
    const float dv = bflo(Dv[dpair + (aw >> 1)]);
    stage_r(L + CV, rv[0], g);
    stage_r(L + CV + CH_BYTES, rv[1], g);
    SB();
#pragma unroll
    for (int i = 2; i < WB; ++i) LOADB(i);
    SB();
    STAMP(7);
    lds_wait_sleep(L + C_MMA, 36);
    if (aw == 0 && lane < 16) *(LAS v4u*)(L + CV3 + (lane >> 3) * CH_HB + (lane & 7) * 16) = v4u{0, 0, 0, 0};
    if (aw == 3) {
#pragma unroll
      for (int i = 0; i < 2; ++i) {
        const int n = lane + 64 * i;
        if (n < 100) { const int cp = n / 25, sl = n - cp * 25; *(LAS v4u*)(L + CV3 + CH_R + cp * CH_RS + 4080 + sl * 16) = v4u{0, 0, 0, 0}; }
      }
    }
    stage_h(L + CV3, cu[2], g);
    stage_r(L + CV3, rv[2], g);
    AUXBAR();
    if (aw == 0) lds_add(L + C_ST, 1u, lane);
    lds_wait_sleep(L + C_MMA, CONV_START);
    __builtin_amdgcn_s_setprio(CONV_PRIO);
    STAMP(8);
    {
      LAS char* Lc = L + CV + (aw >> 1) * CH_BYTES;
      const int cts = aw & 1;
      f32x4 acc[4] = {};
      if (cts == 0) conv8<0>(Lc + aoff, Lc + boff, acc); else conv8<1>(Lc + aoff, Lc + boff, acc);
      STAMP(9);
#pragma unroll
      for (int ti = 0; ti < 4; ++ti) conv_out(Lc, acc[ti], cts == 0 ? (ti < 2 ? ti : ti + 4) : 2 + ti, dpair + (aw >> 1), dv, e, kq, rws);
      WAITV(0); WAITL();
      unsigned old = 0;
      if (lane == 0) old = __hip_atomic_fetch_add((LAS unsigned*)(L + C_Y), 1u, RLX_WG);
      if (__builtin_amdgcn_readfirstlane(old) == 7) set_flag(ws, FL_Y + (nb * 32 + mb) * 4, tag, lane);
    }
    STAMP(10);
    __builtin_amdgcn_s_setprio(0);
    lds_wait_sleep(L + C_HR, 1);
    STAMP(11);
#pragma unroll
    for (int i = 0; i < WA; ++i) LOADA(i);
    SB();
    f32x4 acc2[4][5] = {};
#define LOADA1(i, m) fa[(i) % WA][m] = __builtin_amdgcn_raw_buffer_load_b128(ra, va + (m) * 1024, KIDX(i) * 16384, SC1)
#define LOADB1(i, n) fb[(i) % WB][n] = __builtin_amdgcn_raw_buffer_load_b128(rb, vb + ((n) & 3) * 1024, KIDX(i) * 20480 + ((n) >> 2) * 4096, SC1)
#pragma unroll
    for (int i = 0; i < NKS; ++i) {
#pragma unroll
      for (int mn = 0; mn < 20; ++mn) {
        const int m = mn / 5, n = mn % 5;
        acc2[m][n] = mfma16(fb[i % WB][n], fa[i % WA][m], acc2[m][n]);
        if (i > 0 && (mn & 1)) {
          const int k = mn >> 1;
          SB();
          if (k < 4) { if (i - 1 + WA < NKS) LOADA1(i - 1 + WA, k); }
          else if (k < 9) { if (i - 1 + WB < NKS) LOADB1(i - 1 + WB, k - 4); }
          SB();
        }
      }
      SB();
      if (i == 0) STAMP(12);
    }
    STAMP(13);
    lds_wait(L + C_Y, 8);
    const int ln = __builtin_amdgcn_mbcnt_hi(~0u, __builtin_amdgcn_mbcnt_lo(~0u, opaque0()));
#pragma unroll
    for (int m = 0; m < 4; ++m)
#pragma unroll
      for (int n = 0; n < 5; ++n)
        *(LAS f32x4*)(L + aw * PART + (16 * m + (ln & 15)) * PSTR + n * 64 + (ln >> 4) * 16) = acc2[m][n];
  } else {
    __builtin_amdgcn_s_setprio(MMA_PRIO);
    const __amdgpu_buffer_rsrc_t rw = mkrsrc(w1, D * F * 2);
    unsigned tag;
    if (g1) {
      const __amdgpu_buffer_rsrc_t rx = mkrsrc(x + (size_t)p * 128 * D, 128 * D * 2);
      int vx[2], vw[4];
      {
        const int row = 32 * w + (lane >> 3);
        vx[0] = row * 1280 + (((lane & 7) ^ ((row >> 1) & 7)) << 4);
        vx[1] = (row + 8) * 1280 + (((lane & 7) ^ (((row + 8) >> 1) & 7)) << 4);
#pragma unroll
        for (int ii = 0; ii < 4; ++ii) {
          const int kr = 16 * w + 4 * ii + (lane >> 4);
          vw[ii] = kr * (F * 2) + j * 256 + (((lane & 15) ^ (((kr & 3) << 2) | ((kr >> 2) & 3))) << 4);
        }
      }
      const int rot = ROT_EXPR;
      const unsigned lbase = (unsigned)(uintptr_t)L + w * 4096;
#define KC(s) ((s) + rot >= 10 ? (s) + rot - 10 : (s) + rot)
#define G1LD(s, i) do { \
    if ((i) < 4) glds_fast(rx, lbase + ((s) % NBUF) * 32768 + (i) * 1024, vx[(i) & 1], KC(s) * 128 + ((i) >> 1) * 16 * 1280); \
    else glds_fast(rw, lbase + ((s) % NBUF) * 32768 + 16384 + ((i) - 4) * 1024, vw[((i) - 4) & 3], KC(s) * (64 * F * 2)); \
  } while (0)
#pragma unroll
      for (int i = 0; i < 8; ++i) G1LD(0, i);
      WAITV(8);
      BAR();
      PREP_ITEM(aw, 3); PREP_ITEM(aw, 4);
      WAITL(); lds_add(L + C_AUX, 1u, lane);
      SB();
#pragma unroll
      for (int i = 0; i < 8; ++i) G1LD(1, i);
      STAMP(1);
      const int fh = w & 1, th = w >> 1;
      int xa[4], wa[2][2];
      {
        const int row = 64 * th + (lane & 31);
        const int c0 = hi ^ ((row >> 1) & 7);
        const int blk = (lane >> 4) & 1, q = (lane & 15) >> 2, pp = lane & 3;
#pragma unroll
        for (int s = 0; s < 4; ++s) xa[s] = row * 128 + (((2 * s) ^ c0) << 4);
#pragma unroll
        for (int ft = 0; ft < 2; ++ft)
#pragma unroll
          for (int t = 0; t < 2; ++t) {
            const int kr = 8 * hi + 4 * t + q;
            const int ch = 4 * (2 * fh + ft) + 2 * blk + (pp >> 1);
            wa[ft][t] = 16384 + 256 * kr + ((ch ^ ((q << 2) | ((2 * hi + t) & 3))) << 4) + 8 * (pp & 1);
          }
      }
      f32x16 acc[2][2] = {};
      Frags fA, fB;
#define RDX(f, bo, s, tt) f.x[s][tt] = ldsr128(L + (bo) + xa[s] + (tt) * 4096)
#define RDW(f, bo, s, ft) do { v2u wl_ = trread(L + (bo) + wa[ft][0] + (s) * 4096), wh_ = trread(L + (bo) + wa[ft][1] + (s) * 4096); f.w[s][ft] = v4u{wl_.x, wl_.y, wh_.x, wh_.y}; } while (0)
#define MM(f, s, ft, tt) acc[ft][tt] = mfma32(f.w[s][ft], f.x[s][tt], acc[ft][tt])
#define ITER(s, vm, cur, nxt) do { \
    asm volatile("s_waitcnt vmcnt(" #vm ") lgkmcnt(0)" ::: "memory"); \
    lds_add(L + C_MMA, 1u, lane); SB(); \
    MM(cur, 0, 0, 0); MM(cur, 0, 0, 1); MM(cur, 0, 1, 0); SB(); \
    lds_wait(L + C_MMA, 4 * ((s) + 2)); SB(); \
    constexpr int bo_ = (((s) + 1) % NBUF) * 32768; constexpr bool ld_ = (s) + NBUF < 10; \
    MM(cur, 0, 1, 1); RDX(nxt, bo_, 0, 0); RDX(nxt, bo_, 0, 1); SB(); if (ld_) G1LD((s) + NBUF, 0); SB(); \
    MM(cur, 1, 0, 0); RDW(nxt, bo_, 0, 0); SB(); \
    MM(cur, 1, 0, 1); RDW(nxt, bo_, 0, 1); SB(); if (ld_) G1LD((s) + NBUF, 4); SB(); \
    MM(cur, 1, 1, 0); RDX(nxt, bo_, 1, 0); RDX(nxt, bo_, 1, 1); SB(); if (ld_) G1LD((s) + NBUF, 1); SB(); \
    MM(cur, 1, 1, 1); RDW(nxt, bo_, 1, 0); SB(); \
    MM(cur, 2, 0, 0); RDW(nxt, bo_, 1, 1); SB(); if (ld_) G1LD((s) + NBUF, 5); SB(); \
    MM(cur, 2, 0, 1); RDX(nxt, bo_, 2, 0); RDX(nxt, bo_, 2, 1); SB(); if (ld_) G1LD((s) + NBUF, 2); SB(); \
    MM(cur, 2, 1, 0); RDW(nxt, bo_, 2, 0); SB(); \
    MM(cur, 2, 1, 1); RDW(nxt, bo_, 2, 1); SB(); if (ld_) G1LD((s) + NBUF, 6); SB(); \
    MM(cur, 3, 0, 0); RDX(nxt, bo_, 3, 0); RDX(nxt, bo_, 3, 1); SB(); if (ld_) G1LD((s) + NBUF, 3); SB(); \
    MM(cur, 3, 0, 1); RDW(nxt, bo_, 3, 0); RDW(nxt, bo_, 3, 1); SB(); \
    MM(cur, 3, 1, 0); SB(); if (ld_) G1LD((s) + NBUF, 7); SB(); \
    MM(cur, 3, 1, 1); SB(); \
  } while (0)
      WAITV(8);
      lds_add(L + C_MMA, 1u, lane); lds_wait(L + C_MMA, 4);
      STAMP(2);
#pragma unroll
      for (int s = 0; s < 4; ++s) { RDX(fA, 0, s, 0); RDX(fA, 0, s, 1); RDW(fA, 0, s, 0); RDW(fA, 0, s, 1); }
      SB();
#pragma unroll
      for (int i = 0; i < 8; ++i) G1LD(2, i);
      SB();
      ITER(0, 8, fA, fB);
      ITER(1, 8, fB, fA);
      ITER(2, 8, fA, fB);
      STAMP(3);
      ITER(3, 8, fB, fA);
      ITER(4, 8, fA, fB);
      ITER(5, 8, fB, fA);
      STAMP(4);
      ITER(6, 8, fA, fB);
      ITER(7, 8, fB, fA);
      ITER(8, 0, fA, fB);
#pragma unroll
      for (int s = 0; s < 4; ++s) { MM(fB, s, 0, 0); MM(fB, s, 0, 1); MM(fB, s, 1, 0); MM(fB, s, 1, 1); }
      SB();
      lds_add(L + C_MMA, 1u, lane);
      STAMP(5);
      lds_wait_sleep(L + C_S, 1);
      tag = __builtin_amdgcn_readfirstlane(*(LAS unsigned*)(L + C_TAG));
      STAMP(6);
#pragma unroll
      for (int ft = 0; ft < 2; ++ft) {
        const int fg = 2 * fh + ft;
        const int ks = 4 * j + fg;
        f32x4 c4[4];
#pragma unroll
        for (int g = 0; g < 4; ++g) c4[g] = *(LAS f32x4*)(L + XFC + (32 * fg + 8 * g + 4 * hi) * 4);
#pragma unroll
        for (int tt = 0; tt < 2; ++tt) {
          const int tok = 64 * th + 32 * tt + (lane & 31);
          const f32x2 st = *(LAS f32x2*)(L + XFS + tok * 8);
          const f32x16& a = acc[ft][tt];
          const int mb2 = 2 * p + th, mt = 2 * tt + ((lane >> 4) & 1);
          const int off = ks < KS2 ? HP_OFF + mb2 * HP_MB + ks * 4096 + mt * 1024 + ((2 * hi) * 16 + (lane & 15)) * 16 : -1024;
#pragma unroll
          for (int hh = 0; hh < 2; ++hh) {
            unsigned pk[4];
#pragma unroll
            for (int e2 = 0; e2 < 4; ++e2) {
              const int ee = hh * 4 + e2;
              float v0 = (a[2 * ee] - st.x * c4[ee >> 1][(2 * ee) & 3]) * st.y;
              float v1 = (a[2 * ee + 1] - st.x * c4[ee >> 1][(2 * ee + 1) & 3]) * st.y;
              unsigned hb = pack2(v0, v1);
              pk[e2] = pack2(silu_f(bflo(hb)), silu_f(bfhi(hb)));
            }
            __builtin_amdgcn_raw_buffer_store_b128(v4u{pk[0], pk[1], pk[2], pk[3]}, rws, off + hh * 256, 0, SC1);
            SB();
          }
        }
      }
      STAMP(7);
      WAITV(0);
      {
        unsigned old = 0;
        if (lane == 0) old = __hip_atomic_fetch_add((LAS unsigned*)(L + C_H), 1u, RLX_WG);
        if (__builtin_amdgcn_readfirstlane(old) == 3) set_flag(ws, FL_H + (p * 16 + j) * 4, tag, lane);
      }
      STAMP(8);
    } else {
      const int idx16 = 4 * rg + pi;
      const int srow = 512 * rg + 64 * (4 * hf + pi) + 16 * w + (lane >> 3);
      const int cg = 14 * hf + idx16;
      const bool csv = idx16 < 14 - hf;
      const __amdgpu_buffer_rsrc_t rx = mkrsrc(x, S * D * 2);
      v4u xv[2][10], wv[20];
      WAITV(0);
      BAR();
      if (ABL & 8) for (int i = 0; i < 150; ++i) __builtin_amdgcn_s_sleep(1);
#pragma unroll
      for (int h2 = 0; h2 < 2; ++h2)
#pragma unroll
        for (int i = 0; i < 10; ++i) xv[h2][i] = __builtin_amdgcn_raw_buffer_load_b128(rx, (srow + 8 * h2) * 1280 + (lane & 7) * 16 + i * 128, 0, 0);
      PREP_ITEM(aw, 3); PREP_ITEM(aw, 4);
      WAITL(); lds_add(L + C_AUX, 1u, lane);
      const int wvo = csv ? (160 * w + (lane >> 3)) * (F * 2) + cg * 128 + (lane & 7) * 16 : -16;
#pragma unroll
      for (int i = 0; i < 20; ++i) wv[i] = __builtin_amdgcn_raw_buffer_load_b128(rw, wvo, csv ? i * 8 * F * 2 : 0, 0);
      lds_wait_sleep(L + C_AUX, 8);
      tag = __builtin_amdgcn_readfirstlane(*(LAS unsigned*)(L + C_TAG));
#pragma unroll
      for (int h2 = 0; h2 < 2; ++h2) {
        float s = 0.f;
#pragma unroll
        for (int i = 0; i < 10; ++i)
#pragma unroll
          for (int e2 = 0; e2 < 4; ++e2) s = dot2(xv[h2][i][e2], 0x3f803f80u, s);
        s += __shfl_xor(s, 1); s += __shfl_xor(s, 2); s += __shfl_xor(s, 4);
        const float mean = s * (1.f / D);
        float q0 = 0.f, q1 = 0.f;
#pragma unroll
        for (int i = 0; i < 10; ++i)
#pragma unroll
          for (int e2 = 0; e2 < 4; ++e2) { const float a0 = bflo(xv[h2][i][e2]) - mean, a1 = bfhi(xv[h2][i][e2]) - mean; q0 = fmaf(a0, a0, q0); q1 = fmaf(a1, a1, q1); }
        float q = q0 + q1;
        q += __shfl_xor(q, 1); q += __shfl_xor(q, 2); q += __shfl_xor(q, 4);
        const float den = __builtin_sqrtf(rbf(rbf(q * (1.f / D)) + 1e-5f));
        __builtin_amdgcn_raw_buffer_store_b64(v2u{pack2(mean, den), tag}, rws, (lane & 7) == 0 ? ST_OFF + (srow + 8 * h2) * 8 : -16, 0, SC1);
      }
      float c[8] = {};
#pragma unroll
      for (int i = 0; i < 20; ++i)
#pragma unroll
        for (int e2 = 0; e2 < 4; ++e2) { c[2 * e2] += bflo(wv[i][e2]); c[2 * e2 + 1] += bfhi(wv[i][e2]); }
      *(LAS f32x4*)(L + ((w * 8 + (lane >> 3)) * 64 + (lane & 7) * 8) * 4) = f32x4{c[0], c[1], c[2], c[3]};
      *(LAS f32x4*)(L + ((w * 8 + (lane >> 3)) * 64 + (lane & 7) * 8 + 4) * 4) = f32x4{c[4], c[5], c[6], c[7]};
      WAITL();
      unsigned old = 0;
      if (lane == 0) old = __hip_atomic_fetch_add((LAS unsigned*)(L + C_H), 1u, RLX_WG);
      if (__builtin_amdgcn_readfirstlane(old) == 3) {
        asm volatile("" ::: "memory");
        float t4[4] = {};
#pragma unroll
        for (int i = 0; i < 32; ++i) t4[i & 3] += *(LAS float*)(L + (i * 64 + lane) * 4);
        __builtin_amdgcn_raw_buffer_store_b64(v2u{__float_as_uint((t4[0] + t4[1]) + (t4[2] + t4[3])), tag}, rws, csv ? CS_OFF + (cg * 64 + lane) * 8 : -16, 0, SC1);
      }
      lds_add(L + C_MMA, 11u, lane);
      STAMP(5);
    }
    unsigned hb_ = 0;
#define HBAR() do { WAITL(); lds_add(L + C_G2, 1u, lane); hb_ += 4; lds_wait(L + C_G2, hb_); } while (0)
    {
      const float dv3 = bflo(Dv[d3]);
      lds_wait_sleep(L + C_ST, 1);
      f32x4 ac3[4] = {};
      if (ts3 == 0) conv2<0>(L + CV3 + aoff + aw * 128, L + CV3 + boff + aw * 128, ac3); else conv2<1>(L + CV3 + aoff + aw * 128, L + CV3 + boff + aw * 128, ac3);
#pragma unroll
      for (int ti = 0; ti < 4; ++ti) *(LAS f32x4*)(L + SC3 + (aw * 4 + ti) * 1024 + lane * 16) = ac3[ti];
      HBAR();
      f32x4 a = {};
#pragma unroll
      for (int ww = 0; ww < 4; ++ww) a += *(LAS f32x4*)(L + SC3 + (ww * 4 + aw) * 1024 + lane * 16);
      conv_out(L + CV3, a, ts3 == 0 ? (aw < 2 ? aw : aw + 4) : 2 + aw, d3, dv3, e, kq, rws);
      STAMP(9);
      WAITV(0); WAITL();
      unsigned old = 0;
      if (lane == 0) old = __hip_atomic_fetch_add((LAS unsigned*)(L + C_Y), 1u, RLX_WG);
      if (__builtin_amdgcn_readfirstlane(old) == 7) set_flag(ws, FL_Y + (nb * 32 + mb) * 4, tag, lane);
    }
    STAMP(10);
    if (aw == 1) { poll_flags(ws, FL_H + p * 64, 14, tag, lane); lds_add(L + C_HR, 1u, lane); }
    lds_wait_sleep(L + C_Y, 8);
    {
      const __amdgpu_buffer_rsrc_t rxx = mkrsrc(x, S * D * 2);
#pragma unroll
      for (int i = 0; i < 3; ++i) {
        const int s = aw + 4 * i;
        if (s < 10) {
          const int n = s * 64 + lane; const int row = n / 10, c = n - row * 10;
          GLDS(rxx, L + EX + s * 1024, (64 * mb + row) * 1280 + 160 * nb + c * 16, 0);
        }
      }
    }
    if (aw == 0) { poll_flags(ws, FL_Y + nb * 128, 32, tag, lane); lds_add(L + C_YR, 1u, lane); }
    lds_wait_sleep(L + C_YR, 1);
    STAMP(11);
#pragma unroll
    for (int i = 0; i < 3; ++i) {
      const int ri = aw + 4 * i;
      if (ri < 10) {
        const int ch = 8 * ri + (lane >> 3), cc = lane & 7;
        v4u yv = __builtin_amdgcn_raw_buffer_load_b128(rws, Y_OFF + (80 * nb + ch) * 4096 + mb * 128 + cc * 16, 0, SC1);
        *(LAS v4u*)(L + EY + ch * 144 + cc * 16) = yv;
      }
    }
    WAITV(0);
    HBAR();
    STAMP(12);
    {
      const int q = (lane & 15) >> 2, pp = lane & 3, pos = lane & 15;
      const int tok = 16 * aw + 2 * (pos >> 2) + (pos & 1) + 8 * ((pos >> 1) & 1);
      const f32x2 st = *(LAS f32x2*)(L + XFS + ((mb & 1) * 64 + tok) * 8);
#pragma unroll
      for (int i = 0; i < 5; ++i) {
        const int dq = 4 * i + kq;
        const v2u yv = trread(L + EY + (4 * dq + q) * 144 + aw * 32 + pp * 8);
        const v2u xv2 = *(LAS v2u*)(L + EX + tok * 160 + dq * 8);
        const unsigned d01 = pack2(bflo(xv2.x) - st.x, bfhi(xv2.x) - st.x), d23 = pack2(bflo(xv2.y) - st.x, bfhi(xv2.y) - st.x);
        const unsigned n01 = pack2(bflo(d01) * st.y, bfhi(d01) * st.y), n23 = pack2(bflo(d23) * st.y, bfhi(d23) * st.y);
        const unsigned s01 = pack2(bflo(n01) * bflo(yv.x), bfhi(n01) * bfhi(yv.x)), s23 = pack2(bflo(n23) * bflo(yv.y), bfhi(n23) * bfhi(yv.y));
        *(LAS v4u*)(L + ES + (tok * 20 + dq) * 16) = v4u{s01, s23, xv2.x, xv2.y};
      }
    }
    if (b == 0 && aw == 0) {
#pragma unroll 1
      for (int i = 0; i < 4; ++i) poll_flags(ws, FL_P + i * 256, 64, tag, lane);
      set_flag(ws, FL_GEN, tag, lane);
    }
  }
  STAMP(14);
  BAR();
  const int lane2 = __builtin_amdgcn_mbcnt_hi(~0u, __builtin_amdgcn_mbcnt_lo(~0u, opaque0()));
#pragma unroll
  for (int i = 0; i < 3; ++i) {
    const int it = w + 8 * i;
    if (it < 20) {
      const int n = it * 64 + lane2;
      const int tok = n / 20, dq = n - tok * 20;
      f32x4 o = *(LAS f32x4*)(L + tok * PSTR + dq * 16);
#pragma unroll
      for (int ww = 1; ww < 4; ++ww) o += *(LAS f32x4*)(L + ww * PART + tok * PSTR + dq * 16);
      const v4u sx = *(LAS v4u*)(L + ES + n * 16);
      const unsigned o01 = pack2(o[0], o[1]), o23 = pack2(o[2], o[3]);
      const unsigned g01 = pack2(silu_f(bflo(o01)), silu_f(bfhi(o01))), g23 = pack2(silu_f(bflo(o23)), silu_f(bfhi(o23)));
      const unsigned m01 = pack2(bflo(sx.x) * bflo(g01), bfhi(sx.x) * bfhi(g01)), m23 = pack2(bflo(sx.y) * bflo(g23), bfhi(sx.y) * bfhi(g23));
      v2u res;
      res.x = pack2(bflo(sx.z) + bflo(m01), bfhi(sx.z) + bfhi(m01));
      res.y = pack2(bflo(sx.w) + bflo(m23), bfhi(sx.w) + bfhi(m23));
      *(v2u*)(out + (size_t)(64 * mb + tok) * D + 80 * nb + dq * 4) = res;
    }
  }
  STAMP(15);
  STAMP_OUT(0);
}

extern "C" void kernel_launch(void* const* d_in, const int* in_sizes, int n_in,
                              void* d_out, int out_size, void* d_ws, size_t ws_size,
                              hipStream_t stream) {
  fused<<<256, 512, 0, stream>>>((const u16*)d_in[0], (const u16*)d_in[1], (const u16*)d_in[2], (const u16*)d_in[3],
                                 (const u16*)d_in[4], (const u16*)d_in[5], (const u16*)d_in[7], (const u16*)d_in[8],
                                 (char*)d_ws, (u16*)d_out);
}
```

```cpp
#include <hip/hip_runtime.h>
#include <stdint.h>

typedef unsigned short u16;
typedef unsigned v4u __attribute__((ext_vector_type(4)));
typedef unsigned v2u __attribute__((ext_vector_type(2)));
typedef float f32x2 __attribute__((ext_vector_type(2)));
typedef float f32x4 __attribute__((ext_vector_type(4)));
typedef float f32x16 __attribute__((ext_vector_type(16)));
typedef short bf16x8 __attribute__((ext_vector_type(8)));
typedef short s16x4 __attribute__((ext_vector_type(4)));
typedef __bf16 bf16x2 __attribute__((ext_vector_type(2)));
#define LAS __attribute__((address_space(3)))
#define GAS __attribute__((address_space(1)))
#define DI __device__ __forceinline__
#define RLX_AGENT __ATOMIC_RELAXED, __HIP_MEMORY_SCOPE_AGENT
#define RLX_WG __ATOMIC_RELAXED, __HIP_MEMORY_SCOPE_WORKGROUP

constexpr int S = 2048, D = 640, F = 1728;
constexpr int KS2 = 54;
constexpr int FL_GEN = 0, FL_P = 4096, FL_H = 12288, FL_Y = 16384;
constexpr int ST_OFF = 32768;
constexpr int CS_OFF = ST_OFF + S * 8;
constexpr int HP_OFF = 65536;
constexpr int HP_MB = KS2 * 4096;
constexpr int W2P_OFF = HP_OFF + 32 * HP_MB;
constexpr int W2P_NB = KS2 * 5120;
constexpr int R_OFF = W2P_OFF + 8 * W2P_NB;
constexpr int Y_OFF = R_OFF + D * S * 2;
constexpr int WS_BYTES = Y_OFF + D * S * 2;
constexpr unsigned RSRC3 = 0x00020000u;
constexpr int SC1 = 16;
constexpr unsigned SPIN_CAP = 1u << 18;

DI float bflo(unsigned u) { return __uint_as_float(u << 16); }
DI float bfhi(unsigned u) { return __uint_as_float(u & 0xffff0000u); }
DI unsigned pack2(float a, float b) {
  bf16x2 r = __builtin_convertvector(f32x2{a, b}, bf16x2);
  return __builtin_bit_cast(unsigned, r);
}
DI float rbf(float a) { return bflo(pack2(a, a)); }
DI float silu_f(float x) { return x * __builtin_amdgcn_rcpf(1.f + __builtin_amdgcn_exp2f(-1.44269504088896f * x)); }
DI f32x4 mfma16(v4u a, v4u b, f32x4 c) {
  return __builtin_amdgcn_mfma_f32_16x16x32_bf16(__builtin_bit_cast(bf16x8, a), __builtin_bit_cast(bf16x8, b), c, 0, 0, 0);
}
DI f32x16 mfma32(v4u a, v4u b, f32x16 c) {
  return __builtin_amdgcn_mfma_f32_32x32x16_bf16(__builtin_bit_cast(bf16x8, a), __builtin_bit_cast(bf16x8, b), c, 0, 0, 0);
}
DI float dot2(unsigned a, unsigned b, float c) {
  return __builtin_amdgcn_fdot2_f32_bf16(__builtin_bit_cast(bf16x2, a), __builtin_bit_cast(bf16x2, b), c, false);
}
DI v2u trread(LAS char* p) { return __builtin_bit_cast(v2u, __builtin_amdgcn_ds_read_tr16_b64_v4i16((LAS s16x4*)p)); }
DI v4u ldsr128(LAS char* p) { return *(LAS v4u*)p; }
DI __amdgpu_buffer_rsrc_t mkrsrc(const void* p, int bytes) { return __builtin_amdgcn_make_buffer_rsrc((void*)p, 0, bytes, RSRC3); }
DI void glds_asm(__amdgpu_buffer_rsrc_t rs, unsigned ldsa, int voff, int soff) {
  asm volatile("s_nop 4\n\ts_mov_b32 m0, %0\n\ts_nop 0\n\tbuffer_load_dwordx4 %1, %2, %3 offen lds"
               :: "s"(ldsa), "v"(voff), "s"(rs), "s"(soff) : "m0", "memory");
}
DI void glds_fast(__amdgpu_buffer_rsrc_t rs, unsigned ldsa, int voff, int soff) {
  asm volatile("s_mov_b32 m0, %0\n\ts_nop 0\n\tbuffer_load_dwordx4 %1, %2, %3 offen lds"
               :: "s"(ldsa), "v"(voff), "s"(rs), "s"(soff) : "m0", "memory");
}
#define GLDS(rs, ldsp, voff, soff) glds_asm(rs, (unsigned)(uintptr_t)(ldsp), voff, soff)
#define WAITV(n) asm volatile("s_waitcnt vmcnt(" #n ")" ::: "memory")
#define WAITL() asm volatile("s_waitcnt lgkmcnt(0)" ::: "memory")
#define BAR() do { WAITL(); __builtin_amdgcn_s_barrier(); asm volatile("" ::: "memory"); } while (0)
#define SB() __builtin_amdgcn_sched_barrier(0)

#define STAMP_DECL
#define STAMP(i)
#define STAMP_OUT(kid)

#define ROT_EXPR (5 * ((pi + jj) & 1))
#define WA_N 4
#define POLL_SLEEP 8
#define CODE_PF 1
#define CODE_BYTES 41000u
#define EXACT_PREP 0
#define PREP_PRIO 3
#define CONV_PRIO 1
#define WB_N 4
#define ROT2_EXPR 0
#define DELAY_POLL 24
#define ABL 0
#define AUX_PRIO 0
#define MMA_PRIO 2
#define CONV_START 44
constexpr int NBUF = 3;
constexpr int CH_HA = 0, CH_HB = 4224, CH_R = 8448, CH_RS = 4512, CH_BYTES = CH_R + 4 * CH_RS;
constexpr int CV = 98304;
constexpr int CV3 = 32768;
constexpr int SC3 = 65536;
constexpr int PK = CV, PV = PK + 11264, PW = PV + 10240, PS = PW + 10240, PO = PS + 1024;
constexpr int EY = CV, EX = EY + 80 * 144, ES = EX + 10240;
constexpr int XFS = CV + 2 * CH_BYTES;
constexpr int XFC = XFS + 1024;
constexpr int CNT = XFC + 512;
constexpr int C_AUX = CNT, C_G2 = CNT + 4, C_Y = CNT + 8, C_H = CNT + 12, C_MMA = CNT + 16, C_S = CNT + 20, C_P = CNT + 24,
              C_TAG = CNT + 28, C_ST = CNT + 40, C_HR = CNT + 32, C_YR = CNT + 36;
constexpr int LDS_BYTES = CNT + 64;
static_assert(PO + 10240 <= XFS && ES + 20480 <= XFS && LDS_BYTES <= 163840, "lds");
constexpr int PSTR = 336, PART = 64 * PSTR;

DI void lds_add(LAS char* c, unsigned v, int lane) {
  if (lane == 0) __hip_atomic_fetch_add((LAS unsigned*)c, v, RLX_WG);
}
DI void lds_wait(LAS char* c, unsigned target) {
  while ((unsigned)__builtin_amdgcn_readfirstlane(__hip_atomic_load((LAS unsigned*)c, RLX_WG)) < target) {}
  asm volatile("" ::: "memory");
}
DI void lds_wait_sleep(LAS char* c, unsigned target) {
  while ((unsigned)__builtin_amdgcn_readfirstlane(__hip_atomic_load((LAS unsigned*)c, RLX_WG)) < target) __builtin_amdgcn_s_sleep(1);
  asm volatile("" ::: "memory");
}
DI unsigned flag_load(char* ws, int off, int n, int lane) {
  return __hip_atomic_load((GAS unsigned*)(ws + off) + (lane < n ? lane : 0), RLX_AGENT);
}
DI void poll_flags(char* ws, int off, int n, unsigned tag, int lane) {
#pragma unroll 1
  for (unsigned spins = 0; spins < SPIN_CAP; ++spins) {
    const unsigned v = flag_load(ws, off, n, lane);
    if (__all(v == tag)) break;
    __builtin_amdgcn_s_sleep(POLL_SLEEP);
  }
  __builtin_amdgcn_fence(__ATOMIC_ACQUIRE, "wavefront");
}
DI void set_flag(char* ws, int off, unsigned tag, int lane) {
  if (lane == 0) __hip_atomic_store((GAS unsigned*)(ws + off), tag, RLX_AGENT);
}

DI unsigned opaque0() { unsigned z; asm volatile("v_mov_b32 %0, 0" : "=v"(z)); return z; }
struct Frags { v4u x[4][2], w[4][2]; };

template <int TS> DI void conv_ld(LAS char* pa, LAS char* pb, v4u (&A)[8], v4u (&B)[8]) {
  constexpr int NA = TS == 0 ? 8 : 6;
#pragma unroll
  for (int a = 0; a < NA; ++a) { A[a] = ldsr128(pa + a * 512); B[a] = ldsr128(pb + (7 - a) * 512); }
}
template <int TS> DI void conv_mm(const v4u (&A)[8], const v4u (&B)[8], f32x4 (&acc)[4]) {
#pragma unroll
  for (int ti = 0; ti < 4; ++ti) {
    const int n = TS == 0 ? (ti == 0 ? 0 : ti == 1 ? 1 : ti == 2 ? 6 : 7) : 2 + ti;
#pragma unroll
    for (int a = 0; a <= n; ++a) acc[ti] = mfma16(A[a], B[n - a], acc[ti]);
  }
}
DI void conv_sched() {
#pragma unroll
  for (int i = 0; i < 16; ++i) { __builtin_amdgcn_sched_group_barrier(0x8, 1, 0); __builtin_amdgcn_sched_group_barrier(0x100, 1, 0); }
  __builtin_amdgcn_sched_group_barrier(0x8, 2, 0);
}
template <int TS> DI void conv8(LAS char* pa, LAS char* pb, f32x4 (&acc)[4]) {
  v4u A0[8], B0[8], A1[8], B1[8];
  conv_ld<TS>(pa, pb, A0, B0);
  SB();
#pragma unroll 1
  for (int r = 0; r < 8; r += 2) {
    conv_ld<TS>(pa + 64, pb + 64, A1, B1); conv_mm<TS>(A0, B0, acc); conv_sched(); SB();
    pa += 128; pb += 128;
    conv_ld<TS>(pa, pb, A0, B0); conv_mm<TS>(A1, B1, acc); conv_sched(); SB();
  }
}
template <int TS> DI void conv2(LAS char* pa, LAS char* pb, f32x4 (&acc)[4]) {
#pragma unroll 1
  for (int r = 0; r < 2; ++r) {
    v4u A[8], B[8];
    conv_ld<TS>(pa, pb, A, B); conv_mm<TS>(A, B, acc);
    pa += 64; pb += 64;
  }
}
DI unsigned kshift2(float k1, float k2, float k3, float k4, float k5, float c0, float c1, float c2, float c3) {
  return pack2(fmaf(k4, c0, fmaf(k3, c1, fmaf(k2, c2, k1 * c3))), fmaf(k5, c0, fmaf(k4, c1, fmaf(k3, c2, k2 * c3))));
}
DI void stage_h(LAS char* Lc, v4u h, int g) {
  *(LAS v4u*)(Lc + CH_HB + 112 + g * 16) = h;
  LAS char* a = Lc + CH_HA + 114 + g * 16;
  *(LAS u16*)(a) = (u16)(h.x & 0xffff);
  *(LAS unsigned*)(a + 2) = __builtin_amdgcn_alignbit(h.y, h.x, 16);
  *(LAS v2u*)(a + 6) = v2u{__builtin_amdgcn_alignbit(h.z, h.y, 16), __builtin_amdgcn_alignbit(h.w, h.z, 16)};
  *(LAS u16*)(a + 14) = (u16)(h.w >> 16);
}
DI void stage_r(LAS char* Lc, v4u u, int g) {
  LAS char* rb = Lc + CH_R + g * 16;
  *(LAS v4u*)(rb) = u;
  { LAS char* q = rb + CH_RS - 4; *(LAS unsigned*)(q) = u.x; *(LAS v2u*)(q + 4) = v2u{u.y, u.z}; *(LAS unsigned*)(q + 12) = u.w; }
  { LAS char* q = rb + 2 * CH_RS - 8; *(LAS v2u*)(q) = v2u{u.x, u.y}; *(LAS v2u*)(q + 8) = v2u{u.z, u.w}; }
  { LAS char* q = rb + 3 * CH_RS - 12; *(LAS unsigned*)(q) = u.x; *(LAS v2u*)(q + 4) = v2u{u.y, u.z}; *(LAS unsigned*)(q + 12) = u.w; }
}
DI void conv_out(LAS char* Lc, f32x4 a, int n, int d, float dv, int e, int kq, __amdgpu_buffer_rsrc_t rws) {
  const int t = 256 * n + 64 * (e >> 2) + 2 * (e & 3) + 16 * kq;
  const unsigned ua = *(LAS unsigned*)(Lc + CH_R + 2 * (2046 - t));
  const unsigned ub = *(LAS unsigned*)(Lc + CH_R + 2 * (2038 - t));
  const unsigned c01 = pack2(a[0], a[1]), c23 = pack2(a[2], a[3]);
  const unsigned d01 = pack2(bfhi(ua) * dv, bflo(ua) * dv), d23 = pack2(bfhi(ub) * dv, bflo(ub) * dv);
  v2u o;
  o.x = pack2(bflo(c01) + bflo(d01), bfhi(c01) + bfhi(d01));
  o.y = pack2(bflo(c23) + bflo(d23), bfhi(c23) + bfhi(d23));
  const int off = Y_OFF + d * 4096 + (256 * n + 64 * (e >> 2) + 16 * kq + 4 * (e & 3)) * 2;
  __builtin_amdgcn_raw_buffer_store_b64(o, rws, off, 0, SC1);
}

__global__ __launch_bounds__(512, 2) void fused(const u16* __restrict__ x, const u16* __restrict__ kk, const u16* __restrict__ vv,
                                                const u16* __restrict__ sk, const u16* __restrict__ hk, const u16* __restrict__ Dv,
                                                const u16* __restrict__ w1, const u16* __restrict__ w2,
                                                char* ws, u16* out) {
  __shared__ __attribute__((aligned(16))) char smem[LDS_BYTES];
  LAS char* L = (LAS char*)smem;
  if (__builtin_amdgcn_readfirstlane(threadIdx.x >> 6) == 7) {
    unsigned long long pc;
    asm volatile("s_getpc_b64 %0" : "=s"(pc));
    const unsigned co = min(((blockIdx.x >> 3) & 7) * 8192u + (threadIdx.x & 63) * 128u, CODE_BYTES);
    asm volatile("s_mov_b32 m0, %0\n\ts_nop 0\n\tglobal_load_lds_dword %1, %2" :: "s"((unsigned)(uintptr_t)(L + XFS)), "v"(co), "s"(pc) : "m0");
  }
  const int b = blockIdx.x, xcd = b & 7, r = b >> 3;
  const int rg = xcd >> 1, hf = xcd & 1, pi = r >> 3, jj = r & 7;
  const int p = 4 * rg + pi, j = 7 * hf + jj;
  const bool g1 = jj < 7;
  const int mbl = 2 * pi + ((r >> 2) & 1), nbl = r & 3;
  const int mb = 8 * rg + mbl, nb = 4 * hf + nbl;
  const int lane = threadIdx.x & 63;
  const int w = __builtin_amdgcn_readfirstlane(threadIdx.x >> 6);
  const int hi = lane >> 5, aw = w & 3;
  STAMP_DECL; STAMP(0);
  const __amdgpu_buffer_rsrc_t rws = mkrsrc(ws, WS_BYTES);
  if (threadIdx.x < 16) *(LAS unsigned*)(L + CNT + threadIdx.x * 4) = 0u;
  const int dpair = 80 * nb + 2 * mb, d3 = 80 * nb + 64 + (mb >> 1), ts3 = mb & 1;
  const int e = lane & 15, kq = lane >> 4;
  const int aoff = ((e & 1) ? CH_HB : CH_HA) + (kq + (e >> 1)) * 16;
  const int boff = CH_R + (3 - (e & 3)) * CH_RS + (248 - 32 * 7 - 8 * (e >> 2) + kq) * 16;

  {
    const __amdgpu_buffer_rsrc_t rk = mkrsrc(kk, S * D * 2), rv_ = mkrsrc(vv, S * D * 2), rsk = mkrsrc(sk, D * 8);
    const int sb = 64 * mb * 1280 + 160 * nb;
    int vo[2];
#pragma unroll
    for (int i = 0; i < 2; ++i) { const int n = (8 * i + w) * 64 + lane; const int row = n / 10; vo[i] = row * 1280 + (n - row * 10) * 16; }
    GLDS(rk, L + PK + w * 1024, vo[0] + sb - 5120, 0);
    GLDS(rv_, L + PV + w * 1024, vo[0], sb);
    {
      const int n = (5 + w) * 64 + lane; const int row = n / 10; const int vo3 = row * 1280 + (n - row * 10) * 16;
      if (w < 3) GLDS(rk, L + PK + (8 + w) * 1024, vo[1] + sb - 5120, 0);
      else if (w < 5) GLDS(rv_, L + PV + (5 + w) * 1024, vo3, sb);
      else if (w == 5) GLDS(rsk, L + PS, lane * 16, nb * 640);
    }
  }
#define PREP_ITEM(tg, cg) do { \
        const int q_ = (lane & 15) >> 2, pp_ = lane & 3, tl = 16 * (tg) + 4 * kq; \
        LAS char* ka_ = L + PK + (tl + q_) * 160 + (cg) * 32 + pp_ * 8; \
        v2u k0 = trread(ka_), k1 = trread(ka_ + 4 * 160); \
        v2u v0 = trread(ka_ + (PV - PK)); \
        v2u sw = *(LAS v2u*)(L + PS + ((cg) * 16 + e) * 8); \
        float kf[8] = {bflo(k0.x), bfhi(k0.x), bflo(k0.y), bfhi(k0.y), bflo(k1.x), bfhi(k1.x), bflo(k1.y), bfhi(k1.y)}; \
        float vf[4] = {bflo(v0.x), bfhi(v0.x), bflo(v0.y), bfhi(v0.y)}; \
        const float c0 = bflo(sw.x), c1 = bfhi(sw.x), c2 = bflo(sw.y), c3 = bfhi(sw.y); \
        float u[4]; \
        _Pragma("unroll") for (int e2 = 0; e2 < 4; e2 += 2) { \
          const unsigned a = kshift2(kf[e2 + 1], kf[e2 + 2], kf[e2 + 3], kf[e2 + 4], kf[e2 + 5], c0, c1, c2, c3); \
          u[e2] = bflo(a) * vf[e2]; u[e2 + 1] = bfhi(a) * vf[e2 + 1]; } \
        v2u o = v2u{pack2(u[3], u[2]), pack2(u[1], u[0])}; \
        const int ch = (cg) * 16 + e; \
        const int pos8 = (60 - tl) >> 2; \
        *(LAS v2u*)(L + PO + ch * 128 + ((((pos8 >> 1) ^ (ch & 7)) << 4) | ((pos8 & 1) << 3))) = o; \
      } while (0)

  if (w >= 4) {
    unsigned gen;
    asm volatile("s_load_dword %0, %1, 0x0\n\ts_waitcnt lgkmcnt(0)" : "=s"(gen) : "s"(ws) : "memory");
    const unsigned tag = gen * 0x9E3779B1u + 0x85EBCA6Bu;
    STAMP(1);
    WAITV(0);
    BAR();
    if (aw == 0 && lane == 0) *(LAS unsigned*)(L + C_TAG) = tag;
    unsigned ab = 4;
#define AUXBAR() do { WAITL(); lds_add(L + C_AUX, 1u, lane); ab += 4; lds_wait(L + C_AUX, ab); } while (0)
    STAMP(2);
    if (ABL & 4) lds_wait_sleep(L + C_MMA, 44);
    {
      const __amdgpu_buffer_rsrc_t rw2 = mkrsrc(w2, F * D * 2);
#pragma unroll
      for (int i = 0; i < 3; ++i) if (4 * i + aw < 10) {
        const int n = (4 * i + aw) * 64 + lane; const int row = n / 10;
        GLDS(rw2, L + PW + (4 * i + aw) * 1024, (row + (row >= 32 ? 992 : 0)) * 1280 + (n - row * 10) * 16, 32 * mb * 1280 + 160 * nb);
      }
    }
    {
      const int g = lane >> 4, q = (lane & 15) >> 2, pp = lane & 3;
      PREP_ITEM(aw, 0); PREP_ITEM(aw, 1); PREP_ITEM(aw, 2);
      AUXBAR();
#pragma unroll
      for (int i = 0; i < 3; ++i) {
        const int ri = aw + 4 * i;
        if (ri < 10) {
          const int ch = 8 * ri + (lane >> 3), cc = lane & 7;
          v4u val = ldsr128(L + PO + ch * 128 + ((cc ^ (ch & 7)) << 4));
          __builtin_amdgcn_raw_buffer_store_b128(val, rws, (ABL & 1) ? -16 : R_OFF + (80 * nb + ch) * 4096 + (31 - mb) * 128 + cc * 16, 0, SC1);
        }
      }
      __builtin_amdgcn_s_setprio(AUX_PRIO);
      if (aw < 2) WAITV(3); else WAITV(2);
      AUXBAR();
      STAMP(3);
#pragma unroll
      for (int i = 0; i < 3; ++i) {
        const int fi = aw + 4 * i;
        if (fi < 10) {
          const int half = fi / 5, nt = fi - half * 5, ks = mb + 32 * half;
          const int r0 = 16 * (g & 1) + 4 * (g >> 1);
          LAS char* a = L + PW + half * 5120 + (r0 + q) * 160 + nt * 32 + pp * 8;
          v2u lo = trread(a), hi2 = trread(a + 8 * 160);
          __builtin_amdgcn_raw_buffer_store_b128(v4u{lo.x, lo.y, hi2.x, hi2.y}, rws, (ks < KS2 && !(ABL & 1)) ? W2P_OFF + nb * W2P_NB + ks * 5120 + nt * 1024 + lane * 16 : -16, 0, SC1);
        }
      }
    }
    STAMP(4);
    WAITV(0);
    AUXBAR();
    if (aw == 0) set_flag(ws, FL_P + (nb * 32 + mb) * 4, tag, lane);
    STAMP(5);
    if (ABL & 2) lds_wait_sleep(L + C_MMA, 44);
    const __amdgpu_buffer_rsrc_t rh = mkrsrc(hk, D * S * 2);
    v4u cu[3];
    cu[0] = __builtin_amdgcn_raw_buffer_load_b128(rh, dpair * 4096 + aw * 1024 + lane * 16, 0, 0);
    cu[1] = __builtin_amdgcn_raw_buffer_load_b128(rh, dpair * 4096 + 4096 + aw * 1024 + lane * 16, 0, 0);
    cu[2] = __builtin_amdgcn_raw_buffer_load_b128(rh, d3 * 4096 + aw * 1024 + lane * 16, 0, 0);
    const int g = aw * 64 + lane;
    if (aw == 0 && lane < 32) *(LAS v4u*)(L + CV + (lane >> 4) * CH_BYTES + ((lane >> 3) & 1) * CH_HB + (lane & 7) * 16) = v4u{0, 0, 0, 0};
    if (aw == 3) {
#pragma unroll
      for (int i = 0; i < 4; ++i) {
        const int n = lane + 64 * i;
        if (n < 200) { const int cp = n / 25, sl = n - cp * 25; *(LAS v4u*)(L + CV + (cp >> 2) * CH_BYTES + CH_R + (cp & 3) * CH_RS + 4080 + sl * 16) = v4u{0, 0, 0, 0}; }
      }
    }
    stage_h(L + CV, cu[0], g);
    stage_h(L + CV + CH_BYTES, cu[1], g);
    lds_wait_sleep(L + C_MMA, DELAY_POLL);
    if (aw == 1) {
      const int jc = g1 ? j : 0;
      v2u gr[4];
#define GRL(off) __builtin_bit_cast(v2u, __hip_atomic_load((GAS unsigned long long*)(ws + (off)), RLX_AGENT))
#pragma unroll 1
      for (unsigned sp = 0; sp < SPIN_CAP; ++sp) {
        gr[0] = GRL(ST_OFF + (p * 128 + lane) * 8);
        gr[1] = GRL(ST_OFF + (p * 128 + 64 + lane) * 8);
        gr[2] = GRL(CS_OFF + (jc * 128 + lane) * 8);
        gr[3] = GRL(CS_OFF + (jc * 128 + (j == 13 ? 0 : 64) + lane) * 8);
        if (__all(gr[0].y == tag && gr[1].y == tag && gr[2].y == tag && gr[3].y == tag)) break;
        __builtin_amdgcn_s_sleep(POLL_SLEEP);
      }
      *(LAS f32x2*)(L + XFS + lane * 8) = f32x2{bflo(gr[0].x), 1.f / bfhi(gr[0].x)};
      *(LAS f32x2*)(L + XFS + (64 + lane) * 8) = f32x2{bflo(gr[1].x), 1.f / bfhi(gr[1].x)};
      *(LAS unsigned*)(L + XFC + lane * 4) = gr[2].x;
      *(LAS unsigned*)(L + XFC + (64 + lane) * 4) = gr[3].x;
      WAITL();
      lds_add(L + C_S, 1u, lane);
    }
    if (aw == 2) { poll_flags(ws, FL_P + nb * 128, 32, tag, lane); lds_add(L + C_P, 1u, lane); }
    lds_wait_sleep(L + C_P, 1);
    STAMP(6);
    v4u rv[3];
    rv[0] = __builtin_amdgcn_raw_buffer_load_b128(rws, R_OFF + dpair * 4096 + aw * 1024 + lane * 16, 0, SC1);
    rv[1] = __builtin_amdgcn_raw_buffer_load_b128(rws, R_OFF + dpair * 4096 + 4096 + aw * 1024 + lane * 16, 0, SC1);
    rv[2] = __builtin_amdgcn_raw_buffer_load_b128(rws, R_OFF + d3 * 4096 + aw * 1024 + lane * 16, 0, SC1);
    const int rot2 = ROT2_EXPR;
    const __amdgpu_buffer_rsrc_t ra = mkrsrc(ws + HP_OFF + mb * HP_MB, HP_MB);
    const __amdgpu_buffer_rsrc_t rb = mkrsrc(ws + W2P_OFF + nb * W2P_NB, W2P_NB);
    const int va = lane * 16 + aw * 4096, vb = lane * 16 + aw * 5120;
    constexpr int WA = WA_N, WB = WB_N, NKS = 14;
    v4u fa[WA][4], fb[WB][5];
#define KIDX(i) ((i) + rot2 >= NKS ? (i) + rot2 - NKS : (i) + rot2)
#define LOADA(i) do { const int so_ = KIDX(i) * 16384; \
    _Pragma("unroll") for (int m = 0; m < 4; ++m) fa[(i) % WA][m] = __builtin_amdgcn_raw_buffer_load_b128(ra, va + m * 1024, so_, SC1); } while (0)
#define LOADB(i) do { const int so_ = KIDX(i) * 20480; \
    _Pragma("unroll") for (int n = 0; n < 5; ++n) fb[(i) % WB][n] = __builtin_amdgcn_raw_buffer_load_b128(rb, vb + (n & 3) * 1024, so_ + (n >> 2) * 4096, SC1); } while (0)
    LOADB(0); LOADB(1);
    SB();
    const float dv = bflo(Dv[dpair + (aw >> 1)]);
    stage_r(L + CV, rv[0], g);
    stage_r(L + CV + CH_BYTES, rv[1], g);
    SB();
#pragma unroll
    for (int i = 2; i < WB; ++i) LOADB(i);
    SB();
    STAMP(7);
    lds_wait_sleep(L + C_MMA, 36);
    if (aw == 0 && lane < 16) *(LAS v4u*)(L + CV3 + (lane >> 3) * CH_HB + (lane & 7) * 16) = v4u{0, 0, 0, 0};
    if (aw == 3) {
#pragma unroll
      for (int i = 0; i < 2; ++i) {
        const int n = lane + 64 * i;
        if (n < 100) { const int cp = n / 25, sl = n - cp * 25; *(LAS v4u*)(L + CV3 + CH_R + cp * CH_RS + 4080 + sl * 16) = v4u{0, 0, 0, 0}; }
      }
    }
    stage_h(L + CV3, cu[2], g);
    stage_r(L + CV3, rv[2], g);
    AUXBAR();
    if (aw == 0) lds_add(L + C_ST, 1u, lane);
    lds_wait_sleep(L + C_MMA, CONV_START);
    __builtin_amdgcn_s_setprio(CONV_PRIO);
    STAMP(8);
    {
      LAS char* Lc = L + CV + (aw >> 1) * CH_BYTES;
      const int cts = aw & 1;
      f32x4 acc[4] = {};
      if (cts == 0) conv8<0>(Lc + aoff, Lc + boff, acc); else conv8<1>(Lc + aoff, Lc + boff, acc);
      STAMP(9);
#pragma unroll
      for (int ti = 0; ti < 4; ++ti) conv_out(Lc, acc[ti], cts == 0 ? (ti < 2 ? ti : ti + 4) : 2 + ti, dpair + (aw >> 1), dv, e, kq, rws);
      WAITV(0); WAITL();
      unsigned old = 0;
      if (lane == 0) old = __hip_atomic_fetch_add((LAS unsigned*)(L + C_Y), 1u, RLX_WG);
      if (__builtin_amdgcn_readfirstlane(old) == 7) set_flag(ws, FL_Y + (nb * 32 + mb) * 4, tag, lane);
    }
    STAMP(10);
    __builtin_amdgcn_s_setprio(0);
    lds_wait_sleep(L + C_HR, 1);
    STAMP(11);
#pragma unroll
    for (int i = 0; i < WA; ++i) LOADA(i);
    SB();
    f32x4 acc2[4][5] = {};
#define LOADA1(i, m) fa[(i) % WA][m] = __builtin_amdgcn_raw_buffer_load_b128(ra, va + (m) * 1024, KIDX(i) * 16384, SC1)
#define LOADB1(i, n) fb[(i) % WB][n] = __builtin_amdgcn_raw_buffer_load_b128(rb, vb + ((n) & 3) * 1024, KIDX(i) * 20480 + ((n) >> 2) * 4096, SC1)
#pragma unroll
    for (int i = 0; i < NKS; ++i) {
#pragma unroll
      for (int mn = 0; mn < 20; ++mn) {
        const int m = mn / 5, n = mn % 5;
        acc2[m][n] = mfma16(fb[i % WB][n], fa[i % WA][m], acc2[m][n]);
        if (i > 0 && (mn & 1)) {
          const int k = mn >> 1;
          SB();
          if (k < 4) { if (i - 1 + WA < NKS) LOADA1(i - 1 + WA, k); }
          else if (k < 9) { if (i - 1 + WB < NKS) LOADB1(i - 1 + WB, k - 4); }
          SB();
        }
      }
      SB();
      if (i == 0) STAMP(12);
    }
    STAMP(13);
    lds_wait(L + C_Y, 8);
    const int ln = __builtin_amdgcn_mbcnt_hi(~0u, __builtin_amdgcn_mbcnt_lo(~0u, opaque0()));
#pragma unroll
    for (int m = 0; m < 4; ++m)
#pragma unroll
      for (int n = 0; n < 5; ++n)
        *(LAS f32x4*)(L + aw * PART + (16 * m + (ln & 15)) * PSTR + n * 64 + (ln >> 4) * 16) = acc2[m][n];
  } else {
    __builtin_amdgcn_s_setprio(MMA_PRIO);
    const __amdgpu_buffer_rsrc_t rw = mkrsrc(w1, D * F * 2);
    unsigned tag;
    if (g1) {
      const __amdgpu_buffer_rsrc_t rx = mkrsrc(x + (size_t)p * 128 * D, 128 * D * 2);
      int vx[2], vw[4];
      {
        const int row = 32 * w + (lane >> 3);
        vx[0] = row * 1280 + (((lane & 7) ^ ((row >> 1) & 7)) << 4);
        vx[1] = (row + 8) * 1280 + (((lane & 7) ^ (((row + 8) >> 1) & 7)) << 4);
#pragma unroll
        for (int ii = 0; ii < 4; ++ii) {
          const int kr = 16 * w + 4 * ii + (lane >> 4);
          vw[ii] = kr * (F * 2) + j * 256 + (((lane & 15) ^ (((kr & 3) << 2) | ((kr >> 2) & 3))) << 4);
        }
      }
      const int rot = ROT_EXPR;
      const unsigned lbase = (unsigned)(uintptr_t)L + w * 4096;
#define KC(s) ((s) + rot >= 10 ? (s) + rot - 10 : (s) + rot)
#define G1LD(s, i) do { \
    if ((i) < 4) glds_fast(rx, lbase + ((s) % NBUF) * 32768 + (i) * 1024, vx[(i) & 1], KC(s) * 128 + ((i) >> 1) * 16 * 1280); \
    else glds_fast(rw, lbase + ((s) % NBUF) * 32768 + 16384 + ((i) - 4) * 1024, vw[((i) - 4) & 3], KC(s) * (64 * F * 2)); \
  } while (0)
#pragma unroll
      for (int i = 0; i < 8; ++i) G1LD(0, i);
      WAITV(8);
      BAR();
      PREP_ITEM(aw, 3); PREP_ITEM(aw, 4);
      WAITL(); lds_add(L + C_AUX, 1u, lane);
      SB();
#pragma unroll
      for (int i = 0; i < 8; ++i) G1LD(1, i);
      STAMP(1);
      const int fh = w & 1, th = w >> 1;
      int xa[4], wa[2][2];
      {
        const int row = 64 * th + (lane & 31);
        const int c0 = hi ^ ((row >> 1) & 7);
        const int blk = (lane >> 4) & 1, q = (lane & 15) >> 2, pp = lane & 3;
#pragma unroll
        for (int s = 0; s < 4; ++s) xa[s] = row * 128 + (((2 * s) ^ c0) << 4);
#pragma unroll
        for (int ft = 0; ft < 2; ++ft)
#pragma unroll
          for (int t = 0; t < 2; ++t) {
            const int kr = 8 * hi + 4 * t + q;
            const int ch = 4 * (2 * fh + ft) + 2 * blk + (pp >> 1);
            wa[ft][t] = 16384 + 256 * kr + ((ch ^ ((q << 2) | ((2 * hi + t) & 3))) << 4) + 8 * (pp & 1);
          }
      }
      f32x16 acc[2][2] = {};
      Frags fA, fB;
#define RDX(f, bo, s, tt) f.x[s][tt] = ldsr128(L + (bo) + xa[s] + (tt) * 4096)
#define RDW(f, bo, s, ft) do { v2u wl_ = trread(L + (bo) + wa[ft][0] + (s) * 4096), wh_ = trread(L + (bo) + wa[ft][1] + (s) * 4096); f.w[s][ft] = v4u{wl_.x, wl_.y, wh_.x, wh_.y}; } while (0)
#define MM(f, s, ft, tt) acc[ft][tt] = mfma32(f.w[s][ft], f.x[s][tt], acc[ft][tt])
#define ITER(s, vm, cur, nxt) do { \
    asm volatile("s_waitcnt vmcnt(" #vm ") lgkmcnt(0)" ::: "memory"); \
    lds_add(L + C_MMA, 1u, lane); SB(); \
    MM(cur, 0, 0, 0); MM(cur, 0, 0, 1); MM(cur, 0, 1, 0); SB(); \
    lds_wait(L + C_MMA, 4 * ((s) + 2)); SB(); \
    constexpr int bo_ = (((s) + 1) % NBUF) * 32768; constexpr bool ld_ = (s) + NBUF < 10; \
    MM(cur, 0, 1, 1); RDX(nxt, bo_, 0, 0); RDX(nxt, bo_, 0, 1); SB(); if (ld_) G1LD((s) + NBUF, 0); SB(); \
    MM(cur, 1, 0, 0); RDW(nxt, bo_, 0, 0); SB(); \
    MM(cur, 1, 0, 1); RDW(nxt, bo_, 0, 1); SB(); if (ld_) G1LD((s) + NBUF, 4); SB(); \
    MM(cur, 1, 1, 0); RDX(nxt, bo_, 1, 0); RDX(nxt, bo_, 1, 1); SB(); if (ld_) G1LD((s) + NBUF, 1); SB(); \
    MM(cur, 1, 1, 1); RDW(nxt, bo_, 1, 0); SB(); \
    MM(cur, 2, 0, 0); RDW(nxt, bo_, 1, 1); SB(); if (ld_) G1LD((s) + NBUF, 5); SB(); \
    MM(cur, 2, 0, 1); RDX(nxt, bo_, 2, 0); RDX(nxt, bo_, 2, 1); SB(); if (ld_) G1LD((s) + NBUF, 2); SB(); \
    MM(cur, 2, 1, 0); RDW(nxt, bo_, 2, 0); SB(); \
    MM(cur, 2, 1, 1); RDW(nxt, bo_, 2, 1); SB(); if (ld_) G1LD((s) + NBUF, 6); SB(); \
    MM(cur, 3, 0, 0); RDX(nxt, bo_, 3, 0); RDX(nxt, bo_, 3, 1); SB(); if (ld_) G1LD((s) + NBUF, 3); SB(); \
    MM(cur, 3, 0, 1); RDW(nxt, bo_, 3, 0); RDW(nxt, bo_, 3, 1); SB(); \
    MM(cur, 3, 1, 0); SB(); if (ld_) G1LD((s) + NBUF, 7); SB(); \
    MM(cur, 3, 1, 1); SB(); \
  } while (0)
      WAITV(8);
      lds_add(L + C_MMA, 1u, lane); lds_wait(L + C_MMA, 4);
      STAMP(2);
#pragma unroll
      for (int s = 0; s < 4; ++s) { RDX(fA, 0, s, 0); RDX(fA, 0, s, 1); RDW(fA, 0, s, 0); RDW(fA, 0, s, 1); }
      SB();
#pragma unroll
      for (int i = 0; i < 8; ++i) G1LD(2, i);
      SB();
      ITER(0, 8, fA, fB);
      ITER(1, 8, fB, fA);
      ITER(2, 8, fA, fB);
      STAMP(3);
      ITER(3, 8, fB, fA);
      ITER(4, 8, fA, fB);
      ITER(5, 8, fB, fA);
      STAMP(4);
      ITER(6, 8, fA, fB);
      ITER(7, 8, fB, fA);
      ITER(8, 0, fA, fB);
#pragma unroll
      for (int s = 0; s < 4; ++s) { MM(fB, s, 0, 0); MM(fB, s, 0, 1); MM(fB, s, 1, 0); MM(fB, s, 1, 1); }
      SB();
      lds_add(L + C_MMA, 1u, lane);
      STAMP(5);
      lds_wait_sleep(L + C_S, 1);
      tag = __builtin_amdgcn_readfirstlane(*(LAS unsigned*)(L + C_TAG));
      STAMP(6);
#pragma unroll
      for (int ft = 0; ft < 2; ++ft) {
        const int fg = 2 * fh + ft;
        const int ks = 4 * j + fg;
        f32x4 c4[4];
#pragma unroll
        for (int g = 0; g < 4; ++g) c4[g] = *(LAS f32x4*)(L + XFC + (32 * fg + 8 * g + 4 * hi) * 4);
#pragma unroll
        for (int tt = 0; tt < 2; ++tt) {
          const int tok = 64 * th + 32 * tt + (lane & 31);
          const f32x2 st = *(LAS f32x2*)(L + XFS + tok * 8);
          const f32x16& a = acc[ft][tt];
          const int mb2 = 2 * p + th, mt = 2 * tt + ((lane >> 4) & 1);
          const int off = ks < KS2 ? HP_OFF + mb2 * HP_MB + ks * 4096 + mt * 1024 + ((2 * hi) * 16 + (lane & 15)) * 16 : -1024;
#pragma unroll
          for (int hh = 0; hh < 2; ++hh) {
            unsigned pk[4];
#pragma unroll
            for (int e2 = 0; e2 < 4; ++e2) {
              const int ee = hh * 4 + e2;
              float v0 = (a[2 * ee] - st.x * c4[ee >> 1][(2 * ee) & 3]) * st.y;
              float v1 = (a[2 * ee + 1] - st.x * c4[ee >> 1][(2 * ee + 1) & 3]) * st.y;
              unsigned hb = pack2(v0, v1);
              pk[e2] = pack2(silu_f(bflo(hb)), silu_f(bfhi(hb)));
            }
            __builtin_amdgcn_raw_buffer_store_b128(v4u{pk[0], pk[1], pk[2], pk[3]}, rws, off + hh * 256, 0, SC1);
            SB();
          }
        }
      }
      STAMP(7);
      WAITV(0);
      {
        unsigned old = 0;
        if (lane == 0) old = __hip_atomic_fetch_add((LAS unsigned*)(L + C_H), 1u, RLX_WG);
        if (__builtin_amdgcn_readfirstlane(old) == 3) set_flag(ws, FL_H + (p * 16 + j) * 4, tag, lane);
      }
      STAMP(8);
    } else {
      const int idx16 = 4 * rg + pi;
      const int srow = 512 * rg + 64 * (4 * hf + pi) + 16 * w + (lane >> 3);
      const int cg = 14 * hf + idx16;
      const bool csv = idx16 < 14 - hf;
      const __amdgpu_buffer_rsrc_t rx = mkrsrc(x, S * D * 2);
      v4u xv[2][10], wv[20];
      WAITV(0);
      BAR();
      if (ABL & 8) for (int i = 0; i < 150; ++i) __builtin_amdgcn_s_sleep(1);
#pragma unroll
      for (int h2 = 0; h2 < 2; ++h2)
#pragma unroll
        for (int i = 0; i < 10; ++i) xv[h2][i] = __builtin_amdgcn_raw_buffer_load_b128(rx, (srow + 8 * h2) * 1280 + (lane & 7) * 16 + i * 128, 0, 0);
      PREP_ITEM(aw, 3); PREP_ITEM(aw, 4);
      WAITL(); lds_add(L + C_AUX, 1u, lane);
      const int wvo = csv ? (160 * w + (lane >> 3)) * (F * 2) + cg * 128 + (lane & 7) * 16 : -16;
#pragma unroll
      for (int i = 0; i < 20; ++i) wv[i] = __builtin_amdgcn_raw_buffer_load_b128(rw, wvo, csv ? i * 8 * F * 2 : 0, 0);
      lds_wait_sleep(L + C_AUX, 8);
      tag = __builtin_amdgcn_readfirstlane(*(LAS unsigned*)(L + C_TAG));
#pragma unroll
      for (int h2 = 0; h2 < 2; ++h2) {
        float s = 0.f;
#pragma unroll
        for (int i = 0; i < 10; ++i)
#pragma unroll
          for (int e2 = 0; e2 < 4; ++e2) s = dot2(xv[h2][i][e2], 0x3f803f80u, s);
        s += __shfl_xor(s, 1); s += __shfl_xor(s, 2); s += __shfl_xor(s, 4);
        const float mean = s * (1.f / D);
        float q0 = 0.f, q1 = 0.f;
#pragma unroll
        for (int i = 0; i < 10; ++i)
#pragma unroll
          for (int e2 = 0; e2 < 4; ++e2) { const float a0 = bflo(xv[h2][i][e2]) - mean, a1 = bfhi(xv[h2][i][e2]) - mean; q0 = fmaf(a0, a0, q0); q1 = fmaf(a1, a1, q1); }
        float q = q0 + q1;
        q += __shfl_xor(q, 1); q += __shfl_xor(q, 2); q += __shfl_xor(q, 4);
        const float den = __builtin_sqrtf(rbf(rbf(q * (1.f / D)) + 1e-5f));
        __builtin_amdgcn_raw_buffer_store_b64(v2u{pack2(mean, den), tag}, rws, (lane & 7) == 0 ? ST_OFF + (srow + 8 * h2) * 8 : -16, 0, SC1);
      }
      float c[8] = {};
#pragma unroll
      for (int i = 0; i < 20; ++i)
#pragma unroll
        for (int e2 = 0; e2 < 4; ++e2) { c[2 * e2] += bflo(wv[i][e2]); c[2 * e2 + 1] += bfhi(wv[i][e2]); }
      *(LAS f32x4*)(L + ((w * 8 + (lane >> 3)) * 64 + (lane & 7) * 8) * 4) = f32x4{c[0], c[1], c[2], c[3]};
      *(LAS f32x4*)(L + ((w * 8 + (lane >> 3)) * 64 + (lane & 7) * 8 + 4) * 4) = f32x4{c[4], c[5], c[6], c[7]};
      WAITL();
      unsigned old = 0;
      if (lane == 0) old = __hip_atomic_fetch_add((LAS unsigned*)(L + C_H), 1u, RLX_WG);
      if (__builtin_amdgcn_readfirstlane(old) == 3) {
        asm volatile("" ::: "memory");
        float t4[4] = {};
#pragma unroll
        for (int i = 0; i < 32; ++i) t4[i & 3] += *(LAS float*)(L + (i * 64 + lane) * 4);
        __builtin_amdgcn_raw_buffer_store_b64(v2u{__float_as_uint((t4[0] + t4[1]) + (t4[2] + t4[3])), tag}, rws, csv ? CS_OFF + (cg * 64 + lane) * 8 : -16, 0, SC1);
      }
      lds_add(L + C_MMA, 11u, lane);
      STAMP(5);
    }
    unsigned hb_ = 0;
#define HBAR() do { WAITL(); lds_add(L + C_G2, 1u, lane); hb_ += 4; lds_wait(L + C_G2, hb_); } while (0)
    {
      const float dv3 = bflo(Dv[d3]);
      lds_wait_sleep(L + C_ST, 1);
      f32x4 ac3[4] = {};
      if (ts3 == 0) conv2<0>(L + CV3 + aoff + aw * 128, L + CV3 + boff + aw * 128, ac3); else conv2<1>(L + CV3 + aoff + aw * 128, L + CV3 + boff + aw * 128, ac3);
#pragma unroll
      for (int ti = 0; ti < 4; ++ti) *(LAS f32x4*)(L + SC3 + (aw * 4 + ti) * 1024 + lane * 16) = ac3[ti];
      HBAR();
      f32x4 a = {};
#pragma unroll
      for (int ww = 0; ww < 4; ++ww) a += *(LAS f32x4*)(L + SC3 + (ww * 4 + aw) * 1024 + lane * 16);
      conv_out(L + CV3, a, ts3 == 0 ? (aw < 2 ? aw : aw + 4) : 2 + aw, d3, dv3, e, kq, rws);
      STAMP(9);
      WAITV(0); WAITL();
      unsigned old = 0;
      if (lane == 0) old = __hip_atomic_fetch_add((LAS unsigned*)(L + C_Y), 1u, RLX_WG);
      if (__builtin_amdgcn_readfirstlane(old) == 7) set_flag(ws, FL_Y + (nb * 32 + mb) * 4, tag, lane);
    }
    STAMP(10);
    if (aw == 1) { poll_flags(ws, FL_H + p * 64, 14, tag, lane); lds_add(L + C_HR, 1u, lane); }
    lds_wait_sleep(L + C_Y, 8);
    {
      const __amdgpu_buffer_rsrc_t rxx = mkrsrc(x, S * D * 2);
#pragma unroll
      for (int i = 0; i < 3; ++i) {
        const int s = aw + 4 * i;
        if (s < 10) {
          const int n = s * 64 + lane; const int row = n / 10, c = n - row * 10;
          GLDS(rxx, L + EX + s * 1024, (64 * mb + row) * 1280 + 160 * nb + c * 16, 0);
        }
      }
    }
    if (aw == 0) { poll_flags(ws, FL_Y + nb * 128, 32, tag, lane); lds_add(L + C_YR, 1u, lane); }
    lds_wait_sleep(L + C_YR, 1);
    STAMP(11);
#pragma unroll
    for (int i = 0; i < 3; ++i) {
      const int ri = aw + 4 * i;
      if (ri < 10) {
        const int ch = 8 * ri + (lane >> 3), cc = lane & 7;
        v4u yv = __builtin_amdgcn_raw_buffer_load_b128(rws, Y_OFF + (80 * nb + ch) * 4096 + mb * 128 + cc * 16, 0, SC1);
        *(LAS v4u*)(L + EY + ch * 144 + cc * 16) = yv;
      }
    }
    WAITV(0);
    HBAR();
    STAMP(12);
    {
      const int q = (lane & 15) >> 2, pp = lane & 3, pos = lane & 15;
      const int tok = 16 * aw + 2 * (pos >> 2) + (pos & 1) + 8 * ((pos >> 1) & 1);
      const f32x2 st = *(LAS f32x2*)(L + XFS + ((mb & 1) * 64 + tok) * 8);
#pragma unroll
      for (int i = 0; i < 5; ++i) {
        const int dq = 4 * i + kq;
        const v2u yv = trread(L + EY + (4 * dq + q) * 144 + aw * 32 + pp * 8);
        const v2u xv2 = *(LAS v2u*)(L + EX + tok * 160 + dq * 8);
        const unsigned d01 = pack2(bflo(xv2.x) - st.x, bfhi(xv2.x) - st.x), d23 = pack2(bflo(xv2.y) - st.x, bfhi(xv2.y) - st.x);
        const unsigned n01 = pack2(bflo(d01) * st.y, bfhi(d01) * st.y), n23 = pack2(bflo(d23) * st.y, bfhi(d23) * st.y);
        const unsigned s01 = pack2(bflo(n01) * bflo(yv.x), bfhi(n01) * bfhi(yv.x)), s23 = pack2(bflo(n23) * bflo(yv.y), bfhi(n23) * bfhi(yv.y));
        *(LAS v4u*)(L + ES + (tok * 20 + dq) * 16) = v4u{s01, s23, xv2.x, xv2.y};
      }
    }
    if (b == 0 && aw == 0) {
#pragma unroll 1
      for (int i = 0; i < 4; ++i) poll_flags(ws, FL_P + i * 256, 64, tag, lane);
      set_flag(ws, FL_GEN, tag, lane);
    }
  }
  STAMP(14);
  BAR();
  const int lane2 = __builtin_amdgcn_mbcnt_hi(~0u, __builtin_amdgcn_mbcnt_lo(~0u, opaque0()));
#pragma unroll
  for (int i = 0; i < 3; ++i) {
    const int it = w + 8 * i;
    if (it < 20) {
      const int n = it * 64 + lane2;
      const int tok = n / 20, dq = n - tok * 20;
      f32x4 o = *(LAS f32x4*)(L + tok * PSTR + dq * 16);
#pragma unroll
      for (int ww = 1; ww < 4; ++ww) o += *(LAS f32x4*)(L + ww * PART + tok * PSTR + dq * 16);
      const v4u sx = *(LAS v4u*)(L + ES + n * 16);
      const unsigned o01 = pack2(o[0], o[1]), o23 = pack2(o[2], o[3]);
      const unsigned g01 = pack2(silu_f(bflo(o01)), silu_f(bfhi(o01))), g23 = pack2(silu_f(bflo(o23)), silu_f(bfhi(o23)));
      const unsigned m01 = pack2(bflo(sx.x) * bflo(g01), bfhi(sx.x) * bfhi(g01)), m23 = pack2(bflo(sx.y) * bflo(g23), bfhi(sx.y) * bfhi(g23));
      v2u res;
      res.x = pack2(bflo(sx.z) + bflo(m01), bfhi(sx.z) + bfhi(m01));
      res.y = pack2(bflo(sx.w) + bflo(m23), bfhi(sx.w) + bfhi(m23));
      *(v2u*)(out + (size_t)(64 * mb + tok) * D + 80 * nb + dq * 4) = res;
    }
  }
  STAMP(15);
  STAMP_OUT(0);
}

extern "C" void kernel_launch(void* const* d_in, const int* in_sizes, int n_in,
                              void* d_out, int out_size, void* d_ws, size_t ws_size,
                              hipStream_t stream) {
  fused<<<256, 512, 0, stream>>>((const u16*)d_in[0], (const u16*)d_in[1], (const u16*)d_in[2], (const u16*)d_in[3],
                                 (const u16*)d_in[4], (const u16*)d_in[5], (const u16*)d_in[7], (const u16*)d_in[8],
                                 (char*)d_ws, (u16*)d_out);
}
```

```cpp
#define EXF2
#define EXF_BLK 0
#define PREK 0
#define POSTK 0
#include <hip/hip_runtime.h>
#include <stdint.h>

typedef unsigned short u16;
typedef unsigned v4u __attribute__((ext_vector_type(4)));
typedef unsigned v2u __attribute__((ext_vector_type(2)));
typedef float f32x2 __attribute__((ext_vector_type(2)));
typedef float f32x4 __attribute__((ext_vector_type(4)));
typedef float f32x16 __attribute__((ext_vector_type(16)));
typedef short bf16x8 __attribute__((ext_vector_type(8)));
typedef short s16x4 __attribute__((ext_vector_type(4)));
typedef __bf16 bf16x2 __attribute__((ext_vector_type(2)));
#define LAS __attribute__((address_space(3)))
#define GAS __attribute__((address_space(1)))
#define DI __device__ __forceinline__
#define RLX_AGENT __ATOMIC_RELAXED, __HIP_MEMORY_SCOPE_AGENT
#define RLX_WG __ATOMIC_RELAXED, __HIP_MEMORY_SCOPE_WORKGROUP

constexpr int S = 2048, D = 640, F = 1728;
constexpr int KS2 = 54;
constexpr int FL_GEN = 0, FL_P = 4096, FL_H = 12288, FL_Y = 16384;
constexpr int ST_OFF = 32768;
constexpr int CS_OFF = ST_OFF + S * 8;
constexpr int HP_OFF = 65536;
constexpr int HP_MB = KS2 * 4096;
constexpr int W2P_OFF = HP_OFF + 32 * HP_MB;
constexpr int W2P_NB = KS2 * 5120;
constexpr int R_OFF = W2P_OFF + 8 * W2P_NB;
constexpr int Y_OFF = R_OFF + D * S * 2;
constexpr int WS_BYTES = Y_OFF + D * S * 2;
constexpr unsigned RSRC3 = 0x00020000u;
constexpr int SC1 = 16;
constexpr unsigned SPIN_CAP = 1u << 18;

DI float bflo(unsigned u) { return __uint_as_float(u << 16); }
DI float bfhi(unsigned u) { return __uint_as_float(u & 0xffff0000u); }
DI unsigned pack2(float a, float b) {
  bf16x2 r = __builtin_convertvector(f32x2{a, b}, bf16x2);
  return __builtin_bit_cast(unsigned, r);
}
DI float rbf(float a) { return bflo(pack2(a, a)); }
DI float silu_f(float x) { return x * __builtin_amdgcn_rcpf(1.f + __builtin_amdgcn_exp2f(-1.44269504088896f * x)); }
DI f32x4 mfma16(v4u a, v4u b, f32x4 c) {
  return __builtin_amdgcn_mfma_f32_16x16x32_bf16(__builtin_bit_cast(bf16x8, a), __builtin_bit_cast(bf16x8, b), c, 0, 0, 0);
}
DI f32x16 mfma32(v4u a, v4u b, f32x16 c) {
  return __builtin_amdgcn_mfma_f32_32x32x16_bf16(__builtin_bit_cast(bf16x8, a), __builtin_bit_cast(bf16x8, b), c, 0, 0, 0);
}
DI float dot2(unsigned a, unsigned b, float c) {
  return __builtin_amdgcn_fdot2_f32_bf16(__builtin_bit_cast(bf16x2, a), __builtin_bit_cast(bf16x2, b), c, false);
}
DI v2u trread(LAS char* p) { return __builtin_bit_cast(v2u, __builtin_amdgcn_ds_read_tr16_b64_v4i16((LAS s16x4*)p)); }
DI v4u ldsr128(LAS char* p) { return *(LAS v4u*)p; }
DI __amdgpu_buffer_rsrc_t mkrsrc(const void* p, int bytes) { return __builtin_amdgcn_make_buffer_rsrc((void*)p, 0, bytes, RSRC3); }
DI void glds_asm(__amdgpu_buffer_rsrc_t rs, unsigned ldsa, int voff, int soff) {
  asm volatile("s_nop 4\n\ts_mov_b32 m0, %0\n\ts_nop 0\n\tbuffer_load_dwordx4 %1, %2, %3 offen lds"
               :: "s"(ldsa), "v"(voff), "s"(rs), "s"(soff) : "m0", "memory");
}
DI void glds_fast(__amdgpu_buffer_rsrc_t rs, unsigned ldsa, int voff, int soff) {
  asm volatile("s_mov_b32 m0, %0\n\ts_nop 0\n\tbuffer_load_dwordx4 %1, %2, %3 offen lds"
               :: "s"(ldsa), "v"(voff), "s"(rs), "s"(soff) : "m0", "memory");
}
#define GLDS(rs, ldsp, voff, soff) glds_asm(rs, (unsigned)(uintptr_t)(ldsp), voff, soff)
#define WAITV(n) asm volatile("s_waitcnt vmcnt(" #n ")" ::: "memory")
#define WAITL() asm volatile("s_waitcnt lgkmcnt(0)" ::: "memory")
#define BAR() do { WAITL(); __builtin_amdgcn_s_barrier(); asm volatile("" ::: "memory"); } while (0)
#define SB() __builtin_amdgcn_sched_barrier(0)

#define STAMP_DECL unsigned long long xt0 = 0
#define STAMP(i) do { if ((i) == 0) asm volatile("s_memrealtime %0\n\ts_waitcnt lgkmcnt(0)" : "=s"(xt0) :: "memory"); } while (0)
#define STAMP_OUT(kid) do { if (threadIdx.x == 0) *(unsigned long long*)(ws + (101 << 20) + blockIdx.x * 8) = __builtin_amdgcn_s_memrealtime(); \
   if (PREK && blockIdx.x == EXF_BLK && w == 0) { unsigned long long ta = *(volatile unsigned long long*)(ws + (100 << 20)); unsigned long long t2 = __builtin_amdgcn_s_memrealtime(); const unsigned long long tgt = t2 + PREK * (xt0 - ta); while (__builtin_amdgcn_s_memrealtime() < tgt) __builtin_amdgcn_s_sleep(1); } } while (0)

#define ROT_EXPR (5 * ((pi + jj) & 1))
#define WA_N 4
#define POLL_SLEEP 8
#define CODE_PF 1
#define CODE_BYTES 41000u
#define EXACT_PREP 0
#define PREP_PRIO 3
#define CONV_PRIO 1
#define WB_N 4
#define ROT2_EXPR 0
#define DELAY_POLL 24
#define ABL 0
#define AUX_PRIO 0
#define MMA_PRIO 2
#define CONV_START 44
constexpr int NBUF = 3;
constexpr int CH_HA = 0, CH_HB = 4224, CH_R = 8448, CH_RS = 4512, CH_BYTES = CH_R + 4 * CH_RS;
constexpr int CV = 98304;
constexpr int CV3 = 32768;
constexpr int SC3 = 65536;
constexpr int PK = CV, PV = PK + 11264, PW = PV + 10240, PS = PW + 10240, PO = PS + 1024;
constexpr int EY = CV, EX = EY + 80 * 144, ES = EX + 10240;
constexpr int XFS = CV + 2 * CH_BYTES;
constexpr int XFC = XFS + 1024;
constexpr int CNT = XFC + 512;
constexpr int C_AUX = CNT, C_G2 = CNT + 4, C_Y = CNT + 8, C_H = CNT + 12, C_MMA = CNT + 16, C_S = CNT + 20, C_P = CNT + 24,
              C_TAG = CNT + 28, C_ST = CNT + 40, C_HR = CNT + 32, C_YR = CNT + 36;
constexpr int LDS_BYTES = CNT + 64;
static_assert(PO + 10240 <= XFS && ES + 20480 <= XFS && LDS_BYTES <= 163840, "lds");
constexpr int PSTR = 336, PART = 64 * PSTR;

DI void lds_add(LAS char* c, unsigned v, int lane) {
  if (lane == 0) __hip_atomic_fetch_add((LAS unsigned*)c, v, RLX_WG);
}
DI void lds_wait(LAS char* c, unsigned target) {
  while ((unsigned)__builtin_amdgcn_readfirstlane(__hip_atomic_load((LAS unsigned*)c, RLX_WG)) < target) {}
  asm volatile("" ::: "memory");
}
DI void lds_wait_sleep(LAS char* c, unsigned target) {
  while ((unsigned)__builtin_amdgcn_readfirstlane(__hip_atomic_load((LAS unsigned*)c, RLX_WG)) < target) __builtin_amdgcn_s_sleep(1);
  asm volatile("" ::: "memory");
}
DI unsigned flag_load(char* ws, int off, int n, int lane) {
  return __hip_atomic_load((GAS unsigned*)(ws + off) + (lane < n ? lane : 0), RLX_AGENT);
}
DI void poll_flags(char* ws, int off, int n, unsigned tag, int lane) {
#pragma unroll 1
  for (unsigned spins = 0; spins < SPIN_CAP; ++spins) {
    const unsigned v = flag_load(ws, off, n, lane);
    if (__all(v == tag)) break;
    __builtin_amdgcn_s_sleep(POLL_SLEEP);
  }
  __builtin_amdgcn_fence(__ATOMIC_ACQUIRE, "wavefront");
}
DI void set_flag(char* ws, int off, unsigned tag, int lane) {
  if (lane == 0) __hip_atomic_store((GAS unsigned*)(ws + off), tag, RLX_AGENT);
}

DI unsigned opaque0() { unsigned z; asm volatile("v_mov_b32 %0, 0" : "=v"(z)); return z; }
struct Frags { v4u x[4][2], w[4][2]; };

template <int TS> DI void conv_ld(LAS char* pa, LAS char* pb, v4u (&A)[8], v4u (&B)[8]) {
  constexpr int NA = TS == 0 ? 8 : 6;
#pragma unroll
  for (int a = 0; a < NA; ++a) { A[a] = ldsr128(pa + a * 512); B[a] = ldsr128(pb + (7 - a) * 512); }
}
template <int TS> DI void conv_mm(const v4u (&A)[8], const v4u (&B)[8], f32x4 (&acc)[4]) {
#pragma unroll
  for (int ti = 0; ti < 4; ++ti) {
    const int n = TS == 0 ? (ti == 0 ? 0 : ti == 1 ? 1 : ti == 2 ? 6 : 7) : 2 + ti;
#pragma unroll
    for (int a = 0; a <= n; ++a) acc[ti] = mfma16(A[a], B[n - a], acc[ti]);
  }
}
DI void conv_sched() {
#pragma unroll
  for (int i = 0; i < 16; ++i) { __builtin_amdgcn_sched_group_barrier(0x8, 1, 0); __builtin_amdgcn_sched_group_barrier(0x100, 1, 0); }
  __builtin_amdgcn_sched_group_barrier(0x8, 2, 0);
}
template <int TS> DI void conv8(LAS char* pa, LAS char* pb, f32x4 (&acc)[4]) {
  v4u A0[8], B0[8], A1[8], B1[8];
  conv_ld<TS>(pa, pb, A0, B0);
  SB();
#pragma unroll 1
  for (int r = 0; r < 8; r += 2) {
    conv_ld<TS>(pa + 64, pb + 64, A1, B1); conv_mm<TS>(A0, B0, acc); conv_sched(); SB();
    pa += 128; pb += 128;
    conv_ld<TS>(pa, pb, A0, B0); conv_mm<TS>(A1, B1, acc); conv_sched(); SB();
  }
}
template <int TS> DI void conv2(LAS char* pa, LAS char* pb, f32x4 (&acc)[4]) {
#pragma unroll 1
  for (int r = 0; r < 2; ++r) {
    v4u A[8], B[8];
    conv_ld<TS>(pa, pb, A, B); conv_mm<TS>(A, B, acc);
    pa += 64; pb += 64;
  }
}
DI unsigned kshift2(float k1, float k2, float k3, float k4, float k5, float c0, float c1, float c2, float c3) {
  return pack2(fmaf(k4, c0, fmaf(k3, c1, fmaf(k2, c2, k1 * c3))), fmaf(k5, c0, fmaf(k4, c1, fmaf(k3, c2, k2 * c3))));
}
DI void stage_h(LAS char* Lc, v4u h, int g) {
  *(LAS v4u*)(Lc + CH_HB + 112 + g * 16) = h;
  LAS char* a = Lc + CH_HA + 114 + g * 16;
  *(LAS u16*)(a) = (u16)(h.x & 0xffff);
  *(LAS unsigned*)(a + 2) = __builtin_amdgcn_alignbit(h.y, h.x, 16);
  *(LAS v2u*)(a + 6) = v2u{__builtin_amdgcn_alignbit(h.z, h.y, 16), __builtin_amdgcn_alignbit(h.w, h.z, 16)};
  *(LAS u16*)(a + 14) = (u16)(h.w >> 16);
}
DI void stage_r(LAS char* Lc, v4u u, int g) {
  LAS char* rb = Lc + CH_R + g * 16;
  *(LAS v4u*)(rb) = u;
  { LAS char* q = rb + CH_RS - 4; *(LAS unsigned*)(q) = u.x; *(LAS v2u*)(q + 4) = v2u{u.y, u.z}; *(LAS unsigned*)(q + 12) = u.w; }
  { LAS char* q = rb + 2 * CH_RS - 8; *(LAS v2u*)(q) = v2u{u.x, u.y}; *(LAS v2u*)(q + 8) = v2u{u.z, u.w}; }
  { LAS char* q = rb + 3 * CH_RS - 12; *(LAS unsigned*)(q) = u.x; *(LAS v2u*)(q + 4) = v2u{u.y, u.z}; *(LAS unsigned*)(q + 12) = u.w; }
}
DI void conv_out(LAS char* Lc, f32x4 a, int n, int d, float dv, int e, int kq, __amdgpu_buffer_rsrc_t rws) {
  const int t = 256 * n + 64 * (e >> 2) + 2 * (e & 3) + 16 * kq;
  const unsigned ua = *(LAS unsigned*)(Lc + CH_R + 2 * (2046 - t));
  const unsigned ub = *(LAS unsigned*)(Lc + CH_R + 2 * (2038 - t));
  const unsigned c01 = pack2(a[0], a[1]), c23 = pack2(a[2], a[3]);
  const unsigned d01 = pack2(bfhi(ua) * dv, bflo(ua) * dv), d23 = pack2(bfhi(ub) * dv, bflo(ub) * dv);
  v2u o;
  o.x = pack2(bflo(c01) + bflo(d01), bfhi(c01) + bfhi(d01));
  o.y = pack2(bflo(c23) + bflo(d23), bfhi(c23) + bfhi(d23));
  const int off = Y_OFF + d * 4096 + (256 * n + 64 * (e >> 2) + 16 * kq + 4 * (e & 3)) * 2;
  __builtin_amdgcn_raw_buffer_store_b64(o, rws, off, 0, SC1);
}

__global__ __launch_bounds__(512, 2) void fused(const u16* __restrict__ x, const u16* __restrict__ kk, const u16* __restrict__ vv,
                                                const u16* __restrict__ sk, const u16* __restrict__ hk, const u16* __restrict__ Dv,
                                                const u16* __restrict__ w1, const u16* __restrict__ w2,
                                                char* ws, u16* out) {
  __shared__ __attribute__((aligned(16))) char smem[LDS_BYTES];
  LAS char* L = (LAS char*)smem;
  if (__builtin_amdgcn_readfirstlane(threadIdx.x >> 6) == 7) {
    unsigned long long pc;
    asm volatile("s_getpc_b64 %0" : "=s"(pc));
    const unsigned co = min(((blockIdx.x >> 3) & 7) * 8192u + (threadIdx.x & 63) * 128u, CODE_BYTES);
    asm volatile("s_mov_b32 m0, %0\n\ts_nop 0\n\tglobal_load_lds_dword %1, %2" :: "s"((unsigned)(uintptr_t)(L + XFS)), "v"(co), "s"(pc) : "m0");
  }
  const int b = blockIdx.x, xcd = b & 7, r = b >> 3;
  const int rg = xcd >> 1, hf = xcd & 1, pi = r >> 3, jj = r & 7;
  const int p = 4 * rg + pi, j = 7 * hf + jj;
  const bool g1 = jj < 7;
  const int mbl = 2 * pi + ((r >> 2) & 1), nbl = r & 3;
  const int mb = 8 * rg + mbl, nb = 4 * hf + nbl;
  const int lane = threadIdx.x & 63;
  const int w = __builtin_amdgcn_readfirstlane(threadIdx.x >> 6);
  const int hi = lane >> 5, aw = w & 3;
  STAMP_DECL; STAMP(0);
  const __amdgpu_buffer_rsrc_t rws = mkrsrc(ws, WS_BYTES);
  if (threadIdx.x < 16) *(LAS unsigned*)(L + CNT + threadIdx.x * 4) = 0u;
  const int dpair = 80 * nb + 2 * mb, d3 = 80 * nb + 64 + (mb >> 1), ts3 = mb & 1;
  const int e = lane & 15, kq = lane >> 4;
  const int aoff = ((e & 1) ? CH_HB : CH_HA) + (kq + (e >> 1)) * 16;
  const int boff = CH_R + (3 - (e & 3)) * CH_RS + (248 - 32 * 7 - 8 * (e >> 2) + kq) * 16;

  {
    const __amdgpu_buffer_rsrc_t rk = mkrsrc(kk, S * D * 2), rv_ = mkrsrc(vv, S * D * 2), rsk = mkrsrc(sk, D * 8);
    const int sb = 64 * mb * 1280 + 160 * nb;
    int vo[2];
#pragma unroll
    for (int i = 0; i < 2; ++i) { const int n = (8 * i + w) * 64 + lane; const int row = n / 10; vo[i] = row * 1280 + (n - row * 10) * 16; }
    GLDS(rk, L + PK + w * 1024, vo[0] + sb - 5120, 0);
    GLDS(rv_, L + PV + w * 1024, vo[0], sb);
    {
      const int n = (5 + w) * 64 + lane; const int row = n / 10; const int vo3 = row * 1280 + (n - row * 10) * 16;
      if (w < 3) GLDS(rk, L + PK + (8 + w) * 1024, vo[1] + sb - 5120, 0);
      else if (w < 5) GLDS(rv_, L + PV + (5 + w) * 1024, vo3, sb);
      else if (w == 5) GLDS(rsk, L + PS, lane * 16, nb * 640);
    }
  }
#define PREP_ITEM(tg, cg) do { \
        const int q_ = (lane & 15) >> 2, pp_ = lane & 3, tl = 16 * (tg) + 4 * kq; \
        LAS char* ka_ = L + PK + (tl + q_) * 160 + (cg) * 32 + pp_ * 8; \
        v2u k0 = trread(ka_), k1 = trread(ka_ + 4 * 160); \
        v2u v0 = trread(ka_ + (PV - PK)); \
        v2u sw = *(LAS v2u*)(L + PS + ((cg) * 16 + e) * 8); \
        float kf[8] = {bflo(k0.x), bfhi(k0.x), bflo(k0.y), bfhi(k0.y), bflo(k1.x), bfhi(k1.x), bflo(k1.y), bfhi(k1.y)}; \
        float vf[4] = {bflo(v0.x), bfhi(v0.x), bflo(v0.y), bfhi(v0.y)}; \
        const float c0 = bflo(sw.x), c1 = bfhi(sw.x), c2 = bflo(sw.y), c3 = bfhi(sw.y); \
        float u[4]; \
        _Pragma("unroll") for (int e2 = 0; e2 < 4; e2 += 2) { \
          const unsigned a = kshift2(kf[e2 + 1], kf[e2 + 2], kf[e2 + 3], kf[e2 + 4], kf[e2 + 5], c0, c1, c2, c3); \
          u[e2] = bflo(a) * vf[e2]; u[e2 + 1] = bfhi(a) * vf[e2 + 1]; } \
        v2u o = v2u{pack2(u[3], u[2]), pack2(u[1], u[0])}; \
        const int ch = (cg) * 16 + e; \
        const int pos8 = (60 - tl) >> 2; \
        *(LAS v2u*)(L + PO + ch * 128 + ((((pos8 >> 1) ^ (ch & 7)) << 4) | ((pos8 & 1) << 3))) = o; \
      } while (0)

  if (w >= 4) {
    unsigned gen;
    asm volatile("s_load_dword %0, %1, 0x0\n\ts_waitcnt lgkmcnt(0)" : "=s"(gen) : "s"(ws) : "memory");
    const unsigned tag = gen * 0x9E3779B1u + 0x85EBCA6Bu;
    STAMP(1);
    WAITV(0);
    BAR();
    if (aw == 0 && lane == 0) *(LAS unsigned*)(L + C_TAG) = tag;
    unsigned ab = 4;
#define AUXBAR() do { WAITL(); lds_add(L + C_AUX, 1u, lane); ab += 4; lds_wait(L + C_AUX, ab); } while (0)
    STAMP(2);
    if (ABL & 4) lds_wait_sleep(L + C_MMA, 44);
    {
      const __amdgpu_buffer_rsrc_t rw2 = mkrsrc(w2, F * D * 2);
#pragma unroll
      for (int i = 0; i < 3; ++i) if (4 * i + aw < 10) {
        const int n = (4 * i + aw) * 64 + lane; const int row = n / 10;
        GLDS(rw2, L + PW + (4 * i + aw) * 1024, (row + (row >= 32 ? 992 : 0)) * 1280 + (n - row * 10) * 16, 32 * mb * 1280 + 160 * nb);
      }
    }
    {
      const int g = lane >> 4, q = (lane & 15) >> 2, pp = lane & 3;
      PREP_ITEM(aw, 0); PREP_ITEM(aw, 1); PREP_ITEM(aw, 2);
      AUXBAR();
#pragma unroll
      for (int i = 0; i < 3; ++i) {
        const int ri = aw + 4 * i;
        if (ri < 10) {
          const int ch = 8 * ri + (lane >> 3), cc = lane & 7;
          v4u val = ldsr128(L + PO + ch * 128 + ((cc ^ (ch & 7)) << 4));
          __builtin_amdgcn_raw_buffer_store_b128(val, rws, (ABL & 1) ? -16 : R_OFF + (80 * nb + ch) * 4096 + (31 - mb) * 128 + cc * 16, 0, SC1);
        }
      }
      __builtin_amdgcn_s_setprio(AUX_PRIO);
      if (aw < 2) WAITV(3); else WAITV(2);
      AUXBAR();
      STAMP(3);
#pragma unroll
      for (int i = 0; i < 3; ++i) {
        const int fi = aw + 4 * i;
        if (fi < 10) {
          const int half = fi / 5, nt = fi - half * 5, ks = mb + 32 * half;
          const int r0 = 16 * (g & 1) + 4 * (g >> 1);
          LAS char* a = L + PW + half * 5120 + (r0 + q) * 160 + nt * 32 + pp * 8;
          v2u lo = trread(a), hi2 = trread(a + 8 * 160);
          __builtin_amdgcn_raw_buffer_store_b128(v4u{lo.x, lo.y, hi2.x, hi2.y}, rws, (ks < KS2 && !(ABL & 1)) ? W2P_OFF + nb * W2P_NB + ks * 5120 + nt * 1024 + lane * 16 : -16, 0, SC1);
        }
      }
    }
    STAMP(4);
    WAITV(0);
    AUXBAR();
    if (aw == 0) set_flag(ws, FL_P + (nb * 32 + mb) * 4, tag, lane);
    STAMP(5);
    if (ABL & 2) lds_wait_sleep(L + C_MMA, 44);
    const __amdgpu_buffer_rsrc_t rh = mkrsrc(hk, D * S * 2);
    v4u cu[3];
    cu[0] = __builtin_amdgcn_raw_buffer_load_b128(rh, dpair * 4096 + aw * 1024 + lane * 16, 0, 0);
    cu[1] = __builtin_amdgcn_raw_buffer_load_b128(rh, dpair * 4096 + 4096 + aw * 1024 + lane * 16, 0, 0);
    cu[2] = __builtin_amdgcn_raw_buffer_load_b128(rh, d3 * 4096 + aw * 1024 + lane * 16, 0, 0);
    const int g = aw * 64 + lane;
    if (aw == 0 && lane < 32) *(LAS v4u*)(L + CV + (lane >> 4) * CH_BYTES + ((lane >> 3) & 1) * CH_HB + (lane & 7) * 16) = v4u{0, 0, 0, 0};
    if (aw == 3) {
#pragma unroll
      for (int i = 0; i < 4; ++i) {
        const int n = lane + 64 * i;
        if (n < 200) { const int cp = n / 25, sl = n - cp * 25; *(LAS v4u*)(L + CV + (cp >> 2) * CH_BYTES + CH_R + (cp & 3) * CH_RS + 4080 + sl * 16) = v4u{0, 0, 0, 0}; }
      }
    }
    stage_h(L + CV, cu[0], g);
    stage_h(L + CV + CH_BYTES, cu[1], g);
    lds_wait_sleep(L + C_MMA, DELAY_POLL);
    if (aw == 1) {
      const int jc = g1 ? j : 0;
      v2u gr[4];
#define GRL(off) __builtin_bit_cast(v2u, __hip_atomic_load((GAS unsigned long long*)(ws + (off)), RLX_AGENT))
#pragma unroll 1
      for (unsigned sp = 0; sp < SPIN_CAP; ++sp) {
        gr[0] = GRL(ST_OFF + (p * 128 + lane) * 8);
        gr[1] = GRL(ST_OFF + (p * 128 + 64 + lane) * 8);
        gr[2] = GRL(CS_OFF + (jc * 128 + lane) * 8);
        gr[3] = GRL(CS_OFF + (jc * 128 + (j == 13 ? 0 : 64) + lane) * 8);
        if (__all(gr[0].y == tag && gr[1].y == tag && gr[2].y == tag && gr[3].y == tag)) break;
        __builtin_amdgcn_s_sleep(POLL_SLEEP);
      }
      *(LAS f32x2*)(L + XFS + lane * 8) = f32x2{bflo(gr[0].x), 1.f / bfhi(gr[0].x)};
      *(LAS f32x2*)(L + XFS + (64 + lane) * 8) = f32x2{bflo(gr[1].x), 1.f / bfhi(gr[1].x)};
      *(LAS unsigned*)(L + XFC + lane * 4) = gr[2].x;
      *(LAS unsigned*)(L + XFC + (64 + lane) * 4) = gr[3].x;
      WAITL();
      lds_add(L + C_S, 1u, lane);
    }
    if (aw == 2) { poll_flags(ws, FL_P + nb * 128, 32, tag, lane); lds_add(L + C_P, 1u, lane); }
    lds_wait_sleep(L + C_P, 1);
    STAMP(6);
    v4u rv[3];
    rv[0] = __builtin_amdgcn_raw_buffer_load_b128(rws, R_OFF + dpair * 4096 + aw * 1024 + lane * 16, 0, SC1);
    rv[1] = __builtin_amdgcn_raw_buffer_load_b128(rws, R_OFF + dpair * 4096 + 4096 + aw * 1024 + lane * 16, 0, SC1);
    rv[2] = __builtin_amdgcn_raw_buffer_load_b128(rws, R_OFF + d3 * 4096 + aw * 1024 + lane * 16, 0, SC1);
    const int rot2 = ROT2_EXPR;
    const __amdgpu_buffer_rsrc_t ra = mkrsrc(ws + HP_OFF + mb * HP_MB, HP_MB);
    const __amdgpu_buffer_rsrc_t rb = mkrsrc(ws + W2P_OFF + nb * W2P_NB, W2P_NB);
    const int va = lane * 16 + aw * 4096, vb = lane * 16 + aw * 5120;
    constexpr int WA = WA_N, WB = WB_N, NKS = 14;
    v4u fa[WA][4], fb[WB][5];
#define KIDX(i) ((i) + rot2 >= NKS ? (i) + rot2 - NKS : (i) + rot2)
#define LOADA(i) do { const int so_ = KIDX(i) * 16384; \
    _Pragma("unroll") for (int m = 0; m < 4; ++m) fa[(i) % WA][m] = __builtin_amdgcn_raw_buffer_load_b128(ra, va + m * 1024, so_, SC1); } while (0)
#define LOADB(i) do { const int so_ = KIDX(i) * 20480; \
    _Pragma("unroll") for (int n = 0; n < 5; ++n) fb[(i) % WB][n] = __builtin_amdgcn_raw_buffer_load_b128(rb, vb + (n & 3) * 1024, so_ + (n >> 2) * 4096, SC1); } while (0)
    LOADB(0); LOADB(1);
    SB();
    const float dv = bflo(Dv[dpair + (aw >> 1)]);
    stage_r(L + CV, rv[0], g);
    stage_r(L + CV + CH_BYTES, rv[1], g);
    SB();
#pragma unroll
    for (int i = 2; i < WB; ++i) LOADB(i);
    SB();
    STAMP(7);
    lds_wait_sleep(L + C_MMA, 36);
    if (aw == 0 && lane < 16) *(LAS v4u*)(L + CV3 + (lane >> 3) * CH_HB + (lane & 7) * 16) = v4u{0, 0, 0, 0};
    if (aw == 3) {
#pragma unroll
      for (int i = 0; i < 2; ++i) {
        const int n = lane + 64 * i;
        if (n < 100) { const int cp = n / 25, sl = n - cp * 25; *(LAS v4u*)(L + CV3 + CH_R + cp * CH_RS + 4080 + sl * 16) = v4u{0, 0, 0, 0}; }
      }
    }
    stage_h(L + CV3, cu[2], g);
    stage_r(L + CV3, rv[2], g);
    AUXBAR();
    if (aw == 0) lds_add(L + C_ST, 1u, lane);
    lds_wait_sleep(L + C_MMA, CONV_START);
    __builtin_amdgcn_s_setprio(CONV_PRIO);
    STAMP(8);
    {
      LAS char* Lc = L + CV + (aw >> 1) * CH_BYTES;
      const int cts = aw & 1;
      f32x4 acc[4] = {};
      if (cts == 0) conv8<0>(Lc + aoff, Lc + boff, acc); else conv8<1>(Lc + aoff, Lc + boff, acc);
      STAMP(9);
#pragma unroll
      for (int ti = 0; ti < 4; ++ti) conv_out(Lc, acc[ti], cts == 0 ? (ti < 2 ? ti : ti + 4) : 2 + ti, dpair + (aw >> 1), dv, e, kq, rws);
      WAITV(0); WAITL();
      unsigned old = 0;
      if (lane == 0) old = __hip_atomic_fetch_add((LAS unsigned*)(L + C_Y), 1u, RLX_WG);
      if (__builtin_amdgcn_readfirstlane(old) == 7) set_flag(ws, FL_Y + (nb * 32 + mb) * 4, tag, lane);
    }
    STAMP(10);
    __builtin_amdgcn_s_setprio(0);
    lds_wait_sleep(L + C_HR, 1);
    STAMP(11);
#pragma unroll
    for (int i = 0; i < WA; ++i) LOADA(i);
    SB();
    f32x4 acc2[4][5] = {};
#define LOADA1(i, m) fa[(i) % WA][m] = __builtin_amdgcn_raw_buffer_load_b128(ra, va + (m) * 1024, KIDX(i) * 16384, SC1)
#define LOADB1(i, n) fb[(i) % WB][n] = __builtin_amdgcn_raw_buffer_load_b128(rb, vb + ((n) & 3) * 1024, KIDX(i) * 20480 + ((n) >> 2) * 4096, SC1)
#pragma unroll
    for (int i = 0; i < NKS; ++i) {
#pragma unroll
      for (int mn = 0; mn < 20; ++mn) {
        const int m = mn / 5, n = mn % 5;
        acc2[m][n] = mfma16(fb[i % WB][n], fa[i % WA][m], acc2[m][n]);
        if (i > 0 && (mn & 1)) {
          const int k = mn >> 1;
          SB();
          if (k < 4) { if (i - 1 + WA < NKS) LOADA1(i - 1 + WA, k); }
          else if (k < 9) { if (i - 1 + WB < NKS) LOADB1(i - 1 + WB, k - 4); }
          SB();
        }
      }
      SB();
      if (i == 0) STAMP(12);
    }
    STAMP(13);
    lds_wait(L + C_Y, 8);
    const int ln = __builtin_amdgcn_mbcnt_hi(~0u, __builtin_amdgcn_mbcnt_lo(~0u, opaque0()));
#pragma unroll
    for (int m = 0; m < 4; ++m)
#pragma unroll
      for (int n = 0; n < 5; ++n)
        *(LAS f32x4*)(L + aw * PART + (16 * m + (ln & 15)) * PSTR + n * 64 + (ln >> 4) * 16) = acc2[m][n];
  } else {
    __builtin_amdgcn_s_setprio(MMA_PRIO);
    const __amdgpu_buffer_rsrc_t rw = mkrsrc(w1, D * F * 2);
    unsigned tag;
    if (g1) {
      const __amdgpu_buffer_rsrc_t rx = mkrsrc(x + (size_t)p * 128 * D, 128 * D * 2);
      int vx[2], vw[4];
      {
        const int row = 32 * w + (lane >> 3);
        vx[0] = row * 1280 + (((lane & 7) ^ ((row >> 1) & 7)) << 4);
        vx[1] = (row + 8) * 1280 + (((lane & 7) ^ (((row + 8) >> 1) & 7)) << 4);
#pragma unroll
        for (int ii = 0; ii < 4; ++ii) {
          const int kr = 16 * w + 4 * ii + (lane >> 4);
          vw[ii] = kr * (F * 2) + j * 256 + (((lane & 15) ^ (((kr & 3) << 2) | ((kr >> 2) & 3))) << 4);
        }
      }
      const int rot = ROT_EXPR;
      const unsigned lbase = (unsigned)(uintptr_t)L + w * 4096;
#define KC(s) ((s) + rot >= 10 ? (s) + rot - 10 : (s) + rot)
#define G1LD(s, i) do { \
    if ((i) < 4) glds_fast(rx, lbase + ((s) % NBUF) * 32768 + (i) * 1024, vx[(i) & 1], KC(s) * 128 + ((i) >> 1) * 16 * 1280); \
    else glds_fast(rw, lbase + ((s) % NBUF) * 32768 + 16384 + ((i) - 4) * 1024, vw[((i) - 4) & 3], KC(s) * (64 * F * 2)); \
  } while (0)
#pragma unroll
      for (int i = 0; i < 8; ++i) G1LD(0, i);
      WAITV(8);
      BAR();
      PREP_ITEM(aw, 3); PREP_ITEM(aw, 4);
      WAITL(); lds_add(L + C_AUX, 1u, lane);
      SB();
#pragma unroll
      for (int i = 0; i < 8; ++i) G1LD(1, i);
      STAMP(1);
      const int fh = w & 1, th = w >> 1;
      int xa[4], wa[2][2];
      {
        const int row = 64 * th + (lane & 31);
        const int c0 = hi ^ ((row >> 1) & 7);
        const int blk = (lane >> 4) & 1, q = (lane & 15) >> 2, pp = lane & 3;
#pragma unroll
        for (int s = 0; s < 4; ++s) xa[s] = row * 128 + (((2 * s) ^ c0) << 4);
#pragma unroll
        for (int ft = 0; ft < 2; ++ft)
#pragma unroll
          for (int t = 0; t < 2; ++t) {
            const int kr = 8 * hi + 4 * t + q;
            const int ch = 4 * (2 * fh + ft) + 2 * blk + (pp >> 1);
            wa[ft][t] = 16384 + 256 * kr + ((ch ^ ((q << 2) | ((2 * hi + t) & 3))) << 4) + 8 * (pp & 1);
          }
      }
      f32x16 acc[2][2] = {};
      Frags fA, fB;
#define RDX(f, bo, s, tt) f.x[s][tt] = ldsr128(L + (bo) + xa[s] + (tt) * 4096)
#define RDW(f, bo, s, ft) do { v2u wl_ = trread(L + (bo) + wa[ft][0] + (s) * 4096), wh_ = trread(L + (bo) + wa[ft][1] + (s) * 4096); f.w[s][ft] = v4u{wl_.x, wl_.y, wh_.x, wh_.y}; } while (0)
#define MM(f, s, ft, tt) acc[ft][tt] = mfma32(f.w[s][ft], f.x[s][tt], acc[ft][tt])
#define ITER(s, vm, cur, nxt) do { \
    asm volatile("s_waitcnt vmcnt(" #vm ") lgkmcnt(0)" ::: "memory"); \
    lds_add(L + C_MMA, 1u, lane); SB(); \
    MM(cur, 0, 0, 0); MM(cur, 0, 0, 1); MM(cur, 0, 1, 0); SB(); \
    lds_wait(L + C_MMA, 4 * ((s) + 2)); SB(); \
    constexpr int bo_ = (((s) + 1) % NBUF) * 32768; constexpr bool ld_ = (s) + NBUF < 10; \
    MM(cur, 0, 1, 1); RDX(nxt, bo_, 0, 0); RDX(nxt, bo_, 0, 1); SB(); if (ld_) G1LD((s) + NBUF, 0); SB(); \
    MM(cur, 1, 0, 0); RDW(nxt, bo_, 0, 0); SB(); \
    MM(cur, 1, 0, 1); RDW(nxt, bo_, 0, 1); SB(); if (ld_) G1LD((s) + NBUF, 4); SB(); \
    MM(cur, 1, 1, 0); RDX(nxt, bo_, 1, 0); RDX(nxt, bo_, 1, 1); SB(); if (ld_) G1LD((s) + NBUF, 1); SB(); \
    MM(cur, 1, 1, 1); RDW(nxt, bo_, 1, 0); SB(); \
    MM(cur, 2, 0, 0); RDW(nxt, bo_, 1, 1); SB(); if (ld_) G1LD((s) + NBUF, 5); SB(); \
    MM(cur, 2, 0, 1); RDX(nxt, bo_, 2, 0); RDX(nxt, bo_, 2, 1); SB(); if (ld_) G1LD((s) + NBUF, 2); SB(); \
    MM(cur, 2, 1, 0); RDW(nxt, bo_, 2, 0); SB(); \
    MM(cur, 2, 1, 1); RDW(nxt, bo_, 2, 1); SB(); if (ld_) G1LD((s) + NBUF, 6); SB(); \
    MM(cur, 3, 0, 0); RDX(nxt, bo_, 3, 0); RDX(nxt, bo_, 3, 1); SB(); if (ld_) G1LD((s) + NBUF, 3); SB(); \
    MM(cur, 3, 0, 1); RDW(nxt, bo_, 3, 0); RDW(nxt, bo_, 3, 1); SB(); \
    MM(cur, 3, 1, 0); SB(); if (ld_) G1LD((s) + NBUF, 7); SB(); \
    MM(cur, 3, 1, 1); SB(); \
  } while (0)
      WAITV(8);
      lds_add(L + C_MMA, 1u, lane); lds_wait(L + C_MMA, 4);
      STAMP(2);
#pragma unroll
      for (int s = 0; s < 4; ++s) { RDX(fA, 0, s, 0); RDX(fA, 0, s, 1); RDW(fA, 0, s, 0); RDW(fA, 0, s, 1); }
      SB();
#pragma unroll
      for (int i = 0; i < 8; ++i) G1LD(2, i);
      SB();
      ITER(0, 8, fA, fB);
      ITER(1, 8, fB, fA);
      ITER(2, 8, fA, fB);
      STAMP(3);
      ITER(3, 8, fB, fA);
      ITER(4, 8, fA, fB);
      ITER(5, 8, fB, fA);
      STAMP(4);
      ITER(6, 8, fA, fB);
      ITER(7, 8, fB, fA);
      ITER(8, 0, fA, fB);
#pragma unroll
      for (int s = 0; s < 4; ++s) { MM(fB, s, 0, 0); MM(fB, s, 0, 1); MM(fB, s, 1, 0); MM(fB, s, 1, 1); }
      SB();
      lds_add(L + C_MMA, 1u, lane);
      STAMP(5);
      lds_wait_sleep(L + C_S, 1);
      tag = __builtin_amdgcn_readfirstlane(*(LAS unsigned*)(L + C_TAG));
      STAMP(6);
#pragma unroll
      for (int ft = 0; ft < 2; ++ft) {
        const int fg = 2 * fh + ft;
        const int ks = 4 * j + fg;
        f32x4 c4[4];
#pragma unroll
        for (int g = 0; g < 4; ++g) c4[g] = *(LAS f32x4*)(L + XFC + (32 * fg + 8 * g + 4 * hi) * 4);
#pragma unroll
        for (int tt = 0; tt < 2; ++tt) {
          const int tok = 64 * th + 32 * tt + (lane & 31);
          const f32x2 st = *(LAS f32x2*)(L + XFS + tok * 8);
          const f32x16& a = acc[ft][tt];
          const int mb2 = 2 * p + th, mt = 2 * tt + ((lane >> 4) & 1);
          const int off = ks < KS2 ? HP_OFF + mb2 * HP_MB + ks * 4096 + mt * 1024 + ((2 * hi) * 16 + (lane & 15)) * 16 : -1024;
#pragma unroll
          for (int hh = 0; hh < 2; ++hh) {
            unsigned pk[4];
#pragma unroll
            for (int e2 = 0; e2 < 4; ++e2) {
              const int ee = hh * 4 + e2;
              float v0 = (a[2 * ee] - st.x * c4[ee >> 1][(2 * ee) & 3]) * st.y;
              float v1 = (a[2 * ee + 1] - st.x * c4[ee >> 1][(2 * ee + 1) & 3]) * st.y;
              unsigned hb = pack2(v0, v1);
              pk[e2] = pack2(silu_f(bflo(hb)), silu_f(bfhi(hb)));
            }
            __builtin_amdgcn_raw_buffer_store_b128(v4u{pk[0], pk[1], pk[2], pk[3]}, rws, off + hh * 256, 0, SC1);
            SB();
          }
        }
      }
      STAMP(7);
      WAITV(0);
      {
        unsigned old = 0;
        if (lane == 0) old = __hip_atomic_fetch_add((LAS unsigned*)(L + C_H), 1u, RLX_WG);
        if (__builtin_amdgcn_readfirstlane(old) == 3) set_flag(ws, FL_H + (p * 16 + j) * 4, tag, lane);
      }
      STAMP(8);
    } else {
      const int idx16 = 4 * rg + pi;
      const int srow = 512 * rg + 64 * (4 * hf + pi) + 16 * w + (lane >> 3);
      const int cg = 14 * hf + idx16;
      const bool csv = idx16 < 14 - hf;
      const __amdgpu_buffer_rsrc_t rx = mkrsrc(x, S * D * 2);
      v4u xv[2][10], wv[20];
      WAITV(0);
      BAR();
      if (ABL & 8) for (int i = 0; i < 150; ++i) __builtin_amdgcn_s_sleep(1);
#pragma unroll
      for (int h2 = 0; h2 < 2; ++h2)
#pragma unroll
        for (int i = 0; i < 10; ++i) xv[h2][i] = __builtin_amdgcn_raw_buffer_load_b128(rx, (srow + 8 * h2) * 1280 + (lane & 7) * 16 + i * 128, 0, 0);
      PREP_ITEM(aw, 3); PREP_ITEM(aw, 4);
      WAITL(); lds_add(L + C_AUX, 1u, lane);
      const int wvo = csv ? (160 * w + (lane >> 3)) * (F * 2) + cg * 128 + (lane & 7) * 16 : -16;
#pragma unroll
      for (int i = 0; i < 20; ++i) wv[i] = __builtin_amdgcn_raw_buffer_load_b128(rw, wvo, csv ? i * 8 * F * 2 : 0, 0);
      lds_wait_sleep(L + C_AUX, 8);
      tag = __builtin_amdgcn_readfirstlane(*(LAS unsigned*)(L + C_TAG));
#pragma unroll
      for (int h2 = 0; h2 < 2; ++h2) {
        float s = 0.f;
#pragma unroll
        for (int i = 0; i < 10; ++i)
#pragma unroll
          for (int e2 = 0; e2 < 4; ++e2) s = dot2(xv[h2][i][e2], 0x3f803f80u, s);
        s += __shfl_xor(s, 1); s += __shfl_xor(s, 2); s += __shfl_xor(s, 4);
        const float mean = s * (1.f / D);
        float q0 = 0.f, q1 = 0.f;
#pragma unroll
        for (int i = 0; i < 10; ++i)
#pragma unroll
          for (int e2 = 0; e2 < 4; ++e2) { const float a0 = bflo(xv[h2][i][e2]) - mean, a1 = bfhi(xv[h2][i][e2]) - mean; q0 = fmaf(a0, a0, q0); q1 = fmaf(a1, a1, q1); }
        float q = q0 + q1;
        q += __shfl_xor(q, 1); q += __shfl_xor(q, 2); q += __shfl_xor(q, 4);
        const float den = __builtin_sqrtf(rbf(rbf(q * (1.f / D)) + 1e-5f));
        __builtin_amdgcn_raw_buffer_store_b64(v2u{pack2(mean, den), tag}, rws, (lane & 7) == 0 ? ST_OFF + (srow + 8 * h2) * 8 : -16, 0, SC1);
      }
      float c[8] = {};
#pragma unroll
      for (int i = 0; i < 20; ++i)
#pragma unroll
        for (int e2 = 0; e2 < 4; ++e2) { c[2 * e2] += bflo(wv[i][e2]); c[2 * e2 + 1] += bfhi(wv[i][e2]); }
      *(LAS f32x4*)(L + ((w * 8 + (lane >> 3)) * 64 + (lane & 7) * 8) * 4) = f32x4{c[0], c[1], c[2], c[3]};
      *(LAS f32x4*)(L + ((w * 8 + (lane >> 3)) * 64 + (lane & 7) * 8 + 4) * 4) = f32x4{c[4], c[5], c[6], c[7]};
      WAITL();
      unsigned old = 0;
      if (lane == 0) old = __hip_atomic_fetch_add((LAS unsigned*)(L + C_H), 1u, RLX_WG);
      if (__builtin_amdgcn_readfirstlane(old) == 3) {
        asm volatile("" ::: "memory");
        float t4[4] = {};
#pragma unroll
        for (int i = 0; i < 32; ++i) t4[i & 3] += *(LAS float*)(L + (i * 64 + lane) * 4);
        __builtin_amdgcn_raw_buffer_store_b64(v2u{__float_as_uint((t4[0] + t4[1]) + (t4[2] + t4[3])), tag}, rws, csv ? CS_OFF + (cg * 64 + lane) * 8 : -16, 0, SC1);
      }
      lds_add(L + C_MMA, 11u, lane);
      STAMP(5);
    }
    unsigned hb_ = 0;
#define HBAR() do { WAITL(); lds_add(L + C_G2, 1u, lane); hb_ += 4; lds_wait(L + C_G2, hb_); } while (0)
    {
      const float dv3 = bflo(Dv[d3]);
      lds_wait_sleep(L + C_ST, 1);
      f32x4 ac3[4] = {};
      if (ts3 == 0) conv2<0>(L + CV3 + aoff + aw * 128, L + CV3 + boff + aw * 128, ac3); else conv2<1>(L + CV3 + aoff + aw * 128, L + CV3 + boff + aw * 128, ac3);
#pragma unroll
      for (int ti = 0; ti < 4; ++ti) *(LAS f32x4*)(L + SC3 + (aw * 4 + ti) * 1024 + lane * 16) = ac3[ti];
      HBAR();
      f32x4 a = {};
#pragma unroll
      for (int ww = 0; ww < 4; ++ww) a += *(LAS f32x4*)(L + SC3 + (ww * 4 + aw) * 1024 + lane * 16);
      conv_out(L + CV3, a, ts3 == 0 ? (aw < 2 ? aw : aw + 4) : 2 + aw, d3, dv3, e, kq, rws);
      STAMP(9);
      WAITV(0); WAITL();
      unsigned old = 0;
      if (lane == 0) old = __hip_atomic_fetch_add((LAS unsigned*)(L + C_Y), 1u, RLX_WG);
      if (__builtin_amdgcn_readfirstlane(old) == 7) set_flag(ws, FL_Y + (nb * 32 + mb) * 4, tag, lane);
    }
    STAMP(10);
    if (aw == 1) { poll_flags(ws, FL_H + p * 64, 14, tag, lane); lds_add(L + C_HR, 1u, lane); }
    lds_wait_sleep(L + C_Y, 8);
    {
      const __amdgpu_buffer_rsrc_t rxx = mkrsrc(x, S * D * 2);
#pragma unroll
      for (int i = 0; i < 3; ++i) {
        const int s = aw + 4 * i;
        if (s < 10) {
          const int n = s * 64 + lane; const int row = n / 10, c = n - row * 10;
          GLDS(rxx, L + EX + s * 1024, (64 * mb + row) * 1280 + 160 * nb + c * 16, 0);
        }
      }
    }
    if (aw == 0) { poll_flags(ws, FL_Y + nb * 128, 32, tag, lane); lds_add(L + C_YR, 1u, lane); }
    lds_wait_sleep(L + C_YR, 1);
    STAMP(11);
#pragma unroll
    for (int i = 0; i < 3; ++i) {
      const int ri = aw + 4 * i;
      if (ri < 10) {
        const int ch = 8 * ri + (lane >> 3), cc = lane & 7;
        v4u yv = __builtin_amdgcn_raw_buffer_load_b128(rws, Y_OFF + (80 * nb + ch) * 4096 + mb * 128 + cc * 16, 0, SC1);
        *(LAS v4u*)(L + EY + ch * 144 + cc * 16) = yv;
      }
    }
    WAITV(0);
    HBAR();
    STAMP(12);
    {
      const int q = (lane & 15) >> 2, pp = lane & 3, pos = lane & 15;
      const int tok = 16 * aw + 2 * (pos >> 2) + (pos & 1) + 8 * ((pos >> 1) & 1);
      const f32x2 st = *(LAS f32x2*)(L + XFS + ((mb & 1) * 64 + tok) * 8);
#pragma unroll
      for (int i = 0; i < 5; ++i) {
        const int dq = 4 * i + kq;
        const v2u yv = trread(L + EY + (4 * dq + q) * 144 + aw * 32 + pp * 8);
        const v2u xv2 = *(LAS v2u*)(L + EX + tok * 160 + dq * 8);
        const unsigned d01 = pack2(bflo(xv2.x) - st.x, bfhi(xv2.x) - st.x), d23 = pack2(bflo(xv2.y) - st.x, bfhi(xv2.y) - st.x);
        const unsigned n01 = pack2(bflo(d01) * st.y, bfhi(d01) * st.y), n23 = pack2(bflo(d23) * st.y, bfhi(d23) * st.y);
        const unsigned s01 = pack2(bflo(n01) * bflo(yv.x), bfhi(n01) * bfhi(yv.x)), s23 = pack2(bflo(n23) * bflo(yv.y), bfhi(n23) * bfhi(yv.y));
        *(LAS v4u*)(L + ES + (tok * 20 + dq) * 16) = v4u{s01, s23, xv2.x, xv2.y};
      }
    }
    if (b == 0 && aw == 0) {
#pragma unroll 1
      for (int i = 0; i < 4; ++i) poll_flags(ws, FL_P + i * 256, 64, tag, lane);
      set_flag(ws, FL_GEN, tag, lane);
    }
  }
  STAMP(14);
  BAR();
  const int lane2 = __builtin_amdgcn_mbcnt_hi(~0u, __builtin_amdgcn_mbcnt_lo(~0u, opaque0()));
#pragma unroll
  for (int i = 0; i < 3; ++i) {
    const int it = w + 8 * i;
    if (it < 20) {
      const int n = it * 64 + lane2;
      const int tok = n / 20, dq = n - tok * 20;
      f32x4 o = *(LAS f32x4*)(L + tok * PSTR + dq * 16);
#pragma unroll
      for (int ww = 1; ww < 4; ++ww) o += *(LAS f32x4*)(L + ww * PART + tok * PSTR + dq * 16);
      const v4u sx = *(LAS v4u*)(L + ES + n * 16);
      const unsigned o01 = pack2(o[0], o[1]), o23 = pack2(o[2], o[3]);
      const unsigned g01 = pack2(silu_f(bflo(o01)), silu_f(bfhi(o01))), g23 = pack2(silu_f(bflo(o23)), silu_f(bfhi(o23)));
      const unsigned m01 = pack2(bflo(sx.x) * bflo(g01), bfhi(sx.x) * bfhi(g01)), m23 = pack2(bflo(sx.y) * bflo(g23), bfhi(sx.y) * bfhi(g23));
      v2u res;
      res.x = pack2(bflo(sx.z) + bflo(m01), bfhi(sx.z) + bfhi(m01));
      res.y = pack2(bflo(sx.w) + bflo(m23), bfhi(sx.w) + bfhi(m23));
      *(v2u*)(out + (size_t)(64 * mb + tok) * D + 80 * nb + dq * 4) = res;
    }
  }
  STAMP(15);
  STAMP_OUT(0);
}

__global__ void kpre(char* ws) { if (threadIdx.x == 0) *(unsigned long long*)(ws + (100 << 20)) = __builtin_amdgcn_s_memrealtime(); }
__global__ void kpost(char* ws) {
  unsigned long long now = __builtin_amdgcn_s_memrealtime();
  unsigned long long mx = 0;
  for (int i = 0; i < 4; ++i) { unsigned long long v = *(volatile unsigned long long*)(ws + (101 << 20) + (threadIdx.x * 4 + i) * 8); mx = v > mx ? v : mx; }
  for (int o = 32; o > 0; o >>= 1) { unsigned long long v = __shfl_xor(mx, o); mx = v > mx ? v : mx; }
  const unsigned long long tgt = __builtin_amdgcn_s_memrealtime() + POSTK * (now - mx);
  while (__builtin_amdgcn_s_memrealtime() < tgt) __builtin_amdgcn_s_sleep(1);
}
extern "C" void kernel_launch(void* const* d_in, const int* in_sizes, int n_in,
                              void* d_out, int out_size, void* d_ws, size_t ws_size,
                              hipStream_t stream) {
  kpre<<<1, 64, 0, stream>>>((char*)d_ws);
  fused<<<256, 512, 0, stream>>>((const u16*)d_in[0], (const u16*)d_in[1], (const u16*)d_in[2], (const u16*)d_in[3],
                                 (const u16*)d_in[4], (const u16*)d_in[5], (const u16*)d_in[7], (const u16*)d_in[8],
                                 (char*)d_ws, (u16*)d_out);
  kpost<<<1, 64, 0, stream>>>((char*)d_ws);
}
```

```cpp
#include <hip/hip_runtime.h>
#include <stdint.h>

typedef unsigned short u16;
typedef unsigned v4u __attribute__((ext_vector_type(4)));
typedef unsigned v2u __attribute__((ext_vector_type(2)));
typedef float f32x2 __attribute__((ext_vector_type(2)));
typedef float f32x4 __attribute__((ext_vector_type(4)));
typedef float f32x16 __attribute__((ext_vector_type(16)));
typedef short bf16x8 __attribute__((ext_vector_type(8)));
typedef short s16x4 __attribute__((ext_vector_type(4)));
typedef __bf16 bf16x2 __attribute__((ext_vector_type(2)));
#define LAS __attribute__((address_space(3)))
#define GAS __attribute__((address_space(1)))
#define DI __device__ __forceinline__
#define RLX_AGENT __ATOMIC_RELAXED, __HIP_MEMORY_SCOPE_AGENT
#define RLX_WG __ATOMIC_RELAXED, __HIP_MEMORY_SCOPE_WORKGROUP

#define FSTR_N 128
constexpr int S = 2048, D = 640, F = 1728;
constexpr int KS2 = 54;
constexpr int FSTR = FSTR_N;
constexpr int FL_GEN = 0, FL_P = 131072, FL_H = FL_P + 256 * 256;
constexpr int ST_OFF = 32768;
constexpr int CS_OFF = ST_OFF + S * 8;
constexpr int HP_OFF = 262144;
constexpr int HP_MB = KS2 * 4096;
constexpr int W2P_OFF = HP_OFF + 32 * HP_MB;
constexpr int W2P_NB = KS2 * 5120;
constexpr int R_OFF = W2P_OFF + 8 * W2P_NB;
constexpr int Y_OFF = R_OFF + D * S * 2;
constexpr int WS_BYTES = Y_OFF + D * S * 4;
constexpr unsigned RSRC3 = 0x00020000u;
constexpr int SC1 = 16;
constexpr unsigned SPIN_CAP = 1u << 18;

DI float bflo(unsigned u) { return __uint_as_float(u << 16); }
DI float bfhi(unsigned u) { return __uint_as_float(u & 0xffff0000u); }
DI unsigned pack2(float a, float b) {
  bf16x2 r = __builtin_convertvector(f32x2{a, b}, bf16x2);
  return __builtin_bit_cast(unsigned, r);
}
DI float rbf(float a) { return bflo(pack2(a, a)); }
DI float silu_f(float x) { return x * __builtin_amdgcn_rcpf(1.f + __builtin_amdgcn_exp2f(-1.44269504088896f * x)); }
DI f32x4 mfma16(v4u a, v4u b, f32x4 c) {
  return __builtin_amdgcn_mfma_f32_16x16x32_bf16(__builtin_bit_cast(bf16x8, a), __builtin_bit_cast(bf16x8, b), c, 0, 0, 0);
}
DI f32x16 mfma32(v4u a, v4u b, f32x16 c) {
  return __builtin_amdgcn_mfma_f32_32x32x16_bf16(__builtin_bit_cast(bf16x8, a), __builtin_bit_cast(bf16x8, b), c, 0, 0, 0);
}
DI float dot2(unsigned a, unsigned b, float c) {
  return __builtin_amdgcn_fdot2_f32_bf16(__builtin_bit_cast(bf16x2, a), __builtin_bit_cast(bf16x2, b), c, false);
}
DI v2u trread(LAS char* p) { return __builtin_bit_cast(v2u, __builtin_amdgcn_ds_read_tr16_b64_v4i16((LAS s16x4*)p)); }
DI v4u ldsr128(LAS char* p) { return *(LAS v4u*)p; }
DI __amdgpu_buffer_rsrc_t mkrsrc(const void* p, int bytes) { return __builtin_amdgcn_make_buffer_rsrc((void*)p, 0, bytes, RSRC3); }
DI void glds_asm(__amdgpu_buffer_rsrc_t rs, unsigned ldsa, int voff, int soff) {
  asm volatile("s_nop 4\n\ts_mov_b32 m0, %0\n\ts_nop 0\n\tbuffer_load_dwordx4 %1, %2, %3 offen lds"
               :: "s"(ldsa), "v"(voff), "s"(rs), "s"(soff) : "m0", "memory");
}
DI void glds_fast(__amdgpu_buffer_rsrc_t rs, unsigned ldsa, int voff, int soff) {
  asm volatile("s_mov_b32 m0, %0\n\ts_nop 0\n\tbuffer_load_dwordx4 %1, %2, %3 offen lds"
               :: "s"(ldsa), "v"(voff), "s"(rs), "s"(soff) : "m0", "memory");
}
#define GLDS(rs, ldsp, voff, soff) glds_asm(rs, (unsigned)(uintptr_t)(ldsp), voff, soff)
#define WAITV(n) asm volatile("s_waitcnt vmcnt(" #n ")" ::: "memory")
#define WAITL() asm volatile("s_waitcnt lgkmcnt(0)" ::: "memory")
#define BAR() do { WAITL(); __builtin_amdgcn_s_barrier(); asm volatile("" ::: "memory"); } while (0)
#define SB() __builtin_amdgcn_sched_barrier(0)

#define STAMP_DECL
#define STAMP(i)
#define STAMP_OUT(kid)

#define ROT_EXPR (5 * ((pi + jj) & 1))
#define WA_N 3
#define POLL_SLEEP 8
#define CODE_PF 1
#define CODE_BYTES 41000u
#define EXACT_PREP 0
#define PREP_PRIO 3
#define CONV_PRIO 1
#define WB_N 6
#define ROT2_EXPR 0
#define DELAY_POLL 32
#define ABL 0
#define FH_AUX 1
#define Y_DELAY 2
#define C8_PRIO 0
#define G2_PRIO 0
#define POOL_N 41
#define S1_EARLY 1
#define STAGE_START 44
#define LOOK_N 3
#define AUX_PRIO 0
#define MMA_PRIO 2
#define CONV_START 44
constexpr int NBUF = 3;
constexpr int CH_HA = 0, CH_HB = 4224, CH_R = 8448, CH_RS = 4512, CH_BYTES = CH_R + 4 * CH_RS;
constexpr int CV = 98304;
constexpr int CV3 = 32768;
constexpr int SC3 = 65536;
constexpr int PK = CV, PV = PK + 11264, PW = PV + 10240, PS = PW + 10240, PO = PS + 1024;
constexpr int EY = CV, EX = 0, ES = EY + 80 * 144;
constexpr int XFS = CV + 2 * CH_BYTES;
constexpr int XFC = XFS + 1024;
constexpr int CNT = XFC + 512;
constexpr int C_AUX = CNT, C_G2 = CNT + 4, C_Y = CNT + 8, C_H = CNT + 12, C_MMA = CNT + 16, C_S = CNT + 20, C_P = CNT + 24,
              C_TAG = CNT + 28, C_ST = CNT + 40, C_HR = CNT + 32, C_YR = CNT + 36;
constexpr int LDS_BYTES = CNT + 64;
static_assert(PO + 10240 <= XFS && ES + 20480 <= XFS && LDS_BYTES <= 163840, "lds");
constexpr int PSTR = 336, PART = 64 * PSTR;

DI void lds_add(LAS char* c, unsigned v, int lane) {
  if (lane == 0) __hip_atomic_fetch_add((LAS unsigned*)c, v, RLX_WG);
}
DI void lds_wait(LAS char* c, unsigned target) {
  while ((unsigned)__builtin_amdgcn_readfirstlane(__hip_atomic_load((LAS unsigned*)c, RLX_WG)) < target) {}
  asm volatile("" ::: "memory");
}
DI void lds_wait_sleep(LAS char* c, unsigned target) {
  while ((unsigned)__builtin_amdgcn_readfirstlane(__hip_atomic_load((LAS unsigned*)c, RLX_WG)) < target) __builtin_amdgcn_s_sleep(1);
  asm volatile("" ::: "memory");
}
DI unsigned flag_load(char* ws, int off, int n, int lane) {
  return __hip_atomic_load((GAS unsigned*)(ws + off + (lane < n ? lane : 0) * FSTR), RLX_AGENT);
}
DI void poll_flags(char* ws, int off, int n, unsigned tag, int lane) {
#pragma unroll 1
  for (unsigned spins = 0; spins < SPIN_CAP; ++spins) {
    const unsigned v = flag_load(ws, off, n, lane);
    if (__all(v == tag)) break;
    __builtin_amdgcn_s_sleep(POLL_SLEEP);
  }
  __builtin_amdgcn_fence(__ATOMIC_ACQUIRE, "wavefront");
}
DI void set_flag(char* ws, int off, unsigned tag, int lane) {
  if (lane == 0) __hip_atomic_store((GAS unsigned*)(ws + off), tag, RLX_AGENT);
}

DI unsigned opaque0() { unsigned z; asm volatile("v_mov_b32 %0, 0" : "=v"(z)); return z; }
template <int I> struct IC { static constexpr int value = I; };
template <int B, int E, class Fn> DI void static_for(Fn&& f) { if constexpr (B < E) { f(IC<B>{}); static_for<B + 1, E>(f); } }
struct Frags { v4u x[4][2], w[4][2]; };

template <int TS> DI void conv_ld(LAS char* pa, LAS char* pb, v4u (&A)[8], v4u (&B)[8]) {
  constexpr int NA = TS == 0 ? 8 : 6;
#pragma unroll
  for (int a = 0; a < NA; ++a) { A[a] = ldsr128(pa + a * 512); B[a] = ldsr128(pb + (7 - a) * 512); }
}
template <int TS> DI void conv_mm(const v4u (&A)[8], const v4u (&B)[8], f32x4 (&acc)[4]) {
#pragma unroll
  for (int ti = 0; ti < 4; ++ti) {
    const int n = TS == 0 ? (ti == 0 ? 0 : ti == 1 ? 1 : ti == 2 ? 6 : 7) : 2 + ti;
#pragma unroll
    for (int a = 0; a <= n; ++a) acc[ti] = mfma16(A[a], B[n - a], acc[ti]);
  }
}
DI void conv_sched() {
#pragma unroll
  for (int i = 0; i < 16; ++i) { __builtin_amdgcn_sched_group_barrier(0x8, 1, 0); __builtin_amdgcn_sched_group_barrier(0x100, 1, 0); }
  __builtin_amdgcn_sched_group_barrier(0x8, 2, 0);
}
template <int TS> DI void conv8(LAS char* pa, LAS char* pb, f32x4 (&acc)[4]) {
  v4u A0[8], B0[8], A1[8], B1[8];
  conv_ld<TS>(pa, pb, A0, B0);
  SB();
#pragma unroll 1
  for (int r = 0; r < 8; r += 2) {
    conv_ld<TS>(pa + 64, pb + 64, A1, B1); conv_mm<TS>(A0, B0, acc); conv_sched(); SB();
    pa += 128; pb += 128;
    conv_ld<TS>(pa, pb, A0, B0); conv_mm<TS>(A1, B1, acc); conv_sched(); SB();
  }
}
template <int TS> DI void conv2(LAS char* pa, LAS char* pb, f32x4 (&acc)[4]) {
#pragma unroll 1
  for (int r = 0; r < 2; ++r) {
    v4u A[8], B[8];
    conv_ld<TS>(pa, pb, A, B); conv_mm<TS>(A, B, acc);
    pa += 64; pb += 64;
  }
}
DI unsigned kshift2(float k1, float k2, float k3, float k4, float k5, float c0, float c1, float c2, float c3) {
  return pack2(fmaf(k4, c0, fmaf(k3, c1, fmaf(k2, c2, k1 * c3))), fmaf(k5, c0, fmaf(k4, c1, fmaf(k3, c2, k2 * c3))));
}
DI void stage_h(LAS char* Lc, v4u h, int g) {
  *(LAS v4u*)(Lc + CH_HB + 112 + g * 16) = h;
  LAS char* a = Lc + CH_HA + 114 + g * 16;
  *(LAS u16*)(a) = (u16)(h.x & 0xffff);
  *(LAS unsigned*)(a + 2) = __builtin_amdgcn_alignbit(h.y, h.x, 16);
  *(LAS v2u*)(a + 6) = v2u{__builtin_amdgcn_alignbit(h.z, h.y, 16), __builtin_amdgcn_alignbit(h.w, h.z, 16)};
  *(LAS u16*)(a + 14) = (u16)(h.w >> 16);
}
DI void stage_r(LAS char* Lc, v4u u, int g) {
  LAS char* rb = Lc + CH_R + g * 16;
  *(LAS v4u*)(rb) = u;
  { LAS char* q = rb + CH_RS - 4; *(LAS unsigned*)(q) = u.x; *(LAS v2u*)(q + 4) = v2u{u.y, u.z}; *(LAS unsigned*)(q + 12) = u.w; }
  { LAS char* q = rb + 2 * CH_RS - 8; *(LAS v2u*)(q) = v2u{u.x, u.y}; *(LAS v2u*)(q + 8) = v2u{u.z, u.w}; }
  { LAS char* q = rb + 3 * CH_RS - 12; *(LAS unsigned*)(q) = u.x; *(LAS v2u*)(q + 4) = v2u{u.y, u.z}; *(LAS unsigned*)(q + 12) = u.w; }
}
DI void conv_out(LAS char* Lc, f32x4 a, int n, int d, float dv, int e, int kq, __amdgpu_buffer_rsrc_t rws, unsigned tag) {
  const int t = 256 * n + 64 * (e >> 2) + 2 * (e & 3) + 16 * kq;
  const unsigned ua = *(LAS unsigned*)(Lc + CH_R + 2 * (2046 - t));
  const unsigned ub = *(LAS unsigned*)(Lc + CH_R + 2 * (2038 - t));
  const unsigned c01 = pack2(a[0], a[1]), c23 = pack2(a[2], a[3]);
  const unsigned d01 = pack2(bfhi(ua) * dv, bflo(ua) * dv), d23 = pack2(bfhi(ub) * dv, bflo(ub) * dv);
  v2u o;
  o.x = pack2(bflo(c01) + bflo(d01), bfhi(c01) + bfhi(d01));
  o.y = pack2(bflo(c23) + bflo(d23), bfhi(c23) + bfhi(d23));
  const int off = Y_OFF + d * 8192 + (256 * n + 64 * (e >> 2) + 16 * kq + 4 * (e & 3)) * 4;
  __builtin_amdgcn_raw_buffer_store_b128(v4u{o.x, tag, o.y, tag}, rws, off, 0, SC1);
}

__global__ __launch_bounds__(512, 2) void fused(const u16* __restrict__ x, const u16* __restrict__ kk, const u16* __restrict__ vv,
                                                const u16* __restrict__ sk, const u16* __restrict__ hk, const u16* __restrict__ Dv,
                                                const u16* __restrict__ w1, const u16* __restrict__ w2,
                                                char* ws, u16* out) {
  __shared__ __attribute__((aligned(16))) char smem[LDS_BYTES];
  LAS char* L = (LAS char*)smem;
  if (__builtin_amdgcn_readfirstlane(threadIdx.x >> 6) == 7) {
    unsigned long long pc;
    asm volatile("s_getpc_b64 %0" : "=s"(pc));
    const unsigned co = min(((blockIdx.x >> 3) & 7) * 8192u + (threadIdx.x & 63) * 128u, CODE_BYTES);
    asm volatile("s_mov_b32 m0, %0\n\ts_nop 0\n\tglobal_load_lds_dword %1, %2" :: "s"((unsigned)(uintptr_t)(L + XFS)), "v"(co), "s"(pc) : "m0");
  }
  const int b = blockIdx.x, xcd = b & 7, r = b >> 3;
  const int rg = xcd >> 1, hf = xcd & 1, pi = r >> 3, jj = r & 7;
  const int p = 4 * rg + pi, j = 7 * hf + jj;
  const bool g1 = jj < 7;
  const int mbl = 2 * pi + ((r >> 2) & 1), nbl = r & 3;
  const int mb = 8 * rg + mbl, nb = 4 * hf + nbl;
  const int lane = threadIdx.x & 63;
  const int w = __builtin_amdgcn_readfirstlane(threadIdx.x >> 6);
  const int hi = lane >> 5, aw = w & 3;
  STAMP_DECL; STAMP(0);
  const __amdgpu_buffer_rsrc_t rws = mkrsrc(ws, WS_BYTES);
  if (threadIdx.x < 16) *(LAS unsigned*)(L + CNT + threadIdx.x * 4) = 0u;
  const int dpair = 80 * nb + 2 * mb, d3 = 80 * nb + 64 + (mb >> 1), ts3 = mb & 1;
  const int e = lane & 15, kq = lane >> 4;
  const int aoff = ((e & 1) ? CH_HB : CH_HA) + (kq + (e >> 1)) * 16;
  const int boff = CH_R + (3 - (e & 3)) * CH_RS + (248 - 32 * 7 - 8 * (e >> 2) + kq) * 16;

  {
    const __amdgpu_buffer_rsrc_t rk = mkrsrc(kk, S * D * 2), rv_ = mkrsrc(vv, S * D * 2), rsk = mkrsrc(sk, D * 8);
    const int sb = 64 * mb * 1280 + 160 * nb;
    int vo[2];
#pragma unroll
    for (int i = 0; i < 2; ++i) { const int n = (8 * i + w) * 64 + lane; const int row = n / 10; vo[i] = row * 1280 + (n - row * 10) * 16; }
    GLDS(rk, L + PK + w * 1024, vo[0] + sb - 5120, 0);
    GLDS(rv_, L + PV + w * 1024, vo[0], sb);
    {
      const int n = (5 + w) * 64 + lane; const int row = n / 10; const int vo3 = row * 1280 + (n - row * 10) * 16;
      if (w < 3) GLDS(rk, L + PK + (8 + w) * 1024, vo[1] + sb - 5120, 0);
      else if (w < 5) GLDS(rv_, L + PV + (5 + w) * 1024, vo3, sb);
      else if (w == 5) GLDS(rsk, L + PS, lane * 16, nb * 640);
    }
  }
#define PREP_ITEM(tg, cg) do { \
        const int q_ = (lane & 15) >> 2, pp_ = lane & 3, tl = 16 * (tg) + 4 * kq; \
        LAS char* ka_ = L + PK + (tl + q_) * 160 + (cg) * 32 + pp_ * 8; \
        v2u k0 = trread(ka_), k1 = trread(ka_ + 4 * 160); \
        v2u v0 = trread(ka_ + (PV - PK)); \
        v2u sw = *(LAS v2u*)(L + PS + ((cg) * 16 + e) * 8); \
        float kf[8] = {bflo(k0.x), bfhi(k0.x), bflo(k0.y), bfhi(k0.y), bflo(k1.x), bfhi(k1.x), bflo(k1.y), bfhi(k1.y)}; \
        float vf[4] = {bflo(v0.x), bfhi(v0.x), bflo(v0.y), bfhi(v0.y)}; \
        const float c0 = bflo(sw.x), c1 = bfhi(sw.x), c2 = bflo(sw.y), c3 = bfhi(sw.y); \
        float u[4]; \
        _Pragma("unroll") for (int e2 = 0; e2 < 4; e2 += 2) { \
          const unsigned a = kshift2(kf[e2 + 1], kf[e2 + 2], kf[e2 + 3], kf[e2 + 4], kf[e2 + 5], c0, c1, c2, c3); \
          u[e2] = bflo(a) * vf[e2]; u[e2 + 1] = bfhi(a) * vf[e2 + 1]; } \
        v2u o = v2u{pack2(u[3], u[2]), pack2(u[1], u[0])}; \
        const int ch = (cg) * 16 + e; \
        const int pos8 = (60 - tl) >> 2; \
        *(LAS v2u*)(L + PO + ch * 128 + ((((pos8 >> 1) ^ (ch & 7)) << 4) | ((pos8 & 1) << 3))) = o; \
      } while (0)

  if (w >= 4) {
    unsigned gen;
    asm volatile("s_load_dword %0, %1, 0x0\n\ts_waitcnt lgkmcnt(0)" : "=s"(gen) : "s"(ws) : "memory");
    const unsigned tag = gen * 0x9E3779B1u + 0x85EBCA6Bu;
    STAMP(1);
    WAITV(0);
    BAR();
    if (aw == 0 && lane == 0) *(LAS unsigned*)(L + C_TAG) = tag;
    unsigned ab = 4;
#define AUXBAR() do { WAITL(); lds_add(L + C_AUX, 1u, lane); ab += 4; lds_wait(L + C_AUX, ab); } while (0)
    STAMP(2);
    if (ABL & 4) lds_wait_sleep(L + C_MMA, 44);
    {
      const __amdgpu_buffer_rsrc_t rw2 = mkrsrc(w2, F * D * 2);
#pragma unroll
      for (int i = 0; i < 3; ++i) if (4 * i + aw < 10) {
        const int n = (4 * i + aw) * 64 + lane; const int row = n / 10;
        GLDS(rw2, L + PW + (4 * i + aw) * 1024, (row + (row >= 32 ? 992 : 0)) * 1280 + (n - row * 10) * 16, 32 * mb * 1280 + 160 * nb);
      }
    }
    {
      const int g = lane >> 4, q = (lane & 15) >> 2, pp = lane & 3;
      PREP_ITEM(aw, 0); PREP_ITEM(aw, 1); PREP_ITEM(aw, 2);
      AUXBAR();
#pragma unroll
      for (int i = 0; i < 3; ++i) {
        const int ri = aw + 4 * i;
        if (ri < 10) {
          const int ch = 8 * ri + (lane >> 3), cc = lane & 7;
          v4u val = ldsr128(L + PO + ch * 128 + ((cc ^ (ch & 7)) << 4));
          __builtin_amdgcn_raw_buffer_store_b128(val, rws, (ABL & 1) ? -16 : R_OFF + (80 * nb + ch) * 4096 + (31 - mb) * 128 + cc * 16, 0, SC1);
        }
      }
      __builtin_amdgcn_s_setprio(AUX_PRIO);
      if (aw < 2) WAITV(3); else WAITV(2);
      AUXBAR();
      STAMP(3);
#pragma unroll
      for (int i = 0; i < 3; ++i) {
        const int fi = aw + 4 * i;
        if (fi < 10) {
          const int half = fi / 5, nt = fi - half * 5, ks = mb + 32 * half;
          const int r0 = 16 * (g & 1) + 4 * (g >> 1);
          LAS char* a = L + PW + half * 5120 + (r0 + q) * 160 + nt * 32 + pp * 8;
          v2u lo = trread(a), hi2 = trread(a + 8 * 160);
          __builtin_amdgcn_raw_buffer_store_b128(v4u{lo.x, lo.y, hi2.x, hi2.y}, rws, (ks < KS2 && !(ABL & 1)) ? W2P_OFF + nb * W2P_NB + ks * 5120 + nt * 1024 + lane * 16 : -16, 0, SC1);
        }
      }
    }
    STAMP(4);
    WAITV(0);
    AUXBAR();
    if (aw == 0) set_flag(ws, FL_P + (nb * 32 + mb) * FSTR, tag, lane);
    STAMP(5);
    if (ABL & 2) lds_wait_sleep(L + C_MMA, 44);
    const __amdgpu_buffer_rsrc_t rh = mkrsrc(hk, D * S * 2);
    v4u cu[3];
    cu[0] = __builtin_amdgcn_raw_buffer_load_b128(rh, dpair * 4096 + aw * 1024 + lane * 16, 0, 0);
    cu[1] = __builtin_amdgcn_raw_buffer_load_b128(rh, dpair * 4096 + 4096 + aw * 1024 + lane * 16, 0, 0);
    cu[2] = __builtin_amdgcn_raw_buffer_load_b128(rh, d3 * 4096 + aw * 1024 + lane * 16, 0, 0);
    const int g = aw * 64 + lane;
    lds_wait_sleep(L + C_MMA, DELAY_POLL);
    if (aw == 1) {
      const int jc = g1 ? j : 0;
      v2u gr[4];
#define GRL(off) __builtin_bit_cast(v2u, __hip_atomic_load((GAS unsigned long long*)(ws + (off)), RLX_AGENT))
#pragma unroll 1
      for (unsigned sp = 0; sp < SPIN_CAP; ++sp) {
        gr[0] = GRL(ST_OFF + (p * 128 + lane) * 8);
        gr[1] = GRL(ST_OFF + (p * 128 + 64 + lane) * 8);
        gr[2] = GRL(CS_OFF + (jc * 128 + lane) * 8);
        gr[3] = GRL(CS_OFF + (jc * 128 + (j == 13 ? 0 : 64) + lane) * 8);
        if (__all(gr[0].y == tag && gr[1].y == tag && gr[2].y == tag && gr[3].y == tag)) break;
        __builtin_amdgcn_s_sleep(POLL_SLEEP);
      }
      *(LAS f32x2*)(L + XFS + lane * 8) = f32x2{bflo(gr[0].x), 1.f / bfhi(gr[0].x)};
      *(LAS f32x2*)(L + XFS + (64 + lane) * 8) = f32x2{bflo(gr[1].x), 1.f / bfhi(gr[1].x)};
      *(LAS unsigned*)(L + XFC + lane * 4) = gr[2].x;
      *(LAS unsigned*)(L + XFC + (64 + lane) * 4) = gr[3].x;
      WAITL();
      lds_add(L + C_S, 1u, lane);
    }
    if (aw == 2) { poll_flags(ws, FL_P + nb * 32 * FSTR, 32, tag, lane); lds_add(L + C_P, 1u, lane); }
    lds_wait_sleep(L + C_P, 1);
    STAMP(6);
    v4u rv[3];
    rv[0] = __builtin_amdgcn_raw_buffer_load_b128(rws, R_OFF + dpair * 4096 + aw * 1024 + lane * 16, 0, SC1);
    rv[1] = __builtin_amdgcn_raw_buffer_load_b128(rws, R_OFF + dpair * 4096 + 4096 + aw * 1024 + lane * 16, 0, SC1);
    rv[2] = __builtin_amdgcn_raw_buffer_load_b128(rws, R_OFF + d3 * 4096 + aw * 1024 + lane * 16, 0, SC1);
    const int rot2 = ROT2_EXPR;
    const __amdgpu_buffer_rsrc_t ra = mkrsrc(ws + HP_OFF + mb * HP_MB, HP_MB);
    const __amdgpu_buffer_rsrc_t rb = mkrsrc(ws + W2P_OFF + nb * W2P_NB, W2P_NB);
    const int va = lane * 16 + aw * 4096, vb = lane * 16 + aw * 5120;
    constexpr int WB = WB_N, NKS = 14, POOL = POOL_N;
    v4u fa[NKS][4], fb[NKS][5];
#define KIDX(i) ((i) + rot2 >= NKS ? (i) + rot2 - NKS : (i) + rot2)
#define LOADA(i) do { const int so_ = KIDX(i) * 16384; \
    _Pragma("unroll") for (int m = 0; m < 4; ++m) fa[i][m] = __builtin_amdgcn_raw_buffer_load_b128(ra, va + m * 1024, so_, SC1); } while (0)
#define LOADB(i) do { const int so_ = KIDX(i) * 20480; \
    _Pragma("unroll") for (int n = 0; n < 5; ++n) fb[i][n] = __builtin_amdgcn_raw_buffer_load_b128(rb, vb + (n & 3) * 1024, so_ + (n >> 2) * 4096, SC1); } while (0)
#pragma unroll
    for (int i = 0; i < WB; ++i) LOADB(i);
    SB();
    const float dv = bflo(Dv[d3]);
    STAMP(7);
    lds_wait_sleep(L + C_MMA, STAGE_START);
    if (aw == 0 && lane < 32) *(LAS v4u*)(L + CV + (lane >> 4) * CH_BYTES + ((lane >> 3) & 1) * CH_HB + (lane & 7) * 16) = v4u{0, 0, 0, 0};
    if (aw == 3) {
#pragma unroll
      for (int i = 0; i < 4; ++i) {
        const int n = lane + 64 * i;
        if (n < 200) { const int cp = n / 25, sl = n - cp * 25; *(LAS v4u*)(L + CV + (cp >> 2) * CH_BYTES + CH_R + (cp & 3) * CH_RS + 4080 + sl * 16) = v4u{0, 0, 0, 0}; }
      }
    }
    stage_h(L + CV, cu[0], g);
    stage_h(L + CV + CH_BYTES, cu[1], g);
    stage_r(L + CV, rv[0], g);
    stage_r(L + CV + CH_BYTES, rv[1], g);
    if (aw == 0 && lane < 16) *(LAS v4u*)(L + CV3 + (lane >> 3) * CH_HB + (lane & 7) * 16) = v4u{0, 0, 0, 0};
    if (aw == 3) {
#pragma unroll
      for (int i = 0; i < 2; ++i) {
        const int n = lane + 64 * i;
        if (n < 100) { const int cp = n / 25, sl = n - cp * 25; *(LAS v4u*)(L + CV3 + CH_R + cp * CH_RS + 4080 + sl * 16) = v4u{0, 0, 0, 0}; }
      }
    }
    stage_h(L + CV3, cu[2], g);
    stage_r(L + CV3, rv[2], g);
    AUXBAR();
    if (aw == 0) lds_add(L + C_ST, 1u, lane);
    lds_wait_sleep(L + C_MMA, CONV_START);
    __builtin_amdgcn_s_setprio(CONV_PRIO);
    STAMP(8);
    {
      f32x4 ac3[4] = {};
      if (ts3 == 0) conv2<0>(L + CV3 + aoff + aw * 128, L + CV3 + boff + aw * 128, ac3); else conv2<1>(L + CV3 + aoff + aw * 128, L + CV3 + boff + aw * 128, ac3);
#pragma unroll
      for (int ti = 0; ti < 4; ++ti) *(LAS f32x4*)(L + SC3 + (aw * 4 + ti) * 1024 + lane * 16) = ac3[ti];
      AUXBAR();
      f32x4 a = {};
#pragma unroll
      for (int ww = 0; ww < 4; ++ww) a += *(LAS f32x4*)(L + SC3 + (ww * 4 + aw) * 1024 + lane * 16);
      conv_out(L + CV3, a, ts3 == 0 ? (aw < 2 ? aw : aw + 4) : 2 + aw, d3, dv, e, kq, rws, tag);
      WAITL();
      lds_add(L + C_Y, 1u, lane);
    }
    STAMP(10);
    if (aw == 1) { poll_flags(ws, FL_H + p * 16 * FSTR, 14, tag, lane); lds_add(L + C_HR, 1u, lane); }
    __builtin_amdgcn_s_setprio(G2_PRIO);
    lds_wait_sleep(L + C_HR, 1);
    STAMP(11);
#define ISSUE(q) do { constexpr int q_ = (q); \
      if constexpr (q_ < 4 * WB) { fa[q_ / 4][q_ % 4] = __builtin_amdgcn_raw_buffer_load_b128(ra, va + (q_ % 4) * 1024, KIDX(q_ / 4) * 16384, SC1); } \
      else if constexpr (q_ < 4 * WB + 9 * (NKS - WB)) { constexpr int k_ = WB + (q_ - 4 * WB) / 9, f_ = (q_ - 4 * WB) % 9; \
        if constexpr (f_ < 4) fa[k_][f_] = __builtin_amdgcn_raw_buffer_load_b128(ra, va + f_ * 1024, KIDX(k_) * 16384, SC1); \
        else fb[k_][f_ - 4] = __builtin_amdgcn_raw_buffer_load_b128(rb, vb + ((f_ - 4) & 3) * 1024, KIDX(k_) * 20480 + ((f_ - 4) >> 2) * 4096, SC1); } \
    } while (0)
    constexpr auto need = [](int k) { k = k > NKS ? NKS : k; return k <= WB ? 4 * k : 4 * WB + 9 * (k - WB); };
    constexpr auto cum = [need](int i) { const int a = need(i + LOOK_N), c = POOL - 5 * WB + (i > 0 ? 9 * i - 6 : 0); return a < c ? a : c; };
    static_for<0, cum(0)>([&](auto q) { ISSUE(decltype(q)::value); });
    SB();
    f32x4 acc2[4][5] = {};
    static_for<0, NKS>([&](auto ii) {
      constexpr int i = decltype(ii)::value;
      constexpr int c0 = cum(i), cn = cum(i + 1) - cum(i);
      static_assert(cn <= 9 && c0 >= need(i + 1), "schedule");
      static_for<0, 20>([&](auto mm) {
        constexpr int mn = decltype(mm)::value, m = mn / 5, n = mn % 5;
        acc2[m][n] = mfma16(fb[i][n], fa[i][m], acc2[m][n]);
        constexpr int slot = (mn & 1) ? (mn >> 1) - (9 - cn) : -1;
        if constexpr (slot >= 0 && mn < 19) { SB(); ISSUE(c0 + slot); SB(); }
      });
      SB();
      if (i == 0) STAMP(12);
    });
    STAMP(13);
    lds_wait(L + C_Y, 8);
    const int ln = __builtin_amdgcn_mbcnt_hi(~0u, __builtin_amdgcn_mbcnt_lo(~0u, opaque0()));
#pragma unroll
    for (int m = 0; m < 4; ++m)
#pragma unroll
      for (int n = 0; n < 5; ++n)
        *(LAS f32x4*)(L + aw * PART + (16 * m + (ln & 15)) * PSTR + n * 64 + (ln >> 4) * 16) = acc2[m][n];
  } else {
    __builtin_amdgcn_s_setprio(MMA_PRIO);
    const __amdgpu_buffer_rsrc_t rw = mkrsrc(w1, D * F * 2);
    unsigned tag;
    if (g1) {
      const __amdgpu_buffer_rsrc_t rx = mkrsrc(x + (size_t)p * 128 * D, 128 * D * 2);
      int vx[2], vw[4];
      {
        const int row = 32 * w + (lane >> 3);
        vx[0] = row * 1280 + (((lane & 7) ^ ((row >> 1) & 7)) << 4);
        vx[1] = (row + 8) * 1280 + (((lane & 7) ^ (((row + 8) >> 1) & 7)) << 4);
#pragma unroll
        for (int ii = 0; ii < 4; ++ii) {
          const int kr = 16 * w + 4 * ii + (lane >> 4);
          vw[ii] = kr * (F * 2) + j * 256 + (((lane & 15) ^ (((kr & 3) << 2) | ((kr >> 2) & 3))) << 4);
        }
      }
      const int rot = ROT_EXPR;
      const unsigned lbase = (unsigned)(uintptr_t)L + w * 4096;
#define KC(s) ((s) + rot >= 10 ? (s) + rot - 10 : (s) + rot)
#define G1LD(s, i) do { \
    if ((i) < 4) glds_fast(rx, lbase + ((s) % NBUF) * 32768 + (i) * 1024, vx[(i) & 1], KC(s) * 128 + ((i) >> 1) * 16 * 1280); \
    else glds_fast(rw, lbase + ((s) % NBUF) * 32768 + 16384 + ((i) - 4) * 1024, vw[((i) - 4) & 3], KC(s) * (64 * F * 2)); \
  } while (0)
#pragma unroll
      for (int i = 0; i < 8; ++i) G1LD(0, i);
#pragma unroll
      for (int i = 0; i < 8; ++i) G1LD(1, i);
      WAITV(16);
      BAR();
      PREP_ITEM(aw, 3); PREP_ITEM(aw, 4);
      WAITL(); lds_add(L + C_AUX, 1u, lane);
      SB();
      STAMP(1);
      const int fh = w & 1, th = w >> 1;
      int xa[4], wa[2][2];
      {
        const int row = 64 * th + (lane & 31);
        const int c0 = hi ^ ((row >> 1) & 7);
        const int blk = (lane >> 4) & 1, q = (lane & 15) >> 2, pp = lane & 3;
#pragma unroll
        for (int s = 0; s < 4; ++s) xa[s] = row * 128 + (((2 * s) ^ c0) << 4);
#pragma unroll
        for (int ft = 0; ft < 2; ++ft)
#pragma unroll
          for (int t = 0; t < 2; ++t) {
            const int kr = 8 * hi + 4 * t + q;
            const int ch = 4 * (2 * fh + ft) + 2 * blk + (pp >> 1);
            wa[ft][t] = 16384 + 256 * kr + ((ch ^ ((q << 2) | ((2 * hi + t) & 3))) << 4) + 8 * (pp & 1);
          }
      }
      f32x16 acc[2][2] = {};
      Frags fA, fB;
#define RDX(f, bo, s, tt) f.x[s][tt] = ldsr128(L + (bo) + xa[s] + (tt) * 4096)
#define RDW(f, bo, s, ft) do { v2u wl_ = trread(L + (bo) + wa[ft][0] + (s) * 4096), wh_ = trread(L + (bo) + wa[ft][1] + (s) * 4096); f.w[s][ft] = v4u{wl_.x, wl_.y, wh_.x, wh_.y}; } while (0)
#define MM(f, s, ft, tt) acc[ft][tt] = mfma32(f.w[s][ft], f.x[s][tt], acc[ft][tt])
#define ITER(s, vm, cur, nxt) do { \
    asm volatile("s_waitcnt vmcnt(" #vm ") lgkmcnt(0)" ::: "memory"); \
    lds_add(L + C_MMA, 1u, lane); SB(); \
    MM(cur, 0, 0, 0); MM(cur, 0, 0, 1); MM(cur, 0, 1, 0); SB(); \
    lds_wait(L + C_MMA, 4 * ((s) + 2)); SB(); \
    constexpr int bo_ = (((s) + 1) % NBUF) * 32768; constexpr bool ld_ = (s) + NBUF < 10; \
    MM(cur, 0, 1, 1); RDX(nxt, bo_, 0, 0); RDX(nxt, bo_, 0, 1); SB(); if (ld_) G1LD((s) + NBUF, 0); SB(); \
    MM(cur, 1, 0, 0); RDW(nxt, bo_, 0, 0); SB(); \
    MM(cur, 1, 0, 1); RDW(nxt, bo_, 0, 1); SB(); if (ld_) G1LD((s) + NBUF, 4); SB(); \
    MM(cur, 1, 1, 0); RDX(nxt, bo_, 1, 0); RDX(nxt, bo_, 1, 1); SB(); if (ld_) G1LD((s) + NBUF, 1); SB(); \
    MM(cur, 1, 1, 1); RDW(nxt, bo_, 1, 0); SB(); \
    MM(cur, 2, 0, 0); RDW(nxt, bo_, 1, 1); SB(); if (ld_) G1LD((s) + NBUF, 5); SB(); \
    MM(cur, 2, 0, 1); RDX(nxt, bo_, 2, 0); RDX(nxt, bo_, 2, 1); SB(); if (ld_) G1LD((s) + NBUF, 2); SB(); \
    MM(cur, 2, 1, 0); RDW(nxt, bo_, 2, 0); SB(); \
    MM(cur, 2, 1, 1); RDW(nxt, bo_, 2, 1); SB(); if (ld_) G1LD((s) + NBUF, 6); SB(); \
    MM(cur, 3, 0, 0); RDX(nxt, bo_, 3, 0); RDX(nxt, bo_, 3, 1); SB(); if (ld_) G1LD((s) + NBUF, 3); SB(); \
    MM(cur, 3, 0, 1); RDW(nxt, bo_, 3, 0); RDW(nxt, bo_, 3, 1); SB(); \
    MM(cur, 3, 1, 0); SB(); if (ld_) G1LD((s) + NBUF, 7); SB(); \
    MM(cur, 3, 1, 1); SB(); \
  } while (0)
      WAITV(8);
      lds_add(L + C_MMA, 1u, lane); lds_wait(L + C_MMA, 4);
      STAMP(2);
#pragma unroll
      for (int s = 0; s < 4; ++s) { RDX(fA, 0, s, 0); RDX(fA, 0, s, 1); RDW(fA, 0, s, 0); RDW(fA, 0, s, 1); }
      SB();
#pragma unroll
      for (int i = 0; i < 8; ++i) G1LD(2, i);
      SB();
      ITER(0, 8, fA, fB);
      ITER(1, 8, fB, fA);
      ITER(2, 8, fA, fB);
      STAMP(3);
      ITER(3, 8, fB, fA);
      ITER(4, 8, fA, fB);
      ITER(5, 8, fB, fA);
      STAMP(4);
      ITER(6, 8, fA, fB);
      ITER(7, 8, fB, fA);
      ITER(8, 0, fA, fB);
#pragma unroll
      for (int s = 0; s < 4; ++s) { MM(fB, s, 0, 0); MM(fB, s, 0, 1); MM(fB, s, 1, 0); MM(fB, s, 1, 1); }
      SB();
      lds_add(L + C_MMA, 1u, lane);
      STAMP(5);
      lds_wait_sleep(L + C_S, 1);
      tag = __builtin_amdgcn_readfirstlane(*(LAS unsigned*)(L + C_TAG));
      STAMP(6);
#pragma unroll
      for (int ft = 0; ft < 2; ++ft) {
        const int fg = 2 * fh + ft;
        const int ks = 4 * j + fg;
        f32x4 c4[4];
#pragma unroll
        for (int g = 0; g < 4; ++g) c4[g] = *(LAS f32x4*)(L + XFC + (32 * fg + 8 * g + 4 * hi) * 4);
#pragma unroll
        for (int tt = 0; tt < 2; ++tt) {
          const int tok = 64 * th + 32 * tt + (lane & 31);
          const f32x2 st = *(LAS f32x2*)(L + XFS + tok * 8);
          const f32x16& a = acc[ft][tt];
          const int mb2 = 2 * p + th, mt = 2 * tt + ((lane >> 4) & 1);
          const int off = ks < KS2 ? HP_OFF + mb2 * HP_MB + ks * 4096 + mt * 1024 + ((2 * hi) * 16 + (lane & 15)) * 16 : -1024;
#pragma unroll
          for (int hh = 0; hh < 2; ++hh) {
            unsigned pk[4];
#pragma unroll
            for (int e2 = 0; e2 < 4; ++e2) {
              const int ee = hh * 4 + e2;
              float v0 = (a[2 * ee] - st.x * c4[ee >> 1][(2 * ee) & 3]) * st.y;
              float v1 = (a[2 * ee + 1] - st.x * c4[ee >> 1][(2 * ee + 1) & 3]) * st.y;
              unsigned hb = pack2(v0, v1);
              pk[e2] = pack2(silu_f(bflo(hb)), silu_f(bfhi(hb)));
            }
            __builtin_amdgcn_raw_buffer_store_b128(v4u{pk[0], pk[1], pk[2], pk[3]}, rws, off + hh * 256, 0, SC1);
            SB();
          }
        }
      }
      lds_add(L + C_MMA, 1u, lane);
      STAMP(7);
      WAITV(0);
      {
        unsigned old = 0;
        if (lane == 0) old = __hip_atomic_fetch_add((LAS unsigned*)(L + C_H), 1u, RLX_WG);
        if (__builtin_amdgcn_readfirstlane(old) == 3) set_flag(ws, FL_H + (p * 16 + j) * FSTR, tag, lane);
      }
      STAMP(8);
    } else {
      const int idx16 = 4 * rg + pi;
      const int srow = 512 * rg + 64 * (4 * hf + pi) + 16 * w + (lane >> 3);
      const int cg = 14 * hf + idx16;
      const bool csv = idx16 < 14 - hf;
      const __amdgpu_buffer_rsrc_t rx = mkrsrc(x, S * D * 2);
      v4u xv[2][10], wv[20];
      WAITV(0);
      BAR();
      if (ABL & 8) for (int i = 0; i < 150; ++i) __builtin_amdgcn_s_sleep(1);
#pragma unroll
      for (int h2 = 0; h2 < 2; ++h2)
#pragma unroll
        for (int i = 0; i < 10; ++i) xv[h2][i] = __builtin_amdgcn_raw_buffer_load_b128(rx, (srow + 8 * h2) * 1280 + (lane & 7) * 16 + i * 128, 0, 0);
      PREP_ITEM(aw, 3); PREP_ITEM(aw, 4);
      WAITL(); lds_add(L + C_AUX, 1u, lane);
      const int wvo = csv ? (160 * w + (lane >> 3)) * (F * 2) + cg * 128 + (lane & 7) * 16 : -16;
#pragma unroll
      for (int i = 0; i < 20; ++i) wv[i] = __builtin_amdgcn_raw_buffer_load_b128(rw, wvo, csv ? i * 8 * F * 2 : 0, 0);
      lds_wait_sleep(L + C_AUX, 8);
      tag = __builtin_amdgcn_readfirstlane(*(LAS unsigned*)(L + C_TAG));
#pragma unroll
      for (int h2 = 0; h2 < 2; ++h2) {
        float s = 0.f;
#pragma unroll
        for (int i = 0; i < 10; ++i)
#pragma unroll
          for (int e2 = 0; e2 < 4; ++e2) s = dot2(xv[h2][i][e2], 0x3f803f80u, s);
        s += __shfl_xor(s, 1); s += __shfl_xor(s, 2); s += __shfl_xor(s, 4);
        const float mean = s * (1.f / D);
        float q0 = 0.f, q1 = 0.f;
#pragma unroll
        for (int i = 0; i < 10; ++i)
#pragma unroll
          for (int e2 = 0; e2 < 4; ++e2) { const float a0 = bflo(xv[h2][i][e2]) - mean, a1 = bfhi(xv[h2][i][e2]) - mean; q0 = fmaf(a0, a0, q0); q1 = fmaf(a1, a1, q1); }
        float q = q0 + q1;
        q += __shfl_xor(q, 1); q += __shfl_xor(q, 2); q += __shfl_xor(q, 4);
        const float den = __builtin_sqrtf(rbf(rbf(q * (1.f / D)) + 1e-5f));
        __builtin_amdgcn_raw_buffer_store_b64(v2u{pack2(mean, den), tag}, rws, (lane & 7) == 0 ? ST_OFF + (srow + 8 * h2) * 8 : -16, 0, SC1);
      }
      float c[8] = {};
#pragma unroll
      for (int i = 0; i < 20; ++i)
#pragma unroll
        for (int e2 = 0; e2 < 4; ++e2) { c[2 * e2] += bflo(wv[i][e2]); c[2 * e2 + 1] += bfhi(wv[i][e2]); }
      *(LAS f32x4*)(L + ((w * 8 + (lane >> 3)) * 64 + (lane & 7) * 8) * 4) = f32x4{c[0], c[1], c[2], c[3]};
      *(LAS f32x4*)(L + ((w * 8 + (lane >> 3)) * 64 + (lane & 7) * 8 + 4) * 4) = f32x4{c[4], c[5], c[6], c[7]};
      WAITL();
      unsigned old = 0;
      if (lane == 0) old = __hip_atomic_fetch_add((LAS unsigned*)(L + C_H), 1u, RLX_WG);
      if (__builtin_amdgcn_readfirstlane(old) == 3) {
        asm volatile("" ::: "memory");
        float t4[4] = {};
#pragma unroll
        for (int i = 0; i < 32; ++i) t4[i & 3] += *(LAS float*)(L + (i * 64 + lane) * 4);
        __builtin_amdgcn_raw_buffer_store_b64(v2u{__float_as_uint((t4[0] + t4[1]) + (t4[2] + t4[3])), tag}, rws, csv ? CS_OFF + (cg * 64 + lane) * 8 : -16, 0, SC1);
      }
      lds_add(L + C_MMA, 12u, lane);
      STAMP(5);
    }
    unsigned hb_ = 0;
#define HBAR() do { WAITL(); lds_add(L + C_G2, 1u, lane); hb_ += 4; lds_wait(L + C_G2, hb_); } while (0)
    {
      const __amdgpu_buffer_rsrc_t rxx = mkrsrc(x, S * D * 2);
      lds_wait_sleep(L + C_MMA, 48);
#pragma unroll
      for (int i = 0; i < 3; ++i) {
        const int s = aw + 4 * i;
        if (s < 10) {
          const int n = s * 64 + lane; const int row = n / 10, c = n - row * 10;
          GLDS(rxx, L + EX + s * 1024, (64 * mb + row) * 1280 + 160 * nb + c * 16, 0);
        }
      }
      const float dv = bflo(Dv[dpair + (aw >> 1)]);
      lds_wait_sleep(L + C_ST, 1);
      __builtin_amdgcn_s_setprio(C8_PRIO);
      LAS char* Lc = L + CV + (aw >> 1) * CH_BYTES;
      const int cts = aw & 1;
      f32x4 acc[4] = {};
      if (cts == 0) conv8<0>(Lc + aoff, Lc + boff, acc); else conv8<1>(Lc + aoff, Lc + boff, acc);
      __builtin_amdgcn_s_setprio(MMA_PRIO);
      STAMP(9);
#pragma unroll
      for (int ti = 0; ti < 4; ++ti) conv_out(Lc, acc[ti], cts == 0 ? (ti < 2 ? ti : ti + 4) : 2 + ti, dpair + (aw >> 1), dv, e, kq, rws, tag);
    }
    WAITV(4);
    HBAR();
    STAMP(10);
    {
      const int q = (lane & 15) >> 2, pp = lane & 3, pos = lane & 15;
      const int tok = 16 * aw + 2 * (pos >> 2) + (pos & 1) + 8 * ((pos >> 1) & 1);
      lds_wait_sleep(L + C_S, 1);
      const f32x2 st = *(LAS f32x2*)(L + XFS + ((mb & 1) * 64 + tok) * 8);
      v4u xn[5];
#pragma unroll
      for (int i = 0; i < 5; ++i) {
        const v2u xv2 = *(LAS v2u*)(L + EX + tok * 160 + (4 * i + kq) * 8);
        const unsigned d01 = pack2(bflo(xv2.x) - st.x, bfhi(xv2.x) - st.x), d23 = pack2(bflo(xv2.y) - st.x, bfhi(xv2.y) - st.x);
        xn[i] = v4u{pack2(bflo(d01) * st.y, bfhi(d01) * st.y), pack2(bflo(d23) * st.y, bfhi(d23) * st.y), xv2.x, xv2.y};
      }
      WAITL();
      lds_add(L + C_Y, 1u, lane);
      for (int i = 0; i < Y_DELAY; ++i) __builtin_amdgcn_s_sleep(4);
      STAMP(11);
      {
        v4u yv[5];
        const int yo = Y_OFF + (80 * nb + 4 * aw + kq) * 8192 + mb * 256 + e * 16;
#pragma unroll 1
        for (unsigned sp = 0; sp < SPIN_CAP; ++sp) {
          asm volatile("" ::: "memory");
          bool ok = true;
#pragma unroll
          for (int i = 0; i < 5; ++i) yv[i] = __builtin_amdgcn_raw_buffer_load_b128(rws, yo, i * 16 * 8192, SC1);
#pragma unroll
          for (int i = 0; i < 5; ++i) ok = ok && yv[i].y == tag && yv[i].w == tag;
          if (__all(ok)) break;
          __builtin_amdgcn_s_sleep(POLL_SLEEP);
        }
#pragma unroll
        for (int i = 0; i < 5; ++i) *(LAS v2u*)(L + EY + (16 * i + 4 * aw + kq) * 144 + e * 8) = v2u{yv[i].x, yv[i].z};
      }
      HBAR();
      STAMP(12);
#pragma unroll
      for (int i = 0; i < 5; ++i) {
        const int dq = 4 * i + kq;
        const v2u yv = trread(L + EY + (4 * dq + q) * 144 + aw * 32 + pp * 8);
        const unsigned s01 = pack2(bflo(xn[i].x) * bflo(yv.x), bfhi(xn[i].x) * bfhi(yv.x)), s23 = pack2(bflo(xn[i].y) * bflo(yv.y), bfhi(xn[i].y) * bfhi(yv.y));
        *(LAS v4u*)(L + ES + (tok * 20 + dq) * 16) = v4u{s01, s23, xn[i].z, xn[i].w};
      }
    }
    if (b == 0 && aw == 0) {
#pragma unroll 1
      for (int i = 0; i < 4; ++i) poll_flags(ws, FL_P + i * 64 * FSTR, 64, tag, lane);
      set_flag(ws, FL_GEN, tag, lane);
    }
  }
  STAMP(14);
  BAR();
  const int lane2 = __builtin_amdgcn_mbcnt_hi(~0u, __builtin_amdgcn_mbcnt_lo(~0u, opaque0()));
#pragma unroll
  for (int i = 0; i < 3; ++i) {
    const int it = w + 8 * i;
    if (it < 20) {
      const int n = it * 64 + lane2;
      const int tok = n / 20, dq = n - tok * 20;
      f32x4 o = *(LAS f32x4*)(L + tok * PSTR + dq * 16);
#pragma unroll
      for (int ww = 1; ww < 4; ++ww) o += *(LAS f32x4*)(L + ww * PART + tok * PSTR + dq * 16);
      const v4u sx = *(LAS v4u*)(L + ES + n * 16);
      const unsigned o01 = pack2(o[0], o[1]), o23 = pack2(o[2], o[3]);
      const unsigned g01 = pack2(silu_f(bflo(o01)), silu_f(bfhi(o01))), g23 = pack2(silu_f(bflo(o23)), silu_f(bfhi(o23)));
      const unsigned m01 = pack2(bflo(sx.x) * bflo(g01), bfhi(sx.x) * bfhi(g01)), m23 = pack2(bflo(sx.y) * bflo(g23), bfhi(sx.y) * bfhi(g23));
      v2u res;
      res.x = pack2(bflo(sx.z) + bflo(m01), bfhi(sx.z) + bfhi(m01));
      res.y = pack2(bflo(sx.w) + bflo(m23), bfhi(sx.w) + bfhi(m23));
      *(v2u*)(out + (size_t)(64 * mb + tok) * D + 80 * nb + dq * 4) = res;
    }
  }
  STAMP(15);
  STAMP_OUT(0);
}

extern "C" void kernel_launch(void* const* d_in, const int* in_sizes, int n_in,
                              void* d_out, int out_size, void* d_ws, size_t ws_size,
                              hipStream_t stream) {
  fused<<<256, 512, 0, stream>>>((const u16*)d_in[0], (const u16*)d_in[1], (const u16*)d_in[2], (const u16*)d_in[3],
                                 (const u16*)d_in[4], (const u16*)d_in[5], (const u16*)d_in[7], (const u16*)d_in[8],
                                 (char*)d_ws, (u16*)d_out);
}
```

```cpp
#include <hip/hip_runtime.h>
#include <stdint.h>

typedef unsigned short u16;
typedef unsigned v4u __attribute__((ext_vector_type(4)));
typedef unsigned v2u __attribute__((ext_vector_type(2)));
typedef float f32x2 __attribute__((ext_vector_type(2)));
typedef float f32x4 __attribute__((ext_vector_type(4)));
typedef float f32x16 __attribute__((ext_vector_type(16)));
typedef short bf16x8 __attribute__((ext_vector_type(8)));
typedef short s16x4 __attribute__((ext_vector_type(4)));
typedef __bf16 bf16x2 __attribute__((ext_vector_type(2)));
#define LAS __attribute__((address_space(3)))
#define GAS __attribute__((address_space(1)))
#define DI __device__ __forceinline__
#define RLX_AGENT __ATOMIC_RELAXED, __HIP_MEMORY_SCOPE_AGENT
#define RLX_WG __ATOMIC_RELAXED, __HIP_MEMORY_SCOPE_WORKGROUP

#define FSTR_N 128
constexpr int S = 2048, D = 640, F = 1728;
constexpr int KS2 = 54;
constexpr int FSTR = FSTR_N;
constexpr int FL_GEN = 0, FL_P = 131072, FL_H = FL_P + 256 * 256;
constexpr int ST_OFF = 32768;
constexpr int CS_OFF = ST_OFF + S * 8;
constexpr int HP_OFF = 262144;
constexpr int HP_MB = KS2 * 4096;
constexpr int W2P_OFF = HP_OFF + 32 * HP_MB;
constexpr int W2P_NB = KS2 * 5120;
constexpr int R_OFF = W2P_OFF + 8 * W2P_NB;
constexpr int Y_OFF = R_OFF + D * S * 2;
constexpr int WS_BYTES = Y_OFF + D * S * 4;
constexpr unsigned RSRC3 = 0x00020000u;
constexpr int SC1 = 16;
constexpr unsigned SPIN_CAP = 1u << 18;

DI float bflo(unsigned u) { return __uint_as_float(u << 16); }
DI float bfhi(unsigned u) { return __uint_as_float(u & 0xffff0000u); }
DI unsigned pack2(float a, float b) {
  bf16x2 r = __builtin_convertvector(f32x2{a, b}, bf16x2);
  return __builtin_bit_cast(unsigned, r);
}
DI float rbf(float a) { return bflo(pack2(a, a)); }
DI float silu_f(float x) { return x * __builtin_amdgcn_rcpf(1.f + __builtin_amdgcn_exp2f(-1.44269504088896f * x)); }
DI f32x4 mfma16(v4u a, v4u b, f32x4 c) {
  return __builtin_amdgcn_mfma_f32_16x16x32_bf16(__builtin_bit_cast(bf16x8, a), __builtin_bit_cast(bf16x8, b), c, 0, 0, 0);
}
DI f32x16 mfma32(v4u a, v4u b, f32x16 c) {
  return __builtin_amdgcn_mfma_f32_32x32x16_bf16(__builtin_bit_cast(bf16x8, a), __builtin_bit_cast(bf16x8, b), c, 0, 0, 0);
}
DI float dot2(unsigned a, unsigned b, float c) {
  return __builtin_amdgcn_fdot2_f32_bf16(__builtin_bit_cast(bf16x2, a), __builtin_bit_cast(bf16x2, b), c, false);
}
DI v2u trread(LAS char* p) { return __builtin_bit_cast(v2u, __builtin_amdgcn_ds_read_tr16_b64_v4i16((LAS s16x4*)p)); }
DI v4u ldsr128(LAS char* p) { return *(LAS v4u*)p; }
DI __amdgpu_buffer_rsrc_t mkrsrc(const void* p, int bytes) { return __builtin_amdgcn_make_buffer_rsrc((void*)p, 0, bytes, RSRC3); }
DI void glds_asm(__amdgpu_buffer_rsrc_t rs, unsigned ldsa, int voff, int soff) {
  asm volatile("s_nop 4\n\ts_mov_b32 m0, %0\n\ts_nop 0\n\tbuffer_load_dwordx4 %1, %2, %3 offen lds"
               :: "s"(ldsa), "v"(voff), "s"(rs), "s"(soff) : "m0", "memory");
}
DI void glds_fast(__amdgpu_buffer_rsrc_t rs, unsigned ldsa, int voff, int soff) {
  asm volatile("s_mov_b32 m0, %0\n\ts_nop 0\n\tbuffer_load_dwordx4 %1, %2, %3 offen lds"
               :: "s"(ldsa), "v"(voff), "s"(rs), "s"(soff) : "m0", "memory");
}
#define GLDS(rs, ldsp, voff, soff) glds_asm(rs, (unsigned)(uintptr_t)(ldsp), voff, soff)
#define WAITV(n) asm volatile("s_waitcnt vmcnt(" #n ")" ::: "memory")
#define WAITL() asm volatile("s_waitcnt lgkmcnt(0)" ::: "memory")
#define BAR() do { WAITL(); __builtin_amdgcn_s_barrier(); asm volatile("" ::: "memory"); } while (0)
#define SB() __builtin_amdgcn_sched_barrier(0)

#define STAMP_DECL
#define STAMP(i)
#define STAMP_OUT(kid)

#define ROT_EXPR (5 * ((pi + jj) & 1))
#define WA_N 3
#define POLL_SLEEP 8
#define CODE_PF 1
#define CODE_BYTES 41000u
#define EXACT_PREP 1
#define PREP_PRIO 3
#define CONV_PRIO 1
#define WB_N 6
#define ROT2_EXPR 0
#define DELAY_POLL 32
#define ABL 0
#define FH_AUX 1
#define Y_DELAY 2
#define C8_PRIO 0
#define G2_PRIO 1
#define POOL_N 41
#define S1_EARLY 1
#define STAGE_START 44
#define LOOK_N 5
#define FH_WAIT 4
#define H_DELAY 2
#define C3_PRIO 2
#define FAST_EPI 1
#define AUX_PRIO 0
#define MMA_PRIO 2
#define CONV_START 44
constexpr int NBUF = 3;
constexpr int CH_HA = 0, CH_HB = 4224, CH_R = 8448, CH_RS = 4512, CH_BYTES = CH_R + 4 * CH_RS;
constexpr int CV = 98304;
constexpr int CV3 = 32768;
constexpr int SC3 = 65536;
constexpr int PK = CV, PV = PK + 11264, PW = PV + 10240, PS = PW + 10240, PO = PS + 1024;
constexpr int EY = CV, EX = 0, ES = EY + 80 * 144;
constexpr int XFS = CV + 2 * CH_BYTES;
constexpr int XFC = XFS + 1024;
constexpr int CNT = XFC + 512;
constexpr int C_AUX = CNT, C_G2 = CNT + 4, C_Y = CNT + 8, C_H = CNT + 12, C_MMA = CNT + 16, C_S = CNT + 20, C_P = CNT + 24,
              C_TAG = CNT + 60, C_ST = CNT + 40, C_HR = CNT + 32, C_YR = CNT + 36;
constexpr int LDS_BYTES = CNT + 64;
static_assert(PO + 10240 <= XFS && ES + 20480 <= XFS && LDS_BYTES <= 163840, "lds");
constexpr int PSTR = 336, PART = 64 * PSTR;

DI void lds_add(LAS char* c, unsigned v, int lane) {
  if (lane == 0) __hip_atomic_fetch_add((LAS unsigned*)c, v, RLX_WG);
}
DI void lds_wait(LAS char* c, unsigned target) {
  while ((unsigned)__builtin_amdgcn_readfirstlane(__hip_atomic_load((LAS unsigned*)c, RLX_WG)) < target) {}
  asm volatile("" ::: "memory");
}
DI void lds_wait_sleep(LAS char* c, unsigned target) {
  while ((unsigned)__builtin_amdgcn_readfirstlane(__hip_atomic_load((LAS unsigned*)c, RLX_WG)) < target) __builtin_amdgcn_s_sleep(1);
  asm volatile("" ::: "memory");
}
DI unsigned flag_load(char* ws, int off, int n, int lane) {
  return __hip_atomic_load((GAS unsigned*)(ws + off + (lane < n ? lane : 0) * FSTR), RLX_AGENT);
}
DI void poll_flags(char* ws, int off, int n, unsigned tag, int lane) {
#pragma unroll 1
  for (unsigned spins = 0; spins < SPIN_CAP; ++spins) {
    const unsigned v = flag_load(ws, off, n, lane);
    if (__all(v == tag)) break;
    __builtin_amdgcn_s_sleep(POLL_SLEEP);
  }
  __builtin_amdgcn_fence(__ATOMIC_ACQUIRE, "wavefront");
}
DI void set_flag(char* ws, int off, unsigned tag, int lane) {
  if (lane == 0) __hip_atomic_store((GAS unsigned*)(ws + off), tag, RLX_AGENT);
}

DI unsigned opaque0() { unsigned z; asm volatile("v_mov_b32 %0, 0" : "=v"(z)); return z; }
template <int I> struct IC { static constexpr int value = I; };
template <int B, int E, class Fn> DI void static_for(Fn&& f) { if constexpr (B < E) { f(IC<B>{}); static_for<B + 1, E>(f); } }
struct Frags { v4u x[4][2], w[4][2]; };

template <int TS> DI void conv_ld(LAS char* pa, LAS char* pb, v4u (&A)[8], v4u (&B)[8]) {
  constexpr int NA = TS == 0 ? 8 : 6;
#pragma unroll
  for (int a = 0; a < NA; ++a) { A[a] = ldsr128(pa + a * 512); B[a] = ldsr128(pb + (7 - a) * 512); }
}
template <int TS> DI void conv_mm(const v4u (&A)[8], const v4u (&B)[8], f32x4 (&acc)[4]) {
#pragma unroll
  for (int ti = 0; ti < 4; ++ti) {
    const int n = TS == 0 ? (ti == 0 ? 0 : ti == 1 ? 1 : ti == 2 ? 6 : 7) : 2 + ti;
#pragma unroll
    for (int a = 0; a <= n; ++a) acc[ti] = mfma16(A[a], B[n - a], acc[ti]);
  }
}
DI void conv_sched() {
#pragma unroll
  for (int i = 0; i < 16; ++i) { __builtin_amdgcn_sched_group_barrier(0x8, 1, 0); __builtin_amdgcn_sched_group_barrier(0x100, 1, 0); }
  __builtin_amdgcn_sched_group_barrier(0x8, 2, 0);
}
template <int TS> DI void conv8(LAS char* pa, LAS char* pb, f32x4 (&acc)[4]) {
  v4u A0[8], B0[8], A1[8], B1[8];
  conv_ld<TS>(pa, pb, A0, B0);
  SB();
#pragma unroll 1
  for (int r = 0; r < 8; r += 2) {
    conv_ld<TS>(pa + 64, pb + 64, A1, B1); conv_mm<TS>(A0, B0, acc); conv_sched(); SB();
    pa += 128; pb += 128;
    conv_ld<TS>(pa, pb, A0, B0); conv_mm<TS>(A1, B1, acc); conv_sched(); SB();
  }
}
template <int TS> DI void conv2(LAS char* pa, LAS char* pb, f32x4 (&acc)[4]) {
#pragma unroll 1
  for (int r = 0; r < 2; ++r) {
    v4u A[8], B[8];
    conv_ld<TS>(pa, pb, A, B); conv_mm<TS>(A, B, acc);
    pa += 64; pb += 64;
  }
}
DI unsigned kshift2(float k1, float k2, float k3, float k4, float k5, float c0, float c1, float c2, float c3) {
  unsigned a = pack2(k1 * c3, k2 * c3);
  unsigned t = pack2(k2 * c2, k3 * c2);
  a = pack2(bflo(a) + bflo(t), bfhi(a) + bfhi(t));
  t = pack2(k3 * c1, k4 * c1);
  a = pack2(bflo(a) + bflo(t), bfhi(a) + bfhi(t));
  t = pack2(k4 * c0, k5 * c0);
  return pack2(bflo(a) + bflo(t), bfhi(a) + bfhi(t));
}
DI void stage_h(LAS char* Lc, v4u h, int g) {
  *(LAS v4u*)(Lc + CH_HB + 112 + g * 16) = h;
  LAS char* a = Lc + CH_HA + 114 + g * 16;
  *(LAS u16*)(a) = (u16)(h.x & 0xffff);
  *(LAS unsigned*)(a + 2) = __builtin_amdgcn_alignbit(h.y, h.x, 16);
  *(LAS v2u*)(a + 6) = v2u{__builtin_amdgcn_alignbit(h.z, h.y, 16), __builtin_amdgcn_alignbit(h.w, h.z, 16)};
  *(LAS u16*)(a + 14) = (u16)(h.w >> 16);
}
DI void stage_r(LAS char* Lc, v4u u, int g) {
  LAS char* rb = Lc + CH_R + g * 16;
  *(LAS v4u*)(rb) = u;
  { LAS char* q = rb + CH_RS - 4; *(LAS unsigned*)(q) = u.x; *(LAS v2u*)(q + 4) = v2u{u.y, u.z}; *(LAS unsigned*)(q + 12) = u.w; }
  { LAS char* q = rb + 2 * CH_RS - 8; *(LAS v2u*)(q) = v2u{u.x, u.y}; *(LAS v2u*)(q + 8) = v2u{u.z, u.w}; }
  { LAS char* q = rb + 3 * CH_RS - 12; *(LAS unsigned*)(q) = u.x; *(LAS v2u*)(q + 4) = v2u{u.y, u.z}; *(LAS unsigned*)(q + 12) = u.w; }
}
DI void conv_out(LAS char* Lc, f32x4 a, int n, int d, float dv, int e, int kq, __amdgpu_buffer_rsrc_t rws, unsigned tag) {
  const int t = 256 * n + 64 * (e >> 2) + 2 * (e & 3) + 16 * kq;
  const unsigned ua = *(LAS unsigned*)(Lc + CH_R + 2 * (2046 - t));
  const unsigned ub = *(LAS unsigned*)(Lc + CH_R + 2 * (2038 - t));
  const unsigned c01 = pack2(a[0], a[1]), c23 = pack2(a[2], a[3]);
  const unsigned d01 = pack2(bfhi(ua) * dv, bflo(ua) * dv), d23 = pack2(bfhi(ub) * dv, bflo(ub) * dv);
  v2u o;
  o.x = pack2(bflo(c01) + bflo(d01), bfhi(c01) + bfhi(d01));
  o.y = pack2(bflo(c23) + bflo(d23), bfhi(c23) + bfhi(d23));
  const int off = Y_OFF + d * 8192 + (256 * n + 64 * (e >> 2) + 16 * kq + 4 * (e & 3)) * 4;
  __builtin_amdgcn_raw_buffer_store_b128(v4u{o.x, tag, o.y, tag}, rws, off, 0, SC1);
}

__global__ __launch_bounds__(512, 2) void fused(const u16* __restrict__ x, const u16* __restrict__ kk, const u16* __restrict__ vv,
                                                const u16* __restrict__ sk, const u16* __restrict__ hk, const u16* __restrict__ Dv,
                                                const u16* __restrict__ w1, const u16* __restrict__ w2,
                                                char* ws, u16* out) {
  __shared__ __attribute__((aligned(16))) char smem[LDS_BYTES];
  LAS char* L = (LAS char*)smem;
  if (__builtin_amdgcn_readfirstlane(threadIdx.x >> 6) == 7) {
    unsigned long long pc;
    asm volatile("s_getpc_b64 %0" : "=s"(pc));
    const unsigned co = min(((blockIdx.x >> 3) & 7) * 8192u + (threadIdx.x & 63) * 128u, CODE_BYTES);
    asm volatile("s_mov_b32 m0, %0\n\ts_nop 0\n\tglobal_load_lds_dword %1, %2" :: "s"((unsigned)(uintptr_t)(L + XFS)), "v"(co), "s"(pc) : "m0");
  }
  const int b = blockIdx.x, xcd = b & 7, r = b >> 3;
  const int rg = xcd >> 1, hf = xcd & 1, pi = r >> 3, jj = r & 7;
  const int p = 4 * rg + pi, j = 7 * hf + jj;
  const bool g1 = jj < 7;
  const int mbl = 2 * pi + ((r >> 2) & 1), nbl = r & 3;
  const int mb = 8 * rg + mbl, nb = 4 * hf + nbl;
  const int lane = threadIdx.x & 63;
  const int w = __builtin_amdgcn_readfirstlane(threadIdx.x >> 6);
  const int hi = lane >> 5, aw = w & 3;
  STAMP_DECL; STAMP(0);
  const __amdgpu_buffer_rsrc_t rws = mkrsrc(ws, WS_BYTES);
  if (threadIdx.x < 15) *(LAS unsigned*)(L + CNT + threadIdx.x * 4) = 0u;
  const int dpair = 80 * nb + 2 * mb, d3 = 80 * nb + 64 + (mb >> 1), ts3 = mb & 1;
  const int e = lane & 15, kq = lane >> 4;
  const int aoff = ((e & 1) ? CH_HB : CH_HA) + (kq + (e >> 1)) * 16;
  const int boff = CH_R + (3 - (e & 3)) * CH_RS + (248 - 32 * 7 - 8 * (e >> 2) + kq) * 16;

  {
    const __amdgpu_buffer_rsrc_t rk = mkrsrc(kk, S * D * 2), rv_ = mkrsrc(vv, S * D * 2), rsk = mkrsrc(sk, D * 8);
    const int sb = 64 * mb * 1280 + 160 * nb;
    int vo[2];
#pragma unroll
    for (int i = 0; i < 2; ++i) { const int n = (8 * i + w) * 64 + lane; const int row = n / 10; vo[i] = row * 1280 + (n - row * 10) * 16; }
    GLDS(rk, L + PK + w * 1024, vo[0] + sb - 5120, 0);
    GLDS(rv_, L + PV + w * 1024, vo[0], sb);
    {
      const int n = (5 + w) * 64 + lane; const int row = n / 10; const int vo3 = row * 1280 + (n - row * 10) * 16;
      if (w < 3) GLDS(rk, L + PK + (8 + w) * 1024, vo[1] + sb - 5120, 0);
      else if (w < 5) GLDS(rv_, L + PV + (5 + w) * 1024, vo3, sb);
      else if (w == 5) GLDS(rsk, L + PS, lane * 16, nb * 640);
    }
  }
#define PREP_ITEM(tg, cg) do { \
        const int q_ = (lane & 15) >> 2, pp_ = lane & 3, tl = 16 * (tg) + 4 * kq; \
        LAS char* ka_ = L + PK + (tl + q_) * 160 + (cg) * 32 + pp_ * 8; \
        v2u k0 = trread(ka_), k1 = trread(ka_ + 4 * 160); \
        v2u v0 = trread(ka_ + (PV - PK)); \
        v2u sw = *(LAS v2u*)(L + PS + ((cg) * 16 + e) * 8); \
        float kf[8] = {bflo(k0.x), bfhi(k0.x), bflo(k0.y), bfhi(k0.y), bflo(k1.x), bfhi(k1.x), bflo(k1.y), bfhi(k1.y)}; \
        float vf[4] = {bflo(v0.x), bfhi(v0.x), bflo(v0.y), bfhi(v0.y)}; \
        const float c0 = bflo(sw.x), c1 = bfhi(sw.x), c2 = bflo(sw.y), c3 = bfhi(sw.y); \
        float u[4]; \
        _Pragma("unroll") for (int e2 = 0; e2 < 4; e2 += 2) { \
          const unsigned a = kshift2(kf[e2 + 1], kf[e2 + 2], kf[e2 + 3], kf[e2 + 4], kf[e2 + 5], c0, c1, c2, c3); \
          u[e2] = bflo(a) * vf[e2]; u[e2 + 1] = bfhi(a) * vf[e2 + 1]; } \
        v2u o = v2u{pack2(u[3], u[2]), pack2(u[1], u[0])}; \
        const int ch = (cg) * 16 + e; \
        const int pos8 = (60 - tl) >> 2; \
        *(LAS v2u*)(L + PO + ch * 128 + ((((pos8 >> 1) ^ (ch & 7)) << 4) | ((pos8 & 1) << 3))) = o; \
      } while (0)

  if (w >= 4) {
    unsigned gen;
    asm volatile("s_load_dword %0, %1, 0x0\n\ts_waitcnt lgkmcnt(0)" : "=s"(gen) : "s"(ws) : "memory");
    const unsigned tag = gen * 0x9E3779B1u + 0x85EBCA6Bu;
    STAMP(1);
    if (aw == 0 && lane == 0) *(LAS unsigned*)(L + C_TAG) = tag;
    WAITV(0);
    BAR();
    unsigned ab = 4;
#define AUXBAR() do { WAITL(); lds_add(L + C_AUX, 1u, lane); ab += 4; lds_wait(L + C_AUX, ab); } while (0)
    STAMP(2);
    if (ABL & 4) lds_wait_sleep(L + C_MMA, 44);
    {
      const __amdgpu_buffer_rsrc_t rw2 = mkrsrc(w2, F * D * 2);
#pragma unroll
      for (int i = 0; i < 3; ++i) if (4 * i + aw < 10) {
        const int n = (4 * i + aw) * 64 + lane; const int row = n / 10;
        GLDS(rw2, L + PW + (4 * i + aw) * 1024, (row + (row >= 32 ? 992 : 0)) * 1280 + (n - row * 10) * 16, 32 * mb * 1280 + 160 * nb);
      }
    }
    {
      const int g = lane >> 4, q = (lane & 15) >> 2, pp = lane & 3;
      PREP_ITEM(aw, 0); PREP_ITEM(aw, 1); PREP_ITEM(aw, 2);
      AUXBAR();
#pragma unroll
      for (int i = 0; i < 3; ++i) {
        const int ri = aw + 4 * i;
        if (ri < 10) {
          const int ch = 8 * ri + (lane >> 3), cc = lane & 7;
          v4u val = ldsr128(L + PO + ch * 128 + ((cc ^ (ch & 7)) << 4));
          __builtin_amdgcn_raw_buffer_store_b128(val, rws, (ABL & 1) ? -16 : R_OFF + (80 * nb + ch) * 4096 + (31 - mb) * 128 + cc * 16, 0, SC1);
        }
      }
      __builtin_amdgcn_s_setprio(AUX_PRIO);
      if (aw < 2) WAITV(3); else WAITV(2);
      AUXBAR();
      STAMP(3);
#pragma unroll
      for (int i = 0; i < 3; ++i) {
        const int fi = aw + 4 * i;
        if (fi < 10) {
          const int half = fi / 5, nt = fi - half * 5, ks = mb + 32 * half;
          const int r0 = 16 * (g & 1) + 4 * (g >> 1);
          LAS char* a = L + PW + half * 5120 + (r0 + q) * 160 + nt * 32 + pp * 8;
          v2u lo = trread(a), hi2 = trread(a + 8 * 160);
          __builtin_amdgcn_raw_buffer_store_b128(v4u{lo.x, lo.y, hi2.x, hi2.y}, rws, (ks < KS2 && !(ABL & 1)) ? W2P_OFF + nb * W2P_NB + ks * 5120 + nt * 1024 + lane * 16 : -16, 0, SC1);
        }
      }
    }
    STAMP(4);
    WAITV(0);
    AUXBAR();
    if (aw == 0) set_flag(ws, FL_P + (nb * 32 + mb) * FSTR, tag, lane);
    STAMP(5);
    if (ABL & 2) lds_wait_sleep(L + C_MMA, 44);
    const __amdgpu_buffer_rsrc_t rh = mkrsrc(hk, D * S * 2);
    v4u cu[3];
    cu[0] = __builtin_amdgcn_raw_buffer_load_b128(rh, dpair * 4096 + aw * 1024 + lane * 16, 0, 0);
    cu[1] = __builtin_amdgcn_raw_buffer_load_b128(rh, dpair * 4096 + 4096 + aw * 1024 + lane * 16, 0, 0);
    cu[2] = __builtin_amdgcn_raw_buffer_load_b128(rh, d3 * 4096 + aw * 1024 + lane * 16, 0, 0);
    const int g = aw * 64 + lane;
    lds_wait_sleep(L + C_MMA, DELAY_POLL);
    if (aw == 1) {
      const int jc = g1 ? j : 0;
      v2u gr[4];
#define GRL(off) __builtin_bit_cast(v2u, __hip_atomic_load((GAS unsigned long long*)(ws + (off)), RLX_AGENT))
#pragma unroll 1
      for (unsigned sp = 0; sp < SPIN_CAP; ++sp) {
        gr[0] = GRL(ST_OFF + (p * 128 + lane) * 8);
        gr[1] = GRL(ST_OFF + (p * 128 + 64 + lane) * 8);
        gr[2] = GRL(CS_OFF + (jc * 128 + lane) * 8);
        gr[3] = GRL(CS_OFF + (jc * 128 + (j == 13 ? 0 : 64) + lane) * 8);
        if (__all(gr[0].y == tag && gr[1].y == tag && gr[2].y == tag && gr[3].y == tag)) break;
        __builtin_amdgcn_s_sleep(POLL_SLEEP);
      }
      *(LAS f32x2*)(L + XFS + lane * 8) = f32x2{bflo(gr[0].x), 1.f / bfhi(gr[0].x)};
      *(LAS f32x2*)(L + XFS + (64 + lane) * 8) = f32x2{bflo(gr[1].x), 1.f / bfhi(gr[1].x)};
      *(LAS unsigned*)(L + XFC + lane * 4) = gr[2].x;
      *(LAS unsigned*)(L + XFC + (64 + lane) * 4) = gr[3].x;
      WAITL();
      lds_add(L + C_S, 1u, lane);
    }
    if (aw == 2) { poll_flags(ws, FL_P + nb * 32 * FSTR, 32, tag, lane); lds_add(L + C_P, 1u, lane); }
    lds_wait_sleep(L + C_P, 1);
    STAMP(6);
    v4u rv[3];
    rv[0] = __builtin_amdgcn_raw_buffer_load_b128(rws, R_OFF + dpair * 4096 + aw * 1024 + lane * 16, 0, SC1);
    rv[1] = __builtin_amdgcn_raw_buffer_load_b128(rws, R_OFF + dpair * 4096 + 4096 + aw * 1024 + lane * 16, 0, SC1);
    rv[2] = __builtin_amdgcn_raw_buffer_load_b128(rws, R_OFF + d3 * 4096 + aw * 1024 + lane * 16, 0, SC1);
    const int rot2 = ROT2_EXPR;
    const __amdgpu_buffer_rsrc_t ra = mkrsrc(ws + HP_OFF + mb * HP_MB, HP_MB);
    const __amdgpu_buffer_rsrc_t rb = mkrsrc(ws + W2P_OFF + nb * W2P_NB, W2P_NB);
    const int va = lane * 16 + aw * 4096, vb = lane * 16 + aw * 5120;
    constexpr int WB = WB_N, NKS = 14, POOL = POOL_N;
    v4u fa[NKS][4], fb[NKS][5];
#define KIDX(i) ((i) + rot2 >= NKS ? (i) + rot2 - NKS : (i) + rot2)
#define LOADA(i) do { const int so_ = KIDX(i) * 16384; \
    _Pragma("unroll") for (int m = 0; m < 4; ++m) fa[i][m] = __builtin_amdgcn_raw_buffer_load_b128(ra, va + m * 1024, so_, SC1); } while (0)
#define LOADB(i) do { const int so_ = KIDX(i) * 20480; \
    _Pragma("unroll") for (int n = 0; n < 5; ++n) fb[i][n] = __builtin_amdgcn_raw_buffer_load_b128(rb, vb + (n & 3) * 1024, so_ + (n >> 2) * 4096, SC1); } while (0)
#pragma unroll
    for (int i = 0; i < WB; ++i) LOADB(i);
    SB();
    STAMP(7);
    lds_wait_sleep(L + C_MMA, STAGE_START);
    if (aw == 0 && lane < 32) *(LAS v4u*)(L + CV + (lane >> 4) * CH_BYTES + ((lane >> 3) & 1) * CH_HB + (lane & 7) * 16) = v4u{0, 0, 0, 0};
    if (aw == 3) {
#pragma unroll
      for (int i = 0; i < 4; ++i) {
        const int n = lane + 64 * i;
        if (n < 200) { const int cp = n / 25, sl = n - cp * 25; *(LAS v4u*)(L + CV + (cp >> 2) * CH_BYTES + CH_R + (cp & 3) * CH_RS + 4080 + sl * 16) = v4u{0, 0, 0, 0}; }
      }
    }
    stage_h(L + CV, cu[0], g);
    stage_h(L + CV + CH_BYTES, cu[1], g);
    stage_r(L + CV, rv[0], g);
    stage_r(L + CV + CH_BYTES, rv[1], g);
    if (aw == 0 && lane < 16) *(LAS v4u*)(L + CV3 + (lane >> 3) * CH_HB + (lane & 7) * 16) = v4u{0, 0, 0, 0};
    if (aw == 3) {
#pragma unroll
      for (int i = 0; i < 2; ++i) {
        const int n = lane + 64 * i;
        if (n < 100) { const int cp = n / 25, sl = n - cp * 25; *(LAS v4u*)(L + CV3 + CH_R + cp * CH_RS + 4080 + sl * 16) = v4u{0, 0, 0, 0}; }
      }
    }
    stage_h(L + CV3, cu[2], g);
    stage_r(L + CV3, rv[2], g);
    AUXBAR();
    if (aw == 0) lds_add(L + C_ST, 1u, lane);
    STAMP(8);
    lds_wait_sleep(L + C_H, FH_WAIT);
    for (int i = 0; i < H_DELAY; ++i) __builtin_amdgcn_s_sleep(4);
    STAMP(10);
    if (aw == 1) { poll_flags(ws, FL_H + p * 16 * FSTR, 14, tag, lane); lds_add(L + C_HR, 1u, lane); }
    __builtin_amdgcn_s_setprio(G2_PRIO);
    lds_wait_sleep(L + C_HR, 1);
    STAMP(11);
#define ISSUE(q) do { constexpr int q_ = (q); \
      if constexpr (q_ < 4 * WB) { fa[q_ / 4][q_ % 4] = __builtin_amdgcn_raw_buffer_load_b128(ra, va + (q_ % 4) * 1024, KIDX(q_ / 4) * 16384, SC1); } \
      else if constexpr (q_ < 4 * WB + 9 * (NKS - WB)) { constexpr int k_ = WB + (q_ - 4 * WB) / 9, f_ = (q_ - 4 * WB) % 9; \
        if constexpr (f_ < 4) fa[k_][f_] = __builtin_amdgcn_raw_buffer_load_b128(ra, va + f_ * 1024, KIDX(k_) * 16384, SC1); \
        else fb[k_][f_ - 4] = __builtin_amdgcn_raw_buffer_load_b128(rb, vb + ((f_ - 4) & 3) * 1024, KIDX(k_) * 20480 + ((f_ - 4) >> 2) * 4096, SC1); } \
    } while (0)
    constexpr auto need = [](int k) { k = k > NKS ? NKS : k; return k <= WB ? 4 * k : 4 * WB + 9 * (k - WB); };
    constexpr auto cum = [need](int i) { const int a = need(i + LOOK_N), c = POOL - 5 * WB + (i > 0 ? 9 * i - 6 : 0); return a < c ? a : c; };
    static_for<0, cum(0)>([&](auto q) { ISSUE(decltype(q)::value); });
    SB();
    f32x4 acc2[4][5] = {};
    static_for<0, NKS>([&](auto ii) {
      constexpr int i = decltype(ii)::value;
      constexpr int c0 = cum(i), cn = cum(i + 1) - cum(i);
      static_assert(cn <= 9 && c0 >= need(i + 1), "schedule");
      static_for<0, 20>([&](auto mm) {
        constexpr int mn = decltype(mm)::value, m = mn / 5, n = mn % 5;
        acc2[m][n] = mfma16(fb[i][n], fa[i][m], acc2[m][n]);
        constexpr int slot = (mn & 1) ? (mn >> 1) - (9 - cn) : -1;
        if constexpr (slot >= 0 && mn < 19) { SB(); ISSUE(c0 + slot); SB(); }
      });
      SB();
      if (i == 0) STAMP(12);
    });
    STAMP(13);
    lds_wait(L + C_Y, 4);
    const int ln = __builtin_amdgcn_mbcnt_hi(~0u, __builtin_amdgcn_mbcnt_lo(~0u, opaque0()));
#pragma unroll
    for (int m = 0; m < 4; ++m)
#pragma unroll
      for (int n = 0; n < 5; ++n)
        *(LAS f32x4*)(L + aw * PART + (16 * m + (ln & 15)) * PSTR + n * 64 + (ln >> 4) * 16) = acc2[m][n];
  } else {
    __builtin_amdgcn_s_setprio(MMA_PRIO);
    const __amdgpu_buffer_rsrc_t rw = mkrsrc(w1, D * F * 2);
    unsigned tag;
    if (g1) {
      const __amdgpu_buffer_rsrc_t rx = mkrsrc(x + (size_t)p * 128 * D, 128 * D * 2);
      int vx[2], vw[4];
      {
        const int row = 32 * w + (lane >> 3);
        vx[0] = row * 1280 + (((lane & 7) ^ ((row >> 1) & 7)) << 4);
        vx[1] = (row + 8) * 1280 + (((lane & 7) ^ (((row + 8) >> 1) & 7)) << 4);
#pragma unroll
        for (int ii = 0; ii < 4; ++ii) {
          const int kr = 16 * w + 4 * ii + (lane >> 4);
          vw[ii] = kr * (F * 2) + j * 256 + (((lane & 15) ^ (((kr & 3) << 2) | ((kr >> 2) & 3))) << 4);
        }
      }
      const int rot = ROT_EXPR;
      const unsigned lbase = (unsigned)(uintptr_t)L + w * 4096;
#define KC(s) ((s) + rot >= 10 ? (s) + rot - 10 : (s) + rot)
#define G1LD(s, i) do { \
    if ((i) < 4) glds_fast(rx, lbase + ((s) % NBUF) * 32768 + (i) * 1024, vx[(i) & 1], KC(s) * 128 + ((i) >> 1) * 16 * 1280); \
    else glds_fast(rw, lbase + ((s) % NBUF) * 32768 + 16384 + ((i) - 4) * 1024, vw[((i) - 4) & 3], KC(s) * (64 * F * 2)); \
  } while (0)
#pragma unroll
      for (int i = 0; i < 8; ++i) G1LD(0, i);
#pragma unroll
      for (int i = 0; i < 8; ++i) G1LD(1, i);
      WAITV(16);
      BAR();
      PREP_ITEM(aw, 3); PREP_ITEM(aw, 4);
      WAITL(); lds_add(L + C_AUX, 1u, lane);
      SB();
      STAMP(1);
      const int fh = w & 1, th = w >> 1;
      int xa[4], wa[2][2];
      {
        const int row = 64 * th + (lane & 31);
        const int c0 = hi ^ ((row >> 1) & 7);
        const int blk = (lane >> 4) & 1, q = (lane & 15) >> 2, pp = lane & 3;
#pragma unroll
        for (int s = 0; s < 4; ++s) xa[s] = row * 128 + (((2 * s) ^ c0) << 4);
#pragma unroll
        for (int ft = 0; ft < 2; ++ft)
#pragma unroll
          for (int t = 0; t < 2; ++t) {
            const int kr = 8 * hi + 4 * t + q;
            const int ch = 4 * (2 * fh + ft) + 2 * blk + (pp >> 1);
            wa[ft][t] = 16384 + 256 * kr + ((ch ^ ((q << 2) | ((2 * hi + t) & 3))) << 4) + 8 * (pp & 1);
          }
      }
      f32x16 acc[2][2] = {};
      Frags fA, fB;
#define RDX(f, bo, s, tt) f.x[s][tt] = ldsr128(L + (bo) + xa[s] + (tt) * 4096)
#define RDW(f, bo, s, ft) do { v2u wl_ = trread(L + (bo) + wa[ft][0] + (s) * 4096), wh_ = trread(L + (bo) + wa[ft][1] + (s) * 4096); f.w[s][ft] = v4u{wl_.x, wl_.y, wh_.x, wh_.y}; } while (0)
#define MM(f, s, ft, tt) acc[ft][tt] = mfma32(f.w[s][ft], f.x[s][tt], acc[ft][tt])
#define ITER(s, vm, cur, nxt) do { \
    asm volatile("s_waitcnt vmcnt(" #vm ") lgkmcnt(0)" ::: "memory"); \
    lds_add(L + C_MMA, 1u, lane); SB(); \
    MM(cur, 0, 0, 0); MM(cur, 0, 0, 1); MM(cur, 0, 1, 0); SB(); \
    lds_wait(L + C_MMA, 4 * ((s) + 2)); SB(); \
    constexpr int bo_ = (((s) + 1) % NBUF) * 32768; constexpr bool ld_ = (s) + NBUF < 10; \
    MM(cur, 0, 1, 1); RDX(nxt, bo_, 0, 0); RDX(nxt, bo_, 0, 1); SB(); if (ld_) G1LD((s) + NBUF, 0); SB(); \
    MM(cur, 1, 0, 0); RDW(nxt, bo_, 0, 0); SB(); \
    MM(cur, 1, 0, 1); RDW(nxt, bo_, 0, 1); SB(); if (ld_) G1LD((s) + NBUF, 4); SB(); \
    MM(cur, 1, 1, 0); RDX(nxt, bo_, 1, 0); RDX(nxt, bo_, 1, 1); SB(); if (ld_) G1LD((s) + NBUF, 1); SB(); \
    MM(cur, 1, 1, 1); RDW(nxt, bo_, 1, 0); SB(); \
    MM(cur, 2, 0, 0); RDW(nxt, bo_, 1, 1); SB(); if (ld_) G1LD((s) + NBUF, 5); SB(); \
    MM(cur, 2, 0, 1); RDX(nxt, bo_, 2, 0); RDX(nxt, bo_, 2, 1); SB(); if (ld_) G1LD((s) + NBUF, 2); SB(); \
    MM(cur, 2, 1, 0); RDW(nxt, bo_, 2, 0); SB(); \
    MM(cur, 2, 1, 1); RDW(nxt, bo_, 2, 1); SB(); if (ld_) G1LD((s) + NBUF, 6); SB(); \
    MM(cur, 3, 0, 0); RDX(nxt, bo_, 3, 0); RDX(nxt, bo_, 3, 1); SB(); if (ld_) G1LD((s) + NBUF, 3); SB(); \
    MM(cur, 3, 0, 1); RDW(nxt, bo_, 3, 0); RDW(nxt, bo_, 3, 1); SB(); \
    MM(cur, 3, 1, 0); SB(); if (ld_) G1LD((s) + NBUF, 7); SB(); \
    MM(cur, 3, 1, 1); SB(); \
  } while (0)
      WAITV(8);
      lds_add(L + C_MMA, 1u, lane); lds_wait(L + C_MMA, 4);
      STAMP(2);
#pragma unroll
      for (int s = 0; s < 4; ++s) { RDX(fA, 0, s, 0); RDX(fA, 0, s, 1); RDW(fA, 0, s, 0); RDW(fA, 0, s, 1); }
      SB();
#pragma unroll
      for (int i = 0; i < 8; ++i) G1LD(2, i);
      SB();
      ITER(0, 8, fA, fB);
      ITER(1, 8, fB, fA);
      ITER(2, 8, fA, fB);
      STAMP(3);
      ITER(3, 8, fB, fA);
      ITER(4, 8, fA, fB);
      ITER(5, 8, fB, fA);
      STAMP(4);
      ITER(6, 8, fA, fB);
      ITER(7, 8, fB, fA);
      ITER(8, 0, fA, fB);
#pragma unroll
      for (int s = 0; s < 4; ++s) { MM(fB, s, 0, 0); MM(fB, s, 0, 1); MM(fB, s, 1, 0); MM(fB, s, 1, 1); }
      SB();
      lds_add(L + C_MMA, 1u, lane);
      STAMP(5);
      lds_wait_sleep(L + C_S, 1);
      tag = __builtin_amdgcn_readfirstlane(*(LAS unsigned*)(L + C_TAG));
      STAMP(6);
#pragma unroll
      for (int ft = 0; ft < 2; ++ft) {
        const int fg = 2 * fh + ft;
        const int ks = 4 * j + fg;
        f32x4 c4[4];
#pragma unroll
        for (int g = 0; g < 4; ++g) c4[g] = *(LAS f32x4*)(L + XFC + (32 * fg + 8 * g + 4 * hi) * 4);
#pragma unroll
        for (int tt = 0; tt < 2; ++tt) {
          const int tok = 64 * th + 32 * tt + (lane & 31);
          const f32x2 st = *(LAS f32x2*)(L + XFS + tok * 8);
          const f32x16& a = acc[ft][tt];
          const float zs1 = -1.44269504088896f * st.y, dn1 = __builtin_amdgcn_rcpf(st.y);
          const f32x2 zs = f32x2{zs1, zs1}, dn = f32x2{dn1, dn1};
          const int mb2 = 2 * p + th, mt = 2 * tt + ((lane >> 4) & 1);
          const int off = ks < KS2 ? HP_OFF + mb2 * HP_MB + ks * 4096 + mt * 1024 + ((2 * hi) * 16 + (lane & 15)) * 16 : -1024;
#pragma unroll
          for (int hh = 0; hh < 2; ++hh) {
            unsigned pk[4];
#pragma unroll
            for (int e2 = 0; e2 < 4; ++e2) {
              const int ee = hh * 4 + e2;
              const f32x2 cc = f32x2{c4[ee >> 1][(2 * ee) & 3], c4[ee >> 1][(2 * ee + 1) & 3]};
              const f32x2 dd = __builtin_elementwise_fma(cc, f32x2{-st.x, -st.x}, f32x2{a[2 * ee], a[2 * ee + 1]});
              const f32x2 zz = dd * zs;
              const f32x2 ex = f32x2{__builtin_amdgcn_exp2f(zz.x), __builtin_amdgcn_exp2f(zz.y)};
              const f32x2 ss = __builtin_elementwise_fma(ex, dn, dn);
              const f32x2 hv2 = dd * f32x2{__builtin_amdgcn_rcpf(ss.x), __builtin_amdgcn_rcpf(ss.y)};
              pk[e2] = pack2(hv2.x, hv2.y);
            }
            __builtin_amdgcn_raw_buffer_store_b128(v4u{pk[0], pk[1], pk[2], pk[3]}, rws, off + hh * 256, 0, SC1);
            SB();
          }
        }
      }
      lds_add(L + C_MMA, 1u, lane);
      STAMP(7);
      WAITV(0);
      {
        unsigned old = 0;
        if (lane == 0) old = __hip_atomic_fetch_add((LAS unsigned*)(L + C_H), 1u, RLX_WG);
        if (__builtin_amdgcn_readfirstlane(old) == 3) set_flag(ws, FL_H + (p * 16 + j) * FSTR, tag, lane);
      }
      STAMP(8);
    } else {
      const int idx16 = 4 * rg + pi;
      const int srow = 512 * rg + 64 * (4 * hf + pi) + 16 * w + (lane >> 3);
      const int cg = 14 * hf + idx16;
      const bool csv = idx16 < 14 - hf;
      const __amdgpu_buffer_rsrc_t rx = mkrsrc(x, S * D * 2);
      v4u xv[2][10], wv[20];
#pragma unroll
      for (int h2 = 0; h2 < 2; ++h2)
#pragma unroll
        for (int i = 0; i < 10; ++i) xv[h2][i] = __builtin_amdgcn_raw_buffer_load_b128(rx, (srow + 8 * h2) * 1280 + (lane & 7) * 16 + i * 128, 0, 0);
      SB();
      WAITV(20);
      BAR();
      PREP_ITEM(aw, 3); PREP_ITEM(aw, 4);
      WAITL(); lds_add(L + C_AUX, 1u, lane);
      SB();
      const int wvo = csv ? (160 * w + (lane >> 3)) * (F * 2) + cg * 128 + (lane & 7) * 16 : -16;
#pragma unroll
      for (int i = 0; i < 20; ++i) wv[i] = __builtin_amdgcn_raw_buffer_load_b128(rw, wvo, csv ? i * 8 * F * 2 : 0, 0);
      SB();
      tag = __builtin_amdgcn_readfirstlane(*(LAS unsigned*)(L + C_TAG));
#pragma unroll
      for (int h2 = 0; h2 < 2; ++h2) {
        float s = 0.f;
#pragma unroll
        for (int i = 0; i < 10; ++i)
#pragma unroll
          for (int e2 = 0; e2 < 4; ++e2) s = dot2(xv[h2][i][e2], 0x3f803f80u, s);
        s += __shfl_xor(s, 1); s += __shfl_xor(s, 2); s += __shfl_xor(s, 4);
        const float mean = s * (1.f / D);
        float q0 = 0.f, q1 = 0.f;
#pragma unroll
        for (int i = 0; i < 10; ++i)
#pragma unroll
          for (int e2 = 0; e2 < 4; ++e2) { const float a0 = bflo(xv[h2][i][e2]) - mean, a1 = bfhi(xv[h2][i][e2]) - mean; q0 = fmaf(a0, a0, q0); q1 = fmaf(a1, a1, q1); }
        float q = q0 + q1;
        q += __shfl_xor(q, 1); q += __shfl_xor(q, 2); q += __shfl_xor(q, 4);
        const float den = __builtin_sqrtf(rbf(rbf(q * (1.f / D)) + 1e-5f));
        __builtin_amdgcn_raw_buffer_store_b64(v2u{pack2(mean, den), tag}, rws, (lane & 7) == 0 ? ST_OFF + (srow + 8 * h2) * 8 : -16, 0, SC1);
      }
      float c[8] = {};
#pragma unroll
      for (int i = 0; i < 20; ++i)
#pragma unroll
        for (int e2 = 0; e2 < 4; ++e2) { c[2 * e2] += bflo(wv[i][e2]); c[2 * e2 + 1] += bfhi(wv[i][e2]); }
      *(LAS f32x4*)(L + ((w * 8 + (lane >> 3)) * 64 + (lane & 7) * 8) * 4) = f32x4{c[0], c[1], c[2], c[3]};
      *(LAS f32x4*)(L + ((w * 8 + (lane >> 3)) * 64 + (lane & 7) * 8 + 4) * 4) = f32x4{c[4], c[5], c[6], c[7]};
      WAITL();
      unsigned old = 0;
      if (lane == 0) old = __hip_atomic_fetch_add((LAS unsigned*)(L + C_H), 1u, RLX_WG);
      if (__builtin_amdgcn_readfirstlane(old) == 3) {
        asm volatile("" ::: "memory");
        float t4[4] = {};
#pragma unroll
        for (int i = 0; i < 32; ++i) t4[i & 3] += *(LAS float*)(L + (i * 64 + lane) * 4);
        __builtin_amdgcn_raw_buffer_store_b64(v2u{__float_as_uint((t4[0] + t4[1]) + (t4[2] + t4[3])), tag}, rws, csv ? CS_OFF + (cg * 64 + lane) * 8 : -16, 0, SC1);
      }
      lds_add(L + C_MMA, 12u, lane);
      STAMP(5);
      if (b == 56 && w == 0) {
#pragma unroll 1
        for (unsigned sp = 0; sp < SPIN_CAP; ++sp) {
          bool ok = true;
#pragma unroll
          for (int i = 0; i < 4; ++i) ok = ok && flag_load(ws, FL_P + i * 64 * FSTR, 64, lane) == tag;
          if (__all(ok)) break;
          __builtin_amdgcn_s_sleep(32);
        }
        set_flag(ws, FL_GEN, tag, lane);
      }
    }
    unsigned hb_ = 0;
#define HBAR() do { WAITL(); lds_add(L + C_G2, 1u, lane); hb_ += 4; lds_wait(L + C_G2, hb_); } while (0)
    {
      const __amdgpu_buffer_rsrc_t rxx = mkrsrc(x, S * D * 2);
      lds_wait_sleep(L + C_MMA, 48);
#pragma unroll
      for (int i = 0; i < 3; ++i) {
        const int s = aw + 4 * i;
        if (s < 10) {
          const int n = s * 64 + lane; const int row = n / 10, c = n - row * 10;
          GLDS(rxx, L + EX + s * 1024, (64 * mb + row) * 1280 + 160 * nb + c * 16, 0);
        }
      }
      const float dv = bflo(Dv[dpair + (aw >> 1)]), dv3 = bflo(Dv[d3]);
      lds_wait_sleep(L + C_ST, 1);
      __builtin_amdgcn_s_setprio(C8_PRIO);
      LAS char* Lc = L + CV + (aw >> 1) * CH_BYTES;
      const int cts = aw & 1;
      f32x4 acc[4] = {};
      if (cts == 0) conv8<0>(Lc + aoff, Lc + boff, acc); else conv8<1>(Lc + aoff, Lc + boff, acc);
      __builtin_amdgcn_s_setprio(MMA_PRIO);
      STAMP(9);
#pragma unroll
      for (int ti = 0; ti < 4; ++ti) conv_out(Lc, acc[ti], cts == 0 ? (ti < 2 ? ti : ti + 4) : 2 + ti, dpair + (aw >> 1), dv, e, kq, rws, tag);
      f32x4 ac3[4] = {};
      __builtin_amdgcn_s_setprio(C3_PRIO);
      if (ts3 == 0) conv2<0>(L + CV3 + aoff + aw * 128, L + CV3 + boff + aw * 128, ac3); else conv2<1>(L + CV3 + aoff + aw * 128, L + CV3 + boff + aw * 128, ac3);
      __builtin_amdgcn_s_setprio(MMA_PRIO);
      STAMP(13);
#pragma unroll
      for (int ti = 0; ti < 4; ++ti) *(LAS f32x4*)(L + SC3 + (aw * 4 + ti) * 1024 + lane * 16) = ac3[ti];
      WAITV(4);
      HBAR();
      f32x4 a = {};
#pragma unroll
      for (int ww = 0; ww < 4; ++ww) a += *(LAS f32x4*)(L + SC3 + (ww * 4 + aw) * 1024 + lane * 16);
      conv_out(L + CV3, a, ts3 == 0 ? (aw < 2 ? aw : aw + 4) : 2 + aw, d3, dv3, e, kq, rws, tag);
    }
    STAMP(10);
    {
      const int q = (lane & 15) >> 2, pp = lane & 3, pos = lane & 15;
      const int tok = 16 * aw + 2 * (pos >> 2) + (pos & 1) + 8 * ((pos >> 1) & 1);
      lds_wait_sleep(L + C_S, 1);
      const f32x2 st = *(LAS f32x2*)(L + XFS + ((mb & 1) * 64 + tok) * 8);
      v4u xn[5];
#pragma unroll
      for (int i = 0; i < 5; ++i) {
        const v2u xv2 = *(LAS v2u*)(L + EX + tok * 160 + (4 * i + kq) * 8);
        const unsigned d01 = pack2(bflo(xv2.x) - st.x, bfhi(xv2.x) - st.x), d23 = pack2(bflo(xv2.y) - st.x, bfhi(xv2.y) - st.x);
        xn[i] = v4u{pack2(bflo(d01) * st.y, bfhi(d01) * st.y), pack2(bflo(d23) * st.y, bfhi(d23) * st.y), xv2.x, xv2.y};
      }
      WAITL();
      lds_add(L + C_Y, 1u, lane);
      for (int i = 0; i < Y_DELAY; ++i) __builtin_amdgcn_s_sleep(4);
      STAMP(11);
      {
        v4u yv[5];
        const int yo = Y_OFF + (80 * nb + 4 * aw + kq) * 8192 + mb * 256 + e * 16;
#pragma unroll 1
        for (unsigned sp = 0; sp < SPIN_CAP; ++sp) {
          asm volatile("" ::: "memory");
          bool ok = true;
#pragma unroll
          for (int i = 0; i < 5; ++i) yv[i] = __builtin_amdgcn_raw_buffer_load_b128(rws, yo, i * 16 * 8192, SC1);
#pragma unroll
          for (int i = 0; i < 5; ++i) ok = ok && yv[i].y == tag && yv[i].w == tag;
          if (__all(ok)) break;
          __builtin_amdgcn_s_sleep(POLL_SLEEP);
        }
#pragma unroll
        for (int i = 0; i < 5; ++i) *(LAS v2u*)(L + EY + (16 * i + 4 * aw + kq) * 144 + e * 8) = v2u{yv[i].x, yv[i].z};
      }
      HBAR();
      STAMP(12);
#pragma unroll
      for (int i = 0; i < 5; ++i) {
        const int dq = 4 * i + kq;
        const v2u yv = trread(L + EY + (4 * dq + q) * 144 + aw * 32 + pp * 8);
        const unsigned s01 = pack2(bflo(xn[i].x) * bflo(yv.x), bfhi(xn[i].x) * bfhi(yv.x)), s23 = pack2(bflo(xn[i].y) * bflo(yv.y), bfhi(xn[i].y) * bfhi(yv.y));
        *(LAS v4u*)(L + ES + (tok * 20 + dq) * 16) = v4u{s01, s23, xn[i].z, xn[i].w};
      }
    }
  }
  STAMP(14);
  BAR();
  const int lane2 = __builtin_amdgcn_mbcnt_hi(~0u, __builtin_amdgcn_mbcnt_lo(~0u, opaque0()));
#pragma unroll
  for (int i = 0; i < 3; ++i) {
    const int it = w + 8 * i;
    if (it < 20) {
      const int n = it * 64 + lane2;
      const int tok = n / 20, dq = n - tok * 20;
      f32x4 o = *(LAS f32x4*)(L + tok * PSTR + dq * 16);
#pragma unroll
      for (int ww = 1; ww < 4; ++ww) o += *(LAS f32x4*)(L + ww * PART + tok * PSTR + dq * 16);
      const v4u sx = *(LAS v4u*)(L + ES + n * 16);
      const unsigned o01 = pack2(o[0], o[1]), o23 = pack2(o[2], o[3]);
      const unsigned g01 = pack2(silu_f(bflo(o01)), silu_f(bfhi(o01))), g23 = pack2(silu_f(bflo(o23)), silu_f(bfhi(o23)));
      const unsigned m01 = pack2(bflo(sx.x) * bflo(g01), bfhi(sx.x) * bfhi(g01)), m23 = pack2(bflo(sx.y) * bflo(g23), bfhi(sx.y) * bfhi(g23));
      v2u res;
      res.x = pack2(bflo(sx.z) + bflo(m01), bfhi(sx.z) + bfhi(m01));
      res.y = pack2(bflo(sx.w) + bflo(m23), bfhi(sx.w) + bfhi(m23));
      *(v2u*)(out + (size_t)(64 * mb + tok) * D + 80 * nb + dq * 4) = res;
    }
  }
  STAMP(15);
  STAMP_OUT(0);
}

extern "C" void kernel_launch(void* const* d_in, const int* in_sizes, int n_in,
                              void* d_out, int out_size, void* d_ws, size_t ws_size,
                              hipStream_t stream) {
  fused<<<256, 512, 0, stream>>>((const u16*)d_in[0], (const u16*)d_in[1], (const u16*)d_in[2], (const u16*)d_in[3],
                                 (const u16*)d_in[4], (const u16*)d_in[5], (const u16*)d_in[7], (const u16*)d_in[8],
                                 (char*)d_ws, (u16*)d_out);
}
```

```cpp
#include <hip/hip_runtime.h>
#include <stdint.h>

typedef unsigned short u16;
typedef unsigned v4u __attribute__((ext_vector_type(4)));
typedef unsigned v2u __attribute__((ext_vector_type(2)));
typedef float f32x2 __attribute__((ext_vector_type(2)));
typedef float f32x4 __attribute__((ext_vector_type(4)));
typedef float f32x16 __attribute__((ext_vector_type(16)));
typedef short bf16x8 __attribute__((ext_vector_type(8)));
typedef short s16x4 __attribute__((ext_vector_type(4)));
typedef __bf16 bf16x2 __attribute__((ext_vector_type(2)));
#define LAS __attribute__((address_space(3)))
#define GAS __attribute__((address_space(1)))
#define DI __device__ __forceinline__
#define RLX_AGENT __ATOMIC_RELAXED, __HIP_MEMORY_SCOPE_AGENT
#define RLX_WG __ATOMIC_RELAXED, __HIP_MEMORY_SCOPE_WORKGROUP

#define FSTR_N 128
constexpr int S = 2048, D = 640, F = 1728;
constexpr int KS2 = 54;
constexpr int FSTR = FSTR_N;
constexpr int FL_GEN = 0, FL_P = 131072, FL_H = FL_P + 256 * 256;
constexpr int ST_OFF = 32768;
constexpr int CS_OFF = ST_OFF + S * 8;
constexpr int HP_OFF = 262144;
constexpr int HP_MB = KS2 * 4096;
constexpr int W2P_OFF = HP_OFF + 32 * HP_MB;
constexpr int W2P_NB = KS2 * 5120;
constexpr int R_OFF = W2P_OFF + 8 * W2P_NB;
constexpr int Y_OFF = R_OFF + D * S * 2;
constexpr int WS_BYTES = Y_OFF + D * S * 4;
constexpr unsigned RSRC3 = 0x00020000u;
constexpr int SC1 = 16;
constexpr unsigned SPIN_CAP = 1u << 18;

DI float bflo(unsigned u) { return __uint_as_float(u << 16); }
DI float bfhi(unsigned u) { return __uint_as_float(u & 0xffff0000u); }
DI unsigned pack2(float a, float b) {
  bf16x2 r = __builtin_convertvector(f32x2{a, b}, bf16x2);
  return __builtin_bit_cast(unsigned, r);
}
DI float rbf(float a) { return bflo(pack2(a, a)); }
DI float silu_f(float x) { return x * __builtin_amdgcn_rcpf(1.f + __builtin_amdgcn_exp2f(-1.44269504088896f * x)); }
DI f32x4 mfma16(v4u a, v4u b, f32x4 c) {
  return __builtin_amdgcn_mfma_f32_16x16x32_bf16(__builtin_bit_cast(bf16x8, a), __builtin_bit_cast(bf16x8, b), c, 0, 0, 0);
}
DI f32x16 mfma32(v4u a, v4u b, f32x16 c) {
  return __builtin_amdgcn_mfma_f32_32x32x16_bf16(__builtin_bit_cast(bf16x8, a), __builtin_bit_cast(bf16x8, b), c, 0, 0, 0);
}
DI float dot2(unsigned a, unsigned b, float c) {
  return __builtin_amdgcn_fdot2_f32_bf16(__builtin_bit_cast(bf16x2, a), __builtin_bit_cast(bf16x2, b), c, false);
}
DI v2u trread(LAS char* p) { return __builtin_bit_cast(v2u, __builtin_amdgcn_ds_read_tr16_b64_v4i16((LAS s16x4*)p)); }
DI v4u ldsr128(LAS char* p) { return *(LAS v4u*)p; }
DI __amdgpu_buffer_rsrc_t mkrsrc(const void* p, int bytes) { return __builtin_amdgcn_make_buffer_rsrc((void*)p, 0, bytes, RSRC3); }
DI void glds_asm(__amdgpu_buffer_rsrc_t rs, unsigned ldsa, int voff, int soff) {
  asm volatile("s_nop 4\n\ts_mov_b32 m0, %0\n\ts_nop 0\n\tbuffer_load_dwordx4 %1, %2, %3 offen lds"
               :: "s"(ldsa), "v"(voff), "s"(rs), "s"(soff) : "m0", "memory");
}
DI void glds_fast(__amdgpu_buffer_rsrc_t rs, unsigned ldsa, int voff, int soff) {
  asm volatile("s_mov_b32 m0, %0\n\ts_nop 0\n\tbuffer_load_dwordx4 %1, %2, %3 offen lds"
               :: "s"(ldsa), "v"(voff), "s"(rs), "s"(soff) : "m0", "memory");
}
#define GLDS(rs, ldsp, voff, soff) glds_asm(rs, (unsigned)(uintptr_t)(ldsp), voff, soff)
#define WAITV(n) asm volatile("s_waitcnt vmcnt(" #n ")" ::: "memory")
#define WAITL() asm volatile("s_waitcnt lgkmcnt(0)" ::: "memory")
#define BAR() do { WAITL(); __builtin_amdgcn_s_barrier(); asm volatile("" ::: "memory"); } while (0)
#define SB() __builtin_amdgcn_sched_barrier(0)

#define STAMP_DECL
#define STAMP(i)
#define STAMP_OUT(kid)

#define ROT_EXPR ((((pi + jj) % 3) * 10 + 1) / 3)
#define WA_N 3
#define POLL_SLEEP 8
#define CODE_PF 1
#define CODE_BYTES 43000u
#define EXACT_PREP 1
#define PREP_PRIO 3
#define CONV_PRIO 1
#define WB_N 6
#define ROT2_EXPR 0
#define DELAY_POLL 32
#define ABL 0
#define FH_AUX 1
#define Y_DELAY 2
#define C8_PRIO 1
#define G2_PRIO 0
#define POOL_N 41
#define S1_EARLY 1
#define STAGE_START 44
#define LOOK_N 5
#define FH_WAIT 4
#define H_DELAY 0
#define C3_PRIO 2
#define FAST_EPI 1
#define AUX_PRIO 0
#define MMA_PRIO 2
#define CONV_START 44
constexpr int NBUF = 3;
constexpr int CH_HA = 0, CH_HB = 4224, CH_R = 8448, CH_RS = 4512, CH_BYTES = CH_R + 4 * CH_RS;
constexpr int CV = 98304;
constexpr int CV3 = 32768;
constexpr int SC3 = 65536;
constexpr int PK = CV, PV = PK + 11264, PW = PV + 10240, PS = PW + 10240, PO = PS + 1024;
constexpr int EY = CV, EX = 0, ES = EY + 80 * 144;
constexpr int XFS = CV + 2 * CH_BYTES;
constexpr int XFC = XFS + 1024;
constexpr int CNT = XFC + 512;
constexpr int C_AUX = CNT, C_G2 = CNT + 4, C_Y = CNT + 8, C_H = CNT + 12, C_MMA = CNT + 16, C_S = CNT + 20, C_P = CNT + 24,
              C_TAG = CNT + 60, C_ST = CNT + 40, C_HR = CNT + 32, C_YR = CNT + 36;
constexpr int LDS_BYTES = CNT + 64;
static_assert(PO + 10240 <= XFS && ES + 20480 <= XFS && LDS_BYTES <= 163840, "lds");
constexpr int PSTR = 336, PART = 64 * PSTR;

DI void lds_add(LAS char* c, unsigned v, int lane) {
  if (lane == 0) __hip_atomic_fetch_add((LAS unsigned*)c, v, RLX_WG);
}
DI void lds_wait(LAS char* c, unsigned target) {
  while ((unsigned)__builtin_amdgcn_readfirstlane(__hip_atomic_load((LAS unsigned*)c, RLX_WG)) < target) {}
  asm volatile("" ::: "memory");
}
DI void lds_wait_sleep(LAS char* c, unsigned target) {
  while ((unsigned)__builtin_amdgcn_readfirstlane(__hip_atomic_load((LAS unsigned*)c, RLX_WG)) < target) __builtin_amdgcn_s_sleep(1);
  asm volatile("" ::: "memory");
}
DI unsigned flag_load(char* ws, int off, int n, int lane) {
  return __hip_atomic_load((GAS unsigned*)(ws + off + (lane < n ? lane : 0) * FSTR), RLX_AGENT);
}
DI void poll_flags(char* ws, int off, int n, unsigned tag, int lane) {
#pragma unroll 1
  for (unsigned spins = 0; spins < SPIN_CAP; ++spins) {
    const unsigned v = flag_load(ws, off, n, lane);
    if (__all(v == tag)) break;
    __builtin_amdgcn_s_sleep(POLL_SLEEP);
  }
  __builtin_amdgcn_fence(__ATOMIC_ACQUIRE, "wavefront");
}
DI void set_flag(char* ws, int off, unsigned tag, int lane) {
  if (lane == 0) __hip_atomic_store((GAS unsigned*)(ws + off), tag, RLX_AGENT);
}

DI unsigned opaque0() { unsigned z; asm volatile("v_mov_b32 %0, 0" : "=v"(z)); return z; }
template <int I> struct IC { static constexpr int value = I; };
template <int B, int E, class Fn> DI void static_for(Fn&& f) { if constexpr (B < E) { f(IC<B>{}); static_for<B + 1, E>(f); } }
struct Frags { v4u x[4][2], w[4][2]; };

template <int TS> DI void conv_ld(LAS char* pa, LAS char* pb, v4u (&A)[8], v4u (&B)[8]) {
  constexpr int NA = TS == 0 ? 8 : 6;
#pragma unroll
  for (int a = 0; a < NA; ++a) { A[a] = ldsr128(pa + a * 512); B[a] = ldsr128(pb + (7 - a) * 512); }
}
template <int TS> DI void conv_mm(const v4u (&A)[8], const v4u (&B)[8], f32x4 (&acc)[4]) {
#pragma unroll
  for (int ti = 0; ti < 4; ++ti) {
    const int n = TS == 0 ? (ti == 0 ? 0 : ti == 1 ? 1 : ti == 2 ? 6 : 7) : 2 + ti;
#pragma unroll
    for (int a = 0; a <= n; ++a) acc[ti] = mfma16(A[a], B[n - a], acc[ti]);
  }
}
DI void conv_sched() {
#pragma unroll
  for (int i = 0; i < 16; ++i) { __builtin_amdgcn_sched_group_barrier(0x8, 1, 0); __builtin_amdgcn_sched_group_barrier(0x100, 1, 0); }
  __builtin_amdgcn_sched_group_barrier(0x8, 2, 0);
}
template <int TS> DI void conv8(LAS char* pa, LAS char* pb, f32x4 (&acc)[4]) {
  v4u A0[8], B0[8], A1[8], B1[8];
  conv_ld<TS>(pa, pb, A0, B0);
  SB();
#pragma unroll 1
  for (int r = 0; r < 8; r += 2) {
    conv_ld<TS>(pa + 64, pb + 64, A1, B1); conv_mm<TS>(A0, B0, acc); conv_sched(); SB();
    pa += 128; pb += 128;
    conv_ld<TS>(pa, pb, A0, B0); conv_mm<TS>(A1, B1, acc); conv_sched(); SB();
  }
}
template <int TS> DI void conv2(LAS char* pa, LAS char* pb, f32x4 (&acc)[4]) {
#pragma unroll 1
  for (int r = 0; r < 2; ++r) {
    v4u A[8], B[8];
    conv_ld<TS>(pa, pb, A, B); conv_mm<TS>(A, B, acc);
    pa += 64; pb += 64;
  }
}
DI unsigned kshift2(float k1, float k2, float k3, float k4, float k5, float c0, float c1, float c2, float c3) {
  unsigned a = pack2(k1 * c3, k2 * c3);
  unsigned t = pack2(k2 * c2, k3 * c2);
  a = pack2(bflo(a) + bflo(t), bfhi(a) + bfhi(t));
  t = pack2(k3 * c1, k4 * c1);
  a = pack2(bflo(a) + bflo(t), bfhi(a) + bfhi(t));
  t = pack2(k4 * c0, k5 * c0);
  return pack2(bflo(a) + bflo(t), bfhi(a) + bfhi(t));
}
DI void stage_h(LAS char* Lc, v4u h, int g) {
  *(LAS v4u*)(Lc + CH_HB + 112 + g * 16) = h;
  LAS char* a = Lc + CH_HA + 114 + g * 16;
  *(LAS u16*)(a) = (u16)(h.x & 0xffff);
  *(LAS unsigned*)(a + 2) = __builtin_amdgcn_alignbit(h.y, h.x, 16);
  *(LAS v2u*)(a + 6) = v2u{__builtin_amdgcn_alignbit(h.z, h.y, 16), __builtin_amdgcn_alignbit(h.w, h.z, 16)};
  *(LAS u16*)(a + 14) = (u16)(h.w >> 16);
}
DI void stage_r(LAS char* Lc, v4u u, int g) {
  LAS char* rb = Lc + CH_R + g * 16;
  *(LAS v4u*)(rb) = u;
  { LAS char* q = rb + CH_RS - 4; *(LAS unsigned*)(q) = u.x; *(LAS v2u*)(q + 4) = v2u{u.y, u.z}; *(LAS unsigned*)(q + 12) = u.w; }
  { LAS char* q = rb + 2 * CH_RS - 8; *(LAS v2u*)(q) = v2u{u.x, u.y}; *(LAS v2u*)(q + 8) = v2u{u.z, u.w}; }
  { LAS char* q = rb + 3 * CH_RS - 12; *(LAS unsigned*)(q) = u.x; *(LAS v2u*)(q + 4) = v2u{u.y, u.z}; *(LAS unsigned*)(q + 12) = u.w; }
}
DI void conv_out(LAS char* Lc, f32x4 a, int n, int d, float dv, int e, int kq, __amdgpu_buffer_rsrc_t rws, unsigned tag) {
  const int t = 256 * n + 64 * (e >> 2) + 2 * (e & 3) + 16 * kq;
  const unsigned ua = *(LAS unsigned*)(Lc + CH_R + 2 * (2046 - t));
  const unsigned ub = *(LAS unsigned*)(Lc + CH_R + 2 * (2038 - t));
  const unsigned c01 = pack2(a[0], a[1]), c23 = pack2(a[2], a[3]);
  const unsigned d01 = pack2(bfhi(ua) * dv, bflo(ua) * dv), d23 = pack2(bfhi(ub) * dv, bflo(ub) * dv);
  v2u o;
  o.x = pack2(bflo(c01) + bflo(d01), bfhi(c01) + bfhi(d01));
  o.y = pack2(bflo(c23) + bflo(d23), bfhi(c23) + bfhi(d23));
  const int off = Y_OFF + d * 8192 + (256 * n + 64 * (e >> 2) + 16 * kq + 4 * (e & 3)) * 4;
  __builtin_amdgcn_raw_buffer_store_b128(v4u{o.x, tag, o.y, tag}, rws, off, 0, SC1);
}

__global__ __launch_bounds__(512, 2) void fused(const u16* __restrict__ x, const u16* __restrict__ kk, const u16* __restrict__ vv,
                                                const u16* __restrict__ sk, const u16* __restrict__ hk, const u16* __restrict__ Dv,
                                                const u16* __restrict__ w1, const u16* __restrict__ w2,
                                                char* ws, u16* out) {
  __shared__ __attribute__((aligned(16))) char smem[LDS_BYTES];
  LAS char* L = (LAS char*)smem;
  if (__builtin_amdgcn_readfirstlane(threadIdx.x >> 6) == 7) {
    unsigned long long pc;
    asm volatile("s_getpc_b64 %0" : "=s"(pc));
    const unsigned co = min(((blockIdx.x >> 3) & 7) * 8192u + (threadIdx.x & 63) * 128u, CODE_BYTES);
    asm volatile("s_mov_b32 m0, %0\n\ts_nop 0\n\tglobal_load_lds_dword %1, %2" :: "s"((unsigned)(uintptr_t)(L + XFS)), "v"(co), "s"(pc) : "m0");
  }
  const int b = blockIdx.x, xcd = b & 7, r = b >> 3;
  const int rg = xcd >> 1, hf = xcd & 1, pi = r >> 3, jj = r & 7;
  const int p = 4 * rg + pi, j = 7 * hf + jj;
  const bool g1 = jj < 7;
  const int mbl = 2 * pi + ((r >> 2) & 1), nbl = r & 3;
  const int mb = 8 * rg + mbl, nb = 4 * hf + nbl;
  const int lane = threadIdx.x & 63;
  const int w = __builtin_amdgcn_readfirstlane(threadIdx.x >> 6);
  const int hi = lane >> 5, aw = w & 3;
  STAMP_DECL; STAMP(0);
  const __amdgpu_buffer_rsrc_t rws = mkrsrc(ws, WS_BYTES);
  if (threadIdx.x < 15) *(LAS unsigned*)(L + CNT + threadIdx.x * 4) = 0u;
  const int dpair = 80 * nb + 2 * mb, d3 = 80 * nb + 64 + (mb >> 1), ts3 = mb & 1;
  const int e = lane & 15, kq = lane >> 4;
  const int aoff = ((e & 1) ? CH_HB : CH_HA) + (kq + (e >> 1)) * 16;
  const int boff = CH_R + (3 - (e & 3)) * CH_RS + (248 - 32 * 7 - 8 * (e >> 2) + kq) * 16;

  {
    const __amdgpu_buffer_rsrc_t rk = mkrsrc(kk, S * D * 2), rv_ = mkrsrc(vv, S * D * 2), rsk = mkrsrc(sk, D * 8);
    const int sb = 64 * mb * 1280 + 160 * nb;
    int vo[2];
#pragma unroll
    for (int i = 0; i < 2; ++i) { const int n = (8 * i + w) * 64 + lane; const int row = n / 10; vo[i] = row * 1280 + (n - row * 10) * 16; }
    GLDS(rk, L + PK + w * 1024, vo[0] + sb - 5120, 0);
    GLDS(rv_, L + PV + w * 1024, vo[0], sb);
    {
      const int n = (5 + w) * 64 + lane; const int row = n / 10; const int vo3 = row * 1280 + (n - row * 10) * 16;
      if (w < 3) GLDS(rk, L + PK + (8 + w) * 1024, vo[1] + sb - 5120, 0);
      else if (w < 5) GLDS(rv_, L + PV + (5 + w) * 1024, vo3, sb);
      else if (w == 5) GLDS(rsk, L + PS, lane * 16, nb * 640);
    }
  }
#define PREP_ITEM(tg, cg) do { \
        const int q_ = (lane & 15) >> 2, pp_ = lane & 3, tl = 16 * (tg) + 4 * kq; \
        LAS char* ka_ = L + PK + (tl + q_) * 160 + (cg) * 32 + pp_ * 8; \
        v2u k0 = trread(ka_), k1 = trread(ka_ + 4 * 160); \
        v2u v0 = trread(ka_ + (PV - PK)); \
        v2u sw = *(LAS v2u*)(L + PS + ((cg) * 16 + e) * 8); \
        float kf[8] = {bflo(k0.x), bfhi(k0.x), bflo(k0.y), bfhi(k0.y), bflo(k1.x), bfhi(k1.x), bflo(k1.y), bfhi(k1.y)}; \
        float vf[4] = {bflo(v0.x), bfhi(v0.x), bflo(v0.y), bfhi(v0.y)}; \
        const float c0 = bflo(sw.x), c1 = bfhi(sw.x), c2 = bflo(sw.y), c3 = bfhi(sw.y); \
        float u[4]; \
        _Pragma("unroll") for (int e2 = 0; e2 < 4; e2 += 2) { \
          const unsigned a = kshift2(kf[e2 + 1], kf[e2 + 2], kf[e2 + 3], kf[e2 + 4], kf[e2 + 5], c0, c1, c2, c3); \
          u[e2] = bflo(a) * vf[e2]; u[e2 + 1] = bfhi(a) * vf[e2 + 1]; } \
        v2u o = v2u{pack2(u[3], u[2]), pack2(u[1], u[0])}; \
        const int ch = (cg) * 16 + e; \
        const int pos8 = (60 - tl) >> 2; \
        *(LAS v2u*)(L + PO + ch * 128 + ((((pos8 >> 1) ^ (ch & 7)) << 4) | ((pos8 & 1) << 3))) = o; \
      } while (0)

  if (w >= 4) {
    unsigned gen;
    asm volatile("s_load_dword %0, %1, 0x0\n\ts_waitcnt lgkmcnt(0)" : "=s"(gen) : "s"(ws) : "memory");
    const unsigned tag = gen * 0x9E3779B1u + 0x85EBCA6Bu;
    STAMP(1);
    if (aw == 0 && lane == 0) *(LAS unsigned*)(L + C_TAG) = tag;
    WAITV(0);
    BAR();
    unsigned ab = 4;
#define AUXBAR() do { WAITL(); lds_add(L + C_AUX, 1u, lane); ab += 4; lds_wait(L + C_AUX, ab); } while (0)
    STAMP(2);
    if (ABL & 4) lds_wait_sleep(L + C_MMA, 44);
    {
      const __amdgpu_buffer_rsrc_t rw2 = mkrsrc(w2, F * D * 2);
#pragma unroll
      for (int i = 0; i < 3; ++i) if (4 * i + aw < 10) {
        const int n = (4 * i + aw) * 64 + lane; const int row = n / 10;
        GLDS(rw2, L + PW + (4 * i + aw) * 1024, (row + (row >= 32 ? 992 : 0)) * 1280 + (n - row * 10) * 16, 32 * mb * 1280 + 160 * nb);
      }
    }
    {
      const int g = lane >> 4, q = (lane & 15) >> 2, pp = lane & 3;
      PREP_ITEM(aw, 0); PREP_ITEM(aw, 1); PREP_ITEM(aw, 2);
      AUXBAR();
#pragma unroll
      for (int i = 0; i < 3; ++i) {
        const int ri = aw + 4 * i;
        if (ri < 10) {
          const int ch = 8 * ri + (lane >> 3), cc = lane & 7;
          v4u val = ldsr128(L + PO + ch * 128 + ((cc ^ (ch & 7)) << 4));
          __builtin_amdgcn_raw_buffer_store_b128(val, rws, (ABL & 1) ? -16 : R_OFF + (80 * nb + ch) * 4096 + (31 - mb) * 128 + cc * 16, 0, SC1);
        }
      }
      __builtin_amdgcn_s_setprio(AUX_PRIO);
      if (aw < 2) WAITV(3); else WAITV(2);
      AUXBAR();
      STAMP(3);
#pragma unroll
      for (int i = 0; i < 3; ++i) {
        const int fi = aw + 4 * i;
        if (fi < 10) {
          const int half = fi / 5, nt = fi - half * 5, ks = mb + 32 * half;
          const int r0 = 16 * (g & 1) + 4 * (g >> 1);
          LAS char* a = L + PW + half * 5120 + (r0 + q) * 160 + nt * 32 + pp * 8;
          v2u lo = trread(a), hi2 = trread(a + 8 * 160);
          __builtin_amdgcn_raw_buffer_store_b128(v4u{lo.x, lo.y, hi2.x, hi2.y}, rws, (ks < KS2 && !(ABL & 1)) ? W2P_OFF + nb * W2P_NB + ks * 5120 + nt * 1024 + lane * 16 : -16, 0, SC1);
        }
      }
    }
    STAMP(4);
    WAITV(0);
    AUXBAR();
    if (aw == 0) set_flag(ws, FL_P + (nb * 32 + mb) * FSTR, tag, lane);
    STAMP(5);
    if (ABL & 2) lds_wait_sleep(L + C_MMA, 44);
    const __amdgpu_buffer_rsrc_t rh = mkrsrc(hk, D * S * 2);
    v4u cu[3];
    cu[0] = __builtin_amdgcn_raw_buffer_load_b128(rh, dpair * 4096 + aw * 1024 + lane * 16, 0, 0);
    cu[1] = __builtin_amdgcn_raw_buffer_load_b128(rh, dpair * 4096 + 4096 + aw * 1024 + lane * 16, 0, 0);
    cu[2] = __builtin_amdgcn_raw_buffer_load_b128(rh, d3 * 4096 + aw * 1024 + lane * 16, 0, 0);
    const int g = aw * 64 + lane;
    lds_wait_sleep(L + C_MMA, DELAY_POLL);
    if (aw == 1) {
      const int jc = g1 ? j : 0;
      v2u gr[4];
#define GRL(off) __builtin_bit_cast(v2u, __hip_atomic_load((GAS unsigned long long*)(ws + (off)), RLX_AGENT))
#pragma unroll 1
      for (unsigned sp = 0; sp < SPIN_CAP; ++sp) {
        gr[0] = GRL(ST_OFF + (p * 128 + lane) * 8);
        gr[1] = GRL(ST_OFF + (p * 128 + 64 + lane) * 8);
        gr[2] = GRL(CS_OFF + (jc * 128 + lane) * 8);
        gr[3] = GRL(CS_OFF + (jc * 128 + (j == 13 ? 0 : 64) + lane) * 8);
        if (__all(gr[0].y == tag && gr[1].y == tag && gr[2].y == tag && gr[3].y == tag)) break;
        __builtin_amdgcn_s_sleep(POLL_SLEEP);
      }
      *(LAS f32x2*)(L + XFS + lane * 8) = f32x2{bflo(gr[0].x), 1.f / bfhi(gr[0].x)};
      *(LAS f32x2*)(L + XFS + (64 + lane) * 8) = f32x2{bflo(gr[1].x), 1.f / bfhi(gr[1].x)};
      *(LAS unsigned*)(L + XFC + lane * 4) = gr[2].x;
      *(LAS unsigned*)(L + XFC + (64 + lane) * 4) = gr[3].x;
      WAITL();
      lds_add(L + C_S, 1u, lane);
    }
    if (aw == 2) { poll_flags(ws, FL_P + nb * 32 * FSTR, 32, tag, lane); lds_add(L + C_P, 1u, lane); }
    lds_wait_sleep(L + C_P, 1);
    STAMP(6);
    v4u rv[3];
    rv[0] = __builtin_amdgcn_raw_buffer_load_b128(rws, R_OFF + dpair * 4096 + aw * 1024 + lane * 16, 0, SC1);
    rv[1] = __builtin_amdgcn_raw_buffer_load_b128(rws, R_OFF + dpair * 4096 + 4096 + aw * 1024 + lane * 16, 0, SC1);
    rv[2] = __builtin_amdgcn_raw_buffer_load_b128(rws, R_OFF + d3 * 4096 + aw * 1024 + lane * 16, 0, SC1);
    const int rot2 = ROT2_EXPR;
    const __amdgpu_buffer_rsrc_t ra = mkrsrc(ws + HP_OFF + mb * HP_MB, HP_MB);
    const __amdgpu_buffer_rsrc_t rb = mkrsrc(ws + W2P_OFF + nb * W2P_NB, W2P_NB);
    const int va = lane * 16 + aw * 4096, vb = lane * 16 + aw * 5120;
    constexpr int WB = WB_N, NKS = 14, POOL = POOL_N;
    v4u fa[NKS][4], fb[NKS][5];
#define KIDX(i) ((i) + rot2 >= NKS ? (i) + rot2 - NKS : (i) + rot2)
#define LOADA(i) do { const int so_ = KIDX(i) * 16384; \
    _Pragma("unroll") for (int m = 0; m < 4; ++m) fa[i][m] = __builtin_amdgcn_raw_buffer_load_b128(ra, va + m * 1024, so_, SC1); } while (0)
#define LOADB(i) do { const int so_ = KIDX(i) * 20480; \
    _Pragma("unroll") for (int n = 0; n < 5; ++n) fb[i][n] = __builtin_amdgcn_raw_buffer_load_b128(rb, vb + (n & 3) * 1024, so_ + (n >> 2) * 4096, SC1); } while (0)
#pragma unroll
    for (int i = 0; i < WB; ++i) LOADB(i);
    SB();
    STAMP(7);
    lds_wait_sleep(L + C_MMA, STAGE_START);
    if (aw == 0 && lane < 32) *(LAS v4u*)(L + CV + (lane >> 4) * CH_BYTES + ((lane >> 3) & 1) * CH_HB + (lane & 7) * 16) = v4u{0, 0, 0, 0};
    if (aw == 3) {
#pragma unroll
      for (int i = 0; i < 4; ++i) {
        const int n = lane + 64 * i;
        if (n < 200) { const int cp = n / 25, sl = n - cp * 25; *(LAS v4u*)(L + CV + (cp >> 2) * CH_BYTES + CH_R + (cp & 3) * CH_RS + 4080 + sl * 16) = v4u{0, 0, 0, 0}; }
      }
    }
    stage_h(L + CV, cu[0], g);
    stage_h(L + CV + CH_BYTES, cu[1], g);
    stage_r(L + CV, rv[0], g);
    stage_r(L + CV + CH_BYTES, rv[1], g);
    if (aw == 0 && lane < 16) *(LAS v4u*)(L + CV3 + (lane >> 3) * CH_HB + (lane & 7) * 16) = v4u{0, 0, 0, 0};
    if (aw == 3) {
#pragma unroll
      for (int i = 0; i < 2; ++i) {
        const int n = lane + 64 * i;
        if (n < 100) { const int cp = n / 25, sl = n - cp * 25; *(LAS v4u*)(L + CV3 + CH_R + cp * CH_RS + 4080 + sl * 16) = v4u{0, 0, 0, 0}; }
      }
    }
    stage_h(L + CV3, cu[2], g);
    stage_r(L + CV3, rv[2], g);
    AUXBAR();
    if (aw == 0) lds_add(L + C_ST, 1u, lane);
    STAMP(8);
    lds_wait_sleep(L + C_H, FH_WAIT);
    for (int i = 0; i < H_DELAY; ++i) __builtin_amdgcn_s_sleep(4);
    STAMP(10);
    if (aw == 1) { poll_flags(ws, FL_H + p * 16 * FSTR, 14, tag, lane); lds_add(L + C_HR, 1u, lane); }
    __builtin_amdgcn_s_setprio(G2_PRIO);
    lds_wait_sleep(L + C_HR, 1);
    STAMP(11);
#define ISSUE(q) do { constexpr int q_ = (q); \
      if constexpr (q_ < 4 * WB) { fa[q_ / 4][q_ % 4] = __builtin_amdgcn_raw_buffer_load_b128(ra, va + (q_ % 4) * 1024, KIDX(q_ / 4) * 16384, SC1); } \
      else if constexpr (q_ < 4 * WB + 9 * (NKS - WB)) { constexpr int k_ = WB + (q_ - 4 * WB) / 9, f_ = (q_ - 4 * WB) % 9; \
        if constexpr (f_ < 4) fa[k_][f_] = __builtin_amdgcn_raw_buffer_load_b128(ra, va + f_ * 1024, KIDX(k_) * 16384, SC1); \
        else fb[k_][f_ - 4] = __builtin_amdgcn_raw_buffer_load_b128(rb, vb + ((f_ - 4) & 3) * 1024, KIDX(k_) * 20480 + ((f_ - 4) >> 2) * 4096, SC1); } \
    } while (0)
    constexpr auto need = [](int k) { k = k > NKS ? NKS : k; return k <= WB ? 4 * k : 4 * WB + 9 * (k - WB); };
    constexpr auto cum = [need](int i) { const int a = need(i + LOOK_N), c = POOL - 5 * WB + (i > 0 ? 9 * i - 6 : 0); return a < c ? a : c; };
    static_for<0, cum(0)>([&](auto q) { ISSUE(decltype(q)::value); });
    SB();
    f32x4 acc2[4][5] = {};
    static_for<0, NKS>([&](auto ii) {
      constexpr int i = decltype(ii)::value;
      constexpr int c0 = cum(i), cn = cum(i + 1) - cum(i);
      static_assert(cn <= 9 && c0 >= need(i + 1), "schedule");
      static_for<0, 20>([&](auto mm) {
        constexpr int mn = decltype(mm)::value, m = mn / 5, n = mn % 5;
        acc2[m][n] = mfma16(fb[i][n], fa[i][m], acc2[m][n]);
        constexpr int slot = (mn & 1) ? (mn >> 1) - (9 - cn) : -1;
        if constexpr (slot >= 0 && mn < 19) { SB(); ISSUE(c0 + slot); SB(); }
      });
      SB();
      if (i == 0) STAMP(12);
    });
    STAMP(13);
    lds_wait(L + C_Y, 4);
    const int ln = __builtin_amdgcn_mbcnt_hi(~0u, __builtin_amdgcn_mbcnt_lo(~0u, opaque0()));
#pragma unroll
    for (int m = 0; m < 4; ++m)
#pragma unroll
      for (int n = 0; n < 5; ++n)
        *(LAS f32x4*)(L + aw * PART + (16 * m + (ln & 15)) * PSTR + n * 64 + (ln >> 4) * 16) = acc2[m][n];
  } else {
    __builtin_amdgcn_s_setprio(MMA_PRIO);
    const __amdgpu_buffer_rsrc_t rw = mkrsrc(w1, D * F * 2);
    unsigned tag;
    if (g1) {
      const __amdgpu_buffer_rsrc_t rx = mkrsrc(x + (size_t)p * 128 * D, 128 * D * 2);
      int vx[2], vw[4];
      {
        const int row = 32 * w + (lane >> 3);
        vx[0] = row * 1280 + (((lane & 7) ^ ((row >> 1) & 7)) << 4);
        vx[1] = (row + 8) * 1280 + (((lane & 7) ^ (((row + 8) >> 1) & 7)) << 4);
#pragma unroll
        for (int ii = 0; ii < 4; ++ii) {
          const int kr = 16 * w + 4 * ii + (lane >> 4);
          vw[ii] = kr * (F * 2) + j * 256 + (((lane & 15) ^ (((kr & 3) << 2) | ((kr >> 2) & 3))) << 4);
        }
      }
      const int rot = ROT_EXPR;
      const unsigned lbase = (unsigned)(uintptr_t)L + w * 4096;
#define KC(s) ((s) + rot >= 10 ? (s) + rot - 10 : (s) + rot)
#define G1LD(s, i) do { \
    if ((i) < 4) glds_fast(rx, lbase + ((s) % NBUF) * 32768 + (i) * 1024, vx[(i) & 1], KC(s) * 128 + ((i) >> 1) * 16 * 1280); \
    else glds_fast(rw, lbase + ((s) % NBUF) * 32768 + 16384 + ((i) - 4) * 1024, vw[((i) - 4) & 3], KC(s) * (64 * F * 2)); \
  } while (0)
#pragma unroll
      for (int i = 0; i < 8; ++i) G1LD(0, i);
#pragma unroll
      for (int i = 0; i < 8; ++i) G1LD(1, i);
      WAITV(16);
      BAR();
      PREP_ITEM(aw, 3); PREP_ITEM(aw, 4);
      WAITL(); lds_add(L + C_AUX, 1u, lane);
      SB();
      STAMP(1);
      const int fh = w & 1, th = w >> 1;
      int xa[4], wa[2][2];
      {
        const int row = 64 * th + (lane & 31);
        const int c0 = hi ^ ((row >> 1) & 7);
        const int blk = (lane >> 4) & 1, q = (lane & 15) >> 2, pp = lane & 3;
#pragma unroll
        for (int s = 0; s < 4; ++s) xa[s] = row * 128 + (((2 * s) ^ c0) << 4);
#pragma unroll
        for (int ft = 0; ft < 2; ++ft)
#pragma unroll
          for (int t = 0; t < 2; ++t) {
            const int kr = 8 * hi + 4 * t + q;
            const int ch = 4 * (2 * fh + ft) + 2 * blk + (pp >> 1);
            wa[ft][t] = 16384 + 256 * kr + ((ch ^ ((q << 2) | ((2 * hi + t) & 3))) << 4) + 8 * (pp & 1);
          }
      }
      f32x16 acc[2][2] = {};
      Frags fA, fB;
#define RDX(f, bo, s, tt) f.x[s][tt] = ldsr128(L + (bo) + xa[s] + (tt) * 4096)
#define RDW(f, bo, s, ft) do { v2u wl_ = trread(L + (bo) + wa[ft][0] + (s) * 4096), wh_ = trread(L + (bo) + wa[ft][1] + (s) * 4096); f.w[s][ft] = v4u{wl_.x, wl_.y, wh_.x, wh_.y}; } while (0)
#define MM(f, s, ft, tt) acc[ft][tt] = mfma32(f.w[s][ft], f.x[s][tt], acc[ft][tt])
#define ITER(s, vm, cur, nxt) do { \
    asm volatile("s_waitcnt vmcnt(" #vm ") lgkmcnt(0)" ::: "memory"); \
    lds_add(L + C_MMA, 1u, lane); SB(); \
    MM(cur, 0, 0, 0); MM(cur, 0, 0, 1); MM(cur, 0, 1, 0); SB(); \
    lds_wait(L + C_MMA, 4 * ((s) + 2)); SB(); \
    constexpr int bo_ = (((s) + 1) % NBUF) * 32768; constexpr bool ld_ = (s) + NBUF < 10; \
    MM(cur, 0, 1, 1); RDX(nxt, bo_, 0, 0); RDX(nxt, bo_, 0, 1); SB(); if (ld_) G1LD((s) + NBUF, 0); SB(); \
    MM(cur, 1, 0, 0); RDW(nxt, bo_, 0, 0); SB(); \
    MM(cur, 1, 0, 1); RDW(nxt, bo_, 0, 1); SB(); if (ld_) G1LD((s) + NBUF, 4); SB(); \
    MM(cur, 1, 1, 0); RDX(nxt, bo_, 1, 0); RDX(nxt, bo_, 1, 1); SB(); if (ld_) G1LD((s) + NBUF, 1); SB(); \
    MM(cur, 1, 1, 1); RDW(nxt, bo_, 1, 0); SB(); \
    MM(cur, 2, 0, 0); RDW(nxt, bo_, 1, 1); SB(); if (ld_) G1LD((s) + NBUF, 5); SB(); \
    MM(cur, 2, 0, 1); RDX(nxt, bo_, 2, 0); RDX(nxt, bo_, 2, 1); SB(); if (ld_) G1LD((s) + NBUF, 2); SB(); \
    MM(cur, 2, 1, 0); RDW(nxt, bo_, 2, 0); SB(); \
    MM(cur, 2, 1, 1); RDW(nxt, bo_, 2, 1); SB(); if (ld_) G1LD((s) + NBUF, 6); SB(); \
    MM(cur, 3, 0, 0); RDX(nxt, bo_, 3, 0); RDX(nxt, bo_, 3, 1); SB(); if (ld_) G1LD((s) + NBUF, 3); SB(); \
    MM(cur, 3, 0, 1); RDW(nxt, bo_, 3, 0); RDW(nxt, bo_, 3, 1); SB(); \
    MM(cur, 3, 1, 0); SB(); if (ld_) G1LD((s) + NBUF, 7); SB(); \
    MM(cur, 3, 1, 1); SB(); \
  } while (0)
      WAITV(8);
      lds_add(L + C_MMA, 1u, lane); lds_wait(L + C_MMA, 4);
      STAMP(2);
#pragma unroll
      for (int s = 0; s < 4; ++s) { RDX(fA, 0, s, 0); RDX(fA, 0, s, 1); RDW(fA, 0, s, 0); RDW(fA, 0, s, 1); }
      SB();
#pragma unroll
      for (int i = 0; i < 8; ++i) G1LD(2, i);
      SB();
      ITER(0, 8, fA, fB);
      ITER(1, 8, fB, fA);
      ITER(2, 8, fA, fB);
      STAMP(3);
      ITER(3, 8, fB, fA);
      ITER(4, 8, fA, fB);
      ITER(5, 8, fB, fA);
      STAMP(4);
      ITER(6, 8, fA, fB);
      ITER(7, 8, fB, fA);
      ITER(8, 0, fA, fB);
#pragma unroll
      for (int s = 0; s < 4; ++s) { MM(fB, s, 0, 0); MM(fB, s, 0, 1); MM(fB, s, 1, 0); MM(fB, s, 1, 1); }
      SB();
      lds_add(L + C_MMA, 1u, lane);
      STAMP(5);
      lds_wait_sleep(L + C_S, 1);
      tag = __builtin_amdgcn_readfirstlane(*(LAS unsigned*)(L + C_TAG));
      STAMP(6);
#pragma unroll
      for (int ft = 0; ft < 2; ++ft) {
        const int fg = 2 * fh + ft;
        const int ks = 4 * j + fg;
        f32x4 c4[4];
#pragma unroll
        for (int g = 0; g < 4; ++g) c4[g] = *(LAS f32x4*)(L + XFC + (32 * fg + 8 * g + 4 * hi) * 4);
#pragma unroll
        for (int tt = 0; tt < 2; ++tt) {
          const int tok = 64 * th + 32 * tt + (lane & 31);
          const f32x2 st = *(LAS f32x2*)(L + XFS + tok * 8);
          const f32x16& a = acc[ft][tt];
          const float zs1 = -1.44269504088896f * st.y, dn1 = __builtin_amdgcn_rcpf(st.y);
          const f32x2 zs = f32x2{zs1, zs1}, dn = f32x2{dn1, dn1};
          const int mb2 = 2 * p + th, mt = 2 * tt + ((lane >> 4) & 1);
          const int off = ks < KS2 ? HP_OFF + mb2 * HP_MB + ks * 4096 + mt * 1024 + ((2 * hi) * 16 + (lane & 15)) * 16 : -1024;
#pragma unroll
          for (int hh = 0; hh < 2; ++hh) {
            unsigned pk[4];
#pragma unroll
            for (int e2 = 0; e2 < 4; ++e2) {
              const int ee = hh * 4 + e2;
              const f32x2 cc = f32x2{c4[ee >> 1][(2 * ee) & 3], c4[ee >> 1][(2 * ee + 1) & 3]};
              const f32x2 dd = __builtin_elementwise_fma(cc, f32x2{-st.x, -st.x}, f32x2{a[2 * ee], a[2 * ee + 1]});
              const f32x2 zz = dd * zs;
              const f32x2 ex = f32x2{__builtin_amdgcn_exp2f(zz.x), __builtin_amdgcn_exp2f(zz.y)};
              const f32x2 ss = __builtin_elementwise_fma(ex, dn, dn);
              const f32x2 hv2 = dd * f32x2{__builtin_amdgcn_rcpf(ss.x), __builtin_amdgcn_rcpf(ss.y)};
              pk[e2] = pack2(hv2.x, hv2.y);
            }
            __builtin_amdgcn_raw_buffer_store_b128(v4u{pk[0], pk[1], pk[2], pk[3]}, rws, off + hh * 256, 0, SC1);
            SB();
          }
        }
      }
      lds_add(L + C_MMA, 1u, lane);
      STAMP(7);
      WAITV(0);
      {
        unsigned old = 0;
        if (lane == 0) old = __hip_atomic_fetch_add((LAS unsigned*)(L + C_H), 1u, RLX_WG);
        if (__builtin_amdgcn_readfirstlane(old) == 3) set_flag(ws, FL_H + (p * 16 + j) * FSTR, tag, lane);
      }
      STAMP(8);
    } else {
      const int idx16 = 4 * rg + pi;
      const int srow = 512 * rg + 64 * (4 * hf + pi) + 16 * w + (lane >> 3);
      const int cg = 14 * hf + idx16;
      const bool csv = idx16 < 14 - hf;
      const __amdgpu_buffer_rsrc_t rx = mkrsrc(x, S * D * 2);
      v4u xv[2][10], wv[20];
#pragma unroll
      for (int h2 = 0; h2 < 2; ++h2)
#pragma unroll
        for (int i = 0; i < 10; ++i) xv[h2][i] = __builtin_amdgcn_raw_buffer_load_b128(rx, (srow + 8 * h2) * 1280 + (lane & 7) * 16 + i * 128, 0, 0);
      SB();
      WAITV(20);
      BAR();
      PREP_ITEM(aw, 3); PREP_ITEM(aw, 4);
      WAITL(); lds_add(L + C_AUX, 1u, lane);
      SB();
      const int wvo = csv ? (160 * w + (lane >> 3)) * (F * 2) + cg * 128 + (lane & 7) * 16 : -16;
#pragma unroll
      for (int i = 0; i < 20; ++i) wv[i] = __builtin_amdgcn_raw_buffer_load_b128(rw, wvo, csv ? i * 8 * F * 2 : 0, 0);
      SB();
      tag = __builtin_amdgcn_readfirstlane(*(LAS unsigned*)(L + C_TAG));
#pragma unroll
      for (int h2 = 0; h2 < 2; ++h2) {
        float s = 0.f;
#pragma unroll
        for (int i = 0; i < 10; ++i)
#pragma unroll
          for (int e2 = 0; e2 < 4; ++e2) s = dot2(xv[h2][i][e2], 0x3f803f80u, s);
        s += __shfl_xor(s, 1); s += __shfl_xor(s, 2); s += __shfl_xor(s, 4);
        const float mean = s * (1.f / D);
        float q0 = 0.f, q1 = 0.f;
#pragma unroll
        for (int i = 0; i < 10; ++i)
#pragma unroll
          for (int e2 = 0; e2 < 4; ++e2) { const float a0 = bflo(xv[h2][i][e2]) - mean, a1 = bfhi(xv[h2][i][e2]) - mean; q0 = fmaf(a0, a0, q0); q1 = fmaf(a1, a1, q1); }
        float q = q0 + q1;
        q += __shfl_xor(q, 1); q += __shfl_xor(q, 2); q += __shfl_xor(q, 4);
        const float den = __builtin_sqrtf(rbf(rbf(q * (1.f / D)) + 1e-5f));
        __builtin_amdgcn_raw_buffer_store_b64(v2u{pack2(mean, den), tag}, rws, (lane & 7) == 0 ? ST_OFF + (srow + 8 * h2) * 8 : -16, 0, SC1);
      }
      float c[8] = {};
#pragma unroll
      for (int i = 0; i < 20; ++i)
#pragma unroll
        for (int e2 = 0; e2 < 4; ++e2) { c[2 * e2] += bflo(wv[i][e2]); c[2 * e2 + 1] += bfhi(wv[i][e2]); }
      *(LAS f32x4*)(L + ((w * 8 + (lane >> 3)) * 64 + (lane & 7) * 8) * 4) = f32x4{c[0], c[1], c[2], c[3]};
      *(LAS f32x4*)(L + ((w * 8 + (lane >> 3)) * 64 + (lane & 7) * 8 + 4) * 4) = f32x4{c[4], c[5], c[6], c[7]};
      WAITL();
      unsigned old = 0;
      if (lane == 0) old = __hip_atomic_fetch_add((LAS unsigned*)(L + C_H), 1u, RLX_WG);
      if (__builtin_amdgcn_readfirstlane(old) == 3) {
        asm volatile("" ::: "memory");
        float t4[4] = {};
#pragma unroll
        for (int i = 0; i < 32; ++i) t4[i & 3] += *(LAS float*)(L + (i * 64 + lane) * 4);
        __builtin_amdgcn_raw_buffer_store_b64(v2u{__float_as_uint((t4[0] + t4[1]) + (t4[2] + t4[3])), tag}, rws, csv ? CS_OFF + (cg * 64 + lane) * 8 : -16, 0, SC1);
      }
      lds_add(L + C_MMA, 12u, lane);
      STAMP(5);
      if (b == 56 && w == 0) {
#pragma unroll 1
        for (unsigned sp = 0; sp < SPIN_CAP; ++sp) {
          bool ok = true;
#pragma unroll
          for (int i = 0; i < 4; ++i) ok = ok && flag_load(ws, FL_P + i * 64 * FSTR, 64, lane) == tag;
          if (__all(ok)) break;
          __builtin_amdgcn_s_sleep(32);
        }
        set_flag(ws, FL_GEN, tag, lane);
      }
    }
    unsigned hb_ = 0;
#define HBAR() do { WAITL(); lds_add(L + C_G2, 1u, lane); hb_ += 4; lds_wait(L + C_G2, hb_); } while (0)
    {
      const __amdgpu_buffer_rsrc_t rxx = mkrsrc(x, S * D * 2);
      lds_wait_sleep(L + C_MMA, 48);
#pragma unroll
      for (int i = 0; i < 3; ++i) {
        const int s = aw + 4 * i;
        if (s < 10) {
          const int n = s * 64 + lane; const int row = n / 10, c = n - row * 10;
          GLDS(rxx, L + EX + s * 1024, (64 * mb + row) * 1280 + 160 * nb + c * 16, 0);
        }
      }
      const float dv = bflo(Dv[dpair + (aw >> 1)]), dv3 = bflo(Dv[d3]);
      lds_wait_sleep(L + C_ST, 1);
      __builtin_amdgcn_s_setprio(C8_PRIO);
      LAS char* Lc = L + CV + (aw >> 1) * CH_BYTES;
      const int cts = aw & 1;
      f32x4 acc[4] = {};
      if (cts == 0) conv8<0>(Lc + aoff, Lc + boff, acc); else conv8<1>(Lc + aoff, Lc + boff, acc);
      __builtin_amdgcn_s_setprio(MMA_PRIO);
      STAMP(9);
#pragma unroll
      for (int ti = 0; ti < 4; ++ti) conv_out(Lc, acc[ti], cts == 0 ? (ti < 2 ? ti : ti + 4) : 2 + ti, dpair + (aw >> 1), dv, e, kq, rws, tag);
      f32x4 ac3[4] = {};
      __builtin_amdgcn_s_setprio(C3_PRIO);
      if (ts3 == 0) conv2<0>(L + CV3 + aoff + aw * 128, L + CV3 + boff + aw * 128, ac3); else conv2<1>(L + CV3 + aoff + aw * 128, L + CV3 + boff + aw * 128, ac3);
      __builtin_amdgcn_s_setprio(MMA_PRIO);
      STAMP(13);
#pragma unroll
      for (int ti = 0; ti < 4; ++ti) *(LAS f32x4*)(L + SC3 + (aw * 4 + ti) * 1024 + lane * 16) = ac3[ti];
      WAITV(4);
      HBAR();
      f32x4 a = {};
#pragma unroll
      for (int ww = 0; ww < 4; ++ww) a += *(LAS f32x4*)(L + SC3 + (ww * 4 + aw) * 1024 + lane * 16);
      conv_out(L + CV3, a, ts3 == 0 ? (aw < 2 ? aw : aw + 4) : 2 + aw, d3, dv3, e, kq, rws, tag);
    }
    STAMP(10);
    {
      const int q = (lane & 15) >> 2, pp = lane & 3, pos = lane & 15;
      const int tok = 16 * aw + 2 * (pos >> 2) + (pos & 1) + 8 * ((pos >> 1) & 1);
      lds_wait_sleep(L + C_S, 1);
      const f32x2 st = *(LAS f32x2*)(L + XFS + ((mb & 1) * 64 + tok) * 8);
      v4u xn[5];
#pragma unroll
      for (int i = 0; i < 5; ++i) {
        const v2u xv2 = *(LAS v2u*)(L + EX + tok * 160 + (4 * i + kq) * 8);
        const unsigned d01 = pack2(bflo(xv2.x) - st.x, bfhi(xv2.x) - st.x), d23 = pack2(bflo(xv2.y) - st.x, bfhi(xv2.y) - st.x);
        xn[i] = v4u{pack2(bflo(d01) * st.y, bfhi(d01) * st.y), pack2(bflo(d23) * st.y, bfhi(d23) * st.y), xv2.x, xv2.y};
      }
      WAITL();
      lds_add(L + C_Y, 1u, lane);
      for (int i = 0; i < Y_DELAY; ++i) __builtin_amdgcn_s_sleep(4);
      STAMP(11);
      {
        v4u yv[5];
        const int yo = Y_OFF + (80 * nb + 4 * aw + kq) * 8192 + mb * 256 + e * 16;
#pragma unroll 1
        for (unsigned sp = 0; sp < SPIN_CAP; ++sp) {
          asm volatile("" ::: "memory");
          bool ok = true;
#pragma unroll
          for (int i = 0; i < 5; ++i) yv[i] = __builtin_amdgcn_raw_buffer_load_b128(rws, yo, i * 16 * 8192, SC1);
#pragma unroll
          for (int i = 0; i < 5; ++i) ok = ok && yv[i].y == tag && yv[i].w == tag;
          if (__all(ok)) break;
          __builtin_amdgcn_s_sleep(POLL_SLEEP);
        }
#pragma unroll
        for (int i = 0; i < 5; ++i) *(LAS v2u*)(L + EY + (16 * i + 4 * aw + kq) * 144 + e * 8) = v2u{yv[i].x, yv[i].z};
      }
      HBAR();
      STAMP(12);
#pragma unroll
      for (int i = 0; i < 5; ++i) {
        const int dq = 4 * i + kq;
        const v2u yv = trread(L + EY + (4 * dq + q) * 144 + aw * 32 + pp * 8);
        const unsigned s01 = pack2(bflo(xn[i].x) * bflo(yv.x), bfhi(xn[i].x) * bfhi(yv.x)), s23 = pack2(bflo(xn[i].y) * bflo(yv.y), bfhi(xn[i].y) * bfhi(yv.y));
        *(LAS v4u*)(L + ES + (tok * 20 + dq) * 16) = v4u{s01, s23, xn[i].z, xn[i].w};
      }
    }
  }
  STAMP(14);
  BAR();
  const int lane2 = __builtin_amdgcn_mbcnt_hi(~0u, __builtin_amdgcn_mbcnt_lo(~0u, opaque0()));
#pragma unroll
  for (int i = 0; i < 3; ++i) {
    const int it = w + 8 * i;
    if (it < 20) {
      const int n = it * 64 + lane2;
      const int tok = n / 20, dq = n - tok * 20;
      f32x4 o = *(LAS f32x4*)(L + tok * PSTR + dq * 16);
#pragma unroll
      for (int ww = 1; ww < 4; ++ww) o += *(LAS f32x4*)(L + ww * PART + tok * PSTR + dq * 16);
      const v4u sx = *(LAS v4u*)(L + ES + n * 16);
      const unsigned o01 = pack2(o[0], o[1]), o23 = pack2(o[2], o[3]);
      const unsigned g01 = pack2(silu_f(bflo(o01)), silu_f(bfhi(o01))), g23 = pack2(silu_f(bflo(o23)), silu_f(bfhi(o23)));
      const unsigned m01 = pack2(bflo(sx.x) * bflo(g01), bfhi(sx.x) * bfhi(g01)), m23 = pack2(bflo(sx.y) * bflo(g23), bfhi(sx.y) * bfhi(g23));
      v2u res;
      res.x = pack2(bflo(sx.z) + bflo(m01), bfhi(sx.z) + bfhi(m01));
      res.y = pack2(bflo(sx.w) + bflo(m23), bfhi(sx.w) + bfhi(m23));
      *(v2u*)(out + (size_t)(64 * mb + tok) * D + 80 * nb + dq * 4) = res;
    }
  }
  STAMP(15);
  STAMP_OUT(0);
}

extern "C" void kernel_launch(void* const* d_in, const int* in_sizes, int n_in,
                              void* d_out, int out_size, void* d_ws, size_t ws_size,
                              hipStream_t stream) {
  fused<<<256, 512, 0, stream>>>((const u16*)d_in[0], (const u16*)d_in[1], (const u16*)d_in[2], (const u16*)d_in[3],
                                 (const u16*)d_in[4], (const u16*)d_in[5], (const u16*)d_in[7], (const u16*)d_in[8],
                                 (char*)d_ws, (u16*)d_out);
}
```

```cpp
#define EXF3 0
#include <hip/hip_runtime.h>
#include <stdint.h>

typedef unsigned short u16;
typedef unsigned v4u __attribute__((ext_vector_type(4)));
typedef unsigned v2u __attribute__((ext_vector_type(2)));
typedef float f32x2 __attribute__((ext_vector_type(2)));
typedef float f32x4 __attribute__((ext_vector_type(4)));
typedef float f32x16 __attribute__((ext_vector_type(16)));
typedef short bf16x8 __attribute__((ext_vector_type(8)));
typedef short s16x4 __attribute__((ext_vector_type(4)));
typedef __bf16 bf16x2 __attribute__((ext_vector_type(2)));
#define LAS __attribute__((address_space(3)))
#define GAS __attribute__((address_space(1)))
#define DI __device__ __forceinline__
#define RLX_AGENT __ATOMIC_RELAXED, __HIP_MEMORY_SCOPE_AGENT
#define RLX_WG __ATOMIC_RELAXED, __HIP_MEMORY_SCOPE_WORKGROUP

#define FSTR_N 128
constexpr int S = 2048, D = 640, F = 1728;
constexpr int KS2 = 54;
constexpr int FSTR = FSTR_N;
constexpr int FL_GEN = 0, FL_P = 131072, FL_H = FL_P + 256 * 256;
constexpr int ST_OFF = 32768;
constexpr int CS_OFF = ST_OFF + S * 8;
constexpr int HP_OFF = 262144;
constexpr int HP_MB = KS2 * 4096;
constexpr int W2P_OFF = HP_OFF + 32 * HP_MB;
constexpr int W2P_NB = KS2 * 5120;
constexpr int R_OFF = W2P_OFF + 8 * W2P_NB;
constexpr int Y_OFF = R_OFF + D * S * 2;
constexpr int WS_BYTES = Y_OFF + D * S * 4;
constexpr unsigned RSRC3 = 0x00020000u;
constexpr int SC1 = 16;
constexpr unsigned SPIN_CAP = 1u << 18;

DI float bflo(unsigned u) { return __uint_as_float(u << 16); }
DI float bfhi(unsigned u) { return __uint_as_float(u & 0xffff0000u); }
DI unsigned pack2(float a, float b) {
  bf16x2 r = __builtin_convertvector(f32x2{a, b}, bf16x2);
  return __builtin_bit_cast(unsigned, r);
}
DI float rbf(float a) { return bflo(pack2(a, a)); }
DI float silu_f(float x) { return x * __builtin_amdgcn_rcpf(1.f + __builtin_amdgcn_exp2f(-1.44269504088896f * x)); }
DI f32x4 mfma16(v4u a, v4u b, f32x4 c) {
  return __builtin_amdgcn_mfma_f32_16x16x32_bf16(__builtin_bit_cast(bf16x8, a), __builtin_bit_cast(bf16x8, b), c, 0, 0, 0);
}
DI f32x16 mfma32(v4u a, v4u b, f32x16 c) {
  return __builtin_amdgcn_mfma_f32_32x32x16_bf16(__builtin_bit_cast(bf16x8, a), __builtin_bit_cast(bf16x8, b), c, 0, 0, 0);
}
DI float dot2(unsigned a, unsigned b, float c) {
  return __builtin_amdgcn_fdot2_f32_bf16(__builtin_bit_cast(bf16x2, a), __builtin_bit_cast(bf16x2, b), c, false);
}
DI v2u trread(LAS char* p) { return __builtin_bit_cast(v2u, __builtin_amdgcn_ds_read_tr16_b64_v4i16((LAS s16x4*)p)); }
DI v4u ldsr128(LAS char* p) { return *(LAS v4u*)p; }
DI __amdgpu_buffer_rsrc_t mkrsrc(const void* p, int bytes) { return __builtin_amdgcn_make_buffer_rsrc((void*)p, 0, bytes, RSRC3); }
DI void glds_asm(__amdgpu_buffer_rsrc_t rs, unsigned ldsa, int voff, int soff) {
  asm volatile("s_nop 4\n\ts_mov_b32 m0, %0\n\ts_nop 0\n\tbuffer_load_dwordx4 %1, %2, %3 offen lds"
               :: "s"(ldsa), "v"(voff), "s"(rs), "s"(soff) : "m0", "memory");
}
DI void glds_fast(__amdgpu_buffer_rsrc_t rs, unsigned ldsa, int voff, int soff) {
  asm volatile("s_mov_b32 m0, %0\n\ts_nop 0\n\tbuffer_load_dwordx4 %1, %2, %3 offen lds"
               :: "s"(ldsa), "v"(voff), "s"(rs), "s"(soff) : "m0", "memory");
}
#define GLDS(rs, ldsp, voff, soff) glds_asm(rs, (unsigned)(uintptr_t)(ldsp), voff, soff)
#define WAITV(n) asm volatile("s_waitcnt vmcnt(" #n ")" ::: "memory")
#define WAITL() asm volatile("s_waitcnt lgkmcnt(0)" ::: "memory")
#define BAR() do { WAITL(); __builtin_amdgcn_s_barrier(); asm volatile("" ::: "memory"); } while (0)
#define SB() __builtin_amdgcn_sched_barrier(0)

#define STAMP_DECL unsigned long long xt0 = 0
#define STAMP(i) do { if ((i) == 0) { asm volatile("s_memrealtime %0\n\ts_waitcnt lgkmcnt(0)" : "=s"(xt0) :: "memory"); } if ((i) == 14 && threadIdx.x == 0) __hip_atomic_store((GAS unsigned long long*)(ws + (101 << 20) + blockIdx.x * 8), xt0, RLX_AGENT); } while (0)
#define STAMP_OUT(kid) do { if (blockIdx.x == 73 && w == 0) { unsigned long long te = __builtin_amdgcn_s_memrealtime(); for (int i_ = 0; i_ < 200; ++i_) __builtin_amdgcn_s_sleep(8); \
   unsigned long long mx = 0, mn = ~0ull; for (int i_ = 0; i_ < 4; ++i_) { unsigned long long v = __hip_atomic_load((GAS unsigned long long*)(ws + (101 << 20) + (lane * 4 + i_) * 8), RLX_AGENT); mx = v > mx ? v : mx; mn = v < mn ? v : mn; } \
   for (int o = 32; o > 0; o >>= 1) { unsigned long long v = __shfl_xor(mx, o); mx = v > mx ? v : mx; v = __shfl_xor(mn, o); mn = v < mn ? v : mn; } \
   unsigned long long dd = EXF3 == 0 ? 0 : EXF3 == 1 ? mx - mn : EXF3 == 2 ? te - mn : xt0 - mn; dd = dd > 100000 ? 100000 : dd; \
   const unsigned long long tgt = __builtin_amdgcn_s_memrealtime() + 10 * dd; while (__builtin_amdgcn_s_memrealtime() < tgt) __builtin_amdgcn_s_sleep(1); } } while (0)

#define ROT_EXPR (5 * ((pi + jj) & 1))
#define WA_N 3
#define POLL_SLEEP 8
#define CODE_PF 1
#define CODE_BYTES 43000u
#define EXACT_PREP 1
#define PREP_PRIO 3
#define CONV_PRIO 1
#define WB_N 6
#define ROT2_EXPR 0
#define DELAY_POLL 32
#define ABL 0
#define FH_AUX 1
#define Y_DELAY 2
#define C8_PRIO 1
#define G2_PRIO 0
#define POOL_N 41
#define S1_EARLY 1
#define STAGE_START 44
#define LOOK_N 5
#define FH_WAIT 4
#define H_DELAY 0
#define C3_PRIO 2
#define FAST_EPI 1
#define AUX_PRIO 0
#define MMA_PRIO 2
#define CONV_START 44
constexpr int NBUF = 3;
constexpr int CH_HA = 0, CH_HB = 4224, CH_R = 8448, CH_RS = 4512, CH_BYTES = CH_R + 4 * CH_RS;
constexpr int CV = 98304;
constexpr int CV3 = 32768;
constexpr int SC3 = 65536;
constexpr int PK = CV, PV = PK + 11264, PW = PV + 10240, PS = PW + 10240, PO = PS + 1024;
constexpr int EY = CV, EX = 0, ES = EY + 80 * 144;
constexpr int XFS = CV + 2 * CH_BYTES;
constexpr int XFC = XFS + 1024;
constexpr int CNT = XFC + 512;
constexpr int C_AUX = CNT, C_G2 = CNT + 4, C_Y = CNT + 8, C_H = CNT + 12, C_MMA = CNT + 16, C_S = CNT + 20, C_P = CNT + 24,
              C_TAG = CNT + 60, C_ST = CNT + 40, C_HR = CNT + 32, C_YR = CNT + 36;
constexpr int LDS_BYTES = CNT + 64;
static_assert(PO + 10240 <= XFS && ES + 20480 <= XFS && LDS_BYTES <= 163840, "lds");
constexpr int PSTR = 336, PART = 64 * PSTR;

DI void lds_add(LAS char* c, unsigned v, int lane) {
  if (lane == 0) __hip_atomic_fetch_add((LAS unsigned*)c, v, RLX_WG);
}
DI void lds_wait(LAS char* c, unsigned target) {
  while ((unsigned)__builtin_amdgcn_readfirstlane(__hip_atomic_load((LAS unsigned*)c, RLX_WG)) < target) {}
  asm volatile("" ::: "memory");
}
DI void lds_wait_sleep(LAS char* c, unsigned target) {
  while ((unsigned)__builtin_amdgcn_readfirstlane(__hip_atomic_load((LAS unsigned*)c, RLX_WG)) < target) __builtin_amdgcn_s_sleep(1);
  asm volatile("" ::: "memory");
}
DI unsigned flag_load(char* ws, int off, int n, int lane) {
  return __hip_atomic_load((GAS unsigned*)(ws + off + (lane < n ? lane : 0) * FSTR), RLX_AGENT);
}
DI void poll_flags(char* ws, int off, int n, unsigned tag, int lane) {
#pragma unroll 1
  for (unsigned spins = 0; spins < SPIN_CAP; ++spins) {
    const unsigned v = flag_load(ws, off, n, lane);
    if (__all(v == tag)) break;
    __builtin_amdgcn_s_sleep(POLL_SLEEP);
  }
  __builtin_amdgcn_fence(__ATOMIC_ACQUIRE, "wavefront");
}
DI void set_flag(char* ws, int off, unsigned tag, int lane) {
  if (lane == 0) __hip_atomic_store((GAS unsigned*)(ws + off), tag, RLX_AGENT);
}

DI unsigned opaque0() { unsigned z; asm volatile("v_mov_b32 %0, 0" : "=v"(z)); return z; }
template <int I> struct IC { static constexpr int value = I; };
template <int B, int E, class Fn> DI void static_for(Fn&& f) { if constexpr (B < E) { f(IC<B>{}); static_for<B + 1, E>(f); } }
struct Frags { v4u x[4][2], w[4][2]; };

template <int TS> DI void conv_ld(LAS char* pa, LAS char* pb, v4u (&A)[8], v4u (&B)[8]) {
  constexpr int NA = TS == 0 ? 8 : 6;
#pragma unroll
  for (int a = 0; a < NA; ++a) { A[a] = ldsr128(pa + a * 512); B[a] = ldsr128(pb + (7 - a) * 512); }
}
template <int TS> DI void conv_mm(const v4u (&A)[8], const v4u (&B)[8], f32x4 (&acc)[4]) {
#pragma unroll
  for (int ti = 0; ti < 4; ++ti) {
    const int n = TS == 0 ? (ti == 0 ? 0 : ti == 1 ? 1 : ti == 2 ? 6 : 7) : 2 + ti;
#pragma unroll
    for (int a = 0; a <= n; ++a) acc[ti] = mfma16(A[a], B[n - a], acc[ti]);
  }
}
DI void conv_sched() {
#pragma unroll
  for (int i = 0; i < 16; ++i) { __builtin_amdgcn_sched_group_barrier(0x8, 1, 0); __builtin_amdgcn_sched_group_barrier(0x100, 1, 0); }
  __builtin_amdgcn_sched_group_barrier(0x8, 2, 0);
}
template <int TS> DI void conv8(LAS char* pa, LAS char* pb, f32x4 (&acc)[4]) {
  v4u A0[8], B0[8], A1[8], B1[8];
  conv_ld<TS>(pa, pb, A0, B0);
  SB();
#pragma unroll 1
  for (int r = 0; r < 8; r += 2) {
    conv_ld<TS>(pa + 64, pb + 64, A1, B1); conv_mm<TS>(A0, B0, acc); conv_sched(); SB();
    pa += 128; pb += 128;
    conv_ld<TS>(pa, pb, A0, B0); conv_mm<TS>(A1, B1, acc); conv_sched(); SB();
  }
}
template <int TS> DI void conv2(LAS char* pa, LAS char* pb, f32x4 (&acc)[4]) {
#pragma unroll 1
  for (int r = 0; r < 2; ++r) {
    v4u A[8], B[8];
    conv_ld<TS>(pa, pb, A, B); conv_mm<TS>(A, B, acc);
    pa += 64; pb += 64;
  }
}
DI unsigned kshift2(float k1, float k2, float k3, float k4, float k5, float c0, float c1, float c2, float c3) {
  unsigned a = pack2(k1 * c3, k2 * c3);
  unsigned t = pack2(k2 * c2, k3 * c2);
  a = pack2(bflo(a) + bflo(t), bfhi(a) + bfhi(t));
  t = pack2(k3 * c1, k4 * c1);
  a = pack2(bflo(a) + bflo(t), bfhi(a) + bfhi(t));
  t = pack2(k4 * c0, k5 * c0);
  return pack2(bflo(a) + bflo(t), bfhi(a) + bfhi(t));
}
DI void stage_h(LAS char* Lc, v4u h, int g) {
  *(LAS v4u*)(Lc + CH_HB + 112 + g * 16) = h;
  LAS char* a = Lc + CH_HA + 114 + g * 16;
  *(LAS u16*)(a) = (u16)(h.x & 0xffff);
  *(LAS unsigned*)(a + 2) = __builtin_amdgcn_alignbit(h.y, h.x, 16);
  *(LAS v2u*)(a + 6) = v2u{__builtin_amdgcn_alignbit(h.z, h.y, 16), __builtin_amdgcn_alignbit(h.w, h.z, 16)};
  *(LAS u16*)(a + 14) = (u16)(h.w >> 16);
}
DI void stage_r(LAS char* Lc, v4u u, int g) {
  LAS char* rb = Lc + CH_R + g * 16;
  *(LAS v4u*)(rb) = u;
  { LAS char* q = rb + CH_RS - 4; *(LAS unsigned*)(q) = u.x; *(LAS v2u*)(q + 4) = v2u{u.y, u.z}; *(LAS unsigned*)(q + 12) = u.w; }
  { LAS char* q = rb + 2 * CH_RS - 8; *(LAS v2u*)(q) = v2u{u.x, u.y}; *(LAS v2u*)(q + 8) = v2u{u.z, u.w}; }
  { LAS char* q = rb + 3 * CH_RS - 12; *(LAS unsigned*)(q) = u.x; *(LAS v2u*)(q + 4) = v2u{u.y, u.z}; *(LAS unsigned*)(q + 12) = u.w; }
}
DI void conv_out(LAS char* Lc, f32x4 a, int n, int d, float dv, int e, int kq, __amdgpu_buffer_rsrc_t rws, unsigned tag) {
  const int t = 256 * n + 64 * (e >> 2) + 2 * (e & 3) + 16 * kq;
  const unsigned ua = *(LAS unsigned*)(Lc + CH_R + 2 * (2046 - t));
  const unsigned ub = *(LAS unsigned*)(Lc + CH_R + 2 * (2038 - t));
  const unsigned c01 = pack2(a[0], a[1]), c23 = pack2(a[2], a[3]);
  const unsigned d01 = pack2(bfhi(ua) * dv, bflo(ua) * dv), d23 = pack2(bfhi(ub) * dv, bflo(ub) * dv);
  v2u o;
  o.x = pack2(bflo(c01) + bflo(d01), bfhi(c01) + bfhi(d01));
  o.y = pack2(bflo(c23) + bflo(d23), bfhi(c23) + bfhi(d23));
  const int off = Y_OFF + d * 8192 + (256 * n + 64 * (e >> 2) + 16 * kq + 4 * (e & 3)) * 4;
  __builtin_amdgcn_raw_buffer_store_b128(v4u{o.x, tag, o.y, tag}, rws, off, 0, SC1);
}

__global__ __launch_bounds__(512, 2) void fused(const u16* __restrict__ x, const u16* __restrict__ kk, const u16* __restrict__ vv,
                                                const u16* __restrict__ sk, const u16* __restrict__ hk, const u16* __restrict__ Dv,
                                                const u16* __restrict__ w1, const u16* __restrict__ w2,
                                                char* ws, u16* out) {
  __shared__ __attribute__((aligned(16))) char smem[LDS_BYTES];
  LAS char* L = (LAS char*)smem;
  if (__builtin_amdgcn_readfirstlane(threadIdx.x >> 6) == 7) {
    unsigned long long pc;
    asm volatile("s_getpc_b64 %0" : "=s"(pc));
    const unsigned co = min(((blockIdx.x >> 3) & 7) * 8192u + (threadIdx.x & 63) * 128u, CODE_BYTES);
    asm volatile("s_mov_b32 m0, %0\n\ts_nop 0\n\tglobal_load_lds_dword %1, %2" :: "s"((unsigned)(uintptr_t)(L + XFS)), "v"(co), "s"(pc) : "m0");
  }
  const int b = blockIdx.x, xcd = b & 7, r = b >> 3;
  const int rg = xcd >> 1, hf = xcd & 1, pi = r >> 3, jj = r & 7;
  const int p = 4 * rg + pi, j = 7 * hf + jj;
  const bool g1 = jj < 7;
  const int mbl = 2 * pi + ((r >> 2) & 1), nbl = r & 3;
  const int mb = 8 * rg + mbl, nb = 4 * hf + nbl;
  const int lane = threadIdx.x & 63;
  const int w = __builtin_amdgcn_readfirstlane(threadIdx.x >> 6);
  const int hi = lane >> 5, aw = w & 3;
  STAMP_DECL; STAMP(0);
  const __amdgpu_buffer_rsrc_t rws = mkrsrc(ws, WS_BYTES);
  if (threadIdx.x < 15) *(LAS unsigned*)(L + CNT + threadIdx.x * 4) = 0u;
  const int dpair = 80 * nb + 2 * mb, d3 = 80 * nb + 64 + (mb >> 1), ts3 = mb & 1;
  const int e = lane & 15, kq = lane >> 4;
  const int aoff = ((e & 1) ? CH_HB : CH_HA) + (kq + (e >> 1)) * 16;
  const int boff = CH_R + (3 - (e & 3)) * CH_RS + (248 - 32 * 7 - 8 * (e >> 2) + kq) * 16;

  {
    const __amdgpu_buffer_rsrc_t rk = mkrsrc(kk, S * D * 2), rv_ = mkrsrc(vv, S * D * 2), rsk = mkrsrc(sk, D * 8);
    const int sb = 64 * mb * 1280 + 160 * nb;
    int vo[2];
#pragma unroll
    for (int i = 0; i < 2; ++i) { const int n = (8 * i + w) * 64 + lane; const int row = n / 10; vo[i] = row * 1280 + (n - row * 10) * 16; }
    GLDS(rk, L + PK + w * 1024, vo[0] + sb - 5120, 0);
    GLDS(rv_, L + PV + w * 1024, vo[0], sb);
    {
      const int n = (5 + w) * 64 + lane; const int row = n / 10; const int vo3 = row * 1280 + (n - row * 10) * 16;
      if (w < 3) GLDS(rk, L + PK + (8 + w) * 1024, vo[1] + sb - 5120, 0);
      else if (w < 5) GLDS(rv_, L + PV + (5 + w) * 1024, vo3, sb);
      else if (w == 5) GLDS(rsk, L + PS, lane * 16, nb * 640);
    }
  }
#define PREP_ITEM(tg, cg) do { \
        const int q_ = (lane & 15) >> 2, pp_ = lane & 3, tl = 16 * (tg) + 4 * kq; \
        LAS char* ka_ = L + PK + (tl + q_) * 160 + (cg) * 32 + pp_ * 8; \
        v2u k0 = trread(ka_), k1 = trread(ka_ + 4 * 160); \
        v2u v0 = trread(ka_ + (PV - PK)); \
        v2u sw = *(LAS v2u*)(L + PS + ((cg) * 16 + e) * 8); \
        float kf[8] = {bflo(k0.x), bfhi(k0.x), bflo(k0.y), bfhi(k0.y), bflo(k1.x), bfhi(k1.x), bflo(k1.y), bfhi(k1.y)}; \
        float vf[4] = {bflo(v0.x), bfhi(v0.x), bflo(v0.y), bfhi(v0.y)}; \
        const float c0 = bflo(sw.x), c1 = bfhi(sw.x), c2 = bflo(sw.y), c3 = bfhi(sw.y); \
        float u[4]; \
        _Pragma("unroll") for (int e2 = 0; e2 < 4; e2 += 2) { \
          const unsigned a = kshift2(kf[e2 + 1], kf[e2 + 2], kf[e2 + 3], kf[e2 + 4], kf[e2 + 5], c0, c1, c2, c3); \
          u[e2] = bflo(a) * vf[e2]; u[e2 + 1] = bfhi(a) * vf[e2 + 1]; } \
        v2u o = v2u{pack2(u[3], u[2]), pack2(u[1], u[0])}; \
        const int ch = (cg) * 16 + e; \
        const int pos8 = (60 - tl) >> 2; \
        *(LAS v2u*)(L + PO + ch * 128 + ((((pos8 >> 1) ^ (ch & 7)) << 4) | ((pos8 & 1) << 3))) = o; \
      } while (0)

  if (w >= 4) {
    unsigned gen;
    asm volatile("s_load_dword %0, %1, 0x0\n\ts_waitcnt lgkmcnt(0)" : "=s"(gen) : "s"(ws) : "memory");
    const unsigned tag = gen * 0x9E3779B1u + 0x85EBCA6Bu;
    STAMP(1);
    if (aw == 0 && lane == 0) *(LAS unsigned*)(L + C_TAG) = tag;
    WAITV(0);
    BAR();
    unsigned ab = 4;
#define AUXBAR() do { WAITL(); lds_add(L + C_AUX, 1u, lane); ab += 4; lds_wait(L + C_AUX, ab); } while (0)
    STAMP(2);
    if (ABL & 4) lds_wait_sleep(L + C_MMA, 44);
    {
      const __amdgpu_buffer_rsrc_t rw2 = mkrsrc(w2, F * D * 2);
#pragma unroll
      for (int i = 0; i < 3; ++i) if (4 * i + aw < 10) {
        const int n = (4 * i + aw) * 64 + lane; const int row = n / 10;
        GLDS(rw2, L + PW + (4 * i + aw) * 1024, (row + (row >= 32 ? 992 : 0)) * 1280 + (n - row * 10) * 16, 32 * mb * 1280 + 160 * nb);
      }
    }
    {
      const int g = lane >> 4, q = (lane & 15) >> 2, pp = lane & 3;
      PREP_ITEM(aw, 0); PREP_ITEM(aw, 1); PREP_ITEM(aw, 2);
      AUXBAR();
#pragma unroll
      for (int i = 0; i < 3; ++i) {
        const int ri = aw + 4 * i;
        if (ri < 10) {
          const int ch = 8 * ri + (lane >> 3), cc = lane & 7;
          v4u val = ldsr128(L + PO + ch * 128 + ((cc ^ (ch & 7)) << 4));
          __builtin_amdgcn_raw_buffer_store_b128(val, rws, (ABL & 1) ? -16 : R_OFF + (80 * nb + ch) * 4096 + (31 - mb) * 128 + cc * 16, 0, SC1);
        }
      }
      __builtin_amdgcn_s_setprio(AUX_PRIO);
      if (aw < 2) WAITV(3); else WAITV(2);
      AUXBAR();
      STAMP(3);
#pragma unroll
      for (int i = 0; i < 3; ++i) {
        const int fi = aw + 4 * i;
        if (fi < 10) {
          const int half = fi / 5, nt = fi - half * 5, ks = mb + 32 * half;
          const int r0 = 16 * (g & 1) + 4 * (g >> 1);
          LAS char* a = L + PW + half * 5120 + (r0 + q) * 160 + nt * 32 + pp * 8;
          v2u lo = trread(a), hi2 = trread(a + 8 * 160);
          __builtin_amdgcn_raw_buffer_store_b128(v4u{lo.x, lo.y, hi2.x, hi2.y}, rws, (ks < KS2 && !(ABL & 1)) ? W2P_OFF + nb * W2P_NB + ks * 5120 + nt * 1024 + lane * 16 : -16, 0, SC1);
        }
      }
    }
    STAMP(4);
    WAITV(0);
    AUXBAR();
    if (aw == 0) set_flag(ws, FL_P + (nb * 32 + mb) * FSTR, tag, lane);
    STAMP(5);
    if (ABL & 2) lds_wait_sleep(L + C_MMA, 44);
    const __amdgpu_buffer_rsrc_t rh = mkrsrc(hk, D * S * 2);
    v4u cu[3];
    cu[0] = __builtin_amdgcn_raw_buffer_load_b128(rh, dpair * 4096 + aw * 1024 + lane * 16, 0, 0);
    cu[1] = __builtin_amdgcn_raw_buffer_load_b128(rh, dpair * 4096 + 4096 + aw * 1024 + lane * 16, 0, 0);
    cu[2] = __builtin_amdgcn_raw_buffer_load_b128(rh, d3 * 4096 + aw * 1024 + lane * 16, 0, 0);
    const int g = aw * 64 + lane;
    lds_wait_sleep(L + C_MMA, DELAY_POLL);
    if (aw == 1) {
      const int jc = g1 ? j : 0;
      v2u gr[4];
#define GRL(off) __builtin_bit_cast(v2u, __hip_atomic_load((GAS unsigned long long*)(ws + (off)), RLX_AGENT))
#pragma unroll 1
      for (unsigned sp = 0; sp < SPIN_CAP; ++sp) {
        gr[0] = GRL(ST_OFF + (p * 128 + lane) * 8);
        gr[1] = GRL(ST_OFF + (p * 128 + 64 + lane) * 8);
        gr[2] = GRL(CS_OFF + (jc * 128 + lane) * 8);
        gr[3] = GRL(CS_OFF + (jc * 128 + (j == 13 ? 0 : 64) + lane) * 8);
        if (__all(gr[0].y == tag && gr[1].y == tag && gr[2].y == tag && gr[3].y == tag)) break;
        __builtin_amdgcn_s_sleep(POLL_SLEEP);
      }
      *(LAS f32x2*)(L + XFS + lane * 8) = f32x2{bflo(gr[0].x), 1.f / bfhi(gr[0].x)};
      *(LAS f32x2*)(L + XFS + (64 + lane) * 8) = f32x2{bflo(gr[1].x), 1.f / bfhi(gr[1].x)};
      *(LAS unsigned*)(L + XFC + lane * 4) = gr[2].x;
      *(LAS unsigned*)(L + XFC + (64 + lane) * 4) = gr[3].x;
      WAITL();
      lds_add(L + C_S, 1u, lane);
    }
    if (aw == 2) { poll_flags(ws, FL_P + nb * 32 * FSTR, 32, tag, lane); lds_add(L + C_P, 1u, lane); }
    lds_wait_sleep(L + C_P, 1);
    STAMP(6);
    v4u rv[3];
    rv[0] = __builtin_amdgcn_raw_buffer_load_b128(rws, R_OFF + dpair * 4096 + aw * 1024 + lane * 16, 0, SC1);
    rv[1] = __builtin_amdgcn_raw_buffer_load_b128(rws, R_OFF + dpair * 4096 + 4096 + aw * 1024 + lane * 16, 0, SC1);
    rv[2] = __builtin_amdgcn_raw_buffer_load_b128(rws, R_OFF + d3 * 4096 + aw * 1024 + lane * 16, 0, SC1);
    const int rot2 = ROT2_EXPR;
    const __amdgpu_buffer_rsrc_t ra = mkrsrc(ws + HP_OFF + mb * HP_MB, HP_MB);
    const __amdgpu_buffer_rsrc_t rb = mkrsrc(ws + W2P_OFF + nb * W2P_NB, W2P_NB);
    const int va = lane * 16 + aw * 4096, vb = lane * 16 + aw * 5120;
    constexpr int WB = WB_N, NKS = 14, POOL = POOL_N;
    v4u fa[NKS][4], fb[NKS][5];
#define KIDX(i) ((i) + rot2 >= NKS ? (i) + rot2 - NKS : (i) + rot2)
#define LOADA(i) do { const int so_ = KIDX(i) * 16384; \
    _Pragma("unroll") for (int m = 0; m < 4; ++m) fa[i][m] = __builtin_amdgcn_raw_buffer_load_b128(ra, va + m * 1024, so_, SC1); } while (0)
#define LOADB(i) do { const int so_ = KIDX(i) * 20480; \
    _Pragma("unroll") for (int n = 0; n < 5; ++n) fb[i][n] = __builtin_amdgcn_raw_buffer_load_b128(rb, vb + (n & 3) * 1024, so_ + (n >> 2) * 4096, SC1); } while (0)
#pragma unroll
    for (int i = 0; i < WB; ++i) LOADB(i);
    SB();
    STAMP(7);
    lds_wait_sleep(L + C_MMA, STAGE_START);
    if (aw == 0 && lane < 32) *(LAS v4u*)(L + CV + (lane >> 4) * CH_BYTES + ((lane >> 3) & 1) * CH_HB + (lane & 7) * 16) = v4u{0, 0, 0, 0};
    if (aw == 3) {
#pragma unroll
      for (int i = 0; i < 4; ++i) {
        const int n = lane + 64 * i;
        if (n < 200) { const int cp = n / 25, sl = n - cp * 25; *(LAS v4u*)(L + CV + (cp >> 2) * CH_BYTES + CH_R + (cp & 3) * CH_RS + 4080 + sl * 16) = v4u{0, 0, 0, 0}; }
      }
    }
    stage_h(L + CV, cu[0], g);
    stage_h(L + CV + CH_BYTES, cu[1], g);
    stage_r(L + CV, rv[0], g);
    stage_r(L + CV + CH_BYTES, rv[1], g);
    if (aw == 0 && lane < 16) *(LAS v4u*)(L + CV3 + (lane >> 3) * CH_HB + (lane & 7) * 16) = v4u{0, 0, 0, 0};
    if (aw == 3) {
#pragma unroll
      for (int i = 0; i < 2; ++i) {
        const int n = lane + 64 * i;
        if (n < 100) { const int cp = n / 25, sl = n - cp * 25; *(LAS v4u*)(L + CV3 + CH_R + cp * CH_RS + 4080 + sl * 16) = v4u{0, 0, 0, 0}; }
      }
    }
    stage_h(L + CV3, cu[2], g);
    stage_r(L + CV3, rv[2], g);
    AUXBAR();
    if (aw == 0) lds_add(L + C_ST, 1u, lane);
    STAMP(8);
    lds_wait_sleep(L + C_H, FH_WAIT);
    for (int i = 0; i < H_DELAY; ++i) __builtin_amdgcn_s_sleep(4);
    STAMP(10);
    if (aw == 1) { poll_flags(ws, FL_H + p * 16 * FSTR, 14, tag, lane); lds_add(L + C_HR, 1u, lane); }
    __builtin_amdgcn_s_setprio(G2_PRIO);
    lds_wait_sleep(L + C_HR, 1);
    STAMP(11);
#define ISSUE(q) do { constexpr int q_ = (q); \
      if constexpr (q_ < 4 * WB) { fa[q_ / 4][q_ % 4] = __builtin_amdgcn_raw_buffer_load_b128(ra, va + (q_ % 4) * 1024, KIDX(q_ / 4) * 16384, SC1); } \
      else if constexpr (q_ < 4 * WB + 9 * (NKS - WB)) { constexpr int k_ = WB + (q_ - 4 * WB) / 9, f_ = (q_ - 4 * WB) % 9; \
        if constexpr (f_ < 4) fa[k_][f_] = __builtin_amdgcn_raw_buffer_load_b128(ra, va + f_ * 1024, KIDX(k_) * 16384, SC1); \
        else fb[k_][f_ - 4] = __builtin_amdgcn_raw_buffer_load_b128(rb, vb + ((f_ - 4) & 3) * 1024, KIDX(k_) * 20480 + ((f_ - 4) >> 2) * 4096, SC1); } \
    } while (0)
    constexpr auto need = [](int k) { k = k > NKS ? NKS : k; return k <= WB ? 4 * k : 4 * WB + 9 * (k - WB); };
    constexpr auto cum = [need](int i) { const int a = need(i + LOOK_N), c = POOL - 5 * WB + (i > 0 ? 9 * i - 6 : 0); return a < c ? a : c; };
    static_for<0, cum(0)>([&](auto q) { ISSUE(decltype(q)::value); });
    SB();
    f32x4 acc2[4][5] = {};
    static_for<0, NKS>([&](auto ii) {
      constexpr int i = decltype(ii)::value;
      constexpr int c0 = cum(i), cn = cum(i + 1) - cum(i);
      static_assert(cn <= 9 && c0 >= need(i + 1), "schedule");
      static_for<0, 20>([&](auto mm) {
        constexpr int mn = decltype(mm)::value, m = mn / 5, n = mn % 5;
        acc2[m][n] = mfma16(fb[i][n], fa[i][m], acc2[m][n]);
        constexpr int slot = (mn & 1) ? (mn >> 1) - (9 - cn) : -1;
        if constexpr (slot >= 0 && mn < 19) { SB(); ISSUE(c0 + slot); SB(); }
      });
      SB();
      if (i == 0) STAMP(12);
    });
    STAMP(13);
    lds_wait(L + C_Y, 4);
    const int ln = __builtin_amdgcn_mbcnt_hi(~0u, __builtin_amdgcn_mbcnt_lo(~0u, opaque0()));
#pragma unroll
    for (int m = 0; m < 4; ++m)
#pragma unroll
      for (int n = 0; n < 5; ++n)
        *(LAS f32x4*)(L + aw * PART + (16 * m + (ln & 15)) * PSTR + n * 64 + (ln >> 4) * 16) = acc2[m][n];
  } else {
    __builtin_amdgcn_s_setprio(MMA_PRIO);
    const __amdgpu_buffer_rsrc_t rw = mkrsrc(w1, D * F * 2);
    unsigned tag;
    if (g1) {
      const __amdgpu_buffer_rsrc_t rx = mkrsrc(x + (size_t)p * 128 * D, 128 * D * 2);
      int vx[2], vw[4];
      {
        const int row = 32 * w + (lane >> 3);
        vx[0] = row * 1280 + (((lane & 7) ^ ((row >> 1) & 7)) << 4);
        vx[1] = (row + 8) * 1280 + (((lane & 7) ^ (((row + 8) >> 1) & 7)) << 4);
#pragma unroll
        for (int ii = 0; ii < 4; ++ii) {
          const int kr = 16 * w + 4 * ii + (lane >> 4);
          vw[ii] = kr * (F * 2) + j * 256 + (((lane & 15) ^ (((kr & 3) << 2) | ((kr >> 2) & 3))) << 4);
        }
      }
      const int rot = ROT_EXPR;
      const unsigned lbase = (unsigned)(uintptr_t)L + w * 4096;
#define KC(s) ((s) + rot >= 10 ? (s) + rot - 10 : (s) + rot)
#define G1LD(s, i) do { \
    if ((i) < 4) glds_fast(rx, lbase + ((s) % NBUF) * 32768 + (i) * 1024, vx[(i) & 1], KC(s) * 128 + ((i) >> 1) * 16 * 1280); \
    else glds_fast(rw, lbase + ((s) % NBUF) * 32768 + 16384 + ((i) - 4) * 1024, vw[((i) - 4) & 3], KC(s) * (64 * F * 2)); \
  } while (0)
#pragma unroll
      for (int i = 0; i < 8; ++i) G1LD(0, i);
#pragma unroll
      for (int i = 0; i < 8; ++i) G1LD(1, i);
      WAITV(16);
      BAR();
      PREP_ITEM(aw, 3); PREP_ITEM(aw, 4);
      WAITL(); lds_add(L + C_AUX, 1u, lane);
      SB();
      STAMP(1);
      const int fh = w & 1, th = w >> 1;
      int xa[4], wa[2][2];
      {
        const int row = 64 * th + (lane & 31);
        const int c0 = hi ^ ((row >> 1) & 7);
        const int blk = (lane >> 4) & 1, q = (lane & 15) >> 2, pp = lane & 3;
#pragma unroll
        for (int s = 0; s < 4; ++s) xa[s] = row * 128 + (((2 * s) ^ c0) << 4);
#pragma unroll
        for (int ft = 0; ft < 2; ++ft)
#pragma unroll
          for (int t = 0; t < 2; ++t) {
            const int kr = 8 * hi + 4 * t + q;
            const int ch = 4 * (2 * fh + ft) + 2 * blk + (pp >> 1);
            wa[ft][t] = 16384 + 256 * kr + ((ch ^ ((q << 2) | ((2 * hi + t) & 3))) << 4) + 8 * (pp & 1);
          }
      }
      f32x16 acc[2][2] = {};
      Frags fA, fB;
#define RDX(f, bo, s, tt) f.x[s][tt] = ldsr128(L + (bo) + xa[s] + (tt) * 4096)
#define RDW(f, bo, s, ft) do { v2u wl_ = trread(L + (bo) + wa[ft][0] + (s) * 4096), wh_ = trread(L + (bo) + wa[ft][1] + (s) * 4096); f.w[s][ft] = v4u{wl_.x, wl_.y, wh_.x, wh_.y}; } while (0)
#define MM(f, s, ft, tt) acc[ft][tt] = mfma32(f.w[s][ft], f.x[s][tt], acc[ft][tt])
#define ITER(s, vm, cur, nxt) do { \
    asm volatile("s_waitcnt vmcnt(" #vm ") lgkmcnt(0)" ::: "memory"); \
    lds_add(L + C_MMA, 1u, lane); SB(); \
    MM(cur, 0, 0, 0); MM(cur, 0, 0, 1); MM(cur, 0, 1, 0); SB(); \
    lds_wait(L + C_MMA, 4 * ((s) + 2)); SB(); \
    constexpr int bo_ = (((s) + 1) % NBUF) * 32768; constexpr bool ld_ = (s) + NBUF < 10; \
    MM(cur, 0, 1, 1); RDX(nxt, bo_, 0, 0); RDX(nxt, bo_, 0, 1); SB(); if (ld_) G1LD((s) + NBUF, 0); SB(); \
    MM(cur, 1, 0, 0); RDW(nxt, bo_, 0, 0); SB(); \
    MM(cur, 1, 0, 1); RDW(nxt, bo_, 0, 1); SB(); if (ld_) G1LD((s) + NBUF, 4); SB(); \
    MM(cur, 1, 1, 0); RDX(nxt, bo_, 1, 0); RDX(nxt, bo_, 1, 1); SB(); if (ld_) G1LD((s) + NBUF, 1); SB(); \
    MM(cur, 1, 1, 1); RDW(nxt, bo_, 1, 0); SB(); \
    MM(cur, 2, 0, 0); RDW(nxt, bo_, 1, 1); SB(); if (ld_) G1LD((s) + NBUF, 5); SB(); \
    MM(cur, 2, 0, 1); RDX(nxt, bo_, 2, 0); RDX(nxt, bo_, 2, 1); SB(); if (ld_) G1LD((s) + NBUF, 2); SB(); \
    MM(cur, 2, 1, 0); RDW(nxt, bo_, 2, 0); SB(); \
    MM(cur, 2, 1, 1); RDW(nxt, bo_, 2, 1); SB(); if (ld_) G1LD((s) + NBUF, 6); SB(); \
    MM(cur, 3, 0, 0); RDX(nxt, bo_, 3, 0); RDX(nxt, bo_, 3, 1); SB(); if (ld_) G1LD((s) + NBUF, 3); SB(); \
    MM(cur, 3, 0, 1); RDW(nxt, bo_, 3, 0); RDW(nxt, bo_, 3, 1); SB(); \
    MM(cur, 3, 1, 0); SB(); if (ld_) G1LD((s) + NBUF, 7); SB(); \
    MM(cur, 3, 1, 1); SB(); \
  } while (0)
      WAITV(8);
      lds_add(L + C_MMA, 1u, lane); lds_wait(L + C_MMA, 4);
      STAMP(2);
#pragma unroll
      for (int s = 0; s < 4; ++s) { RDX(fA, 0, s, 0); RDX(fA, 0, s, 1); RDW(fA, 0, s, 0); RDW(fA, 0, s, 1); }
      SB();
#pragma unroll
      for (int i = 0; i < 8; ++i) G1LD(2, i);
      SB();
      ITER(0, 8, fA, fB);
      ITER(1, 8, fB, fA);
      ITER(2, 8, fA, fB);
      STAMP(3);
      ITER(3, 8, fB, fA);
      ITER(4, 8, fA, fB);
      ITER(5, 8, fB, fA);
      STAMP(4);
      ITER(6, 8, fA, fB);
      ITER(7, 8, fB, fA);
      ITER(8, 0, fA, fB);
#pragma unroll
      for (int s = 0; s < 4; ++s) { MM(fB, s, 0, 0); MM(fB, s, 0, 1); MM(fB, s, 1, 0); MM(fB, s, 1, 1); }
      SB();
      lds_add(L + C_MMA, 1u, lane);
      STAMP(5);
      lds_wait_sleep(L + C_S, 1);
      tag = __builtin_amdgcn_readfirstlane(*(LAS unsigned*)(L + C_TAG));
      STAMP(6);
#pragma unroll
      for (int ft = 0; ft < 2; ++ft) {
        const int fg = 2 * fh + ft;
        const int ks = 4 * j + fg;
        f32x4 c4[4];
#pragma unroll
        for (int g = 0; g < 4; ++g) c4[g] = *(LAS f32x4*)(L + XFC + (32 * fg + 8 * g + 4 * hi) * 4);
#pragma unroll
        for (int tt = 0; tt < 2; ++tt) {
          const int tok = 64 * th + 32 * tt + (lane & 31);
          const f32x2 st = *(LAS f32x2*)(L + XFS + tok * 8);
          const f32x16& a = acc[ft][tt];
          const float zs1 = -1.44269504088896f * st.y, dn1 = __builtin_amdgcn_rcpf(st.y);
          const f32x2 zs = f32x2{zs1, zs1}, dn = f32x2{dn1, dn1};
          const int mb2 = 2 * p + th, mt = 2 * tt + ((lane >> 4) & 1);
          const int off = ks < KS2 ? HP_OFF + mb2 * HP_MB + ks * 4096 + mt * 1024 + ((2 * hi) * 16 + (lane & 15)) * 16 : -1024;
#pragma unroll
          for (int hh = 0; hh < 2; ++hh) {
            unsigned pk[4];
#pragma unroll
            for (int e2 = 0; e2 < 4; ++e2) {
              const int ee = hh * 4 + e2;
              const f32x2 cc = f32x2{c4[ee >> 1][(2 * ee) & 3], c4[ee >> 1][(2 * ee + 1) & 3]};
              const f32x2 dd = __builtin_elementwise_fma(cc, f32x2{-st.x, -st.x}, f32x2{a[2 * ee], a[2 * ee + 1]});
              const f32x2 zz = dd * zs;
              const f32x2 ex = f32x2{__builtin_amdgcn_exp2f(zz.x), __builtin_amdgcn_exp2f(zz.y)};
              const f32x2 ss = __builtin_elementwise_fma(ex, dn, dn);
              const f32x2 hv2 = dd * f32x2{__builtin_amdgcn_rcpf(ss.x), __builtin_amdgcn_rcpf(ss.y)};
              pk[e2] = pack2(hv2.x, hv2.y);
            }
            __builtin_amdgcn_raw_buffer_store_b128(v4u{pk[0], pk[1], pk[2], pk[3]}, rws, off + hh * 256, 0, SC1);
            SB();
          }
        }
      }
      lds_add(L + C_MMA, 1u, lane);
      STAMP(7);
      WAITV(0);
      {
        unsigned old = 0;
        if (lane == 0) old = __hip_atomic_fetch_add((LAS unsigned*)(L + C_H), 1u, RLX_WG);
        if (__builtin_amdgcn_readfirstlane(old) == 3) set_flag(ws, FL_H + (p * 16 + j) * FSTR, tag, lane);
      }
      STAMP(8);
    } else {
      const int idx16 = 4 * rg + pi;
      const int srow = 512 * rg + 64 * (4 * hf + pi) + 16 * w + (lane >> 3);
      const int cg = 14 * hf + idx16;
      const bool csv = idx16 < 14 - hf;
      const __amdgpu_buffer_rsrc_t rx = mkrsrc(x, S * D * 2);
      v4u xv[2][10], wv[20];
#pragma unroll
      for (int h2 = 0; h2 < 2; ++h2)
#pragma unroll
        for (int i = 0; i < 10; ++i) xv[h2][i] = __builtin_amdgcn_raw_buffer_load_b128(rx, (srow + 8 * h2) * 1280 + (lane & 7) * 16 + i * 128, 0, 0);
      SB();
      WAITV(20);
      BAR();
      PREP_ITEM(aw, 3); PREP_ITEM(aw, 4);
      WAITL(); lds_add(L + C_AUX, 1u, lane);
      SB();
      const int wvo = csv ? (160 * w + (lane >> 3)) * (F * 2) + cg * 128 + (lane & 7) * 16 : -16;
#pragma unroll
      for (int i = 0; i < 20; ++i) wv[i] = __builtin_amdgcn_raw_buffer_load_b128(rw, wvo, csv ? i * 8 * F * 2 : 0, 0);
      SB();
      tag = __builtin_amdgcn_readfirstlane(*(LAS unsigned*)(L + C_TAG));
#pragma unroll
      for (int h2 = 0; h2 < 2; ++h2) {
        float s = 0.f;
#pragma unroll
        for (int i = 0; i < 10; ++i)
#pragma unroll
          for (int e2 = 0; e2 < 4; ++e2) s = dot2(xv[h2][i][e2], 0x3f803f80u, s);
        s += __shfl_xor(s, 1); s += __shfl_xor(s, 2); s += __shfl_xor(s, 4);
        const float mean = s * (1.f / D);
        float q0 = 0.f, q1 = 0.f;
#pragma unroll
        for (int i = 0; i < 10; ++i)
#pragma unroll
          for (int e2 = 0; e2 < 4; ++e2) { const float a0 = bflo(xv[h2][i][e2]) - mean, a1 = bfhi(xv[h2][i][e2]) - mean; q0 = fmaf(a0, a0, q0); q1 = fmaf(a1, a1, q1); }
        float q = q0 + q1;
        q += __shfl_xor(q, 1); q += __shfl_xor(q, 2); q += __shfl_xor(q, 4);
        const float den = __builtin_sqrtf(rbf(rbf(q * (1.f / D)) + 1e-5f));
        __builtin_amdgcn_raw_buffer_store_b64(v2u{pack2(mean, den), tag}, rws, (lane & 7) == 0 ? ST_OFF + (srow + 8 * h2) * 8 : -16, 0, SC1);
      }
      float c[8] = {};
#pragma unroll
      for (int i = 0; i < 20; ++i)
#pragma unroll
        for (int e2 = 0; e2 < 4; ++e2) { c[2 * e2] += bflo(wv[i][e2]); c[2 * e2 + 1] += bfhi(wv[i][e2]); }
      *(LAS f32x4*)(L + ((w * 8 + (lane >> 3)) * 64 + (lane & 7) * 8) * 4) = f32x4{c[0], c[1], c[2], c[3]};
      *(LAS f32x4*)(L + ((w * 8 + (lane >> 3)) * 64 + (lane & 7) * 8 + 4) * 4) = f32x4{c[4], c[5], c[6], c[7]};
      WAITL();
      unsigned old = 0;
      if (lane == 0) old = __hip_atomic_fetch_add((LAS unsigned*)(L + C_H), 1u, RLX_WG);
      if (__builtin_amdgcn_readfirstlane(old) == 3) {
        asm volatile("" ::: "memory");
        float t4[4] = {};
#pragma unroll
        for (int i = 0; i < 32; ++i) t4[i & 3] += *(LAS float*)(L + (i * 64 + lane) * 4);
        __builtin_amdgcn_raw_buffer_store_b64(v2u{__float_as_uint((t4[0] + t4[1]) + (t4[2] + t4[3])), tag}, rws, csv ? CS_OFF + (cg * 64 + lane) * 8 : -16, 0, SC1);
      }
      lds_add(L + C_MMA, 12u, lane);
      STAMP(5);
      if (b == 56 && w == 0) {
#pragma unroll 1
        for (unsigned sp = 0; sp < SPIN_CAP; ++sp) {
          bool ok = true;
#pragma unroll
          for (int i = 0; i < 4; ++i) ok = ok && flag_load(ws, FL_P + i * 64 * FSTR, 64, lane) == tag;
          if (__all(ok)) break;
          __builtin_amdgcn_s_sleep(32);
        }
        set_flag(ws, FL_GEN, tag, lane);
      }
    }
    unsigned hb_ = 0;
#define HBAR() do { WAITL(); lds_add(L + C_G2, 1u, lane); hb_ += 4; lds_wait(L + C_G2, hb_); } while (0)
    {
      const __amdgpu_buffer_rsrc_t rxx = mkrsrc(x, S * D * 2);
      lds_wait_sleep(L + C_MMA, 48);
#pragma unroll
      for (int i = 0; i < 3; ++i) {
        const int s = aw + 4 * i;
        if (s < 10) {
          const int n = s * 64 + lane; const int row = n / 10, c = n - row * 10;
          GLDS(rxx, L + EX + s * 1024, (64 * mb + row) * 1280 + 160 * nb + c * 16, 0);
        }
      }
      const float dv = bflo(Dv[dpair + (aw >> 1)]), dv3 = bflo(Dv[d3]);
      lds_wait_sleep(L + C_ST, 1);
      __builtin_amdgcn_s_setprio(C8_PRIO);
      LAS char* Lc = L + CV + (aw >> 1) * CH_BYTES;
      const int cts = aw & 1;
      f32x4 acc[4] = {};
      if (cts == 0) conv8<0>(Lc + aoff, Lc + boff, acc); else conv8<1>(Lc + aoff, Lc + boff, acc);
      __builtin_amdgcn_s_setprio(MMA_PRIO);
      STAMP(9);
#pragma unroll
      for (int ti = 0; ti < 4; ++ti) conv_out(Lc, acc[ti], cts == 0 ? (ti < 2 ? ti : ti + 4) : 2 + ti, dpair + (aw >> 1), dv, e, kq, rws, tag);
      f32x4 ac3[4] = {};
      __builtin_amdgcn_s_setprio(C3_PRIO);
      if (ts3 == 0) conv2<0>(L + CV3 + aoff + aw * 128, L + CV3 + boff + aw * 128, ac3); else conv2<1>(L + CV3 + aoff + aw * 128, L + CV3 + boff + aw * 128, ac3);
      __builtin_amdgcn_s_setprio(MMA_PRIO);
      STAMP(13);
#pragma unroll
      for (int ti = 0; ti < 4; ++ti) *(LAS f32x4*)(L + SC3 + (aw * 4 + ti) * 1024 + lane * 16) = ac3[ti];
      WAITV(4);
      HBAR();
      f32x4 a = {};
#pragma unroll
      for (int ww = 0; ww < 4; ++ww) a += *(LAS f32x4*)(L + SC3 + (ww * 4 + aw) * 1024 + lane * 16);
      conv_out(L + CV3, a, ts3 == 0 ? (aw < 2 ? aw : aw + 4) : 2 + aw, d3, dv3, e, kq, rws, tag);
    }
    STAMP(10);
    {
      const int q = (lane & 15) >> 2, pp = lane & 3, pos = lane & 15;
      const int tok = 16 * aw + 2 * (pos >> 2) + (pos & 1) + 8 * ((pos >> 1) & 1);
      lds_wait_sleep(L + C_S, 1);
      const f32x2 st = *(LAS f32x2*)(L + XFS + ((mb & 1) * 64 + tok) * 8);
      v4u xn[5];
#pragma unroll
      for (int i = 0; i < 5; ++i) {
        const v2u xv2 = *(LAS v2u*)(L + EX + tok * 160 + (4 * i + kq) * 8);
        const unsigned d01 = pack2(bflo(xv2.x) - st.x, bfhi(xv2.x) - st.x), d23 = pack2(bflo(xv2.y) - st.x, bfhi(xv2.y) - st.x);
        xn[i] = v4u{pack2(bflo(d01) * st.y, bfhi(d01) * st.y), pack2(bflo(d23) * st.y, bfhi(d23) * st.y), xv2.x, xv2.y};
      }
      WAITL();
      lds_add(L + C_Y, 1u, lane);
      for (int i = 0; i < Y_DELAY; ++i) __builtin_amdgcn_s_sleep(4);
      STAMP(11);
      {
        v4u yv[5];
        const int yo = Y_OFF + (80 * nb + 4 * aw + kq) * 8192 + mb * 256 + e * 16;
#pragma unroll 1
        for (unsigned sp = 0; sp < SPIN_CAP; ++sp) {
          asm volatile("" ::: "memory");
          bool ok = true;
#pragma unroll
          for (int i = 0; i < 5; ++i) yv[i] = __builtin_amdgcn_raw_buffer_load_b128(rws, yo, i * 16 * 8192, SC1);
#pragma unroll
          for (int i = 0; i < 5; ++i) ok = ok && yv[i].y == tag && yv[i].w == tag;
          if (__all(ok)) break;
          __builtin_amdgcn_s_sleep(POLL_SLEEP);
        }
#pragma unroll
        for (int i = 0; i < 5; ++i) *(LAS v2u*)(L + EY + (16 * i + 4 * aw + kq) * 144 + e * 8) = v2u{yv[i].x, yv[i].z};
      }
      HBAR();
      STAMP(12);
#pragma unroll
      for (int i = 0; i < 5; ++i) {
        const int dq = 4 * i + kq;
        const v2u yv = trread(L + EY + (4 * dq + q) * 144 + aw * 32 + pp * 8);
        const unsigned s01 = pack2(bflo(xn[i].x) * bflo(yv.x), bfhi(xn[i].x) * bfhi(yv.x)), s23 = pack2(bflo(xn[i].y) * bflo(yv.y), bfhi(xn[i].y) * bfhi(yv.y));
        *(LAS v4u*)(L + ES + (tok * 20 + dq) * 16) = v4u{s01, s23, xn[i].z, xn[i].w};
      }
    }
  }
  STAMP(14);
  BAR();
  const int lane2 = __builtin_amdgcn_mbcnt_hi(~0u, __builtin_amdgcn_mbcnt_lo(~0u, opaque0()));
#pragma unroll
  for (int i = 0; i < 3; ++i) {
    const int it = w + 8 * i;
    if (it < 20) {
      const int n = it * 64 + lane2;
      const int tok = n / 20, dq = n - tok * 20;
      f32x4 o = *(LAS f32x4*)(L + tok * PSTR + dq * 16);
#pragma unroll
      for (int ww = 1; ww < 4; ++ww) o += *(LAS f32x4*)(L + ww * PART + tok * PSTR + dq * 16);
      const v4u sx = *(LAS v4u*)(L + ES + n * 16);
      const unsigned o01 = pack2(o[0], o[1]), o23 = pack2(o[2], o[3]);
      const unsigned g01 = pack2(silu_f(bflo(o01)), silu_f(bfhi(o01))), g23 = pack2(silu_f(bflo(o23)), silu_f(bfhi(o23)));
      const unsigned m01 = pack2(bflo(sx.x) * bflo(g01), bfhi(sx.x) * bfhi(g01)), m23 = pack2(bflo(sx.y) * bflo(g23), bfhi(sx.y) * bfhi(g23));
      v2u res;
      res.x = pack2(bflo(sx.z) + bflo(m01), bfhi(sx.z) + bfhi(m01));
      res.y = pack2(bflo(sx.w) + bflo(m23), bfhi(sx.w) + bfhi(m23));
      *(v2u*)(out + (size_t)(64 * mb + tok) * D + 80 * nb + dq * 4) = res;
    }
  }
  STAMP(15);
  STAMP_OUT(0);
}

extern "C" void kernel_launch(void* const* d_in, const int* in_sizes, int n_in,
                              void* d_out, int out_size, void* d_ws, size_t ws_size,
                              hipStream_t stream) {
  fused<<<256, 512, 0, stream>>>((const u16*)d_in[0], (const u16*)d_in[1], (const u16*)d_in[2], (const u16*)d_in[3],
                                 (const u16*)d_in[4], (const u16*)d_in[5], (const u16*)d_in[7], (const u16*)d_in[8],
                                 (char*)d_ws, (u16*)d_out);
}
```

```cpp
#include <hip/hip_runtime.h>
#include <stdint.h>

typedef unsigned short u16;
typedef unsigned v4u __attribute__((ext_vector_type(4)));
typedef unsigned v2u __attribute__((ext_vector_type(2)));
typedef float f32x2 __attribute__((ext_vector_type(2)));
typedef float f32x4 __attribute__((ext_vector_type(4)));
typedef float f32x16 __attribute__((ext_vector_type(16)));
typedef short bf16x8 __attribute__((ext_vector_type(8)));
typedef short s16x4 __attribute__((ext_vector_type(4)));
typedef __bf16 bf16x2 __attribute__((ext_vector_type(2)));
#define LAS __attribute__((address_space(3)))
#define GAS __attribute__((address_space(1)))
#define DI __device__ __forceinline__
#define RLX_AGENT __ATOMIC_RELAXED, __HIP_MEMORY_SCOPE_AGENT
#define RLX_WG __ATOMIC_RELAXED, __HIP_MEMORY_SCOPE_WORKGROUP

constexpr int S = 2048, D = 640, F = 1728;
constexpr int KS2 = 54;
constexpr int FSTR = 128;
constexpr int FL_GEN = 0, FL_P = 131072, FL_H = FL_P + 256 * 256;
constexpr int ST_OFF = 32768;
constexpr int CS_OFF = ST_OFF + S * 8;
constexpr int HP_OFF = 262144;
constexpr int HP_MB = KS2 * 4096;
constexpr int W2P_OFF = HP_OFF + 32 * HP_MB;
constexpr int W2P_NB = KS2 * 5120;
constexpr int R_OFF = W2P_OFF + 8 * W2P_NB;
constexpr int Y_OFF = R_OFF + D * S * 2;
constexpr int WS_BYTES = Y_OFF + D * S * 4;
constexpr unsigned RSRC3 = 0x00020000u;
constexpr int SC1 = 16;
constexpr unsigned SPIN_CAP = 1u << 18;

DI float bflo(unsigned u) { return __uint_as_float(u << 16); }
DI float bfhi(unsigned u) { return __uint_as_float(u & 0xffff0000u); }
DI unsigned pack2(float a, float b) {
  bf16x2 r = __builtin_convertvector(f32x2{a, b}, bf16x2);
  return __builtin_bit_cast(unsigned, r);
}
DI float rbf(float a) { return bflo(pack2(a, a)); }
DI float silu_f(float x) { return x * __builtin_amdgcn_rcpf(1.f + __builtin_amdgcn_exp2f(-1.44269504088896f * x)); }
DI f32x4 mfma16(v4u a, v4u b, f32x4 c) {
  return __builtin_amdgcn_mfma_f32_16x16x32_bf16(__builtin_bit_cast(bf16x8, a), __builtin_bit_cast(bf16x8, b), c, 0, 0, 0);
}
DI f32x16 mfma32(v4u a, v4u b, f32x16 c) {
  return __builtin_amdgcn_mfma_f32_32x32x16_bf16(__builtin_bit_cast(bf16x8, a), __builtin_bit_cast(bf16x8, b), c, 0, 0, 0);
}
DI float dot2(unsigned a, unsigned b, float c) {
  return __builtin_amdgcn_fdot2_f32_bf16(__builtin_bit_cast(bf16x2, a), __builtin_bit_cast(bf16x2, b), c, false);
}
DI v2u trread(LAS char* p) { return __builtin_bit_cast(v2u, __builtin_amdgcn_ds_read_tr16_b64_v4i16((LAS s16x4*)p)); }
DI v4u ldsr128(LAS char* p) { return *(LAS v4u*)p; }
DI __amdgpu_buffer_rsrc_t mkrsrc(const void* p, int bytes) { return __builtin_amdgcn_make_buffer_rsrc((void*)p, 0, bytes, RSRC3); }
DI void glds_asm(__amdgpu_buffer_rsrc_t rs, unsigned ldsa, int voff, int soff) {
  asm volatile("s_nop 4\n\ts_mov_b32 m0, %0\n\ts_nop 0\n\tbuffer_load_dwordx4 %1, %2, %3 offen lds"
               :: "s"(ldsa), "v"(voff), "s"(rs), "s"(soff) : "m0", "memory");
}
DI void glds_fast(__amdgpu_buffer_rsrc_t rs, unsigned ldsa, int voff, int soff) {
  asm volatile("s_mov_b32 m0, %0\n\ts_nop 0\n\tbuffer_load_dwordx4 %1, %2, %3 offen lds"
               :: "s"(ldsa), "v"(voff), "s"(rs), "s"(soff) : "m0", "memory");
}
#define GLDS(rs, ldsp, voff, soff) glds_asm(rs, (unsigned)(uintptr_t)(ldsp), voff, soff)
#define WAITV(n) asm volatile("s_waitcnt vmcnt(" #n ")" ::: "memory")
#define WAITL() asm volatile("s_waitcnt lgkmcnt(0)" ::: "memory")
#define BAR() do { WAITL(); __builtin_amdgcn_s_barrier(); asm volatile("" ::: "memory"); } while (0)
#define SB() __builtin_amdgcn_sched_barrier(0)


constexpr int NBUF = 3;
constexpr int CH_HA = 0, CH_HB = 4224, CH_R = 8448, CH_RS = 4512, CH_BYTES = CH_R + 4 * CH_RS;
constexpr int CV = 98304;
constexpr int CV3 = 32768;
constexpr int SC3 = 65536;
constexpr int PK = CV, PV = PK + 11264, PW = PV + 10240, PS = PW + 10240, PO = PS + 1024;
constexpr int EY = CV, EX = 0, ES = EY + 80 * 144;
constexpr int XFS = CV + 2 * CH_BYTES;
constexpr int XFC = XFS + 1024;
constexpr int CNT = XFC + 512;
constexpr int C_AUX = CNT, C_G2 = CNT + 4, C_Y = CNT + 8, C_H = CNT + 12, C_MMA = CNT + 16, C_S = CNT + 20, C_P = CNT + 24,
              C_TAG = CNT + 60, C_ST = CNT + 40, C_HR = CNT + 32, C_YR = CNT + 36;
constexpr int LDS_BYTES = CNT + 64;
static_assert(PO + 10240 <= XFS && ES + 20480 <= XFS && LDS_BYTES <= 163840, "lds");
constexpr int PSTR = 336, PART = 64 * PSTR;

DI void lds_add(LAS char* c, unsigned v, int lane) {
  if (lane == 0) __hip_atomic_fetch_add((LAS unsigned*)c, v, RLX_WG);
}
DI void lds_wait(LAS char* c, unsigned target) {
  while ((unsigned)__builtin_amdgcn_readfirstlane(__hip_atomic_load((LAS unsigned*)c, RLX_WG)) < target) {}
  asm volatile("" ::: "memory");
}
DI void lds_wait_sleep(LAS char* c, unsigned target) {
  while ((unsigned)__builtin_amdgcn_readfirstlane(__hip_atomic_load((LAS unsigned*)c, RLX_WG)) < target) __builtin_amdgcn_s_sleep(1);
  asm volatile("" ::: "memory");
}
DI unsigned flag_load(char* ws, int off, int n, int lane) {
  return __hip_atomic_load((GAS unsigned*)(ws + off + (lane < n ? lane : 0) * FSTR), RLX_AGENT);
}
DI void poll_flags(char* ws, int off, int n, unsigned tag, int lane) {
#pragma unroll 1
  for (unsigned spins = 0; spins < SPIN_CAP; ++spins) {
    const unsigned v = flag_load(ws, off, n, lane);
    if (__all(v == tag)) break;
    __builtin_amdgcn_s_sleep(8);
  }
  __builtin_amdgcn_fence(__ATOMIC_ACQUIRE, "wavefront");
}
DI void set_flag(char* ws, int off, unsigned tag, int lane) {
  if (lane == 0) __hip_atomic_store((GAS unsigned*)(ws + off), tag, RLX_AGENT);
}

DI unsigned opaque0() { unsigned z; asm volatile("v_mov_b32 %0, 0" : "=v"(z)); return z; }
template <int I> struct IC { static constexpr int value = I; };
template <int B, int E, class Fn> DI void static_for(Fn&& f) { if constexpr (B < E) { f(IC<B>{}); static_for<B + 1, E>(f); } }
struct Frags { v4u x[4][2], w[4][2]; };

template <int TS> DI void conv_ld(LAS char* pa, LAS char* pb, v4u (&A)[8], v4u (&B)[8]) {
  constexpr int NA = TS == 0 ? 8 : 6;
#pragma unroll
  for (int a = 0; a < NA; ++a) { A[a] = ldsr128(pa + a * 512); B[a] = ldsr128(pb + (7 - a) * 512); }
}
template <int TS> DI void conv_mm(const v4u (&A)[8], const v4u (&B)[8], f32x4 (&acc)[4]) {
#pragma unroll
  for (int ti = 0; ti < 4; ++ti) {
    const int n = TS == 0 ? (ti == 0 ? 0 : ti == 1 ? 1 : ti == 2 ? 6 : 7) : 2 + ti;
#pragma unroll
    for (int a = 0; a <= n; ++a) acc[ti] = mfma16(A[a], B[n - a], acc[ti]);
  }
}
DI void conv_sched() {
#pragma unroll
  for (int i = 0; i < 16; ++i) { __builtin_amdgcn_sched_group_barrier(0x8, 1, 0); __builtin_amdgcn_sched_group_barrier(0x100, 1, 0); }
  __builtin_amdgcn_sched_group_barrier(0x8, 2, 0);
}
template <int TS> DI void conv8(LAS char* pa, LAS char* pb, f32x4 (&acc)[4]) {
  v4u A0[8], B0[8], A1[8], B1[8];
  conv_ld<TS>(pa, pb, A0, B0);
  SB();
#pragma unroll 1
  for (int r = 0; r < 8; r += 2) {
    conv_ld<TS>(pa + 64, pb + 64, A1, B1); conv_mm<TS>(A0, B0, acc); conv_sched(); SB();
    pa += 128; pb += 128;
    conv_ld<TS>(pa, pb, A0, B0); conv_mm<TS>(A1, B1, acc); conv_sched(); SB();
  }
}
template <int TS> DI void conv2(LAS char* pa, LAS char* pb, f32x4 (&acc)[4]) {
#pragma unroll 1
  for (int r = 0; r < 2; ++r) {
    v4u A[8], B[8];
    conv_ld<TS>(pa, pb, A, B); conv_mm<TS>(A, B, acc);
    pa += 64; pb += 64;
  }
}
DI unsigned kshift2(float k1, float k2, float k3, float k4, float k5, float c0, float c1, float c2, float c3) {
  unsigned a = pack2(k1 * c3, k2 * c3);
  unsigned t = pack2(k2 * c2, k3 * c2);
  a = pack2(bflo(a) + bflo(t), bfhi(a) + bfhi(t));
  t = pack2(k3 * c1, k4 * c1);
  a = pack2(bflo(a) + bflo(t), bfhi(a) + bfhi(t));
  t = pack2(k4 * c0, k5 * c0);
  return pack2(bflo(a) + bflo(t), bfhi(a) + bfhi(t));
}
DI void stage_h(LAS char* Lc, v4u h, int g) {
  *(LAS v4u*)(Lc + CH_HB + 112 + g * 16) = h;
  LAS char* a = Lc + CH_HA + 114 + g * 16;
  *(LAS u16*)(a) = (u16)(h.x & 0xffff);
  *(LAS unsigned*)(a + 2) = __builtin_amdgcn_alignbit(h.y, h.x, 16);
  *(LAS v2u*)(a + 6) = v2u{__builtin_amdgcn_alignbit(h.z, h.y, 16), __builtin_amdgcn_alignbit(h.w, h.z, 16)};
  *(LAS u16*)(a + 14) = (u16)(h.w >> 16);
}
DI void stage_r(LAS char* Lc, v4u u, int g) {
  LAS char* rb = Lc + CH_R + g * 16;
  *(LAS v4u*)(rb) = u;
  { LAS char* q = rb + CH_RS - 4; *(LAS unsigned*)(q) = u.x; *(LAS v2u*)(q + 4) = v2u{u.y, u.z}; *(LAS unsigned*)(q + 12) = u.w; }
  { LAS char* q = rb + 2 * CH_RS - 8; *(LAS v2u*)(q) = v2u{u.x, u.y}; *(LAS v2u*)(q + 8) = v2u{u.z, u.w}; }
  { LAS char* q = rb + 3 * CH_RS - 12; *(LAS unsigned*)(q) = u.x; *(LAS v2u*)(q + 4) = v2u{u.y, u.z}; *(LAS unsigned*)(q + 12) = u.w; }
}
DI void conv_out(LAS char* Lc, f32x4 a, int n, int d, float dv, int e, int kq, __amdgpu_buffer_rsrc_t rws, unsigned tag) {
  const int t = 256 * n + 64 * (e >> 2) + 2 * (e & 3) + 16 * kq;
  const unsigned ua = *(LAS unsigned*)(Lc + CH_R + 2 * (2046 - t));
  const unsigned ub = *(LAS unsigned*)(Lc + CH_R + 2 * (2038 - t));
  const unsigned c01 = pack2(a[0], a[1]), c23 = pack2(a[2], a[3]);
  const unsigned d01 = pack2(bfhi(ua) * dv, bflo(ua) * dv), d23 = pack2(bfhi(ub) * dv, bflo(ub) * dv);
  v2u o;
  o.x = pack2(bflo(c01) + bflo(d01), bfhi(c01) + bfhi(d01));
  o.y = pack2(bflo(c23) + bflo(d23), bfhi(c23) + bfhi(d23));
  const int off = Y_OFF + d * 8192 + (256 * n + 64 * (e >> 2) + 16 * kq + 4 * (e & 3)) * 4;
  __builtin_amdgcn_raw_buffer_store_b128(v4u{o.x, tag, o.y, tag}, rws, off, 0, SC1);
}

__global__ __launch_bounds__(512, 2) void fused(const u16* __restrict__ x, const u16* __restrict__ kk, const u16* __restrict__ vv,
                                                const u16* __restrict__ sk, const u16* __restrict__ hk, const u16* __restrict__ Dv,
                                                const u16* __restrict__ w1, const u16* __restrict__ w2,
                                                char* ws, u16* out) {
  __shared__ __attribute__((aligned(16))) char smem[LDS_BYTES];
  LAS char* L = (LAS char*)smem;
  if (__builtin_amdgcn_readfirstlane(threadIdx.x >> 6) == 7) {
    unsigned long long pc;
    asm volatile("s_getpc_b64 %0" : "=s"(pc));
    const unsigned co = min(((blockIdx.x >> 3) & 7) * 8192u + (threadIdx.x & 63) * 128u, 43000u);
    asm volatile("s_mov_b32 m0, %0\n\ts_nop 0\n\tglobal_load_lds_dword %1, %2" :: "s"((unsigned)(uintptr_t)(L + XFS)), "v"(co), "s"(pc) : "m0");
  }
  const int b = blockIdx.x, xcd = b & 7, r = b >> 3;
  const int rg = xcd >> 1, hf = xcd & 1, pi = r >> 3, jj = r & 7;
  const int p = 4 * rg + pi, j = 7 * hf + jj;
  const bool g1 = jj < 7;
  const int mbl = 2 * pi + ((r >> 2) & 1), nbl = r & 3;
  const int mb = 8 * rg + mbl, nb = 4 * hf + nbl;
  const int lane = threadIdx.x & 63;
  const int w = __builtin_amdgcn_readfirstlane(threadIdx.x >> 6);
  const int hi = lane >> 5, aw = w & 3;
  const __amdgpu_buffer_rsrc_t rws = mkrsrc(ws, WS_BYTES);
  if (threadIdx.x < 15) *(LAS unsigned*)(L + CNT + threadIdx.x * 4) = 0u;
  const int dpair = 80 * nb + 2 * mb, d3 = 80 * nb + 64 + (mb >> 1), ts3 = mb & 1;
  const int e = lane & 15, kq = lane >> 4;
  const int aoff = ((e & 1) ? CH_HB : CH_HA) + (kq + (e >> 1)) * 16;
  const int boff = CH_R + (3 - (e & 3)) * CH_RS + (248 - 32 * 7 - 8 * (e >> 2) + kq) * 16;

  {
    const __amdgpu_buffer_rsrc_t rk = mkrsrc(kk, S * D * 2), rv_ = mkrsrc(vv, S * D * 2), rsk = mkrsrc(sk, D * 8);
    const int sb = 64 * mb * 1280 + 160 * nb;
    int vo[2];
#pragma unroll
    for (int i = 0; i < 2; ++i) { const int n = (8 * i + w) * 64 + lane; const int row = n / 10; vo[i] = row * 1280 + (n - row * 10) * 16; }
    GLDS(rk, L + PK + w * 1024, vo[0] + sb - 5120, 0);
    GLDS(rv_, L + PV + w * 1024, vo[0], sb);
    {
      const int n = (5 + w) * 64 + lane; const int row = n / 10; const int vo3 = row * 1280 + (n - row * 10) * 16;
      if (w < 3) GLDS(rk, L + PK + (8 + w) * 1024, vo[1] + sb - 5120, 0);
      else if (w < 5) GLDS(rv_, L + PV + (5 + w) * 1024, vo3, sb);
      else if (w == 5) GLDS(rsk, L + PS, lane * 16, nb * 640);
    }
  }
#define PREP_ITEM(tg, cg) do { \
        const int q_ = (lane & 15) >> 2, pp_ = lane & 3, tl = 16 * (tg) + 4 * kq; \
        LAS char* ka_ = L + PK + (tl + q_) * 160 + (cg) * 32 + pp_ * 8; \
        v2u k0 = trread(ka_), k1 = trread(ka_ + 4 * 160); \
        v2u v0 = trread(ka_ + (PV - PK)); \
        v2u sw = *(LAS v2u*)(L + PS + ((cg) * 16 + e) * 8); \
        float kf[8] = {bflo(k0.x), bfhi(k0.x), bflo(k0.y), bfhi(k0.y), bflo(k1.x), bfhi(k1.x), bflo(k1.y), bfhi(k1.y)}; \
        float vf[4] = {bflo(v0.x), bfhi(v0.x), bflo(v0.y), bfhi(v0.y)}; \
        const float c0 = bflo(sw.x), c1 = bfhi(sw.x), c2 = bflo(sw.y), c3 = bfhi(sw.y); \
        float u[4]; \
        _Pragma("unroll") for (int e2 = 0; e2 < 4; e2 += 2) { \
          const unsigned a = kshift2(kf[e2 + 1], kf[e2 + 2], kf[e2 + 3], kf[e2 + 4], kf[e2 + 5], c0, c1, c2, c3); \
          u[e2] = bflo(a) * vf[e2]; u[e2 + 1] = bfhi(a) * vf[e2 + 1]; } \
        v2u o = v2u{pack2(u[3], u[2]), pack2(u[1], u[0])}; \
        const int ch = (cg) * 16 + e; \
        const int pos8 = (60 - tl) >> 2; \
        *(LAS v2u*)(L + PO + ch * 128 + ((((pos8 >> 1) ^ (ch & 7)) << 4) | ((pos8 & 1) << 3))) = o; \
      } while (0)

  if (w >= 4) {
    const unsigned gen = __hip_atomic_load((GAS unsigned*)(ws + FL_GEN), RLX_AGENT);
    WAITV(0);
    const unsigned tag = (unsigned)__builtin_amdgcn_readfirstlane(gen) * 0x9E3779B1u + 0x85EBCA6Bu;
    if (aw == 0 && lane == 0) *(LAS unsigned*)(L + C_TAG) = tag;
#define AUXBAR() do { WAITL(); lds_add(L + C_AUX, 1u, lane); ab += 4; lds_wait(L + C_AUX, ab); } while (0)
    BAR();
    unsigned ab = 4;
    {
      const __amdgpu_buffer_rsrc_t rw2 = mkrsrc(w2, F * D * 2);
#pragma unroll
      for (int i = 0; i < 3; ++i) if (4 * i + aw < 10) {
        const int n = (4 * i + aw) * 64 + lane; const int row = n / 10;
        GLDS(rw2, L + PW + (4 * i + aw) * 1024, (row + (row >= 32 ? 992 : 0)) * 1280 + (n - row * 10) * 16, 32 * mb * 1280 + 160 * nb);
      }
    }
    {
      const int g = lane >> 4, q = (lane & 15) >> 2, pp = lane & 3;
      PREP_ITEM(aw, 0); PREP_ITEM(aw, 1); PREP_ITEM(aw, 2);
      AUXBAR();
#pragma unroll
      for (int i = 0; i < 3; ++i) {
        const int ri = aw + 4 * i;
        if (ri < 10) {
          const int ch = 8 * ri + (lane >> 3), cc = lane & 7;
          v4u val = ldsr128(L + PO + ch * 128 + ((cc ^ (ch & 7)) << 4));
          __builtin_amdgcn_raw_buffer_store_b128(val, rws, R_OFF + (80 * nb + ch) * 4096 + (31 - mb) * 128 + cc * 16, 0, SC1);
        }
      }
      __builtin_amdgcn_s_setprio(0);
      if (aw < 2) WAITV(3); else WAITV(2);
      AUXBAR();
#pragma unroll
      for (int i = 0; i < 3; ++i) {
        const int fi = aw + 4 * i;
        if (fi < 10) {
          const int half = fi / 5, nt = fi - half * 5, ks = mb + 32 * half;
          const int r0 = 16 * (g & 1) + 4 * (g >> 1);
          LAS char* a = L + PW + half * 5120 + (r0 + q) * 160 + nt * 32 + pp * 8;
          v2u lo = trread(a), hi2 = trread(a + 8 * 160);
          __builtin_amdgcn_raw_buffer_store_b128(v4u{lo.x, lo.y, hi2.x, hi2.y}, rws, ks < KS2 ? W2P_OFF + nb * W2P_NB + ks * 5120 + nt * 1024 + lane * 16 : -16, 0, SC1);
        }
      }
    }
    WAITV(0);
    AUXBAR();
    if (aw == 0) set_flag(ws, FL_P + (nb * 32 + mb) * FSTR, tag, lane);
    const __amdgpu_buffer_rsrc_t rh = mkrsrc(hk, D * S * 2);
    v4u cu[3];
    cu[0] = __builtin_amdgcn_raw_buffer_load_b128(rh, dpair * 4096 + aw * 1024 + lane * 16, 0, 0);
    cu[1] = __builtin_amdgcn_raw_buffer_load_b128(rh, dpair * 4096 + 4096 + aw * 1024 + lane * 16, 0, 0);
    cu[2] = __builtin_amdgcn_raw_buffer_load_b128(rh, d3 * 4096 + aw * 1024 + lane * 16, 0, 0);
    const int g = aw * 64 + lane;
    lds_wait_sleep(L + C_MMA, 32);
    if (aw == 1) {
      const int jc = g1 ? j : 0;
      v2u gr[4];
#define GRL(off) __builtin_bit_cast(v2u, __hip_atomic_load((GAS unsigned long long*)(ws + (off)), RLX_AGENT))
#pragma unroll 1
      for (unsigned sp = 0; sp < SPIN_CAP; ++sp) {
        gr[0] = GRL(ST_OFF + (p * 128 + lane) * 8);
        gr[1] = GRL(ST_OFF + (p * 128 + 64 + lane) * 8);
        gr[2] = GRL(CS_OFF + (jc * 128 + lane) * 8);
        gr[3] = GRL(CS_OFF + (jc * 128 + (j == 13 ? 0 : 64) + lane) * 8);
        if (__all(gr[0].y == tag && gr[1].y == tag && gr[2].y == tag && gr[3].y == tag)) break;
        __builtin_amdgcn_s_sleep(8);
      }
      *(LAS f32x2*)(L + XFS + lane * 8) = f32x2{bflo(gr[0].x), 1.f / bfhi(gr[0].x)};
      *(LAS f32x2*)(L + XFS + (64 + lane) * 8) = f32x2{bflo(gr[1].x), 1.f / bfhi(gr[1].x)};
      *(LAS unsigned*)(L + XFC + lane * 4) = gr[2].x;
      *(LAS unsigned*)(L + XFC + (64 + lane) * 4) = gr[3].x;
      WAITL();
      lds_add(L + C_S, 1u, lane);
    }
    if (aw == 2) { poll_flags(ws, FL_P + nb * 32 * FSTR, 32, tag, lane); lds_add(L + C_P, 1u, lane); }
    lds_wait_sleep(L + C_P, 1);
    v4u rv[3];
    rv[0] = __builtin_amdgcn_raw_buffer_load_b128(rws, R_OFF + dpair * 4096 + aw * 1024 + lane * 16, 0, SC1);
    rv[1] = __builtin_amdgcn_raw_buffer_load_b128(rws, R_OFF + dpair * 4096 + 4096 + aw * 1024 + lane * 16, 0, SC1);
    rv[2] = __builtin_amdgcn_raw_buffer_load_b128(rws, R_OFF + d3 * 4096 + aw * 1024 + lane * 16, 0, SC1);
    const int rot2 = 0;
    const __amdgpu_buffer_rsrc_t ra = mkrsrc(ws + HP_OFF + mb * HP_MB, HP_MB);
    const __amdgpu_buffer_rsrc_t rb = mkrsrc(ws + W2P_OFF + nb * W2P_NB, W2P_NB);
    const int va = lane * 16 + aw * 4096, vb = lane * 16 + aw * 5120;
    constexpr int WB = 6, NKS = 14, POOL = 41;
    v4u fa[NKS][4], fb[NKS][5];
#define KIDX(i) ((i) + rot2 >= NKS ? (i) + rot2 - NKS : (i) + rot2)
#define LOADA(i) do { const int so_ = KIDX(i) * 16384; \
    _Pragma("unroll") for (int m = 0; m < 4; ++m) fa[i][m] = __builtin_amdgcn_raw_buffer_load_b128(ra, va + m * 1024, so_, SC1); } while (0)
#define LOADB(i) do { const int so_ = KIDX(i) * 20480; \
    _Pragma("unroll") for (int n = 0; n < 5; ++n) fb[i][n] = __builtin_amdgcn_raw_buffer_load_b128(rb, vb + (n & 3) * 1024, so_ + (n >> 2) * 4096, SC1); } while (0)
#pragma unroll
    for (int i = 0; i < WB; ++i) LOADB(i);
    SB();
    lds_wait_sleep(L + C_MMA, 44);
    if (aw == 0 && lane < 32) *(LAS v4u*)(L + CV + (lane >> 4) * CH_BYTES + ((lane >> 3) & 1) * CH_HB + (lane & 7) * 16) = v4u{0, 0, 0, 0};
    if (aw == 3) {
#pragma unroll
      for (int i = 0; i < 4; ++i) {
        const int n = lane + 64 * i;
        if (n < 200) { const int cp = n / 25, sl = n - cp * 25; *(LAS v4u*)(L + CV + (cp >> 2) * CH_BYTES + CH_R + (cp & 3) * CH_RS + 4080 + sl * 16) = v4u{0, 0, 0, 0}; }
      }
    }
    stage_h(L + CV, cu[0], g);
    stage_h(L + CV + CH_BYTES, cu[1], g);
    stage_r(L + CV, rv[0], g);
    stage_r(L + CV + CH_BYTES, rv[1], g);
    if (aw == 0 && lane < 16) *(LAS v4u*)(L + CV3 + (lane >> 3) * CH_HB + (lane & 7) * 16) = v4u{0, 0, 0, 0};
    if (aw == 3) {
#pragma unroll
      for (int i = 0; i < 2; ++i) {
        const int n = lane + 64 * i;
        if (n < 100) { const int cp = n / 25, sl = n - cp * 25; *(LAS v4u*)(L + CV3 + CH_R + cp * CH_RS + 4080 + sl * 16) = v4u{0, 0, 0, 0}; }
      }
    }
    stage_h(L + CV3, cu[2], g);
    stage_r(L + CV3, rv[2], g);
    AUXBAR();
    if (aw == 0) lds_add(L + C_ST, 1u, lane);
    lds_wait_sleep(L + C_H, 4);
    if (aw == 1) { poll_flags(ws, FL_H + p * 16 * FSTR, 14, tag, lane); lds_add(L + C_HR, 1u, lane); }
    __builtin_amdgcn_s_setprio(0);
    lds_wait_sleep(L + C_HR, 1);
#define ISSUE(q) do { constexpr int q_ = (q); \
      if constexpr (q_ < 4 * WB) { fa[q_ / 4][q_ % 4] = __builtin_amdgcn_raw_buffer_load_b128(ra, va + (q_ % 4) * 1024, KIDX(q_ / 4) * 16384, SC1); } \
      else if constexpr (q_ < 4 * WB + 9 * (NKS - WB)) { constexpr int k_ = WB + (q_ - 4 * WB) / 9, f_ = (q_ - 4 * WB) % 9; \
        if constexpr (f_ < 4) fa[k_][f_] = __builtin_amdgcn_raw_buffer_load_b128(ra, va + f_ * 1024, KIDX(k_) * 16384, SC1); \
        else fb[k_][f_ - 4] = __builtin_amdgcn_raw_buffer_load_b128(rb, vb + ((f_ - 4) & 3) * 1024, KIDX(k_) * 20480 + ((f_ - 4) >> 2) * 4096, SC1); } \
    } while (0)
    constexpr auto need = [](int k) { k = k > NKS ? NKS : k; return k <= WB ? 4 * k : 4 * WB + 9 * (k - WB); };
    constexpr auto cum = [need](int i) { const int a = need(i + 5), c = POOL - 5 * WB + (i > 0 ? 9 * i - 6 : 0); return a < c ? a : c; };
    static_for<0, cum(0)>([&](auto q) { ISSUE(decltype(q)::value); });
    SB();
#define PIN_ACC() do { _Pragma("unroll") for (int m_ = 0; m_ < 4; ++m_) _Pragma("unroll") for (int n_ = 0; n_ < 5; ++n_) asm volatile("" : "+v"(acc2[m_][n_])); } while (0)
    f32x4 acc2[4][5] = {};
    LAS char* pw = L;
    static_for<0, NKS>([&](auto ii) {
      constexpr int i = decltype(ii)::value;
      constexpr int c0 = cum(i), cn = cum(i + 1) - cum(i);
      static_assert(cn <= 9 && c0 >= need(i + 1), "schedule");
      static_for<0, 20>([&](auto mm) {
        constexpr int mn = decltype(mm)::value, m = mn / 5, n = mn % 5;
        acc2[m][n] = mfma16(fb[i][n], fa[i][m], acc2[m][n]);
        constexpr int slot = (mn & 1) ? (mn >> 1) - (9 - cn) : -1;
        if constexpr (slot >= 0 && mn < 19) { SB(); ISSUE(c0 + slot); SB(); }
        if constexpr (i == NKS - 1 && mn >= 2) { constexpr int m2 = (mn - 2) / 5, n2 = (mn - 2) % 5; SB(); *(LAS f32x4*)(pw + 16 * m2 * PSTR + n2 * 64) = acc2[m2][n2]; SB(); }
      });
      SB();
      if constexpr (i == NKS - 2) {
        PIN_ACC();
        lds_wait(L + C_Y, 4);
        const int ln = __builtin_amdgcn_mbcnt_hi(~0u, __builtin_amdgcn_mbcnt_lo(~0u, opaque0()));
        pw = L + aw * PART + (ln & 15) * PSTR + (ln >> 4) * 16;
        SB();
      }
    });
    *(LAS f32x4*)(pw + 48 * PSTR + 3 * 64) = acc2[3][3];
    *(LAS f32x4*)(pw + 48 * PSTR + 4 * 64) = acc2[3][4];
  } else {
    __builtin_amdgcn_s_setprio(2);
    const __amdgpu_buffer_rsrc_t rw = mkrsrc(w1, D * F * 2);
    unsigned tag;
    if (g1) {
      const __amdgpu_buffer_rsrc_t rx = mkrsrc(x + (size_t)p * 128 * D, 128 * D * 2);
      int vx[2], vw[4];
      {
        const int row = 32 * w + (lane >> 3);
        vx[0] = row * 1280 + (((lane & 7) ^ ((row >> 1) & 7)) << 4);
        vx[1] = (row + 8) * 1280 + (((lane & 7) ^ (((row + 8) >> 1) & 7)) << 4);
#pragma unroll
        for (int ii = 0; ii < 4; ++ii) {
          const int kr = 16 * w + 4 * ii + (lane >> 4);
          vw[ii] = kr * (F * 2) + j * 256 + (((lane & 15) ^ (((kr & 3) << 2) | ((kr >> 2) & 3))) << 4);
        }
      }
      const int rot = (5 * ((pi + jj) & 1));
      const unsigned lbase = (unsigned)(uintptr_t)L + w * 4096;
#define KC(s) ((s) + rot >= 10 ? (s) + rot - 10 : (s) + rot)
#define G1LD(s, i) do { \
    if ((i) < 4) glds_fast(rx, lbase + ((s) % NBUF) * 32768 + (i) * 1024, vx[(i) & 1], KC(s) * 128 + ((i) >> 1) * 16 * 1280); \
    else glds_fast(rw, lbase + ((s) % NBUF) * 32768 + 16384 + ((i) - 4) * 1024, vw[((i) - 4) & 3], KC(s) * (64 * F * 2)); \
  } while (0)
#pragma unroll
      for (int i = 0; i < 8; ++i) G1LD(0, i);
#pragma unroll
      for (int i = 0; i < 8; ++i) G1LD(1, i);
      WAITV(16);
      BAR();
      PREP_ITEM(aw, 3); PREP_ITEM(aw, 4);
      WAITL(); lds_add(L + C_AUX, 1u, lane);
      SB();
      const int fh = w & 1, th = w >> 1;
      int xa[4], wa[2][2];
      {
        const int row = 64 * th + (lane & 31);
        const int c0 = hi ^ ((row >> 1) & 7);
        const int blk = (lane >> 4) & 1, q = (lane & 15) >> 2, pp = lane & 3;
#pragma unroll
        for (int s = 0; s < 4; ++s) xa[s] = row * 128 + (((2 * s) ^ c0) << 4);
#pragma unroll
        for (int ft = 0; ft < 2; ++ft)
#pragma unroll
          for (int t = 0; t < 2; ++t) {
            const int kr = 8 * hi + 4 * t + q;
            const int ch = 4 * (2 * fh + ft) + 2 * blk + (pp >> 1);
            wa[ft][t] = 16384 + 256 * kr + ((ch ^ ((q << 2) | ((2 * hi + t) & 3))) << 4) + 8 * (pp & 1);
          }
      }
      f32x16 acc[2][2] = {};
      Frags fA, fB;
#define RDX(f, bo, s, tt) f.x[s][tt] = ldsr128(L + (bo) + xa[s] + (tt) * 4096)
#define RDW(f, bo, s, ft) do { v2u wl_ = trread(L + (bo) + wa[ft][0] + (s) * 4096), wh_ = trread(L + (bo) + wa[ft][1] + (s) * 4096); f.w[s][ft] = v4u{wl_.x, wl_.y, wh_.x, wh_.y}; } while (0)
#define MM(f, s, ft, tt) acc[ft][tt] = mfma32(f.w[s][ft], f.x[s][tt], acc[ft][tt])
#define ITER(s, vm, cur, nxt) do { \
    asm volatile("s_waitcnt vmcnt(" #vm ") lgkmcnt(0)" ::: "memory"); \
    lds_add(L + C_MMA, 1u, lane); SB(); \
    MM(cur, 0, 0, 0); MM(cur, 0, 0, 1); MM(cur, 0, 1, 0); SB(); \
    lds_wait(L + C_MMA, 4 * ((s) + 2)); SB(); \
    constexpr int bo_ = (((s) + 1) % NBUF) * 32768; constexpr bool ld_ = (s) + NBUF < 10; \
    MM(cur, 0, 1, 1); RDX(nxt, bo_, 0, 0); RDX(nxt, bo_, 0, 1); SB(); if (ld_) G1LD((s) + NBUF, 0); SB(); \
    MM(cur, 1, 0, 0); RDW(nxt, bo_, 0, 0); SB(); \
    MM(cur, 1, 0, 1); RDW(nxt, bo_, 0, 1); SB(); if (ld_) G1LD((s) + NBUF, 4); SB(); \
    MM(cur, 1, 1, 0); RDX(nxt, bo_, 1, 0); RDX(nxt, bo_, 1, 1); SB(); if (ld_) G1LD((s) + NBUF, 1); SB(); \
    MM(cur, 1, 1, 1); RDW(nxt, bo_, 1, 0); SB(); \
    MM(cur, 2, 0, 0); RDW(nxt, bo_, 1, 1); SB(); if (ld_) G1LD((s) + NBUF, 5); SB(); \
    MM(cur, 2, 0, 1); RDX(nxt, bo_, 2, 0); RDX(nxt, bo_, 2, 1); SB(); if (ld_) G1LD((s) + NBUF, 2); SB(); \
    MM(cur, 2, 1, 0); RDW(nxt, bo_, 2, 0); SB(); \
    MM(cur, 2, 1, 1); RDW(nxt, bo_, 2, 1); SB(); if (ld_) G1LD((s) + NBUF, 6); SB(); \
    MM(cur, 3, 0, 0); RDX(nxt, bo_, 3, 0); RDX(nxt, bo_, 3, 1); SB(); if (ld_) G1LD((s) + NBUF, 3); SB(); \
    MM(cur, 3, 0, 1); RDW(nxt, bo_, 3, 0); RDW(nxt, bo_, 3, 1); SB(); \
    MM(cur, 3, 1, 0); SB(); if (ld_) G1LD((s) + NBUF, 7); SB(); \
    MM(cur, 3, 1, 1); SB(); \
  } while (0)
      WAITV(8);
      lds_add(L + C_MMA, 1u, lane); lds_wait(L + C_MMA, 4);
#pragma unroll
      for (int s = 0; s < 4; ++s) { RDX(fA, 0, s, 0); RDX(fA, 0, s, 1); RDW(fA, 0, s, 0); RDW(fA, 0, s, 1); }
      SB();
#pragma unroll
      for (int i = 0; i < 8; ++i) G1LD(2, i);
      SB();
      ITER(0, 8, fA, fB);
      ITER(1, 8, fB, fA);
      ITER(2, 8, fA, fB);
      ITER(3, 8, fB, fA);
      ITER(4, 8, fA, fB);
      ITER(5, 8, fB, fA);
      ITER(6, 8, fA, fB);
      ITER(7, 8, fB, fA);
      ITER(8, 0, fA, fB);
#pragma unroll
      for (int s = 0; s < 4; ++s) { MM(fB, s, 0, 0); MM(fB, s, 0, 1); MM(fB, s, 1, 0); MM(fB, s, 1, 1); }
      SB();
      lds_add(L + C_MMA, 1u, lane);
      lds_wait_sleep(L + C_S, 1);
      tag = __builtin_amdgcn_readfirstlane(*(LAS unsigned*)(L + C_TAG));
#pragma unroll
      for (int ft = 0; ft < 2; ++ft) {
        const int fg = 2 * fh + ft;
        const int ks = 4 * j + fg;
        f32x4 c4[4];
#pragma unroll
        for (int g = 0; g < 4; ++g) c4[g] = *(LAS f32x4*)(L + XFC + (32 * fg + 8 * g + 4 * hi) * 4);
#pragma unroll
        for (int tt = 0; tt < 2; ++tt) {
          const int tok = 64 * th + 32 * tt + (lane & 31);
          const f32x2 st = *(LAS f32x2*)(L + XFS + tok * 8);
          const f32x16& a = acc[ft][tt];
          const float zs1 = -1.44269504088896f * st.y, dn1 = __builtin_amdgcn_rcpf(st.y);
          const f32x2 zs = f32x2{zs1, zs1}, dn = f32x2{dn1, dn1};
          const int mb2 = 2 * p + th, mt = 2 * tt + ((lane >> 4) & 1);
          const int off = ks < KS2 ? HP_OFF + mb2 * HP_MB + ks * 4096 + mt * 1024 + ((2 * hi) * 16 + (lane & 15)) * 16 : -1024;
#pragma unroll
          for (int hh = 0; hh < 2; ++hh) {
            unsigned pk[4];
#pragma unroll
            for (int e2 = 0; e2 < 4; ++e2) {
              const int ee = hh * 4 + e2;
              const f32x2 cc = f32x2{c4[ee >> 1][(2 * ee) & 3], c4[ee >> 1][(2 * ee + 1) & 3]};
              const f32x2 dd = __builtin_elementwise_fma(cc, f32x2{-st.x, -st.x}, f32x2{a[2 * ee], a[2 * ee + 1]});
              const f32x2 zz = dd * zs;
              const f32x2 ex = f32x2{__builtin_amdgcn_exp2f(zz.x), __builtin_amdgcn_exp2f(zz.y)};
              const f32x2 ss = __builtin_elementwise_fma(ex, dn, dn);
              const f32x2 hv2 = dd * f32x2{__builtin_amdgcn_rcpf(ss.x), __builtin_amdgcn_rcpf(ss.y)};
              pk[e2] = pack2(hv2.x, hv2.y);
            }
            __builtin_amdgcn_raw_buffer_store_b128(v4u{pk[0], pk[1], pk[2], pk[3]}, rws, off + hh * 256, 0, SC1);
            SB();
          }
        }
      }
      lds_add(L + C_MMA, 1u, lane);
      WAITV(0);
      {
        unsigned old = 0;
        if (lane == 0) old = __hip_atomic_fetch_add((LAS unsigned*)(L + C_H), 1u, RLX_WG);
        if (__builtin_amdgcn_readfirstlane(old) == 3) set_flag(ws, FL_H + (p * 16 + j) * FSTR, tag, lane);
      }
    } else {
      const int idx16 = 4 * rg + pi;
      const int srow = 512 * rg + 64 * (4 * hf + pi) + 16 * w + (lane >> 3);
      const int cg = 14 * hf + idx16;
      const bool csv = idx16 < 14 - hf;
      const __amdgpu_buffer_rsrc_t rx = mkrsrc(x, S * D * 2);
      v4u xv[2][10], wv[20];
#pragma unroll
      for (int h2 = 0; h2 < 2; ++h2)
#pragma unroll
        for (int i = 0; i < 10; ++i) xv[h2][i] = __builtin_amdgcn_raw_buffer_load_b128(rx, (srow + 8 * h2) * 1280 + (lane & 7) * 16 + i * 128, 0, 0);
      SB();
      WAITV(20);
      BAR();
      PREP_ITEM(aw, 3); PREP_ITEM(aw, 4);
      WAITL(); lds_add(L + C_AUX, 1u, lane);
      SB();
      const int wvo = csv ? (160 * w + (lane >> 3)) * (F * 2) + cg * 128 + (lane & 7) * 16 : -16;
#pragma unroll
      for (int i = 0; i < 20; ++i) wv[i] = __builtin_amdgcn_raw_buffer_load_b128(rw, wvo, csv ? i * 8 * F * 2 : 0, 0);
      SB();
      tag = __builtin_amdgcn_readfirstlane(*(LAS unsigned*)(L + C_TAG));
#pragma unroll
      for (int h2 = 0; h2 < 2; ++h2) {
        float s = 0.f;
#pragma unroll
        for (int i = 0; i < 10; ++i)
#pragma unroll
          for (int e2 = 0; e2 < 4; ++e2) s = dot2(xv[h2][i][e2], 0x3f803f80u, s);
        s += __shfl_xor(s, 1); s += __shfl_xor(s, 2); s += __shfl_xor(s, 4);
        const float mean = s * (1.f / D);
        float q0 = 0.f, q1 = 0.f;
#pragma unroll
        for (int i = 0; i < 10; ++i)
#pragma unroll
          for (int e2 = 0; e2 < 4; ++e2) { const float a0 = bflo(xv[h2][i][e2]) - mean, a1 = bfhi(xv[h2][i][e2]) - mean; q0 = fmaf(a0, a0, q0); q1 = fmaf(a1, a1, q1); }
        float q = q0 + q1;
        q += __shfl_xor(q, 1); q += __shfl_xor(q, 2); q += __shfl_xor(q, 4);
        const float den = __builtin_sqrtf(rbf(rbf(q * (1.f / D)) + 1e-5f));
        __builtin_amdgcn_raw_buffer_store_b64(v2u{pack2(mean, den), tag}, rws, (lane & 7) == 0 ? ST_OFF + (srow + 8 * h2) * 8 : -16, 0, SC1);
      }
      float c[8] = {};
#pragma unroll
      for (int i = 0; i < 20; ++i)
#pragma unroll
        for (int e2 = 0; e2 < 4; ++e2) { c[2 * e2] += bflo(wv[i][e2]); c[2 * e2 + 1] += bfhi(wv[i][e2]); }
      *(LAS f32x4*)(L + ((w * 8 + (lane >> 3)) * 64 + (lane & 7) * 8) * 4) = f32x4{c[0], c[1], c[2], c[3]};
      *(LAS f32x4*)(L + ((w * 8 + (lane >> 3)) * 64 + (lane & 7) * 8 + 4) * 4) = f32x4{c[4], c[5], c[6], c[7]};
      WAITL();
      unsigned old = 0;
      if (lane == 0) old = __hip_atomic_fetch_add((LAS unsigned*)(L + C_H), 1u, RLX_WG);
      if (__builtin_amdgcn_readfirstlane(old) == 3) {
        asm volatile("" ::: "memory");
        float t4[4] = {};
#pragma unroll
        for (int i = 0; i < 32; ++i) t4[i & 3] += *(LAS float*)(L + (i * 64 + lane) * 4);
        __builtin_amdgcn_raw_buffer_store_b64(v2u{__float_as_uint((t4[0] + t4[1]) + (t4[2] + t4[3])), tag}, rws, csv ? CS_OFF + (cg * 64 + lane) * 8 : -16, 0, SC1);
      }
      lds_add(L + C_MMA, 12u, lane);
      if (b == 56 && w == 0) {
#pragma unroll 1
        for (unsigned sp = 0; sp < SPIN_CAP; ++sp) {
          bool ok = true;
#pragma unroll
          for (int i = 0; i < 4; ++i) ok = ok && flag_load(ws, FL_P + i * 64 * FSTR, 64, lane) == tag;
          if (__all(ok)) break;
          __builtin_amdgcn_s_sleep(32);
        }
        set_flag(ws, FL_GEN, tag, lane);
      }
    }
    unsigned hb_ = 0;
#define HBAR() do { WAITL(); lds_add(L + C_G2, 1u, lane); hb_ += 4; lds_wait(L + C_G2, hb_); } while (0)
    {
      const __amdgpu_buffer_rsrc_t rxx = mkrsrc(x, S * D * 2);
      lds_wait_sleep(L + C_MMA, 48);
#pragma unroll
      for (int i = 0; i < 3; ++i) {
        const int s = aw + 4 * i;
        if (s < 10) {
          const int n = s * 64 + lane; const int row = n / 10, c = n - row * 10;
          GLDS(rxx, L + EX + s * 1024, (64 * mb + row) * 1280 + 160 * nb + c * 16, 0);
        }
      }
      const float dv = bflo(Dv[dpair + (aw >> 1)]), dv3 = bflo(Dv[d3]);
      lds_wait_sleep(L + C_ST, 1);
      __builtin_amdgcn_s_setprio(1);
      LAS char* Lc = L + CV + (aw >> 1) * CH_BYTES;
      const int cts = aw & 1;
      f32x4 acc[4] = {};
      if (cts == 0) conv8<0>(Lc + aoff, Lc + boff, acc); else conv8<1>(Lc + aoff, Lc + boff, acc);
      __builtin_amdgcn_s_setprio(2);
#pragma unroll
      for (int ti = 0; ti < 4; ++ti) conv_out(Lc, acc[ti], cts == 0 ? (ti < 2 ? ti : ti + 4) : 2 + ti, dpair + (aw >> 1), dv, e, kq, rws, tag);
      f32x4 ac3[4] = {};
      __builtin_amdgcn_s_setprio(2);
      if (ts3 == 0) conv2<0>(L + CV3 + aoff + aw * 128, L + CV3 + boff + aw * 128, ac3); else conv2<1>(L + CV3 + aoff + aw * 128, L + CV3 + boff + aw * 128, ac3);
      __builtin_amdgcn_s_setprio(2);
#pragma unroll
      for (int ti = 0; ti < 4; ++ti) *(LAS f32x4*)(L + SC3 + (aw * 4 + ti) * 1024 + lane * 16) = ac3[ti];
      WAITV(4);
      HBAR();
      f32x4 a = {};
#pragma unroll
      for (int ww = 0; ww < 4; ++ww) a += *(LAS f32x4*)(L + SC3 + (ww * 4 + aw) * 1024 + lane * 16);
      conv_out(L + CV3, a, ts3 == 0 ? (aw < 2 ? aw : aw + 4) : 2 + aw, d3, dv3, e, kq, rws, tag);
    }
    {
      const int q = (lane & 15) >> 2, pp = lane & 3, pos = lane & 15;
      const int tok = 16 * aw + 2 * (pos >> 2) + (pos & 1) + 8 * ((pos >> 1) & 1);
      lds_wait_sleep(L + C_S, 1);
      const f32x2 st = *(LAS f32x2*)(L + XFS + ((mb & 1) * 64 + tok) * 8);
      v4u xn[5];
#pragma unroll
      for (int i = 0; i < 5; ++i) {
        const v2u xv2 = *(LAS v2u*)(L + EX + tok * 160 + (4 * i + kq) * 8);
        const unsigned d01 = pack2(bflo(xv2.x) - st.x, bfhi(xv2.x) - st.x), d23 = pack2(bflo(xv2.y) - st.x, bfhi(xv2.y) - st.x);
        xn[i] = v4u{pack2(bflo(d01) * st.y, bfhi(d01) * st.y), pack2(bflo(d23) * st.y, bfhi(d23) * st.y), xv2.x, xv2.y};
      }
      WAITL();
      lds_add(L + C_Y, 1u, lane);
      for (int i = 0; i < 2; ++i) __builtin_amdgcn_s_sleep(4);
      {
        v4u yv[5];
        const int yo = Y_OFF + (80 * nb + 4 * aw + kq) * 8192 + mb * 256 + e * 16;
#pragma unroll 1
        for (unsigned sp = 0; sp < SPIN_CAP; ++sp) {
          asm volatile("" ::: "memory");
          bool ok = true;
#pragma unroll
          for (int i = 0; i < 5; ++i) yv[i] = __builtin_amdgcn_raw_buffer_load_b128(rws, yo, i * 16 * 8192, SC1);
#pragma unroll
          for (int i = 0; i < 5; ++i) ok = ok && yv[i].y == tag && yv[i].w == tag;
          if (__all(ok)) break;
          __builtin_amdgcn_s_sleep(8);
        }
#pragma unroll
        for (int i = 0; i < 5; ++i) *(LAS v2u*)(L + EY + (16 * i + 4 * aw + kq) * 144 + e * 8) = v2u{yv[i].x, yv[i].z};
      }
      HBAR();
#pragma unroll
      for (int i = 0; i < 5; ++i) {
        const int dq = 4 * i + kq;
        const v2u yv = trread(L + EY + (4 * dq + q) * 144 + aw * 32 + pp * 8);
        const unsigned s01 = pack2(bflo(xn[i].x) * bflo(yv.x), bfhi(xn[i].x) * bfhi(yv.x)), s23 = pack2(bflo(xn[i].y) * bflo(yv.y), bfhi(xn[i].y) * bfhi(yv.y));
        *(LAS v4u*)(L + ES + (tok * 20 + dq) * 16) = v4u{s01, s23, xn[i].z, xn[i].w};
      }
    }
  }
  BAR();
  const int lane2 = __builtin_amdgcn_mbcnt_hi(~0u, __builtin_amdgcn_mbcnt_lo(~0u, opaque0()));
#pragma unroll
  for (int i = 0; i < 3; ++i) {
    const int it = w + 8 * i;
    if (it < 20) {
      const int n = it * 64 + lane2;
      const int tok = n / 20, dq = n - tok * 20;
      f32x4 o = *(LAS f32x4*)(L + tok * PSTR + dq * 16);
#pragma unroll
      for (int ww = 1; ww < 4; ++ww) o += *(LAS f32x4*)(L + ww * PART + tok * PSTR + dq * 16);
      const v4u sx = *(LAS v4u*)(L + ES + n * 16);
      const unsigned o01 = pack2(o[0], o[1]), o23 = pack2(o[2], o[3]);
      const unsigned g01 = pack2(silu_f(bflo(o01)), silu_f(bfhi(o01))), g23 = pack2(silu_f(bflo(o23)), silu_f(bfhi(o23)));
      const unsigned m01 = pack2(bflo(sx.x) * bflo(g01), bfhi(sx.x) * bfhi(g01)), m23 = pack2(bflo(sx.y) * bflo(g23), bfhi(sx.y) * bfhi(g23));
      v2u res;
      res.x = pack2(bflo(sx.z) + bflo(m01), bfhi(sx.z) + bfhi(m01));
      res.y = pack2(bflo(sx.w) + bflo(m23), bfhi(sx.w) + bfhi(m23));
      *(v2u*)(out + (size_t)(64 * mb + tok) * D + 80 * nb + dq * 4) = res;
    }
  }
}

extern "C" void kernel_launch(void* const* d_in, const int* in_sizes, int n_in,
                              void* d_out, int out_size, void* d_ws, size_t ws_size,
                              hipStream_t stream) {
  fused<<<256, 512, 0, stream>>>((const u16*)d_in[0], (const u16*)d_in[1], (const u16*)d_in[2], (const u16*)d_in[3],
                                 (const u16*)d_in[4], (const u16*)d_in[5], (const u16*)d_in[7], (const u16*)d_in[8],
                                 (char*)d_ws, (u16*)d_out);
}
```

```cpp
#include <hip/hip_runtime.h>
#include <stdint.h>

typedef unsigned short u16;
typedef unsigned v4u __attribute__((ext_vector_type(4)));
typedef unsigned v2u __attribute__((ext_vector_type(2)));
typedef float f32x2 __attribute__((ext_vector_type(2)));
typedef float f32x4 __attribute__((ext_vector_type(4)));
typedef float f32x16 __attribute__((ext_vector_type(16)));
typedef short bf16x8 __attribute__((ext_vector_type(8)));
typedef short s16x4 __attribute__((ext_vector_type(4)));
typedef __bf16 bf16x2 __attribute__((ext_vector_type(2)));
#define LAS __attribute__((address_space(3)))
#define GAS __attribute__((address_space(1)))
#define DI __device__ __forceinline__
#define RLX_AGENT __ATOMIC_RELAXED, __HIP_MEMORY_SCOPE_AGENT
#define RLX_WG __ATOMIC_RELAXED, __HIP_MEMORY_SCOPE_WORKGROUP

#define FSTR_N 128
constexpr int S = 2048, D = 640, F = 1728;
constexpr int KS2 = 54;
constexpr int FSTR = FSTR_N;
constexpr int FL_GEN = 0, FL_P = 131072, FL_H = FL_P + 256 * 256;
constexpr int ST_OFF = 32768;
constexpr int CS_OFF = ST_OFF + S * 8;
constexpr int HP_OFF = 262144;
constexpr int HP_MB = KS2 * 4096;
constexpr int W2P_OFF = HP_OFF + 32 * HP_MB;
constexpr int W2P_NB = KS2 * 5120;
constexpr int R_OFF = W2P_OFF + 8 * W2P_NB;
constexpr int Y_OFF = R_OFF + D * S * 2;
constexpr int WS_BYTES = Y_OFF + D * S * 4;
constexpr unsigned RSRC3 = 0x00020000u;
constexpr int SC1 = 16;
constexpr unsigned SPIN_CAP = 1u << 18;

DI float bflo(unsigned u) { return __uint_as_float(u << 16); }
DI float bfhi(unsigned u) { return __uint_as_float(u & 0xffff0000u); }
DI unsigned pack2(float a, float b) {
  bf16x2 r = __builtin_convertvector(f32x2{a, b}, bf16x2);
  return __builtin_bit_cast(unsigned, r);
}
DI float rbf(float a) { return bflo(pack2(a, a)); }
DI float silu_f(float x) { return x * __builtin_amdgcn_rcpf(1.f + __builtin_amdgcn_exp2f(-1.44269504088896f * x)); }
DI f32x4 mfma16(v4u a, v4u b, f32x4 c) {
  return __builtin_amdgcn_mfma_f32_16x16x32_bf16(__builtin_bit_cast(bf16x8, a), __builtin_bit_cast(bf16x8, b), c, 0, 0, 0);
}
DI f32x16 mfma32(v4u a, v4u b, f32x16 c) {
  return __builtin_amdgcn_mfma_f32_32x32x16_bf16(__builtin_bit_cast(bf16x8, a), __builtin_bit_cast(bf16x8, b), c, 0, 0, 0);
}
DI float dot2(unsigned a, unsigned b, float c) {
  return __builtin_amdgcn_fdot2_f32_bf16(__builtin_bit_cast(bf16x2, a), __builtin_bit_cast(bf16x2, b), c, false);
}
DI v2u trread(LAS char* p) { return __builtin_bit_cast(v2u, __builtin_amdgcn_ds_read_tr16_b64_v4i16((LAS s16x4*)p)); }
DI v4u ldsr128(LAS char* p) { return *(LAS v4u*)p; }
DI __amdgpu_buffer_rsrc_t mkrsrc(const void* p, int bytes) { return __builtin_amdgcn_make_buffer_rsrc((void*)p, 0, bytes, RSRC3); }
DI void glds_asm(__amdgpu_buffer_rsrc_t rs, unsigned ldsa, int voff, int soff) {
  asm volatile("s_nop 4\n\ts_mov_b32 m0, %0\n\ts_nop 0\n\tbuffer_load_dwordx4 %1, %2, %3 offen lds"
               :: "s"(ldsa), "v"(voff), "s"(rs), "s"(soff) : "m0", "memory");
}
DI void glds_fast(__amdgpu_buffer_rsrc_t rs, unsigned ldsa, int voff, int soff) {
  asm volatile("s_mov_b32 m0, %0\n\ts_nop 0\n\tbuffer_load_dwordx4 %1, %2, %3 offen lds"
               :: "s"(ldsa), "v"(voff), "s"(rs), "s"(soff) : "m0", "memory");
}
#define GLDS(rs, ldsp, voff, soff) glds_asm(rs, (unsigned)(uintptr_t)(ldsp), voff, soff)
#define WAITV(n) asm volatile("s_waitcnt vmcnt(" #n ")" ::: "memory")
#define WAITL() asm volatile("s_waitcnt lgkmcnt(0)" ::: "memory")
#define BAR() do { WAITL(); __builtin_amdgcn_s_barrier(); asm volatile("" ::: "memory"); } while (0)
#define SB() __builtin_amdgcn_sched_barrier(0)

#define STAMP_DECL
#define STAMP(i)
#define STAMP_OUT(kid)

#define ROT_EXPR (5 * ((pi + jj) & 1))
#define WA_N 3
#define POLL_SLEEP 8
#define CODE_PF 1
#define CODE_BYTES 43000u
#define EXACT_PREP 1
#define PREP_PRIO 3
#define CONV_PRIO 1
#define WB_N 6
#define ROT2_EXPR 0
#define DELAY_POLL 32
#define ABL 0
#define FH_AUX 1
#define Y_DELAY 2
#define C8_PRIO 1
#define G2_PRIO 0
#define POOL_N 41
#define S1_EARLY 1
#define STAGE_START 44
#define LOOK_N 5
#define FH_WAIT 4
#define H_DELAY 0
#define C3_PRIO 2
#define FAST_EPI 1
#define AUX_PRIO 0
#define MMA_PRIO 2
#define CONV_START 44
constexpr int NBUF = 3;
constexpr int CH_HA = 0, CH_HB = 4224, CH_R = 8448, CH_RS = 4512, CH_BYTES = CH_R + 4 * CH_RS;
constexpr int CV = 98304;
constexpr int CV3 = 32768;
constexpr int SC3 = 65536;
constexpr int PK = CV, PV = PK + 11264, PW = PV + 10240, PS = PW + 10240, PO = PS + 1024;
constexpr int EY = CV, EX = 0, ES = EY + 80 * 144;
constexpr int XFS = CV + 2 * CH_BYTES;
constexpr int XFC = XFS + 1024;
constexpr int CNT = XFC + 512;
constexpr int C_AUX = CNT, C_G2 = CNT + 4, C_Y = CNT + 8, C_H = CNT + 12, C_MMA = CNT + 16, C_S = CNT + 20, C_P = CNT + 24,
              C_TAG = CNT + 60, C_ST = CNT + 40, C_HR = CNT + 32, C_YR = CNT + 36;
constexpr int LDS_BYTES = CNT + 64;
static_assert(PO + 10240 <= XFS && ES + 20480 <= XFS && LDS_BYTES <= 163840, "lds");
constexpr int PSTR = 336, PART = 64 * PSTR;

DI void lds_add(LAS char* c, unsigned v, int lane) {
  if (lane == 0) __hip_atomic_fetch_add((LAS unsigned*)c, v, RLX_WG);
}
DI void lds_wait(LAS char* c, unsigned target) {
  while ((unsigned)__builtin_amdgcn_readfirstlane(__hip_atomic_load((LAS unsigned*)c, RLX_WG)) < target) {}
  asm volatile("" ::: "memory");
}
DI void lds_wait_sleep(LAS char* c, unsigned target) {
  while ((unsigned)__builtin_amdgcn_readfirstlane(__hip_atomic_load((LAS unsigned*)c, RLX_WG)) < target) __builtin_amdgcn_s_sleep(1);
  asm volatile("" ::: "memory");
}
DI unsigned flag_load(char* ws, int off, int n, int lane) {
  return __hip_atomic_load((GAS unsigned*)(ws + off + (lane < n ? lane : 0) * FSTR), RLX_AGENT);
}
DI void poll_flags(char* ws, int off, int n, unsigned tag, int lane) {
#pragma unroll 1
  for (unsigned spins = 0; spins < SPIN_CAP; ++spins) {
    const unsigned v = flag_load(ws, off, n, lane);
    if (__all(v == tag)) break;
    __builtin_amdgcn_s_sleep(POLL_SLEEP);
  }
  __builtin_amdgcn_fence(__ATOMIC_ACQUIRE, "wavefront");
}
DI void set_flag(char* ws, int off, unsigned tag, int lane) {
  if (lane == 0) __hip_atomic_store((GAS unsigned*)(ws + off), tag, RLX_AGENT);
}

DI unsigned opaque0() { unsigned z; asm volatile("v_mov_b32 %0, 0" : "=v"(z)); return z; }
template <int I> struct IC { static constexpr int value = I; };
template <int B, int E, class Fn> DI void static_for(Fn&& f) { if constexpr (B < E) { f(IC<B>{}); static_for<B + 1, E>(f); } }
struct Frags { v4u x[4][2], w[4][2]; };

template <int TS> DI void conv_ld(LAS char* pa, LAS char* pb, v4u (&A)[8], v4u (&B)[8]) {
  constexpr int NA = TS == 0 ? 8 : 6;
#pragma unroll
  for (int a = 0; a < NA; ++a) { A[a] = ldsr128(pa + a * 512); B[a] = ldsr128(pb + (7 - a) * 512); }
}
template <int TS> DI void conv_mm(const v4u (&A)[8], const v4u (&B)[8], f32x4 (&acc)[4]) {
#pragma unroll
  for (int ti = 0; ti < 4; ++ti) {
    const int n = TS == 0 ? (ti == 0 ? 0 : ti == 1 ? 1 : ti == 2 ? 6 : 7) : 2 + ti;
#pragma unroll
    for (int a = 0; a <= n; ++a) acc[ti] = mfma16(A[a], B[n - a], acc[ti]);
  }
}
DI void conv_sched() {
#pragma unroll
  for (int i = 0; i < 16; ++i) { __builtin_amdgcn_sched_group_barrier(0x8, 1, 0); __builtin_amdgcn_sched_group_barrier(0x100, 1, 0); }
  __builtin_amdgcn_sched_group_barrier(0x8, 2, 0);
}
template <int TS> DI void conv8(LAS char* pa, LAS char* pb, f32x4 (&acc)[4]) {
  v4u A0[8], B0[8], A1[8], B1[8];
  conv_ld<TS>(pa, pb, A0, B0);
  SB();
#pragma unroll 1
  for (int r = 0; r < 8; r += 2) {
    conv_ld<TS>(pa + 64, pb + 64, A1, B1); conv_mm<TS>(A0, B0, acc); conv_sched(); SB();
    pa += 128; pb += 128;
    conv_ld<TS>(pa, pb, A0, B0); conv_mm<TS>(A1, B1, acc); conv_sched(); SB();
  }
}
template <int TS> DI void conv2(LAS char* pa, LAS char* pb, f32x4 (&acc)[4]) {
#pragma unroll 1
  for (int r = 0; r < 2; ++r) {
    v4u A[8], B[8];
    conv_ld<TS>(pa, pb, A, B); conv_mm<TS>(A, B, acc);
    pa += 64; pb += 64;
  }
}
DI unsigned kshift2(float k1, float k2, float k3, float k4, float k5, float c0, float c1, float c2, float c3) {
  unsigned a = pack2(k1 * c3, k2 * c3);
  unsigned t = pack2(k2 * c2, k3 * c2);
  a = pack2(bflo(a) + bflo(t), bfhi(a) + bfhi(t));
  t = pack2(k3 * c1, k4 * c1);
  a = pack2(bflo(a) + bflo(t), bfhi(a) + bfhi(t));
  t = pack2(k4 * c0, k5 * c0);
  return pack2(bflo(a) + bflo(t), bfhi(a) + bfhi(t));
}
DI void stage_h(LAS char* Lc, v4u h, int g) {
  *(LAS v4u*)(Lc + CH_HB + 112 + g * 16) = h;
  LAS char* a = Lc + CH_HA + 114 + g * 16;
  *(LAS u16*)(a) = (u16)(h.x & 0xffff);
  *(LAS unsigned*)(a + 2) = __builtin_amdgcn_alignbit(h.y, h.x, 16);
  *(LAS v2u*)(a + 6) = v2u{__builtin_amdgcn_alignbit(h.z, h.y, 16), __builtin_amdgcn_alignbit(h.w, h.z, 16)};
  *(LAS u16*)(a + 14) = (u16)(h.w >> 16);
}
DI void stage_r(LAS char* Lc, v4u u, int g) {
  LAS char* rb = Lc + CH_R + g * 16;
  *(LAS v4u*)(rb) = u;
  { LAS char* q = rb + CH_RS - 4; *(LAS unsigned*)(q) = u.x; *(LAS v2u*)(q + 4) = v2u{u.y, u.z}; *(LAS unsigned*)(q + 12) = u.w; }
  { LAS char* q = rb + 2 * CH_RS - 8; *(LAS v2u*)(q) = v2u{u.x, u.y}; *(LAS v2u*)(q + 8) = v2u{u.z, u.w}; }
  { LAS char* q = rb + 3 * CH_RS - 12; *(LAS unsigned*)(q) = u.x; *(LAS v2u*)(q + 4) = v2u{u.y, u.z}; *(LAS unsigned*)(q + 12) = u.w; }
}
DI void conv_out(LAS char* Lc, f32x4 a, int n, int d, float dv, int e, int kq, __amdgpu_buffer_rsrc_t rws, unsigned tag) {
  const int t = 256 * n + 64 * (e >> 2) + 2 * (e & 3) + 16 * kq;
  const unsigned ua = *(LAS unsigned*)(Lc + CH_R + 2 * (2046 - t));
  const unsigned ub = *(LAS unsigned*)(Lc + CH_R + 2 * (2038 - t));
  const unsigned c01 = pack2(a[0], a[1]), c23 = pack2(a[2], a[3]);
  const unsigned d01 = pack2(bfhi(ua) * dv, bflo(ua) * dv), d23 = pack2(bfhi(ub) * dv, bflo(ub) * dv);
  v2u o;
  o.x = pack2(bflo(c01) + bflo(d01), bfhi(c01) + bfhi(d01));
  o.y = pack2(bflo(c23) + bflo(d23), bfhi(c23) + bfhi(d23));
  const int off = Y_OFF + d * 8192 + (256 * n + 64 * (e >> 2) + 16 * kq + 4 * (e & 3)) * 4;
  __builtin_amdgcn_raw_buffer_store_b128(v4u{o.x, tag, o.y, tag}, rws, off, 0, SC1);
}

__global__ __launch_bounds__(512, 2) void fused(const u16* __restrict__ x, const u16* __restrict__ kk, const u16* __restrict__ vv,
                                                const u16* __restrict__ sk, const u16* __restrict__ hk, const u16* __restrict__ Dv,
                                                const u16* __restrict__ w1, const u16* __restrict__ w2,
                                                char* ws, u16* out) {
  __shared__ __attribute__((aligned(16))) char smem[LDS_BYTES];
  LAS char* L = (LAS char*)smem;
  if (__builtin_amdgcn_readfirstlane(threadIdx.x >> 6) == 7) {
    unsigned long long pc;
    asm volatile("s_getpc_b64 %0" : "=s"(pc));
    const unsigned co = min(((blockIdx.x >> 3) & 7) * 8192u + (threadIdx.x & 63) * 128u, CODE_BYTES);
    asm volatile("s_mov_b32 m0, %0\n\ts_nop 0\n\tglobal_load_lds_dword %1, %2" :: "s"((unsigned)(uintptr_t)(L + XFS)), "v"(co), "s"(pc) : "m0");
  }
  const int b = blockIdx.x, xcd = b & 7, r = b >> 3;
  const int rg = xcd >> 1, hf = xcd & 1, pi = r >> 3, jj = r & 7;
  const int p = 4 * rg + pi, j = 7 * hf + jj;
  const bool g1 = jj < 7;
  const int mbl = 2 * pi + ((r >> 2) & 1), nbl = r & 3;
  const int mb = 8 * rg + mbl, nb = 4 * hf + nbl;
  const int lane = threadIdx.x & 63;
  const int w = __builtin_amdgcn_readfirstlane(threadIdx.x >> 6);
  const int hi = lane >> 5, aw = w & 3;
  STAMP_DECL; STAMP(0);
  const __amdgpu_buffer_rsrc_t rws = mkrsrc(ws, WS_BYTES);
  if (threadIdx.x < 15) *(LAS unsigned*)(L + CNT + threadIdx.x * 4) = 0u;
  const int dpair = 80 * nb + 2 * mb, d3 = 80 * nb + 64 + (mb >> 1), ts3 = mb & 1;
  const int e = lane & 15, kq = lane >> 4;
  const int aoff = ((e & 1) ? CH_HB : CH_HA) + (kq + (e >> 1)) * 16;
  const int boff = CH_R + (3 - (e & 3)) * CH_RS + (248 - 32 * 7 - 8 * (e >> 2) + kq) * 16;

  {
    const __amdgpu_buffer_rsrc_t rk = mkrsrc(kk, S * D * 2), rv_ = mkrsrc(vv, S * D * 2), rsk = mkrsrc(sk, D * 8);
    const int sb = 64 * mb * 1280 + 160 * nb;
    int vo[2];
#pragma unroll
    for (int i = 0; i < 2; ++i) { const int n = (8 * i + w) * 64 + lane; const int row = n / 10; vo[i] = row * 1280 + (n - row * 10) * 16; }
    GLDS(rk, L + PK + w * 1024, vo[0] + sb - 5120, 0);
    GLDS(rv_, L + PV + w * 1024, vo[0], sb);
    {
      const int n = (5 + w) * 64 + lane; const int row = n / 10; const int vo3 = row * 1280 + (n - row * 10) * 16;
      if (w < 3) GLDS(rk, L + PK + (8 + w) * 1024, vo[1] + sb - 5120, 0);
      else if (w < 5) GLDS(rv_, L + PV + (5 + w) * 1024, vo3, sb);
      else if (w == 5) GLDS(rsk, L + PS, lane * 16, nb * 640);
    }
  }
#define PREP_ITEM(tg, cg) do { \
        const int q_ = (lane & 15) >> 2, pp_ = lane & 3, tl = 16 * (tg) + 4 * kq; \
        LAS char* ka_ = L + PK + (tl + q_) * 160 + (cg) * 32 + pp_ * 8; \
        v2u k0 = trread(ka_), k1 = trread(ka_ + 4 * 160); \
        v2u v0 = trread(ka_ + (PV - PK)); \
        v2u sw = *(LAS v2u*)(L + PS + ((cg) * 16 + e) * 8); \
        float kf[8] = {bflo(k0.x), bfhi(k0.x), bflo(k0.y), bfhi(k0.y), bflo(k1.x), bfhi(k1.x), bflo(k1.y), bfhi(k1.y)}; \
        float vf[4] = {bflo(v0.x), bfhi(v0.x), bflo(v0.y), bfhi(v0.y)}; \
        const float c0 = bflo(sw.x), c1 = bfhi(sw.x), c2 = bflo(sw.y), c3 = bfhi(sw.y); \
        float u[4]; \
        _Pragma("unroll") for (int e2 = 0; e2 < 4; e2 += 2) { \
          const unsigned a = kshift2(kf[e2 + 1], kf[e2 + 2], kf[e2 + 3], kf[e2 + 4], kf[e2 + 5], c0, c1, c2, c3); \
          u[e2] = bflo(a) * vf[e2]; u[e2 + 1] = bfhi(a) * vf[e2 + 1]; } \
        v2u o = v2u{pack2(u[3], u[2]), pack2(u[1], u[0])}; \
        const int ch = (cg) * 16 + e; \
        const int pos8 = (60 - tl) >> 2; \
        *(LAS v2u*)(L + PO + ch * 128 + ((((pos8 >> 1) ^ (ch & 7)) << 4) | ((pos8 & 1) << 3))) = o; \
      } while (0)

  if (w >= 4) {
    unsigned gen;
    asm volatile("s_load_dword %0, %1, 0x0\n\ts_waitcnt lgkmcnt(0)" : "=s"(gen) : "s"(ws) : "memory");
    const unsigned tag = gen * 0x9E3779B1u + 0x85EBCA6Bu;
    STAMP(1);
    if (aw == 0 && lane == 0) *(LAS unsigned*)(L + C_TAG) = tag;
    WAITV(0);
    BAR();
    unsigned ab = 4;
#define AUXBAR() do { WAITL(); lds_add(L + C_AUX, 1u, lane); ab += 4; lds_wait(L + C_AUX, ab); } while (0)
    STAMP(2);
    if (ABL & 4) lds_wait_sleep(L + C_MMA, 44);
    {
      const __amdgpu_buffer_rsrc_t rw2 = mkrsrc(w2, F * D * 2);
#pragma unroll
      for (int i = 0; i < 3; ++i) if (4 * i + aw < 10) {
        const int n = (4 * i + aw) * 64 + lane; const int row = n / 10;
        GLDS(rw2, L + PW + (4 * i + aw) * 1024, (row + (row >= 32 ? 992 : 0)) * 1280 + (n - row * 10) * 16, 32 * mb * 1280 + 160 * nb);
      }
    }
    {
      const int g = lane >> 4, q = (lane & 15) >> 2, pp = lane & 3;
      PREP_ITEM(aw, 0); PREP_ITEM(aw, 1); PREP_ITEM(aw, 2);
      AUXBAR();
#pragma unroll
      for (int i = 0; i < 3; ++i) {
        const int ri = aw + 4 * i;
        if (ri < 10) {
          const int ch = 8 * ri + (lane >> 3), cc = lane & 7;
          v4u val = ldsr128(L + PO + ch * 128 + ((cc ^ (ch & 7)) << 4));
          __builtin_amdgcn_raw_buffer_store_b128(val, rws, (ABL & 1) ? -16 : R_OFF + (80 * nb + ch) * 4096 + (31 - mb) * 128 + cc * 16, 0, SC1);
        }
      }
      __builtin_amdgcn_s_setprio(AUX_PRIO);
      if (aw < 2) WAITV(3); else WAITV(2);
      AUXBAR();
      STAMP(3);
#pragma unroll
      for (int i = 0; i < 3; ++i) {
        const int fi = aw + 4 * i;
        if (fi < 10) {
          const int half = fi / 5, nt = fi - half * 5, ks = mb + 32 * half;
          const int r0 = 16 * (g & 1) + 4 * (g >> 1);
          LAS char* a = L + PW + half * 5120 + (r0 + q) * 160 + nt * 32 + pp * 8;
          v2u lo = trread(a), hi2 = trread(a + 8 * 160);
          __builtin_amdgcn_raw_buffer_store_b128(v4u{lo.x, lo.y, hi2.x, hi2.y}, rws, (ks < KS2 && !(ABL & 1)) ? W2P_OFF + nb * W2P_NB + ks * 5120 + nt * 1024 + lane * 16 : -16, 0, SC1);
        }
      }
    }
    STAMP(4);
    WAITV(0);
    AUXBAR();
    if (aw == 0) set_flag(ws, FL_P + (nb * 32 + mb) * FSTR, tag, lane);
    STAMP(5);
    if (ABL & 2) lds_wait_sleep(L + C_MMA, 44);
    const __amdgpu_buffer_rsrc_t rh = mkrsrc(hk, D * S * 2);
    v4u cu[3];
    cu[0] = __builtin_amdgcn_raw_buffer_load_b128(rh, dpair * 4096 + aw * 1024 + lane * 16, 0, 0);
    cu[1] = __builtin_amdgcn_raw_buffer_load_b128(rh, dpair * 4096 + 4096 + aw * 1024 + lane * 16, 0, 0);
    cu[2] = __builtin_amdgcn_raw_buffer_load_b128(rh, d3 * 4096 + aw * 1024 + lane * 16, 0, 0);
    const int g = aw * 64 + lane;
    lds_wait_sleep(L + C_MMA, DELAY_POLL);
    if (aw == 1) {
      const int jc = g1 ? j : 0;
      v2u gr[4];
#define GRL(off) __builtin_bit_cast(v2u, __hip_atomic_load((GAS unsigned long long*)(ws + (off)), RLX_AGENT))
#pragma unroll 1
      for (unsigned sp = 0; sp < SPIN_CAP; ++sp) {
        gr[0] = GRL(ST_OFF + (p * 128 + lane) * 8);
        gr[1] = GRL(ST_OFF + (p * 128 + 64 + lane) * 8);
        gr[2] = GRL(CS_OFF + (jc * 128 + lane) * 8);
        gr[3] = GRL(CS_OFF + (jc * 128 + (j == 13 ? 0 : 64) + lane) * 8);
        if (__all(gr[0].y == tag && gr[1].y == tag && gr[2].y == tag && gr[3].y == tag)) break;
        __builtin_amdgcn_s_sleep(POLL_SLEEP);
      }
      *(LAS f32x2*)(L + XFS + lane * 8) = f32x2{bflo(gr[0].x), 1.f / bfhi(gr[0].x)};
      *(LAS f32x2*)(L + XFS + (64 + lane) * 8) = f32x2{bflo(gr[1].x), 1.f / bfhi(gr[1].x)};
      *(LAS unsigned*)(L + XFC + lane * 4) = gr[2].x;
      *(LAS unsigned*)(L + XFC + (64 + lane) * 4) = gr[3].x;
      WAITL();
      lds_add(L + C_S, 1u, lane);
    }
    if (aw == 2) { poll_flags(ws, FL_P + nb * 32 * FSTR, 32, tag, lane); lds_add(L + C_P, 1u, lane); }
    lds_wait_sleep(L + C_P, 1);
    STAMP(6);
    v4u rv[3];
    rv[0] = __builtin_amdgcn_raw_buffer_load_b128(rws, R_OFF + dpair * 4096 + aw * 1024 + lane * 16, 0, SC1);
    rv[1] = __builtin_amdgcn_raw_buffer_load_b128(rws, R_OFF + dpair * 4096 + 4096 + aw * 1024 + lane * 16, 0, SC1);
    rv[2] = __builtin_amdgcn_raw_buffer_load_b128(rws, R_OFF + d3 * 4096 + aw * 1024 + lane * 16, 0, SC1);
    const int rot2 = ROT2_EXPR;
    const __amdgpu_buffer_rsrc_t ra = mkrsrc(ws + HP_OFF + mb * HP_MB, HP_MB);
    const __amdgpu_buffer_rsrc_t rb = mkrsrc(ws + W2P_OFF + nb * W2P_NB, W2P_NB);
    const int va = lane * 16 + aw * 4096, vb = lane * 16 + aw * 5120;
    constexpr int WB = WB_N, NKS = 14, POOL = POOL_N;
    v4u fa[NKS][4], fb[NKS][5];
#define KIDX(i) ((i) + rot2 >= NKS ? (i) + rot2 - NKS : (i) + rot2)
#define LOADA(i) do { const int so_ = KIDX(i) * 16384; \
    _Pragma("unroll") for (int m = 0; m < 4; ++m) fa[i][m] = __builtin_amdgcn_raw_buffer_load_b128(ra, va + m * 1024, so_, SC1); } while (0)
#define LOADB(i) do { const int so_ = KIDX(i) * 20480; \
    _Pragma("unroll") for (int n = 0; n < 5; ++n) fb[i][n] = __builtin_amdgcn_raw_buffer_load_b128(rb, vb + (n & 3) * 1024, so_ + (n >> 2) * 4096, SC1); } while (0)
#pragma unroll
    for (int i = 0; i < WB; ++i) LOADB(i);
    SB();
    STAMP(7);
    lds_wait_sleep(L + C_MMA, STAGE_START);
    if (aw == 0 && lane < 32) *(LAS v4u*)(L + CV + (lane >> 4) * CH_BYTES + ((lane >> 3) & 1) * CH_HB + (lane & 7) * 16) = v4u{0, 0, 0, 0};
    if (aw == 3) {
#pragma unroll
      for (int i = 0; i < 4; ++i) {
        const int n = lane + 64 * i;
        if (n < 200) { const int cp = n / 25, sl = n - cp * 25; *(LAS v4u*)(L + CV + (cp >> 2) * CH_BYTES + CH_R + (cp & 3) * CH_RS + 4080 + sl * 16) = v4u{0, 0, 0, 0}; }
      }
    }
    stage_h(L + CV, cu[0], g);
    stage_h(L + CV + CH_BYTES, cu[1], g);
    stage_r(L + CV, rv[0], g);
    stage_r(L + CV + CH_BYTES, rv[1], g);
    if (aw == 0 && lane < 16) *(LAS v4u*)(L + CV3 + (lane >> 3) * CH_HB + (lane & 7) * 16) = v4u{0, 0, 0, 0};
    if (aw == 3) {
#pragma unroll
      for (int i = 0; i < 2; ++i) {
        const int n = lane + 64 * i;
        if (n < 100) { const int cp = n / 25, sl = n - cp * 25; *(LAS v4u*)(L + CV3 + CH_R + cp * CH_RS + 4080 + sl * 16) = v4u{0, 0, 0, 0}; }
      }
    }
    stage_h(L + CV3, cu[2], g);
    stage_r(L + CV3, rv[2], g);
    AUXBAR();
    if (aw == 0) lds_add(L + C_ST, 1u, lane);
    STAMP(8);
    lds_wait_sleep(L + C_H, FH_WAIT);
    for (int i = 0; i < H_DELAY; ++i) __builtin_amdgcn_s_sleep(4);
    STAMP(10);
    if (aw == 1) { poll_flags(ws, FL_H + p * 16 * FSTR, 14, tag, lane); lds_add(L + C_HR, 1u, lane); }
    __builtin_amdgcn_s_setprio(G2_PRIO);
    lds_wait_sleep(L + C_HR, 1);
    STAMP(11);
#define ISSUE(q) do { constexpr int q_ = (q); \
      if constexpr (q_ < 4 * WB) { fa[q_ / 4][q_ % 4] = __builtin_amdgcn_raw_buffer_load_b128(ra, va + (q_ % 4) * 1024, KIDX(q_ / 4) * 16384, SC1); } \
      else if constexpr (q_ < 4 * WB + 9 * (NKS - WB)) { constexpr int k_ = WB + (q_ - 4 * WB) / 9, f_ = (q_ - 4 * WB) % 9; \
        if constexpr (f_ < 4) fa[k_][f_] = __builtin_amdgcn_raw_buffer_load_b128(ra, va + f_ * 1024, KIDX(k_) * 16384, SC1); \
        else fb[k_][f_ - 4] = __builtin_amdgcn_raw_buffer_load_b128(rb, vb + ((f_ - 4) & 3) * 1024, KIDX(k_) * 20480 + ((f_ - 4) >> 2) * 4096, SC1); } \
    } while (0)
    constexpr auto need = [](int k) { k = k > NKS ? NKS : k; return k <= WB ? 4 * k : 4 * WB + 9 * (k - WB); };
    constexpr auto cum = [need](int i) { const int a = need(i + LOOK_N), c = POOL - 5 * WB + (i > 0 ? 9 * i - 6 : 0); return a < c ? a : c; };
    static_for<0, cum(0)>([&](auto q) { ISSUE(decltype(q)::value); });
    SB();
    f32x4 acc2[4][5] = {};
    static_for<0, NKS>([&](auto ii) {
      constexpr int i = decltype(ii)::value;
      constexpr int c0 = cum(i), cn = cum(i + 1) - cum(i);
      static_assert(cn <= 9 && c0 >= need(i + 1), "schedule");
      static_for<0, 20>([&](auto mm) {
        constexpr int mn = decltype(mm)::value, m = mn / 5, n = mn % 5;
        acc2[m][n] = mfma16(fb[i][n], fa[i][m], acc2[m][n]);
        constexpr int slot = (mn & 1) ? (mn >> 1) - (9 - cn) : -1;
        if constexpr (slot >= 0 && mn < 19) { SB(); ISSUE(c0 + slot); SB(); }
      });
      SB();
      if (i == 0) STAMP(12);
    });
    STAMP(13);
    lds_wait(L + C_Y, 4);
    const int ln = __builtin_amdgcn_mbcnt_hi(~0u, __builtin_amdgcn_mbcnt_lo(~0u, opaque0()));
#pragma unroll
    for (int m = 0; m < 4; ++m)
#pragma unroll
      for (int n = 0; n < 5; ++n)
        *(LAS f32x4*)(L + aw * PART + (16 * m + (ln & 15)) * PSTR + n * 64 + (ln >> 4) * 16) = acc2[m][n];
  } else {
    __builtin_amdgcn_s_setprio(MMA_PRIO);
    const __amdgpu_buffer_rsrc_t rw = mkrsrc(w1, D * F * 2);
    unsigned tag;
    if (g1) {
      const __amdgpu_buffer_rsrc_t rx = mkrsrc(x + (size_t)p * 128 * D, 128 * D * 2);
      int vx[2], vw[4];
      {
        const int row = 32 * w + (lane >> 3);
        vx[0] = row * 1280 + (((lane & 7) ^ ((row >> 1) & 7)) << 4);
        vx[1] = (row + 8) * 1280 + (((lane & 7) ^ (((row + 8) >> 1) & 7)) << 4);
#pragma unroll
        for (int ii = 0; ii < 4; ++ii) {
          const int kr = 16 * w + 4 * ii + (lane >> 4);
          vw[ii] = kr * (F * 2) + j * 256 + (((lane & 15) ^ (((kr & 3) << 2) | ((kr >> 2) & 3))) << 4);
        }
      }
      const int rot = ROT_EXPR;
      const unsigned lbase = (unsigned)(uintptr_t)L + w * 4096;
#define KC(s) ((s) + rot >= 10 ? (s) + rot - 10 : (s) + rot)
#define G1LD(s, i) do { \
    if ((i) < 4) glds_fast(rx, lbase + ((s) % NBUF) * 32768 + (i) * 1024, vx[(i) & 1], KC(s) * 128 + ((i) >> 1) * 16 * 1280); \
    else glds_fast(rw, lbase + ((s) % NBUF) * 32768 + 16384 + ((i) - 4) * 1024, vw[((i) - 4) & 3], KC(s) * (64 * F * 2)); \
  } while (0)
#pragma unroll
      for (int i = 0; i < 8; ++i) G1LD(0, i);
#pragma unroll
      for (int i = 0; i < 8; ++i) G1LD(1, i);
      WAITV(16);
      BAR();
      PREP_ITEM(aw, 3); PREP_ITEM(aw, 4);
      WAITL(); lds_add(L + C_AUX, 1u, lane);
      SB();
      STAMP(1);
      const int fh = w & 1, th = w >> 1;
      int xa[4], wa[2][2];
      {
        const int row = 64 * th + (lane & 31);
        const int c0 = hi ^ ((row >> 1) & 7);
        const int blk = (lane >> 4) & 1, q = (lane & 15) >> 2, pp = lane & 3;
#pragma unroll
        for (int s = 0; s < 4; ++s) xa[s] = row * 128 + (((2 * s) ^ c0) << 4);
#pragma unroll
        for (int ft = 0; ft < 2; ++ft)
#pragma unroll
          for (int t = 0; t < 2; ++t) {
            const int kr = 8 * hi + 4 * t + q;
            const int ch = 4 * (2 * fh + ft) + 2 * blk + (pp >> 1);
            wa[ft][t] = 16384 + 256 * kr + ((ch ^ ((q << 2) | ((2 * hi + t) & 3))) << 4) + 8 * (pp & 1);
          }
      }
      f32x16 acc[2][2] = {};
      Frags fA, fB;
#define RDX(f, bo, s, tt) f.x[s][tt] = ldsr128(L + (bo) + xa[s] + (tt) * 4096)
#define RDW(f, bo, s, ft) do { v2u wl_ = trread(L + (bo) + wa[ft][0] + (s) * 4096), wh_ = trread(L + (bo) + wa[ft][1] + (s) * 4096); f.w[s][ft] = v4u{wl_.x, wl_.y, wh_.x, wh_.y}; } while (0)
#define MM(f, s, ft, tt) acc[ft][tt] = mfma32(f.w[s][ft], f.x[s][tt], acc[ft][tt])
#define ITER(s, vm, cur, nxt) do { \
    asm volatile("s_waitcnt vmcnt(" #vm ") lgkmcnt(0)" ::: "memory"); \
    lds_add(L + C_MMA, 1u, lane); SB(); \
    MM(cur, 0, 0, 0); MM(cur, 0, 0, 1); MM(cur, 0, 1, 0); SB(); \
    lds_wait(L + C_MMA, 4 * ((s) + 2)); SB(); \
    constexpr int bo_ = (((s) + 1) % NBUF) * 32768; constexpr bool ld_ = (s) + NBUF < 10; \
    MM(cur, 0, 1, 1); RDX(nxt, bo_, 0, 0); RDX(nxt, bo_, 0, 1); SB(); if (ld_) G1LD((s) + NBUF, 0); SB(); \
    MM(cur, 1, 0, 0); RDW(nxt, bo_, 0, 0); SB(); \
    MM(cur, 1, 0, 1); RDW(nxt, bo_, 0, 1); SB(); if (ld_) G1LD((s) + NBUF, 4); SB(); \
    MM(cur, 1, 1, 0); RDX(nxt, bo_, 1, 0); RDX(nxt, bo_, 1, 1); SB(); if (ld_) G1LD((s) + NBUF, 1); SB(); \
    MM(cur, 1, 1, 1); RDW(nxt, bo_, 1, 0); SB(); \
    MM(cur, 2, 0, 0); RDW(nxt, bo_, 1, 1); SB(); if (ld_) G1LD((s) + NBUF, 5); SB(); \
    MM(cur, 2, 0, 1); RDX(nxt, bo_, 2, 0); RDX(nxt, bo_, 2, 1); SB(); if (ld_) G1LD((s) + NBUF, 2); SB(); \
    MM(cur, 2, 1, 0); RDW(nxt, bo_, 2, 0); SB(); \
    MM(cur, 2, 1, 1); RDW(nxt, bo_, 2, 1); SB(); if (ld_) G1LD((s) + NBUF, 6); SB(); \
    MM(cur, 3, 0, 0); RDX(nxt, bo_, 3, 0); RDX(nxt, bo_, 3, 1); SB(); if (ld_) G1LD((s) + NBUF, 3); SB(); \
    MM(cur, 3, 0, 1); RDW(nxt, bo_, 3, 0); RDW(nxt, bo_, 3, 1); SB(); \
    MM(cur, 3, 1, 0); SB(); if (ld_) G1LD((s) + NBUF, 7); SB(); \
    MM(cur, 3, 1, 1); SB(); \
  } while (0)
      WAITV(8);
      lds_add(L + C_MMA, 1u, lane); lds_wait(L + C_MMA, 4);
      STAMP(2);
#pragma unroll
      for (int s = 0; s < 4; ++s) { RDX(fA, 0, s, 0); RDX(fA, 0, s, 1); RDW(fA, 0, s, 0); RDW(fA, 0, s, 1); }
      SB();
#pragma unroll
      for (int i = 0; i < 8; ++i) G1LD(2, i);
      SB();
      ITER(0, 8, fA, fB);
      ITER(1, 8, fB, fA);
      ITER(2, 8, fA, fB);
      STAMP(3);
      ITER(3, 8, fB, fA);
      ITER(4, 8, fA, fB);
      ITER(5, 8, fB, fA);
      STAMP(4);
      ITER(6, 8, fA, fB);
      ITER(7, 8, fB, fA);
      ITER(8, 0, fA, fB);
#pragma unroll
      for (int s = 0; s < 4; ++s) { MM(fB, s, 0, 0); MM(fB, s, 0, 1); MM(fB, s, 1, 0); MM(fB, s, 1, 1); }
      SB();
      lds_add(L + C_MMA, 1u, lane);
      STAMP(5);
      lds_wait_sleep(L + C_S, 1);
      tag = __builtin_amdgcn_readfirstlane(*(LAS unsigned*)(L + C_TAG));
      STAMP(6);
#pragma unroll
      for (int ft = 0; ft < 2; ++ft) {
        const int fg = 2 * fh + ft;
        const int ks = 4 * j + fg;
        f32x4 c4[4];
#pragma unroll
        for (int g = 0; g < 4; ++g) c4[g] = *(LAS f32x4*)(L + XFC + (32 * fg + 8 * g + 4 * hi) * 4);
#pragma unroll
        for (int tt = 0; tt < 2; ++tt) {
          const int tok = 64 * th + 32 * tt + (lane & 31);
          const f32x2 st = *(LAS f32x2*)(L + XFS + tok * 8);
          const f32x16& a = acc[ft][tt];
          const float zs1 = -1.44269504088896f * st.y, dn1 = __builtin_amdgcn_rcpf(st.y);
          const f32x2 zs = f32x2{zs1, zs1}, dn = f32x2{dn1, dn1};
          const int mb2 = 2 * p + th, mt = 2 * tt + ((lane >> 4) & 1);
          const int off = ks < KS2 ? HP_OFF + mb2 * HP_MB + ks * 4096 + mt * 1024 + ((2 * hi) * 16 + (lane & 15)) * 16 : -1024;
#pragma unroll
          for (int hh = 0; hh < 2; ++hh) {
            unsigned pk[4];
#pragma unroll
            for (int e2 = 0; e2 < 4; ++e2) {
              const int ee = hh * 4 + e2;
              const f32x2 cc = f32x2{c4[ee >> 1][(2 * ee) & 3], c4[ee >> 1][(2 * ee + 1) & 3]};
              const f32x2 dd = __builtin_elementwise_fma(cc, f32x2{-st.x, -st.x}, f32x2{a[2 * ee], a[2 * ee + 1]});
              const f32x2 zz = dd * zs;
              const f32x2 ex = f32x2{__builtin_amdgcn_exp2f(zz.x), __builtin_amdgcn_exp2f(zz.y)};
              const f32x2 ss = __builtin_elementwise_fma(ex, dn, dn);
              const f32x2 hv2 = dd * f32x2{__builtin_amdgcn_rcpf(ss.x), __builtin_amdgcn_rcpf(ss.y)};
              pk[e2] = pack2(hv2.x, hv2.y);
            }
            __builtin_amdgcn_raw_buffer_store_b128(v4u{pk[0], pk[1], pk[2], pk[3]}, rws, off + hh * 256, 0, SC1);
            SB();
          }
        }
      }
      lds_add(L + C_MMA, 1u, lane);
      STAMP(7);
      WAITV(0);
      {
        unsigned old = 0;
        if (lane == 0) old = __hip_atomic_fetch_add((LAS unsigned*)(L + C_H), 1u, RLX_WG);
        if (__builtin_amdgcn_readfirstlane(old) == 3) set_flag(ws, FL_H + (p * 16 + j) * FSTR, tag, lane);
      }
      STAMP(8);
    } else {
      const int idx16 = 4 * rg + pi;
      const int srow = 512 * rg + 64 * (4 * hf + pi) + 16 * w + (lane >> 3);
      const int cg = 14 * hf + idx16;
      const bool csv = idx16 < 14 - hf;
      const __amdgpu_buffer_rsrc_t rx = mkrsrc(x, S * D * 2);
      v4u xv[2][10], wv[20];
#pragma unroll
      for (int h2 = 0; h2 < 2; ++h2)
#pragma unroll
        for (int i = 0; i < 10; ++i) xv[h2][i] = __builtin_amdgcn_raw_buffer_load_b128(rx, (srow + 8 * h2) * 1280 + (lane & 7) * 16 + i * 128, 0, 0);
      SB();
      WAITV(20);
      BAR();
      PREP_ITEM(aw, 3); PREP_ITEM(aw, 4);
      WAITL(); lds_add(L + C_AUX, 1u, lane);
      SB();
      const int wvo = csv ? (160 * w + (lane >> 3)) * (F * 2) + cg * 128 + (lane & 7) * 16 : -16;
#pragma unroll
      for (int i = 0; i < 20; ++i) wv[i] = __builtin_amdgcn_raw_buffer_load_b128(rw, wvo, csv ? i * 8 * F * 2 : 0, 0);
      SB();
      tag = __builtin_amdgcn_readfirstlane(*(LAS unsigned*)(L + C_TAG));
#pragma unroll
      for (int h2 = 0; h2 < 2; ++h2) {
        float s = 0.f;
#pragma unroll
        for (int i = 0; i < 10; ++i)
#pragma unroll
          for (int e2 = 0; e2 < 4; ++e2) s = dot2(xv[h2][i][e2], 0x3f803f80u, s);
        s += __shfl_xor(s, 1); s += __shfl_xor(s, 2); s += __shfl_xor(s, 4);
        const float mean = s * (1.f / D);
        float q0 = 0.f, q1 = 0.f;
#pragma unroll
        for (int i = 0; i < 10; ++i)
#pragma unroll
          for (int e2 = 0; e2 < 4; ++e2) { const float a0 = bflo(xv[h2][i][e2]) - mean, a1 = bfhi(xv[h2][i][e2]) - mean; q0 = fmaf(a0, a0, q0); q1 = fmaf(a1, a1, q1); }
        float q = q0 + q1;
        q += __shfl_xor(q, 1); q += __shfl_xor(q, 2); q += __shfl_xor(q, 4);
        const float den = __builtin_sqrtf(rbf(rbf(q * (1.f / D)) + 1e-5f));
        __builtin_amdgcn_raw_buffer_store_b64(v2u{pack2(mean, den), tag}, rws, (lane & 7) == 0 ? ST_OFF + (srow + 8 * h2) * 8 : -16, 0, SC1);
      }
      float c[8] = {};
#pragma unroll
      for (int i = 0; i < 20; ++i)
#pragma unroll
        for (int e2 = 0; e2 < 4; ++e2) { c[2 * e2] += bflo(wv[i][e2]); c[2 * e2 + 1] += bfhi(wv[i][e2]); }
      *(LAS f32x4*)(L + ((w * 8 + (lane >> 3)) * 64 + (lane & 7) * 8) * 4) = f32x4{c[0], c[1], c[2], c[3]};
      *(LAS f32x4*)(L + ((w * 8 + (lane >> 3)) * 64 + (lane & 7) * 8 + 4) * 4) = f32x4{c[4], c[5], c[6], c[7]};
      WAITL();
      unsigned old = 0;
      if (lane == 0) old = __hip_atomic_fetch_add((LAS unsigned*)(L + C_H), 1u, RLX_WG);
      if (__builtin_amdgcn_readfirstlane(old) == 3) {
        asm volatile("" ::: "memory");
        float t4[4] = {};
#pragma unroll
        for (int i = 0; i < 32; ++i) t4[i & 3] += *(LAS float*)(L + (i * 64 + lane) * 4);
        __builtin_amdgcn_raw_buffer_store_b64(v2u{__float_as_uint((t4[0] + t4[1]) + (t4[2] + t4[3])), tag}, rws, csv ? CS_OFF + (cg * 64 + lane) * 8 : -16, 0, SC1);
      }
      lds_add(L + C_MMA, 12u, lane);
      STAMP(5);
      if (b == 56 && w == 0) {
#pragma unroll 1
        for (unsigned sp = 0; sp < SPIN_CAP; ++sp) {
          bool ok = true;
#pragma unroll
          for (int i = 0; i < 4; ++i) ok = ok && flag_load(ws, FL_P + i * 64 * FSTR, 64, lane) == tag;
          if (__all(ok)) break;
          __builtin_amdgcn_s_sleep(32);
        }
        set_flag(ws, FL_GEN, tag, lane);
      }
    }
    unsigned hb_ = 0;
#define HBAR() do { WAITL(); lds_add(L + C_G2, 1u, lane); hb_ += 4; lds_wait(L + C_G2, hb_); } while (0)
    {
      const __amdgpu_buffer_rsrc_t rxx = mkrsrc(x, S * D * 2);
      lds_wait_sleep(L + C_MMA, 48);
#pragma unroll
      for (int i = 0; i < 3; ++i) {
        const int s = aw + 4 * i;
        if (s < 10) {
          const int n = s * 64 + lane; const int row = n / 10, c = n - row * 10;
          GLDS(rxx, L + EX + s * 1024, (64 * mb + row) * 1280 + 160 * nb + c * 16, 0);
        }
      }
      const float dv = bflo(Dv[dpair + (aw >> 1)]), dv3 = bflo(Dv[d3]);
      lds_wait_sleep(L + C_ST, 1);
      __builtin_amdgcn_s_setprio(C8_PRIO);
      LAS char* Lc = L + CV + (aw >> 1) * CH_BYTES;
      const int cts = aw & 1;
      f32x4 acc[4] = {};
      if (cts == 0) conv8<0>(Lc + aoff, Lc + boff, acc); else conv8<1>(Lc + aoff, Lc + boff, acc);
      __builtin_amdgcn_s_setprio(MMA_PRIO);
      STAMP(9);
#pragma unroll
      for (int ti = 0; ti < 4; ++ti) conv_out(Lc, acc[ti], cts == 0 ? (ti < 2 ? ti : ti + 4) : 2 + ti, dpair + (aw >> 1), dv, e, kq, rws, tag);
      f32x4 ac3[4] = {};
      __builtin_amdgcn_s_setprio(C3_PRIO);
      if (ts3 == 0) conv2<0>(L + CV3 + aoff + aw * 128, L + CV3 + boff + aw * 128, ac3); else conv2<1>(L + CV3 + aoff + aw * 128, L + CV3 + boff + aw * 128, ac3);
      __builtin_amdgcn_s_setprio(MMA_PRIO);
      STAMP(13);
#pragma unroll
      for (int ti = 0; ti < 4; ++ti) *(LAS f32x4*)(L + SC3 + (aw * 4 + ti) * 1024 + lane * 16) = ac3[ti];
      WAITV(4);
      HBAR();
      f32x4 a = {};
#pragma unroll
      for (int ww = 0; ww < 4; ++ww) a += *(LAS f32x4*)(L + SC3 + (ww * 4 + aw) * 1024 + lane * 16);
      conv_out(L + CV3, a, ts3 == 0 ? (aw < 2 ? aw : aw + 4) : 2 + aw, d3, dv3, e, kq, rws, tag);
    }
    STAMP(10);
    {
      const int q = (lane & 15) >> 2, pp = lane & 3, pos = lane & 15;
      const int tok = 16 * aw + 2 * (pos >> 2) + (pos & 1) + 8 * ((pos >> 1) & 1);
      lds_wait_sleep(L + C_S, 1);
      const f32x2 st = *(LAS f32x2*)(L + XFS + ((mb & 1) * 64 + tok) * 8);
      v4u xn[5];
#pragma unroll
      for (int i = 0; i < 5; ++i) {
        const v2u xv2 = *(LAS v2u*)(L + EX + tok * 160 + (4 * i + kq) * 8);
        const unsigned d01 = pack2(bflo(xv2.x) - st.x, bfhi(xv2.x) - st.x), d23 = pack2(bflo(xv2.y) - st.x, bfhi(xv2.y) - st.x);
        xn[i] = v4u{pack2(bflo(d01) * st.y, bfhi(d01) * st.y), pack2(bflo(d23) * st.y, bfhi(d23) * st.y), xv2.x, xv2.y};
      }
      WAITL();
      lds_add(L + C_Y, 1u, lane);
      for (int i = 0; i < Y_DELAY; ++i) __builtin_amdgcn_s_sleep(4);
      STAMP(11);
      {
        v4u yv[5];
        const int yo = Y_OFF + (80 * nb + 4 * aw + kq) * 8192 + mb * 256 + e * 16;
#pragma unroll 1
        for (unsigned sp = 0; sp < SPIN_CAP; ++sp) {
          asm volatile("" ::: "memory");
          bool ok = true;
#pragma unroll
          for (int i = 0; i < 5; ++i) yv[i] = __builtin_amdgcn_raw_buffer_load_b128(rws, yo, i * 16 * 8192, SC1);
#pragma unroll
          for (int i = 0; i < 5; ++i) ok = ok && yv[i].y == tag && yv[i].w == tag;
          if (__all(ok)) break;
          __builtin_amdgcn_s_sleep(POLL_SLEEP);
        }
#pragma unroll
        for (int i = 0; i < 5; ++i) *(LAS v2u*)(L + EY + (16 * i + 4 * aw + kq) * 144 + e * 8) = v2u{yv[i].x, yv[i].z};
      }
      HBAR();
      STAMP(12);
#pragma unroll
      for (int i = 0; i < 5; ++i) {
        const int dq = 4 * i + kq;
        const v2u yv = trread(L + EY + (4 * dq + q) * 144 + aw * 32 + pp * 8);
        const unsigned s01 = pack2(bflo(xn[i].x) * bflo(yv.x), bfhi(xn[i].x) * bfhi(yv.x)), s23 = pack2(bflo(xn[i].y) * bflo(yv.y), bfhi(xn[i].y) * bfhi(yv.y));
        *(LAS v4u*)(L + ES + (tok * 20 + dq) * 16) = v4u{s01, s23, xn[i].z, xn[i].w};
      }
    }
  }
  STAMP(14);
  BAR();
  const int lane2 = __builtin_amdgcn_mbcnt_hi(~0u, __builtin_amdgcn_mbcnt_lo(~0u, opaque0()));
#pragma unroll
  for (int i = 0; i < 3; ++i) {
    const int it = w + 8 * i;
    if (it < 20) {
      const int n = it * 64 + lane2;
      const int tok = n / 20, dq = n - tok * 20;
      f32x4 o = *(LAS f32x4*)(L + tok * PSTR + dq * 16);
#pragma unroll
      for (int ww = 1; ww < 4; ++ww) o += *(LAS f32x4*)(L + ww * PART + tok * PSTR + dq * 16);
      const v4u sx = *(LAS v4u*)(L + ES + n * 16);
      const unsigned o01 = pack2(o[0], o[1]), o23 = pack2(o[2], o[3]);
      const unsigned g01 = pack2(silu_f(bflo(o01)), silu_f(bfhi(o01))), g23 = pack2(silu_f(bflo(o23)), silu_f(bfhi(o23)));
      const unsigned m01 = pack2(bflo(sx.x) * bflo(g01), bfhi(sx.x) * bfhi(g01)), m23 = pack2(bflo(sx.y) * bflo(g23), bfhi(sx.y) * bfhi(g23));
      v2u res;
      res.x = pack2(bflo(sx.z) + bflo(m01), bfhi(sx.z) + bfhi(m01));
      res.y = pack2(bflo(sx.w) + bflo(m23), bfhi(sx.w) + bfhi(m23));
      *(v2u*)(out + (size_t)(64 * mb + tok) * D + 80 * nb + dq * 4) = res;
    }
  }
  STAMP(15);
  STAMP_OUT(0);
}

extern "C" void kernel_launch(void* const* d_in, const int* in_sizes, int n_in,
                              void* d_out, int out_size, void* d_ws, size_t ws_size,
                              hipStream_t stream) {
  fused<<<256, 512, 0, stream>>>((const u16*)d_in[0], (const u16*)d_in[1], (const u16*)d_in[2], (const u16*)d_in[3],
                                 (const u16*)d_in[4], (const u16*)d_in[5], (const u16*)d_in[7], (const u16*)d_in[8],
                                 (char*)d_ws, (u16*)d_out);
}
```

```cpp
#include <hip/hip_runtime.h>
#include <stdint.h>

typedef unsigned short u16;
typedef unsigned v4u __attribute__((ext_vector_type(4)));
typedef unsigned v2u __attribute__((ext_vector_type(2)));
typedef float f32x2 __attribute__((ext_vector_type(2)));
typedef float f32x4 __attribute__((ext_vector_type(4)));
typedef float f32x16 __attribute__((ext_vector_type(16)));
typedef short bf16x8 __attribute__((ext_vector_type(8)));
typedef short s16x4 __attribute__((ext_vector_type(4)));
typedef __bf16 bf16x2 __attribute__((ext_vector_type(2)));
#define LAS __attribute__((address_space(3)))
#define GAS __attribute__((address_space(1)))
#define DI __device__ __forceinline__
#define RLX_AGENT __ATOMIC_RELAXED, __HIP_MEMORY_SCOPE_AGENT
#define RLX_WG __ATOMIC_RELAXED, __HIP_MEMORY_SCOPE_WORKGROUP

constexpr int S = 2048, D = 640, F = 1728;
constexpr int KS2 = 54;
constexpr int FSTR = 128;
constexpr int FL_GEN = 0, FL_P = 131072, FL_H = FL_P + 256 * 256;
constexpr int ST_OFF = 32768;
constexpr int CS_OFF = ST_OFF + S * 8;
constexpr int HP_OFF = 262144;
constexpr int HP_MB = KS2 * 4096;
constexpr int W2P_OFF = HP_OFF + 32 * HP_MB;
constexpr int W2P_NB = KS2 * 5120;
constexpr int R_OFF = W2P_OFF + 8 * W2P_NB;
constexpr int Y_OFF = R_OFF + D * S * 2;
constexpr int WS_BYTES = Y_OFF + D * S * 4;
constexpr unsigned RSRC3 = 0x00020000u;
constexpr int SC1 = 16;
constexpr unsigned SPIN_CAP = 1u << 18;

DI float bflo(unsigned u) { return __uint_as_float(u << 16); }
DI float bfhi(unsigned u) { return __uint_as_float(u & 0xffff0000u); }
DI unsigned pack2(float a, float b) {
  bf16x2 r = __builtin_convertvector(f32x2{a, b}, bf16x2);
  return __builtin_bit_cast(unsigned, r);
}
DI float rbf(float a) { return bflo(pack2(a, a)); }
DI float silu_f(float x) { return x * __builtin_amdgcn_rcpf(1.f + __builtin_amdgcn_exp2f(-1.44269504088896f * x)); }
DI f32x4 mfma16(v4u a, v4u b, f32x4 c) {
  return __builtin_amdgcn_mfma_f32_16x16x32_bf16(__builtin_bit_cast(bf16x8, a), __builtin_bit_cast(bf16x8, b), c, 0, 0, 0);
}
DI f32x16 mfma32(v4u a, v4u b, f32x16 c) {
  return __builtin_amdgcn_mfma_f32_32x32x16_bf16(__builtin_bit_cast(bf16x8, a), __builtin_bit_cast(bf16x8, b), c, 0, 0, 0);
}
DI float dot2(unsigned a, unsigned b, float c) {
  return __builtin_amdgcn_fdot2_f32_bf16(__builtin_bit_cast(bf16x2, a), __builtin_bit_cast(bf16x2, b), c, false);
}
DI v2u trread(LAS char* p) { return __builtin_bit_cast(v2u, __builtin_amdgcn_ds_read_tr16_b64_v4i16((LAS s16x4*)p)); }
DI v4u ldsr128(LAS char* p) { return *(LAS v4u*)p; }
DI __amdgpu_buffer_rsrc_t mkrsrc(const void* p, int bytes) { return __builtin_amdgcn_make_buffer_rsrc((void*)p, 0, bytes, RSRC3); }
DI void glds_asm(__amdgpu_buffer_rsrc_t rs, unsigned ldsa, int voff, int soff) {
  asm volatile("s_nop 4\n\ts_mov_b32 m0, %0\n\ts_nop 0\n\tbuffer_load_dwordx4 %1, %2, %3 offen lds"
               :: "s"(ldsa), "v"(voff), "s"(rs), "s"(soff) : "m0", "memory");
}
DI void glds_fast(__amdgpu_buffer_rsrc_t rs, unsigned ldsa, int voff, int soff) {
  asm volatile("s_mov_b32 m0, %0\n\ts_nop 0\n\tbuffer_load_dwordx4 %1, %2, %3 offen lds"
               :: "s"(ldsa), "v"(voff), "s"(rs), "s"(soff) : "m0", "memory");
}
#define GLDS(rs, ldsp, voff, soff) glds_asm(rs, (unsigned)(uintptr_t)(ldsp), voff, soff)
#define WAITV(n) asm volatile("s_waitcnt vmcnt(" #n ")" ::: "memory")
#define WAITL() asm volatile("s_waitcnt lgkmcnt(0)" ::: "memory")
#define BAR() do { WAITL(); __builtin_amdgcn_s_barrier(); asm volatile("" ::: "memory"); } while (0)
#define SB() __builtin_amdgcn_sched_barrier(0)


constexpr int NBUF = 3;
constexpr int CH_HA = 0, CH_HB = 4224, CH_R = 8448, CH_RS = 4512, CH_BYTES = CH_R + 4 * CH_RS;
constexpr int CV = 98304;
constexpr int CV3 = 32768;
constexpr int SC3 = 65536;
constexpr int PK = CV, PV = PK + 11264, PW = PV + 10240, PS = PW + 10240, PO = PS + 1024;
constexpr int EY = CV, EX = 0, ES = EY + 80 * 144;
constexpr int XFS = CV + 2 * CH_BYTES;
constexpr int XFC = XFS + 1024;
constexpr int CNT = XFC + 512;
constexpr int C_AUX = CNT, C_G2 = CNT + 4, C_Y = CNT + 8, C_H = CNT + 12, C_MMA = CNT + 16, C_S = CNT + 20, C_P = CNT + 24,
              C_TAG = CNT + 60, C_ST = CNT + 40, C_HR = CNT + 32, C_YR = CNT + 36;
constexpr int LDS_BYTES = CNT + 64;
static_assert(PO + 10240 <= XFS && ES + 20480 <= XFS && LDS_BYTES <= 163840, "lds");
constexpr int PSTR = 336, PART = 64 * PSTR;

DI void lds_add(LAS char* c, unsigned v, int lane) {
  if (lane == 0) __hip_atomic_fetch_add((LAS unsigned*)c, v, RLX_WG);
}
DI void lds_wait(LAS char* c, unsigned target) {
  while ((unsigned)__builtin_amdgcn_readfirstlane(__hip_atomic_load((LAS unsigned*)c, RLX_WG)) < target) {}
  asm volatile("" ::: "memory");
}
DI void lds_wait_sleep(LAS char* c, unsigned target) {
  while ((unsigned)__builtin_amdgcn_readfirstlane(__hip_atomic_load((LAS unsigned*)c, RLX_WG)) < target) __builtin_amdgcn_s_sleep(1);
  asm volatile("" ::: "memory");
}
DI unsigned flag_load(char* ws, int off, int n, int lane) {
  return __hip_atomic_load((GAS unsigned*)(ws + off + (lane < n ? lane : 0) * FSTR), RLX_AGENT);
}
DI void poll_flags(char* ws, int off, int n, unsigned tag, int lane) {
#pragma unroll 1
  for (unsigned spins = 0; spins < SPIN_CAP; ++spins) {
    const unsigned v = flag_load(ws, off, n, lane);
    if (__all(v == tag)) break;
    __builtin_amdgcn_s_sleep(8);
  }
  __builtin_amdgcn_fence(__ATOMIC_ACQUIRE, "wavefront");
}
DI void set_flag(char* ws, int off, unsigned tag, int lane) {
  if (lane == 0) __hip_atomic_store((GAS unsigned*)(ws + off), tag, RLX_AGENT);
}

DI unsigned opaque0() { unsigned z; asm volatile("v_mov_b32 %0, 0" : "=v"(z)); return z; }
template <int I> struct IC { static constexpr int value = I; };
template <int B, int E, class Fn> DI void static_for(Fn&& f) { if constexpr (B < E) { f(IC<B>{}); static_for<B + 1, E>(f); } }
struct Frags { v4u x[4][2], w[4][2]; };

template <int TS> DI void conv_ld(LAS char* pa, LAS char* pb, v4u (&A)[8], v4u (&B)[8]) {
  constexpr int NA = TS == 0 ? 8 : 6;
#pragma unroll
  for (int a = 0; a < NA; ++a) { A[a] = ldsr128(pa + a * 512); B[a] = ldsr128(pb + (7 - a) * 512); }
}
template <int TS> DI void conv_mm(const v4u (&A)[8], const v4u (&B)[8], f32x4 (&acc)[4]) {
#pragma unroll
  for (int ti = 0; ti < 4; ++ti) {
    const int n = TS == 0 ? (ti == 0 ? 0 : ti == 1 ? 1 : ti == 2 ? 6 : 7) : 2 + ti;
#pragma unroll
    for (int a = 0; a <= n; ++a) acc[ti] = mfma16(A[a], B[n - a], acc[ti]);
  }
}
DI void conv_sched() {
#pragma unroll
  for (int i = 0; i < 16; ++i) { __builtin_amdgcn_sched_group_barrier(0x8, 1, 0); __builtin_amdgcn_sched_group_barrier(0x100, 1, 0); }
  __builtin_amdgcn_sched_group_barrier(0x8, 2, 0);
}
template <int TS> DI void conv8(LAS char* pa, LAS char* pb, f32x4 (&acc)[4]) {
  v4u A0[8], B0[8], A1[8], B1[8];
  conv_ld<TS>(pa, pb, A0, B0);
  SB();
#pragma unroll 1
  for (int r = 0; r < 8; r += 2) {
    conv_ld<TS>(pa + 64, pb + 64, A1, B1); conv_mm<TS>(A0, B0, acc); conv_sched(); SB();
    pa += 128; pb += 128;
    conv_ld<TS>(pa, pb, A0, B0); conv_mm<TS>(A1, B1, acc); conv_sched(); SB();
  }
}
template <int TS> DI void conv2(LAS char* pa, LAS char* pb, f32x4 (&acc)[4]) {
#pragma unroll 1
  for (int r = 0; r < 2; ++r) {
    v4u A[8], B[8];
    conv_ld<TS>(pa, pb, A, B); conv_mm<TS>(A, B, acc);
    pa += 64; pb += 64;
  }
}
DI unsigned kshift2(float k1, float k2, float k3, float k4, float k5, float c0, float c1, float c2, float c3) {
  unsigned a = pack2(k1 * c3, k2 * c3);
  unsigned t = pack2(k2 * c2, k3 * c2);
  a = pack2(bflo(a) + bflo(t), bfhi(a) + bfhi(t));
  t = pack2(k3 * c1, k4 * c1);
  a = pack2(bflo(a) + bflo(t), bfhi(a) + bfhi(t));
  t = pack2(k4 * c0, k5 * c0);
  return pack2(bflo(a) + bflo(t), bfhi(a) + bfhi(t));
}
DI void stage_h(LAS char* Lc, v4u h, int g) {
  *(LAS v4u*)(Lc + CH_HB + 112 + g * 16) = h;
  LAS char* a = Lc + CH_HA + 114 + g * 16;
  *(LAS u16*)(a) = (u16)(h.x & 0xffff);
  *(LAS unsigned*)(a + 2) = __builtin_amdgcn_alignbit(h.y, h.x, 16);
  *(LAS v2u*)(a + 6) = v2u{__builtin_amdgcn_alignbit(h.z, h.y, 16), __builtin_amdgcn_alignbit(h.w, h.z, 16)};
  *(LAS u16*)(a + 14) = (u16)(h.w >> 16);
}
DI void stage_r(LAS char* Lc, v4u u, int g) {
  LAS char* rb = Lc + CH_R + g * 16;
  *(LAS v4u*)(rb) = u;
  { LAS char* q = rb + CH_RS - 4; *(LAS unsigned*)(q) = u.x; *(LAS v2u*)(q + 4) = v2u{u.y, u.z}; *(LAS unsigned*)(q + 12) = u.w; }
  { LAS char* q = rb + 2 * CH_RS - 8; *(LAS v2u*)(q) = v2u{u.x, u.y}; *(LAS v2u*)(q + 8) = v2u{u.z, u.w}; }
  { LAS char* q = rb + 3 * CH_RS - 12; *(LAS unsigned*)(q) = u.x; *(LAS v2u*)(q + 4) = v2u{u.y, u.z}; *(LAS unsigned*)(q + 12) = u.w; }
}
DI void conv_out(LAS char* Lc, f32x4 a, int n, int d, float dv, int e, int kq, __amdgpu_buffer_rsrc_t rws, unsigned tag) {
  const int t = 256 * n + 64 * (e >> 2) + 2 * (e & 3) + 16 * kq;
  const unsigned ua = *(LAS unsigned*)(Lc + CH_R + 2 * (2046 - t));
  const unsigned ub = *(LAS unsigned*)(Lc + CH_R + 2 * (2038 - t));
  const unsigned c01 = pack2(a[0], a[1]), c23 = pack2(a[2], a[3]);
  const unsigned d01 = pack2(bfhi(ua) * dv, bflo(ua) * dv), d23 = pack2(bfhi(ub) * dv, bflo(ub) * dv);
  v2u o;
  o.x = pack2(bflo(c01) + bflo(d01), bfhi(c01) + bfhi(d01));
  o.y = pack2(bflo(c23) + bflo(d23), bfhi(c23) + bfhi(d23));
  const int off = Y_OFF + d * 8192 + (256 * n + 64 * (e >> 2) + 16 * kq + 4 * (e & 3)) * 4;
  __builtin_amdgcn_raw_buffer_store_b128(v4u{o.x, tag, o.y, tag}, rws, off, 0, SC1);
}

__global__ __launch_bounds__(512, 2) void fused(const u16* __restrict__ x, const u16* __restrict__ kk, const u16* __restrict__ vv,
                                                const u16* __restrict__ sk, const u16* __restrict__ hk, const u16* __restrict__ Dv,
                                                const u16* __restrict__ w1, const u16* __restrict__ w2,
                                                char* ws, u16* out) {
  __shared__ __attribute__((aligned(16))) char smem[LDS_BYTES];
  LAS char* L = (LAS char*)smem;
  if (__builtin_amdgcn_readfirstlane(threadIdx.x >> 6) == 7) {
    unsigned long long pc;
    asm volatile("s_getpc_b64 %0" : "=s"(pc));
    const unsigned co = min(((blockIdx.x >> 3) & 7) * 8192u + (threadIdx.x & 63) * 128u, 39000u);
    asm volatile("s_mov_b32 m0, %0\n\ts_nop 0\n\tglobal_load_lds_dword %1, %2" :: "s"((unsigned)(uintptr_t)(L + XFS)), "v"(co), "s"(pc) : "m0");
  }
  const int b = blockIdx.x, xcd = b & 7, r = b >> 3;
  const int rg = xcd >> 1, hf = xcd & 1, pi = r >> 3, jj = r & 7;
  const int p = 4 * rg + pi, j = 7 * hf + jj;
  const bool g1 = jj < 7;
  const int mbl = 2 * pi + ((r >> 2) & 1), nbl = r & 3;
  const int mb = 8 * rg + mbl, nb = 4 * hf + nbl;
  const int lane = threadIdx.x & 63;
  const int w = __builtin_amdgcn_readfirstlane(threadIdx.x >> 6);
  const int hi = lane >> 5, aw = w & 3;
  const __amdgpu_buffer_rsrc_t rws = mkrsrc(ws, WS_BYTES);
  if (threadIdx.x < 15) *(LAS unsigned*)(L + CNT + threadIdx.x * 4) = 0u;
  const int dpair = 80 * nb + 2 * mb, d3 = 80 * nb + 64 + (mb >> 1), ts3 = mb & 1;
  const int e = lane & 15, kq = lane >> 4;
  const int aoff = ((e & 1) ? CH_HB : CH_HA) + (kq + (e >> 1)) * 16;
  const int boff = CH_R + (3 - (e & 3)) * CH_RS + (248 - 32 * 7 - 8 * (e >> 2) + kq) * 16;

  {
    const __amdgpu_buffer_rsrc_t rk = mkrsrc(kk, S * D * 2), rv_ = mkrsrc(vv, S * D * 2), rsk = mkrsrc(sk, D * 8);
    const int sb = 64 * mb * 1280 + 160 * nb;
    int vo[2];
#pragma unroll
    for (int i = 0; i < 2; ++i) { const int n = (8 * i + w) * 64 + lane; const int row = n / 10; vo[i] = row * 1280 + (n - row * 10) * 16; }
    GLDS(rk, L + PK + w * 1024, vo[0] + sb - 5120, 0);
    GLDS(rv_, L + PV + w * 1024, vo[0], sb);
    {
      const int n = (5 + w) * 64 + lane; const int row = n / 10; const int vo3 = row * 1280 + (n - row * 10) * 16;
      if (w < 3) GLDS(rk, L + PK + (8 + w) * 1024, vo[1] + sb - 5120, 0);
      else if (w < 5) GLDS(rv_, L + PV + (5 + w) * 1024, vo3, sb);
      else if (w == 5) GLDS(rsk, L + PS, lane * 16, nb * 640);
    }
  }
#define PREP_ITEM(tg, cg) do { \
        const int q_ = (lane & 15) >> 2, pp_ = lane & 3, tl = 16 * (tg) + 4 * kq; \
        LAS char* ka_ = L + PK + (tl + q_) * 160 + (cg) * 32 + pp_ * 8; \
        v2u k0 = trread(ka_), k1 = trread(ka_ + 4 * 160); \
        v2u v0 = trread(ka_ + (PV - PK)); \
        v2u sw = *(LAS v2u*)(L + PS + ((cg) * 16 + e) * 8); \
        float kf[8] = {bflo(k0.x), bfhi(k0.x), bflo(k0.y), bfhi(k0.y), bflo(k1.x), bfhi(k1.x), bflo(k1.y), bfhi(k1.y)}; \
        float vf[4] = {bflo(v0.x), bfhi(v0.x), bflo(v0.y), bfhi(v0.y)}; \
        const float c0 = bflo(sw.x), c1 = bfhi(sw.x), c2 = bflo(sw.y), c3 = bfhi(sw.y); \
        float u[4]; \
        _Pragma("unroll") for (int e2 = 0; e2 < 4; e2 += 2) { \
          const unsigned a = kshift2(kf[e2 + 1], kf[e2 + 2], kf[e2 + 3], kf[e2 + 4], kf[e2 + 5], c0, c1, c2, c3); \
          u[e2] = bflo(a) * vf[e2]; u[e2 + 1] = bfhi(a) * vf[e2 + 1]; } \
        v2u o = v2u{pack2(u[3], u[2]), pack2(u[1], u[0])}; \
        const int ch = (cg) * 16 + e; \
        const int pos8 = (60 - tl) >> 2; \
        *(LAS v2u*)(L + PO + ch * 128 + ((((pos8 >> 1) ^ (ch & 7)) << 4) | ((pos8 & 1) << 3))) = o; \
      } while (0)

  if (w >= 4) {
    const unsigned gen = __hip_atomic_load((GAS unsigned*)(ws + FL_GEN), RLX_AGENT);
    WAITV(0);
    const unsigned tag = (unsigned)__builtin_amdgcn_readfirstlane(gen) * 0x9E3779B1u + 0x85EBCA6Bu;
    if (aw == 0 && lane == 0) *(LAS unsigned*)(L + C_TAG) = tag;
#define AUXBAR() do { WAITL(); lds_add(L + C_AUX, 1u, lane); ab += 4; lds_wait(L + C_AUX, ab); } while (0)
    BAR();
    unsigned ab = 4;
    {
      const __amdgpu_buffer_rsrc_t rw2 = mkrsrc(w2, F * D * 2);
#pragma unroll
      for (int i = 0; i < 3; ++i) if (4 * i + aw < 10) {
        const int n = (4 * i + aw) * 64 + lane; const int row = n / 10;
        GLDS(rw2, L + PW + (4 * i + aw) * 1024, (row + (row >= 32 ? 992 : 0)) * 1280 + (n - row * 10) * 16, 32 * mb * 1280 + 160 * nb);
      }
    }
    {
      const int g = lane >> 4, q = (lane & 15) >> 2, pp = lane & 3;
      PREP_ITEM(aw, 0); PREP_ITEM(aw, 1); PREP_ITEM(aw, 2);
      AUXBAR();
#pragma unroll
      for (int i = 0; i < 3; ++i) {
        const int ri = aw + 4 * i;
        if (ri < 10) {
          const int ch = 8 * ri + (lane >> 3), cc = lane & 7;
          v4u val = ldsr128(L + PO + ch * 128 + ((cc ^ (ch & 7)) << 4));
          __builtin_amdgcn_raw_buffer_store_b128(val, rws, R_OFF + (80 * nb + ch) * 4096 + (31 - mb) * 128 + cc * 16, 0, SC1);
        }
      }
      __builtin_amdgcn_s_setprio(0);
      if (aw < 2) WAITV(3); else WAITV(2);
      AUXBAR();
#pragma unroll
      for (int i = 0; i < 3; ++i) {
        const int fi = aw + 4 * i;
        if (fi < 10) {
          const int half = fi / 5, nt = fi - half * 5, ks = mb + 32 * half;
          const int r0 = 16 * (g & 1) + 4 * (g >> 1);
          LAS char* a = L + PW + half * 5120 + (r0 + q) * 160 + nt * 32 + pp * 8;
          v2u lo = trread(a), hi2 = trread(a + 8 * 160);
          __builtin_amdgcn_raw_buffer_store_b128(v4u{lo.x, lo.y, hi2.x, hi2.y}, rws, ks < KS2 ? W2P_OFF + nb * W2P_NB + ks * 5120 + nt * 1024 + lane * 16 : -16, 0, SC1);
        }
      }
    }
    WAITV(0);
    AUXBAR();
    if (aw == 0) set_flag(ws, FL_P + (nb * 32 + mb) * FSTR, tag, lane);
    const __amdgpu_buffer_rsrc_t rh = mkrsrc(hk, D * S * 2);
    v4u cu[3];
    cu[0] = __builtin_amdgcn_raw_buffer_load_b128(rh, dpair * 4096 + aw * 1024 + lane * 16, 0, 0);
    cu[1] = __builtin_amdgcn_raw_buffer_load_b128(rh, dpair * 4096 + 4096 + aw * 1024 + lane * 16, 0, 0);
    cu[2] = __builtin_amdgcn_raw_buffer_load_b128(rh, d3 * 4096 + aw * 1024 + lane * 16, 0, 0);
    const int g = aw * 64 + lane;
    lds_wait_sleep(L + C_MMA, 32);
    if (aw == 1) {
      const int jc = g1 ? j : 0;
      v2u gr[4];
#define GRL(off) __builtin_bit_cast(v2u, __hip_atomic_load((GAS unsigned long long*)(ws + (off)), RLX_AGENT))
#pragma unroll 1
      for (unsigned sp = 0; sp < SPIN_CAP; ++sp) {
        gr[0] = GRL(ST_OFF + (p * 128 + lane) * 8);
        gr[1] = GRL(ST_OFF + (p * 128 + 64 + lane) * 8);
        gr[2] = GRL(CS_OFF + (jc * 128 + lane) * 8);
        gr[3] = GRL(CS_OFF + (jc * 128 + (j == 13 ? 0 : 64) + lane) * 8);
        if (__all(gr[0].y == tag && gr[1].y == tag && gr[2].y == tag && gr[3].y == tag)) break;
        __builtin_amdgcn_s_sleep(8);
      }
      *(LAS f32x2*)(L + XFS + lane * 8) = f32x2{bflo(gr[0].x), 1.f / bfhi(gr[0].x)};
      *(LAS f32x2*)(L + XFS + (64 + lane) * 8) = f32x2{bflo(gr[1].x), 1.f / bfhi(gr[1].x)};
      *(LAS unsigned*)(L + XFC + lane * 4) = gr[2].x;
      *(LAS unsigned*)(L + XFC + (64 + lane) * 4) = gr[3].x;
      WAITL();
      lds_add(L + C_S, 1u, lane);
    }
    if (aw == 2) { poll_flags(ws, FL_P + nb * 32 * FSTR, 32, tag, lane); lds_add(L + C_P, 1u, lane); }
    lds_wait_sleep(L + C_P, 1);
    v4u rv[3];
    rv[0] = __builtin_amdgcn_raw_buffer_load_b128(rws, R_OFF + dpair * 4096 + aw * 1024 + lane * 16, 0, SC1);
    rv[1] = __builtin_amdgcn_raw_buffer_load_b128(rws, R_OFF + dpair * 4096 + 4096 + aw * 1024 + lane * 16, 0, SC1);
    rv[2] = __builtin_amdgcn_raw_buffer_load_b128(rws, R_OFF + d3 * 4096 + aw * 1024 + lane * 16, 0, SC1);
    const int rot2 = 0;
    const __amdgpu_buffer_rsrc_t ra = mkrsrc(ws + HP_OFF + mb * HP_MB, HP_MB);
    const __amdgpu_buffer_rsrc_t rb = mkrsrc(ws + W2P_OFF + nb * W2P_NB, W2P_NB);
    const int va = lane * 16 + aw * 4096, vb = lane * 16 + aw * 5120;
    constexpr int WB = 6, NKS = 14, POOL = 41;
    v4u fa[NKS][4], fb[NKS][5];
#define KIDX(i) ((i) + rot2 >= NKS ? (i) + rot2 - NKS : (i) + rot2)
#define LOADA(i) do { const int so_ = KIDX(i) * 16384; \
    _Pragma("unroll") for (int m = 0; m < 4; ++m) fa[i][m] = __builtin_amdgcn_raw_buffer_load_b128(ra, va + m * 1024, so_, SC1); } while (0)
#define LOADB(i) do { const int so_ = KIDX(i) * 20480; \
    _Pragma("unroll") for (int n = 0; n < 5; ++n) fb[i][n] = __builtin_amdgcn_raw_buffer_load_b128(rb, vb + (n & 3) * 1024, so_ + (n >> 2) * 4096, SC1); } while (0)
#pragma unroll
    for (int i = 0; i < WB; ++i) LOADB(i);
    SB();
    lds_wait_sleep(L + C_MMA, 44);
    if (aw == 0 && lane < 32) *(LAS v4u*)(L + CV + (lane >> 4) * CH_BYTES + ((lane >> 3) & 1) * CH_HB + (lane & 7) * 16) = v4u{0, 0, 0, 0};
    if (aw == 3) {
#pragma unroll
      for (int i = 0; i < 4; ++i) {
        const int n = lane + 64 * i;
        if (n < 200) { const int cp = n / 25, sl = n - cp * 25; *(LAS v4u*)(L + CV + (cp >> 2) * CH_BYTES + CH_R + (cp & 3) * CH_RS + 4080 + sl * 16) = v4u{0, 0, 0, 0}; }
      }
    }
    stage_h(L + CV, cu[0], g);
    stage_h(L + CV + CH_BYTES, cu[1], g);
    stage_r(L + CV, rv[0], g);
    stage_r(L + CV + CH_BYTES, rv[1], g);
    if (aw == 0 && lane < 16) *(LAS v4u*)(L + CV3 + (lane >> 3) * CH_HB + (lane & 7) * 16) = v4u{0, 0, 0, 0};
    if (aw == 3) {
#pragma unroll
      for (int i = 0; i < 2; ++i) {
        const int n = lane + 64 * i;
        if (n < 100) { const int cp = n / 25, sl = n - cp * 25; *(LAS v4u*)(L + CV3 + CH_R + cp * CH_RS + 4080 + sl * 16) = v4u{0, 0, 0, 0}; }
      }
    }
    stage_h(L + CV3, cu[2], g);
    stage_r(L + CV3, rv[2], g);
    AUXBAR();
    if (aw == 0) lds_add(L + C_ST, 1u, lane);
    lds_wait_sleep(L + C_H, 4);
    if (aw == 1) { poll_flags(ws, FL_H + p * 16 * FSTR, 14, tag, lane); lds_add(L + C_HR, 1u, lane); }
    __builtin_amdgcn_s_setprio(0);
    lds_wait_sleep(L + C_HR, 1);
#define ISSUE(q) do { constexpr int q_ = (q); \
      if constexpr (q_ < 4 * WB) { fa[q_ / 4][q_ % 4] = __builtin_amdgcn_raw_buffer_load_b128(ra, va + (q_ % 4) * 1024, KIDX(q_ / 4) * 16384, SC1); } \
      else if constexpr (q_ < 4 * WB + 9 * (NKS - WB)) { constexpr int k_ = WB + (q_ - 4 * WB) / 9, f_ = (q_ - 4 * WB) % 9; \
        if constexpr (f_ < 4) fa[k_][f_] = __builtin_amdgcn_raw_buffer_load_b128(ra, va + f_ * 1024, KIDX(k_) * 16384, SC1); \
        else fb[k_][f_ - 4] = __builtin_amdgcn_raw_buffer_load_b128(rb, vb + ((f_ - 4) & 3) * 1024, KIDX(k_) * 20480 + ((f_ - 4) >> 2) * 4096, SC1); } \
    } while (0)
    constexpr auto need = [](int k) { k = k > NKS ? NKS : k; return k <= WB ? 4 * k : 4 * WB + 9 * (k - WB); };
    constexpr auto cum = [need](int i) { const int a = need(i + 5), c = POOL - 5 * WB + (i > 0 ? 9 * i - 6 : 0); return a < c ? a : c; };
    static_for<0, cum(0)>([&](auto q) { ISSUE(decltype(q)::value); });
    SB();
#define PIN_ACC() do { _Pragma("unroll") for (int m_ = 0; m_ < 4; ++m_) _Pragma("unroll") for (int n_ = 0; n_ < 5; ++n_) asm volatile("" : "+v"(acc2[m_][n_])); } while (0)
    f32x4 acc2[4][5] = {};
    LAS char* pw = L;
    static_for<0, NKS>([&](auto ii) {
      constexpr int i = decltype(ii)::value;
      constexpr int c0 = cum(i), cn = cum(i + 1) - cum(i);
      static_assert(cn <= 9 && c0 >= need(i + 1), "schedule");
      static_for<0, 20>([&](auto mm) {
        constexpr int mn = decltype(mm)::value, m = mn / 5, n = mn % 5;
        acc2[m][n] = mfma16(fb[i][n], fa[i][m], acc2[m][n]);
        constexpr int slot = (mn & 1) ? (mn >> 1) - (9 - cn) : -1;
        if constexpr (slot >= 0 && mn < 19) { SB(); ISSUE(c0 + slot); SB(); }
        if constexpr (i == NKS - 1 && mn >= 2) { constexpr int m2 = (mn - 2) / 5, n2 = (mn - 2) % 5; SB(); *(LAS f32x4*)(pw + 16 * m2 * PSTR + n2 * 64) = acc2[m2][n2]; SB(); }
      });
      SB();
      if constexpr (i == NKS - 2) {
        PIN_ACC();
        lds_wait(L + C_Y, 4);
        const int ln = __builtin_amdgcn_mbcnt_hi(~0u, __builtin_amdgcn_mbcnt_lo(~0u, opaque0()));
        pw = L + aw * PART + (ln & 15) * PSTR + (ln >> 4) * 16;
        SB();
      }
    });
    *(LAS f32x4*)(pw + 48 * PSTR + 3 * 64) = acc2[3][3];
    *(LAS f32x4*)(pw + 48 * PSTR + 4 * 64) = acc2[3][4];
  } else {
    __builtin_amdgcn_s_setprio(2);
    const __amdgpu_buffer_rsrc_t rw = mkrsrc(w1, D * F * 2);
    unsigned tag;
    if (g1) {
      const __amdgpu_buffer_rsrc_t rx = mkrsrc(x + (size_t)p * 128 * D, 128 * D * 2);
      int vx[2], vw[4];
      {
        const int row = 32 * w + (lane >> 3);
        vx[0] = row * 1280 + (((lane & 7) ^ ((row >> 1) & 7)) << 4);
        vx[1] = (row + 8) * 1280 + (((lane & 7) ^ (((row + 8) >> 1) & 7)) << 4);
#pragma unroll
        for (int ii = 0; ii < 4; ++ii) {
          const int kr = 16 * w + 4 * ii + (lane >> 4);
          vw[ii] = kr * (F * 2) + j * 256 + (((lane & 15) ^ (((kr & 3) << 2) | ((kr >> 2) & 3))) << 4);
        }
      }
      const int rot = (5 * ((pi + jj) & 1) + (5 * xcd) / 8) % 10;
      const unsigned lbase = (unsigned)(uintptr_t)L + w * 4096;
#define KC(s) ((s) + rot >= 10 ? (s) + rot - 10 : (s) + rot)
#define G1LD(s, i) do { \
    if ((i) < 4) glds_fast(rx, lbase + ((s) % NBUF) * 32768 + (i) * 1024, vx[(i) & 1], KC(s) * 128 + ((i) >> 1) * 16 * 1280); \
    else glds_fast(rw, lbase + ((s) % NBUF) * 32768 + 16384 + ((i) - 4) * 1024, vw[((i) - 4) & 3], KC(s) * (64 * F * 2)); \
  } while (0)
#pragma unroll
      for (int i = 0; i < 8; ++i) G1LD(0, i);
#pragma unroll
      for (int i = 0; i < 8; ++i) G1LD(1, i);
      WAITV(16);
      BAR();
      PREP_ITEM(aw, 3); PREP_ITEM(aw, 4);
      WAITL(); lds_add(L + C_AUX, 1u, lane);
      SB();
      const int fh = w & 1, th = w >> 1;
      int xa[4], wa[2][2];
      {
        const int row = 64 * th + (lane & 31);
        const int c0 = hi ^ ((row >> 1) & 7);
        const int blk = (lane >> 4) & 1, q = (lane & 15) >> 2, pp = lane & 3;
#pragma unroll
        for (int s = 0; s < 4; ++s) xa[s] = row * 128 + (((2 * s) ^ c0) << 4);
#pragma unroll
        for (int ft = 0; ft < 2; ++ft)
#pragma unroll
          for (int t = 0; t < 2; ++t) {
            const int kr = 8 * hi + 4 * t + q;
            const int ch = 4 * (2 * fh + ft) + 2 * blk + (pp >> 1);
            wa[ft][t] = 16384 + 256 * kr + ((ch ^ ((q << 2) | ((2 * hi + t) & 3))) << 4) + 8 * (pp & 1);
          }
      }
      f32x16 acc[2][2] = {};
      Frags fA, fB;
#define RDX(f, bo, s, tt) f.x[s][tt] = ldsr128(L + (bo) + xa[s] + (tt) * 4096)
#define RDW(f, bo, s, ft) do { v2u wl_ = trread(L + (bo) + wa[ft][0] + (s) * 4096), wh_ = trread(L + (bo) + wa[ft][1] + (s) * 4096); f.w[s][ft] = v4u{wl_.x, wl_.y, wh_.x, wh_.y}; } while (0)
#define MM(f, s, ft, tt) acc[ft][tt] = mfma32(f.w[s][ft], f.x[s][tt], acc[ft][tt])
#define ITER(s, vm, cur, nxt) do { \
    asm volatile("s_waitcnt vmcnt(" #vm ") lgkmcnt(0)" ::: "memory"); \
    lds_add(L + C_MMA, 1u, lane); SB(); \
    MM(cur, 0, 0, 0); MM(cur, 0, 0, 1); MM(cur, 0, 1, 0); SB(); \
    lds_wait(L + C_MMA, 4 * ((s) + 2)); SB(); \
    constexpr int bo_ = (((s) + 1) % NBUF) * 32768; constexpr bool ld_ = (s) + NBUF < 10; \
    MM(cur, 0, 1, 1); RDX(nxt, bo_, 0, 0); RDX(nxt, bo_, 0, 1); SB(); if (ld_) G1LD((s) + NBUF, 0); SB(); \
    MM(cur, 1, 0, 0); RDW(nxt, bo_, 0, 0); SB(); \
    MM(cur, 1, 0, 1); RDW(nxt, bo_, 0, 1); SB(); if (ld_) G1LD((s) + NBUF, 4); SB(); \
    MM(cur, 1, 1, 0); RDX(nxt, bo_, 1, 0); RDX(nxt, bo_, 1, 1); SB(); if (ld_) G1LD((s) + NBUF, 1); SB(); \
    MM(cur, 1, 1, 1); RDW(nxt, bo_, 1, 0); SB(); \
    MM(cur, 2, 0, 0); RDW(nxt, bo_, 1, 1); SB(); if (ld_) G1LD((s) + NBUF, 5); SB(); \
    MM(cur, 2, 0, 1); RDX(nxt, bo_, 2, 0); RDX(nxt, bo_, 2, 1); SB(); if (ld_) G1LD((s) + NBUF, 2); SB(); \
    MM(cur, 2, 1, 0); RDW(nxt, bo_, 2, 0); SB(); \
    MM(cur, 2, 1, 1); RDW(nxt, bo_, 2, 1); SB(); if (ld_) G1LD((s) + NBUF, 6); SB(); \
    MM(cur, 3, 0, 0); RDX(nxt, bo_, 3, 0); RDX(nxt, bo_, 3, 1); SB(); if (ld_) G1LD((s) + NBUF, 3); SB(); \
    MM(cur, 3, 0, 1); RDW(nxt, bo_, 3, 0); RDW(nxt, bo_, 3, 1); SB(); \
    MM(cur, 3, 1, 0); SB(); if (ld_) G1LD((s) + NBUF, 7); SB(); \
    MM(cur, 3, 1, 1); SB(); \
  } while (0)
      WAITV(8);
      lds_add(L + C_MMA, 1u, lane); lds_wait(L + C_MMA, 4);
#pragma unroll
      for (int s = 0; s < 4; ++s) { RDX(fA, 0, s, 0); RDX(fA, 0, s, 1); RDW(fA, 0, s, 0); RDW(fA, 0, s, 1); }
      SB();
#pragma unroll
      for (int i = 0; i < 8; ++i) G1LD(2, i);
      SB();
      ITER(0, 8, fA, fB);
      ITER(1, 8, fB, fA);
      ITER(2, 8, fA, fB);
      ITER(3, 8, fB, fA);
      ITER(4, 8, fA, fB);
      ITER(5, 8, fB, fA);
      ITER(6, 8, fA, fB);
      ITER(7, 8, fB, fA);
      ITER(8, 0, fA, fB);
#pragma unroll
      for (int s = 0; s < 4; ++s) { MM(fB, s, 0, 0); MM(fB, s, 0, 1); MM(fB, s, 1, 0); MM(fB, s, 1, 1); }
      SB();
      lds_add(L + C_MMA, 1u, lane);
      lds_wait_sleep(L + C_S, 1);
      tag = __builtin_amdgcn_readfirstlane(*(LAS unsigned*)(L + C_TAG));
#pragma unroll
      for (int ft = 0; ft < 2; ++ft) {
        const int fg = 2 * fh + ft;
        const int ks = 4 * j + fg;
        f32x4 c4[4];
#pragma unroll
        for (int g = 0; g < 4; ++g) c4[g] = *(LAS f32x4*)(L + XFC + (32 * fg + 8 * g + 4 * hi) * 4);
#pragma unroll
        for (int tt = 0; tt < 2; ++tt) {
          const int tok = 64 * th + 32 * tt + (lane & 31);
          const f32x2 st = *(LAS f32x2*)(L + XFS + tok * 8);
          const f32x16& a = acc[ft][tt];
          const float zs1 = -1.44269504088896f * st.y, dn1 = __builtin_amdgcn_rcpf(st.y);
          const f32x2 zs = f32x2{zs1, zs1}, dn = f32x2{dn1, dn1};
          const int mb2 = 2 * p + th, mt = 2 * tt + ((lane >> 4) & 1);
          const int off = ks < KS2 ? HP_OFF + mb2 * HP_MB + ks * 4096 + mt * 1024 + ((2 * hi) * 16 + (lane & 15)) * 16 : -1024;
#pragma unroll
          for (int hh = 0; hh < 2; ++hh) {
            unsigned pk[4];
#pragma unroll
            for (int e2 = 0; e2 < 4; ++e2) {
              const int ee = hh * 4 + e2;
              const f32x2 cc = f32x2{c4[ee >> 1][(2 * ee) & 3], c4[ee >> 1][(2 * ee + 1) & 3]};
              const f32x2 dd = __builtin_elementwise_fma(cc, f32x2{-st.x, -st.x}, f32x2{a[2 * ee], a[2 * ee + 1]});
              const f32x2 zz = dd * zs;
              const f32x2 ex = f32x2{__builtin_amdgcn_exp2f(zz.x), __builtin_amdgcn_exp2f(zz.y)};
              const f32x2 ss = __builtin_elementwise_fma(ex, dn, dn);
              const f32x2 hv2 = dd * f32x2{__builtin_amdgcn_rcpf(ss.x), __builtin_amdgcn_rcpf(ss.y)};
              pk[e2] = pack2(hv2.x, hv2.y);
            }
            __builtin_amdgcn_raw_buffer_store_b128(v4u{pk[0], pk[1], pk[2], pk[3]}, rws, off + hh * 256, 0, SC1);
            SB();
          }
        }
      }
      lds_add(L + C_MMA, 1u, lane);
      WAITV(0);
      {
        unsigned old = 0;
        if (lane == 0) old = __hip_atomic_fetch_add((LAS unsigned*)(L + C_H), 1u, RLX_WG);
        if (__builtin_amdgcn_readfirstlane(old) == 3) set_flag(ws, FL_H + (p * 16 + j) * FSTR, tag, lane);
      }
    } else {
      const int idx16 = 4 * rg + pi;
      const int srow = 512 * rg + 64 * (4 * hf + pi) + 16 * w + (lane >> 3);
      const int cg = 14 * hf + idx16;
      const bool csv = idx16 < 14 - hf;
      const __amdgpu_buffer_rsrc_t rx = mkrsrc(x, S * D * 2);
      v4u xv[2][10], wv[20];
#pragma unroll
      for (int h2 = 0; h2 < 2; ++h2)
#pragma unroll
        for (int i = 0; i < 10; ++i) xv[h2][i] = __builtin_amdgcn_raw_buffer_load_b128(rx, (srow + 8 * h2) * 1280 + (lane & 7) * 16 + i * 128, 0, 0);
      SB();
      WAITV(20);
      BAR();
      PREP_ITEM(aw, 3); PREP_ITEM(aw, 4);
      WAITL(); lds_add(L + C_AUX, 1u, lane);
      SB();
      const int wvo = csv ? (160 * w + (lane >> 3)) * (F * 2) + cg * 128 + (lane & 7) * 16 : -16;
#pragma unroll
      for (int i = 0; i < 20; ++i) wv[i] = __builtin_amdgcn_raw_buffer_load_b128(rw, wvo, csv ? i * 8 * F * 2 : 0, 0);
      SB();
      tag = __builtin_amdgcn_readfirstlane(*(LAS unsigned*)(L + C_TAG));
#pragma unroll
      for (int h2 = 0; h2 < 2; ++h2) {
        float s = 0.f;
#pragma unroll
        for (int i = 0; i < 10; ++i)
#pragma unroll
          for (int e2 = 0; e2 < 4; ++e2) s = dot2(xv[h2][i][e2], 0x3f803f80u, s);
        s += __shfl_xor(s, 1); s += __shfl_xor(s, 2); s += __shfl_xor(s, 4);
        const float mean = s * (1.f / D);
        float q0 = 0.f, q1 = 0.f;
#pragma unroll
        for (int i = 0; i < 10; ++i)
#pragma unroll
          for (int e2 = 0; e2 < 4; ++e2) { const float a0 = bflo(xv[h2][i][e2]) - mean, a1 = bfhi(xv[h2][i][e2]) - mean; q0 = fmaf(a0, a0, q0); q1 = fmaf(a1, a1, q1); }
        float q = q0 + q1;
        q += __shfl_xor(q, 1); q += __shfl_xor(q, 2); q += __shfl_xor(q, 4);
        const float den = __builtin_sqrtf(rbf(rbf(q * (1.f / D)) + 1e-5f));
        __builtin_amdgcn_raw_buffer_store_b64(v2u{pack2(mean, den), tag}, rws, (lane & 7) == 0 ? ST_OFF + (srow + 8 * h2) * 8 : -16, 0, SC1);
      }
      float c[8] = {};
#pragma unroll
      for (int i = 0; i < 20; ++i)
#pragma unroll
        for (int e2 = 0; e2 < 4; ++e2) { c[2 * e2] += bflo(wv[i][e2]); c[2 * e2 + 1] += bfhi(wv[i][e2]); }
      *(LAS f32x4*)(L + ((w * 8 + (lane >> 3)) * 64 + (lane & 7) * 8) * 4) = f32x4{c[0], c[1], c[2], c[3]};
      *(LAS f32x4*)(L + ((w * 8 + (lane >> 3)) * 64 + (lane & 7) * 8 + 4) * 4) = f32x4{c[4], c[5], c[6], c[7]};
      WAITL();
      unsigned old = 0;
      if (lane == 0) old = __hip_atomic_fetch_add((LAS unsigned*)(L + C_H), 1u, RLX_WG);
      if (__builtin_amdgcn_readfirstlane(old) == 3) {
        asm volatile("" ::: "memory");
        float t4[4] = {};
#pragma unroll
        for (int i = 0; i < 32; ++i) t4[i & 3] += *(LAS float*)(L + (i * 64 + lane) * 4);
        __builtin_amdgcn_raw_buffer_store_b64(v2u{__float_as_uint((t4[0] + t4[1]) + (t4[2] + t4[3])), tag}, rws, csv ? CS_OFF + (cg * 64 + lane) * 8 : -16, 0, SC1);
      }
      lds_add(L + C_MMA, 12u, lane);
      if (b == 56 && w == 0) {
#pragma unroll 1
        for (unsigned sp = 0; sp < SPIN_CAP; ++sp) {
          bool ok = true;
#pragma unroll
          for (int i = 0; i < 4; ++i) ok = ok && flag_load(ws, FL_P + i * 64 * FSTR, 64, lane) == tag;
          if (__all(ok)) break;
          __builtin_amdgcn_s_sleep(32);
        }
        set_flag(ws, FL_GEN, tag, lane);
      }
    }
    unsigned hb_ = 0;
#define HBAR() do { WAITL(); lds_add(L + C_G2, 1u, lane); hb_ += 4; lds_wait(L + C_G2, hb_); } while (0)
    {
      const __amdgpu_buffer_rsrc_t rxx = mkrsrc(x, S * D * 2);
      lds_wait_sleep(L + C_MMA, 48);
#pragma unroll
      for (int i = 0; i < 3; ++i) {
        const int s = aw + 4 * i;
        if (s < 10) {
          const int n = s * 64 + lane; const int row = n / 10, c = n - row * 10;
          GLDS(rxx, L + EX + s * 1024, (64 * mb + row) * 1280 + 160 * nb + c * 16, 0);
        }
      }
      const float dv = bflo(Dv[dpair + (aw >> 1)]), dv3 = bflo(Dv[d3]);
      lds_wait_sleep(L + C_ST, 1);
      __builtin_amdgcn_s_setprio(1);
      LAS char* Lc = L + CV + (aw >> 1) * CH_BYTES;
      const int cts = aw & 1;
      f32x4 acc[4] = {};
      if (cts == 0) conv8<0>(Lc + aoff, Lc + boff, acc); else conv8<1>(Lc + aoff, Lc + boff, acc);
      __builtin_amdgcn_s_setprio(2);
#pragma unroll
      for (int ti = 0; ti < 4; ++ti) conv_out(Lc, acc[ti], cts == 0 ? (ti < 2 ? ti : ti + 4) : 2 + ti, dpair + (aw >> 1), dv, e, kq, rws, tag);
      f32x4 ac3[4] = {};
      __builtin_amdgcn_s_setprio(2);
      if (ts3 == 0) conv2<0>(L + CV3 + aoff + aw * 128, L + CV3 + boff + aw * 128, ac3); else conv2<1>(L + CV3 + aoff + aw * 128, L + CV3 + boff + aw * 128, ac3);
      __builtin_amdgcn_s_setprio(2);
#pragma unroll
      for (int ti = 0; ti < 4; ++ti) *(LAS f32x4*)(L + SC3 + (aw * 4 + ti) * 1024 + lane * 16) = ac3[ti];
      WAITV(4);
      HBAR();
      f32x4 a = {};
#pragma unroll
      for (int ww = 0; ww < 4; ++ww) a += *(LAS f32x4*)(L + SC3 + (ww * 4 + aw) * 1024 + lane * 16);
      conv_out(L + CV3, a, ts3 == 0 ? (aw < 2 ? aw : aw + 4) : 2 + aw, d3, dv3, e, kq, rws, tag);
    }
    {
      const int q = (lane & 15) >> 2, pp = lane & 3, pos = lane & 15;
      const int tok = 16 * aw + 2 * (pos >> 2) + (pos & 1) + 8 * ((pos >> 1) & 1);
      lds_wait_sleep(L + C_S, 1);
      const f32x2 st = *(LAS f32x2*)(L + XFS + ((mb & 1) * 64 + tok) * 8);
      v4u xn[5];
#pragma unroll
      for (int i = 0; i < 5; ++i) {
        const v2u xv2 = *(LAS v2u*)(L + EX + tok * 160 + (4 * i + kq) * 8);
        const unsigned d01 = pack2(bflo(xv2.x) - st.x, bfhi(xv2.x) - st.x), d23 = pack2(bflo(xv2.y) - st.x, bfhi(xv2.y) - st.x);
        xn[i] = v4u{pack2(bflo(d01) * st.y, bfhi(d01) * st.y), pack2(bflo(d23) * st.y, bfhi(d23) * st.y), xv2.x, xv2.y};
      }
      WAITL();
      lds_add(L + C_Y, 1u, lane);
      for (int i = 0; i < 2; ++i) __builtin_amdgcn_s_sleep(4);
      {
        v4u yv[5];
        const int yo = Y_OFF + (80 * nb + 4 * aw + kq) * 8192 + mb * 256 + e * 16;
#pragma unroll 1
        for (unsigned sp = 0; sp < SPIN_CAP; ++sp) {
          asm volatile("" ::: "memory");
          bool ok = true;
#pragma unroll
          for (int i = 0; i < 5; ++i) yv[i] = __builtin_amdgcn_raw_buffer_load_b128(rws, yo, i * 16 * 8192, SC1);
#pragma unroll
          for (int i = 0; i < 5; ++i) ok = ok && yv[i].y == tag && yv[i].w == tag;
          if (__all(ok)) break;
          __builtin_amdgcn_s_sleep(8);
        }
#pragma unroll
        for (int i = 0; i < 5; ++i) *(LAS v2u*)(L + EY + (16 * i + 4 * aw + kq) * 144 + e * 8) = v2u{yv[i].x, yv[i].z};
      }
      HBAR();
#pragma unroll
      for (int i = 0; i < 5; ++i) {
        const int dq = 4 * i + kq;
        const v2u yv = trread(L + EY + (4 * dq + q) * 144 + aw * 32 + pp * 8);
        const unsigned s01 = pack2(bflo(xn[i].x) * bflo(yv.x), bfhi(xn[i].x) * bfhi(yv.x)), s23 = pack2(bflo(xn[i].y) * bflo(yv.y), bfhi(xn[i].y) * bfhi(yv.y));
        *(LAS v4u*)(L + ES + (tok * 20 + dq) * 16) = v4u{s01, s23, xn[i].z, xn[i].w};
      }
    }
  }
  BAR();
  const int lane2 = __builtin_amdgcn_mbcnt_hi(~0u, __builtin_amdgcn_mbcnt_lo(~0u, opaque0()));
#pragma unroll
  for (int i = 0; i < 3; ++i) {
    const int it = w + 8 * i;
    if (it < 20) {
      const int n = it * 64 + lane2;
      const int tok = n / 20, dq = n - tok * 20;
      f32x4 o = *(LAS f32x4*)(L + tok * PSTR + dq * 16);
#pragma unroll
      for (int ww = 1; ww < 4; ++ww) o += *(LAS f32x4*)(L + ww * PART + tok * PSTR + dq * 16);
      const v4u sx = *(LAS v4u*)(L + ES + n * 16);
      const unsigned o01 = pack2(o[0], o[1]), o23 = pack2(o[2], o[3]);
      const unsigned g01 = pack2(silu_f(bflo(o01)), silu_f(bfhi(o01))), g23 = pack2(silu_f(bflo(o23)), silu_f(bfhi(o23)));
      const unsigned m01 = pack2(bflo(sx.x) * bflo(g01), bfhi(sx.x) * bfhi(g01)), m23 = pack2(bflo(sx.y) * bflo(g23), bfhi(sx.y) * bfhi(g23));
      v2u res;
      res.x = pack2(bflo(sx.z) + bflo(m01), bfhi(sx.z) + bfhi(m01));
      res.y = pack2(bflo(sx.w) + bflo(m23), bfhi(sx.w) + bfhi(m23));
      *(v2u*)(out + (size_t)(64 * mb + tok) * D + 80 * nb + dq * 4) = res;
    }
  }
}

extern "C" void kernel_launch(void* const* d_in, const int* in_sizes, int n_in,
                              void* d_out, int out_size, void* d_ws, size_t ws_size,
                              hipStream_t stream) {
  fused<<<256, 512, 0, stream>>>((const u16*)d_in[0], (const u16*)d_in[1], (const u16*)d_in[2], (const u16*)d_in[3],
                                 (const u16*)d_in[4], (const u16*)d_in[5], (const u16*)d_in[7], (const u16*)d_in[8],
                                 (char*)d_ws, (u16*)d_out);
}
```

```cpp
#include <hip/hip_runtime.h>
#include <stdint.h>

typedef unsigned short u16;
typedef unsigned v4u __attribute__((ext_vector_type(4)));
typedef unsigned v2u __attribute__((ext_vector_type(2)));
typedef float f32x2 __attribute__((ext_vector_type(2)));
typedef float f32x4 __attribute__((ext_vector_type(4)));
typedef float f32x16 __attribute__((ext_vector_type(16)));
typedef short bf16x8 __attribute__((ext_vector_type(8)));
typedef short s16x4 __attribute__((ext_vector_type(4)));
typedef __bf16 bf16x2 __attribute__((ext_vector_type(2)));
#define LAS __attribute__((address_space(3)))
#define GAS __attribute__((address_space(1)))
#define DI __device__ __forceinline__
#define RLX_AGENT __ATOMIC_RELAXED, __HIP_MEMORY_SCOPE_AGENT
#define RLX_WG __ATOMIC_RELAXED, __HIP_MEMORY_SCOPE_WORKGROUP

#define FSTR_N 128
constexpr int S = 2048, D = 640, F = 1728;
constexpr int KS2 = 54;
constexpr int FSTR = FSTR_N;
constexpr int FL_GEN = 0, FL_P = 131072, FL_H = FL_P + 256 * 256;
constexpr int ST_OFF = 32768;
constexpr int CS_OFF = ST_OFF + S * 8;
constexpr int HP_OFF = 262144;
constexpr int HP_MB = KS2 * 4096;
constexpr int W2P_OFF = HP_OFF + 32 * HP_MB;
constexpr int W2P_NB = KS2 * 5120;
constexpr int R_OFF = W2P_OFF + 8 * W2P_NB;
constexpr int Y_OFF = R_OFF + D * S * 2;
constexpr int WS_BYTES = Y_OFF + D * S * 4;
constexpr unsigned RSRC3 = 0x00020000u;
constexpr int SC1 = 16;
constexpr unsigned SPIN_CAP = 1u << 18;

DI float bflo(unsigned u) { return __uint_as_float(u << 16); }
DI float bfhi(unsigned u) { return __uint_as_float(u & 0xffff0000u); }
DI unsigned pack2(float a, float b) {
  bf16x2 r = __builtin_convertvector(f32x2{a, b}, bf16x2);
  return __builtin_bit_cast(unsigned, r);
}
DI float rbf(float a) { return bflo(pack2(a, a)); }
DI float silu_f(float x) { return x * __builtin_amdgcn_rcpf(1.f + __builtin_amdgcn_exp2f(-1.44269504088896f * x)); }
DI f32x4 mfma16(v4u a, v4u b, f32x4 c) {
  return __builtin_amdgcn_mfma_f32_16x16x32_bf16(__builtin_bit_cast(bf16x8, a), __builtin_bit_cast(bf16x8, b), c, 0, 0, 0);
}
DI f32x16 mfma32(v4u a, v4u b, f32x16 c) {
  return __builtin_amdgcn_mfma_f32_32x32x16_bf16(__builtin_bit_cast(bf16x8, a), __builtin_bit_cast(bf16x8, b), c, 0, 0, 0);
}
DI float dot2(unsigned a, unsigned b, float c) {
  return __builtin_amdgcn_fdot2_f32_bf16(__builtin_bit_cast(bf16x2, a), __builtin_bit_cast(bf16x2, b), c, false);
}
DI v2u trread(LAS char* p) { return __builtin_bit_cast(v2u, __builtin_amdgcn_ds_read_tr16_b64_v4i16((LAS s16x4*)p)); }
DI v4u ldsr128(LAS char* p) { return *(LAS v4u*)p; }
DI __amdgpu_buffer_rsrc_t mkrsrc(const void* p, int bytes) { return __builtin_amdgcn_make_buffer_rsrc((void*)p, 0, bytes, RSRC3); }
DI void glds_asm(__amdgpu_buffer_rsrc_t rs, unsigned ldsa, int voff, int soff) {
  asm volatile("s_nop 4\n\ts_mov_b32 m0, %0\n\ts_nop 0\n\tbuffer_load_dwordx4 %1, %2, %3 offen lds"
               :: "s"(ldsa), "v"(voff), "s"(rs), "s"(soff) : "m0", "memory");
}
DI void glds_fast(__amdgpu_buffer_rsrc_t rs, unsigned ldsa, int voff, int soff) {
  asm volatile("s_mov_b32 m0, %0\n\ts_nop 0\n\tbuffer_load_dwordx4 %1, %2, %3 offen lds"
               :: "s"(ldsa), "v"(voff), "s"(rs), "s"(soff) : "m0", "memory");
}
#define GLDS(rs, ldsp, voff, soff) glds_asm(rs, (unsigned)(uintptr_t)(ldsp), voff, soff)
#define WAITV(n) asm volatile("s_waitcnt vmcnt(" #n ")" ::: "memory")
#define WAITL() asm volatile("s_waitcnt lgkmcnt(0)" ::: "memory")
#define BAR() do { WAITL(); __builtin_amdgcn_s_barrier(); asm volatile("" ::: "memory"); } while (0)
#define SB() __builtin_amdgcn_sched_barrier(0)

#define STAMP_DECL
#define STAMP(i)
#define STAMP_OUT(kid)

#define ROT_EXPR ((5 * ((pi + jj) & 1) + (5 * xcd) / 8) % 10)
#define WA_N 3
#define POLL_SLEEP 8
#define CODE_PF 1
#define CODE_BYTES 39000u
#define EXACT_PREP 1
#define PREP_PRIO 3
#define CONV_PRIO 1
#define WB_N 6
#define ROT2_EXPR 0
#define DELAY_POLL 32
#define ABL 0
#define FH_AUX 1
#define Y_DELAY 2
#define C8_PRIO 1
#define G2_PRIO 0
#define POOL_N 41
#define S1_EARLY 1
#define STAGE_START 44
#define LOOK_N 5
#define FH_WAIT 4
#define H_DELAY 0
#define C3_PRIO 2
#define FAST_EPI 1
#define AUX_PRIO 0
#define MMA_PRIO 2
#define CONV_START 44
constexpr int NBUF = 3;
constexpr int CH_HA = 0, CH_HB = 4224, CH_R = 8448, CH_RS = 4512, CH_BYTES = CH_R + 4 * CH_RS;
constexpr int CV = 98304;
constexpr int CV3 = 32768;
constexpr int SC3 = 65536;
constexpr int PK = CV, PV = PK + 11264, PW = PV + 10240, PS = PW + 10240, PO = PS + 1024;
constexpr int EY = CV, EX = 0, ES = EY + 80 * 144;
constexpr int XFS = CV + 2 * CH_BYTES;
constexpr int XFC = XFS + 1024;
constexpr int CNT = XFC + 512;
constexpr int C_AUX = CNT, C_G2 = CNT + 4, C_Y = CNT + 8, C_H = CNT + 12, C_MMA = CNT + 16, C_S = CNT + 20, C_P = CNT + 24,
              C_TAG = CNT + 60, C_ST = CNT + 40, C_HR = CNT + 32, C_YR = CNT + 36;
constexpr int LDS_BYTES = CNT + 64;
static_assert(PO + 10240 <= XFS && ES + 20480 <= XFS && LDS_BYTES <= 163840, "lds");
constexpr int PSTR = 336, PART = 64 * PSTR;

DI void lds_add(LAS char* c, unsigned v, int lane) {
  if (lane == 0) __hip_atomic_fetch_add((LAS unsigned*)c, v, RLX_WG);
}
DI void lds_wait(LAS char* c, unsigned target) {
  while ((unsigned)__builtin_amdgcn_readfirstlane(__hip_atomic_load((LAS unsigned*)c, RLX_WG)) < target) {}
  asm volatile("" ::: "memory");
}
DI void lds_wait_sleep(LAS char* c, unsigned target) {
  while ((unsigned)__builtin_amdgcn_readfirstlane(__hip_atomic_load((LAS unsigned*)c, RLX_WG)) < target) __builtin_amdgcn_s_sleep(1);
  asm volatile("" ::: "memory");
}
DI unsigned flag_load(char* ws, int off, int n, int lane) {
  return __hip_atomic_load((GAS unsigned*)(ws + off + (lane < n ? lane : 0) * FSTR), RLX_AGENT);
}
DI void poll_flags(char* ws, int off, int n, unsigned tag, int lane) {
#pragma unroll 1
  for (unsigned spins = 0; spins < SPIN_CAP; ++spins) {
    const unsigned v = flag_load(ws, off, n, lane);
    if (__all(v == tag)) break;
    __builtin_amdgcn_s_sleep(POLL_SLEEP);
  }
  __builtin_amdgcn_fence(__ATOMIC_ACQUIRE, "wavefront");
}
DI void set_flag(char* ws, int off, unsigned tag, int lane) {
  if (lane == 0) __hip_atomic_store((GAS unsigned*)(ws + off), tag, RLX_AGENT);
}

DI unsigned opaque0() { unsigned z; asm volatile("v_mov_b32 %0, 0" : "=v"(z)); return z; }
template <int I> struct IC { static constexpr int value = I; };
template <int B, int E, class Fn> DI void static_for(Fn&& f) { if constexpr (B < E) { f(IC<B>{}); static_for<B + 1, E>(f); } }
struct Frags { v4u x[4][2], w[4][2]; };

template <int TS> DI void conv_ld(LAS char* pa, LAS char* pb, v4u (&A)[8], v4u (&B)[8]) {
  constexpr int NA = TS == 0 ? 8 : 6;
#pragma unroll
  for (int a = 0; a < NA; ++a) { A[a] = ldsr128(pa + a * 512); B[a] = ldsr128(pb + (7 - a) * 512); }
}
template <int TS> DI void conv_mm(const v4u (&A)[8], const v4u (&B)[8], f32x4 (&acc)[4]) {
#pragma unroll
  for (int ti = 0; ti < 4; ++ti) {
    const int n = TS == 0 ? (ti == 0 ? 0 : ti == 1 ? 1 : ti == 2 ? 6 : 7) : 2 + ti;
#pragma unroll
    for (int a = 0; a <= n; ++a) acc[ti] = mfma16(A[a], B[n - a], acc[ti]);
  }
}
DI void conv_sched() {
#pragma unroll
  for (int i = 0; i < 16; ++i) { __builtin_amdgcn_sched_group_barrier(0x8, 1, 0); __builtin_amdgcn_sched_group_barrier(0x100, 1, 0); }
  __builtin_amdgcn_sched_group_barrier(0x8, 2, 0);
}
template <int TS> DI void conv8(LAS char* pa, LAS char* pb, f32x4 (&acc)[4]) {
  v4u A0[8], B0[8], A1[8], B1[8];
  conv_ld<TS>(pa, pb, A0, B0);
  SB();
#pragma unroll 1
  for (int r = 0; r < 8; r += 2) {
    conv_ld<TS>(pa + 64, pb + 64, A1, B1); conv_mm<TS>(A0, B0, acc); conv_sched(); SB();
    pa += 128; pb += 128;
    conv_ld<TS>(pa, pb, A0, B0); conv_mm<TS>(A1, B1, acc); conv_sched(); SB();
  }
}
template <int TS> DI void conv2(LAS char* pa, LAS char* pb, f32x4 (&acc)[4]) {
#pragma unroll 1
  for (int r = 0; r < 2; ++r) {
    v4u A[8], B[8];
    conv_ld<TS>(pa, pb, A, B); conv_mm<TS>(A, B, acc);
    pa += 64; pb += 64;
  }
}
DI unsigned kshift2(float k1, float k2, float k3, float k4, float k5, float c0, float c1, float c2, float c3) {
  unsigned a = pack2(k1 * c3, k2 * c3);
  unsigned t = pack2(k2 * c2, k3 * c2);
  a = pack2(bflo(a) + bflo(t), bfhi(a) + bfhi(t));
  t = pack2(k3 * c1, k4 * c1);
  a = pack2(bflo(a) + bflo(t), bfhi(a) + bfhi(t));
  t = pack2(k4 * c0, k5 * c0);
  return pack2(bflo(a) + bflo(t), bfhi(a) + bfhi(t));
}
DI void stage_h(LAS char* Lc, v4u h, int g) {
  *(LAS v4u*)(Lc + CH_HB + 112 + g * 16) = h;
  LAS char* a = Lc + CH_HA + 114 + g * 16;
  *(LAS u16*)(a) = (u16)(h.x & 0xffff);
  *(LAS unsigned*)(a + 2) = __builtin_amdgcn_alignbit(h.y, h.x, 16);
  *(LAS v2u*)(a + 6) = v2u{__builtin_amdgcn_alignbit(h.z, h.y, 16), __builtin_amdgcn_alignbit(h.w, h.z, 16)};
  *(LAS u16*)(a + 14) = (u16)(h.w >> 16);
}
DI void stage_r(LAS char* Lc, v4u u, int g) {
  LAS char* rb = Lc + CH_R + g * 16;
  *(LAS v4u*)(rb) = u;
  { LAS char* q = rb + CH_RS - 4; *(LAS unsigned*)(q) = u.x; *(LAS v2u*)(q + 4) = v2u{u.y, u.z}; *(LAS unsigned*)(q + 12) = u.w; }
  { LAS char* q = rb + 2 * CH_RS - 8; *(LAS v2u*)(q) = v2u{u.x, u.y}; *(LAS v2u*)(q + 8) = v2u{u.z, u.w}; }
  { LAS char* q = rb + 3 * CH_RS - 12; *(LAS unsigned*)(q) = u.x; *(LAS v2u*)(q + 4) = v2u{u.y, u.z}; *(LAS unsigned*)(q + 12) = u.w; }
}
DI void conv_out(LAS char* Lc, f32x4 a, int n, int d, float dv, int e, int kq, __amdgpu_buffer_rsrc_t rws, unsigned tag) {
  const int t = 256 * n + 64 * (e >> 2) + 2 * (e & 3) + 16 * kq;
  const unsigned ua = *(LAS unsigned*)(Lc + CH_R + 2 * (2046 - t));
  const unsigned ub = *(LAS unsigned*)(Lc + CH_R + 2 * (2038 - t));
  const unsigned c01 = pack2(a[0], a[1]), c23 = pack2(a[2], a[3]);
  const unsigned d01 = pack2(bfhi(ua) * dv, bflo(ua) * dv), d23 = pack2(bfhi(ub) * dv, bflo(ub) * dv);
  v2u o;
  o.x = pack2(bflo(c01) + bflo(d01), bfhi(c01) + bfhi(d01));
  o.y = pack2(bflo(c23) + bflo(d23), bfhi(c23) + bfhi(d23));
  const int off = Y_OFF + d * 8192 + (256 * n + 64 * (e >> 2) + 16 * kq + 4 * (e & 3)) * 4;
  __builtin_amdgcn_raw_buffer_store_b128(v4u{o.x, tag, o.y, tag}, rws, off, 0, SC1);
}

__global__ __launch_bounds__(512, 2) void fused(const u16* __restrict__ x, const u16* __restrict__ kk, const u16* __restrict__ vv,
                                                const u16* __restrict__ sk, const u16* __restrict__ hk, const u16* __restrict__ Dv,
                                                const u16* __restrict__ w1, const u16* __restrict__ w2,
                                                char* ws, u16* out) {
  __shared__ __attribute__((aligned(16))) char smem[LDS_BYTES];
  LAS char* L = (LAS char*)smem;
  if (__builtin_amdgcn_readfirstlane(threadIdx.x >> 6) == 7) {
    unsigned long long pc;
    asm volatile("s_getpc_b64 %0" : "=s"(pc));
    const unsigned co = min(((blockIdx.x >> 3) & 7) * 8192u + (threadIdx.x & 63) * 128u, CODE_BYTES);
    asm volatile("s_mov_b32 m0, %0\n\ts_nop 0\n\tglobal_load_lds_dword %1, %2" :: "s"((unsigned)(uintptr_t)(L + XFS)), "v"(co), "s"(pc) : "m0");
  }
  const int b = blockIdx.x, xcd = b & 7, r = b >> 3;
  const int rg = xcd >> 1, hf = xcd & 1, pi = r >> 3, jj = r & 7;
  const int p = 4 * rg + pi, j = 7 * hf + jj;
  const bool g1 = jj < 7;
  const int mbl = 2 * pi + ((r >> 2) & 1), nbl = r & 3;
  const int mb = 8 * rg + mbl, nb = 4 * hf + nbl;
  const int lane = threadIdx.x & 63;
  const int w = __builtin_amdgcn_readfirstlane(threadIdx.x >> 6);
  const int hi = lane >> 5, aw = w & 3;
  STAMP_DECL; STAMP(0);
  const __amdgpu_buffer_rsrc_t rws = mkrsrc(ws, WS_BYTES);
  if (threadIdx.x < 15) *(LAS unsigned*)(L + CNT + threadIdx.x * 4) = 0u;
  const int dpair = 80 * nb + 2 * mb, d3 = 80 * nb + 64 + (mb >> 1), ts3 = mb & 1;
  const int e = lane & 15, kq = lane >> 4;
  const int aoff = ((e & 1) ? CH_HB : CH_HA) + (kq + (e >> 1)) * 16;
  const int boff = CH_R + (3 - (e & 3)) * CH_RS + (248 - 32 * 7 - 8 * (e >> 2) + kq) * 16;

  {
    const __amdgpu_buffer_rsrc_t rk = mkrsrc(kk, S * D * 2), rv_ = mkrsrc(vv, S * D * 2), rsk = mkrsrc(sk, D * 8);
    const int sb = 64 * mb * 1280 + 160 * nb;
    int vo[2];
#pragma unroll
    for (int i = 0; i < 2; ++i) { const int n = (8 * i + w) * 64 + lane; const int row = n / 10; vo[i] = row * 1280 + (n - row * 10) * 16; }
    GLDS(rk, L + PK + w * 1024, vo[0] + sb - 5120, 0);
    GLDS(rv_, L + PV + w * 1024, vo[0], sb);
    {
      const int n = (5 + w) * 64 + lane; const int row = n / 10; const int vo3 = row * 1280 + (n - row * 10) * 16;
      if (w < 3) GLDS(rk, L + PK + (8 + w) * 1024, vo[1] + sb - 5120, 0);
      else if (w < 5) GLDS(rv_, L + PV + (5 + w) * 1024, vo3, sb);
      else if (w == 5) GLDS(rsk, L + PS, lane * 16, nb * 640);
    }
  }
#define PREP_ITEM(tg, cg) do { \
        const int q_ = (lane & 15) >> 2, pp_ = lane & 3, tl = 16 * (tg) + 4 * kq; \
        LAS char* ka_ = L + PK + (tl + q_) * 160 + (cg) * 32 + pp_ * 8; \
        v2u k0 = trread(ka_), k1 = trread(ka_ + 4 * 160); \
        v2u v0 = trread(ka_ + (PV - PK)); \
        v2u sw = *(LAS v2u*)(L + PS + ((cg) * 16 + e) * 8); \
        float kf[8] = {bflo(k0.x), bfhi(k0.x), bflo(k0.y), bfhi(k0.y), bflo(k1.x), bfhi(k1.x), bflo(k1.y), bfhi(k1.y)}; \
        float vf[4] = {bflo(v0.x), bfhi(v0.x), bflo(v0.y), bfhi(v0.y)}; \
        const float c0 = bflo(sw.x), c1 = bfhi(sw.x), c2 = bflo(sw.y), c3 = bfhi(sw.y); \
        float u[4]; \
        _Pragma("unroll") for (int e2 = 0; e2 < 4; e2 += 2) { \
          const unsigned a = kshift2(kf[e2 + 1], kf[e2 + 2], kf[e2 + 3], kf[e2 + 4], kf[e2 + 5], c0, c1, c2, c3); \
          u[e2] = bflo(a) * vf[e2]; u[e2 + 1] = bfhi(a) * vf[e2 + 1]; } \
        v2u o = v2u{pack2(u[3], u[2]), pack2(u[1], u[0])}; \
        const int ch = (cg) * 16 + e; \
        const int pos8 = (60 - tl) >> 2; \
        *(LAS v2u*)(L + PO + ch * 128 + ((((pos8 >> 1) ^ (ch & 7)) << 4) | ((pos8 & 1) << 3))) = o; \
      } while (0)

  if (w >= 4) {
    unsigned gen;
    asm volatile("s_load_dword %0, %1, 0x0\n\ts_waitcnt lgkmcnt(0)" : "=s"(gen) : "s"(ws) : "memory");
    const unsigned tag = gen * 0x9E3779B1u + 0x85EBCA6Bu;
    STAMP(1);
    if (aw == 0 && lane == 0) *(LAS unsigned*)(L + C_TAG) = tag;
    WAITV(0);
    BAR();
    unsigned ab = 4;
#define AUXBAR() do { WAITL(); lds_add(L + C_AUX, 1u, lane); ab += 4; lds_wait(L + C_AUX, ab); } while (0)
    STAMP(2);
    if (ABL & 4) lds_wait_sleep(L + C_MMA, 44);
    {
      const __amdgpu_buffer_rsrc_t rw2 = mkrsrc(w2, F * D * 2);
#pragma unroll
      for (int i = 0; i < 3; ++i) if (4 * i + aw < 10) {
        const int n = (4 * i + aw) * 64 + lane; const int row = n / 10;
        GLDS(rw2, L + PW + (4 * i + aw) * 1024, (row + (row >= 32 ? 992 : 0)) * 1280 + (n - row * 10) * 16, 32 * mb * 1280 + 160 * nb);
      }
    }
    {
      const int g = lane >> 4, q = (lane & 15) >> 2, pp = lane & 3;
      PREP_ITEM(aw, 0); PREP_ITEM(aw, 1); PREP_ITEM(aw, 2);
      AUXBAR();
#pragma unroll
      for (int i = 0; i < 3; ++i) {
        const int ri = aw + 4 * i;
        if (ri < 10) {
          const int ch = 8 * ri + (lane >> 3), cc = lane & 7;
          v4u val = ldsr128(L + PO + ch * 128 + ((cc ^ (ch & 7)) << 4));
          __builtin_amdgcn_raw_buffer_store_b128(val, rws, (ABL & 1) ? -16 : R_OFF + (80 * nb + ch) * 4096 + (31 - mb) * 128 + cc * 16, 0, SC1);
        }
      }
      __builtin_amdgcn_s_setprio(AUX_PRIO);
      if (aw < 2) WAITV(3); else WAITV(2);
      AUXBAR();
      STAMP(3);
#pragma unroll
      for (int i = 0; i < 3; ++i) {
        const int fi = aw + 4 * i;
        if (fi < 10) {
          const int half = fi / 5, nt = fi - half * 5, ks = mb + 32 * half;
          const int r0 = 16 * (g & 1) + 4 * (g >> 1);
          LAS char* a = L + PW + half * 5120 + (r0 + q) * 160 + nt * 32 + pp * 8;
          v2u lo = trread(a), hi2 = trread(a + 8 * 160);
          __builtin_amdgcn_raw_buffer_store_b128(v4u{lo.x, lo.y, hi2.x, hi2.y}, rws, (ks < KS2 && !(ABL & 1)) ? W2P_OFF + nb * W2P_NB + ks * 5120 + nt * 1024 + lane * 16 : -16, 0, SC1);
        }
      }
    }
    STAMP(4);
    WAITV(0);
    AUXBAR();
    if (aw == 0) set_flag(ws, FL_P + (nb * 32 + mb) * FSTR, tag, lane);
    STAMP(5);
    if (ABL & 2) lds_wait_sleep(L + C_MMA, 44);
    const __amdgpu_buffer_rsrc_t rh = mkrsrc(hk, D * S * 2);
    v4u cu[3];
    cu[0] = __builtin_amdgcn_raw_buffer_load_b128(rh, dpair * 4096 + aw * 1024 + lane * 16, 0, 0);
    cu[1] = __builtin_amdgcn_raw_buffer_load_b128(rh, dpair * 4096 + 4096 + aw * 1024 + lane * 16, 0, 0);
    cu[2] = __builtin_amdgcn_raw_buffer_load_b128(rh, d3 * 4096 + aw * 1024 + lane * 16, 0, 0);
    const int g = aw * 64 + lane;
    lds_wait_sleep(L + C_MMA, DELAY_POLL);
    if (aw == 1) {
      const int jc = g1 ? j : 0;
      v2u gr[4];
#define GRL(off) __builtin_bit_cast(v2u, __hip_atomic_load((GAS unsigned long long*)(ws + (off)), RLX_AGENT))
#pragma unroll 1
      for (unsigned sp = 0; sp < SPIN_CAP; ++sp) {
        gr[0] = GRL(ST_OFF + (p * 128 + lane) * 8);
        gr[1] = GRL(ST_OFF + (p * 128 + 64 + lane) * 8);
        gr[2] = GRL(CS_OFF + (jc * 128 + lane) * 8);
        gr[3] = GRL(CS_OFF + (jc * 128 + (j == 13 ? 0 : 64) + lane) * 8);
        if (__all(gr[0].y == tag && gr[1].y == tag && gr[2].y == tag && gr[3].y == tag)) break;
        __builtin_amdgcn_s_sleep(POLL_SLEEP);
      }
      *(LAS f32x2*)(L + XFS + lane * 8) = f32x2{bflo(gr[0].x), 1.f / bfhi(gr[0].x)};
      *(LAS f32x2*)(L + XFS + (64 + lane) * 8) = f32x2{bflo(gr[1].x), 1.f / bfhi(gr[1].x)};
      *(LAS unsigned*)(L + XFC + lane * 4) = gr[2].x;
      *(LAS unsigned*)(L + XFC + (64 + lane) * 4) = gr[3].x;
      WAITL();
      lds_add(L + C_S, 1u, lane);
    }
    if (aw == 2) { poll_flags(ws, FL_P + nb * 32 * FSTR, 32, tag, lane); lds_add(L + C_P, 1u, lane); }
    lds_wait_sleep(L + C_P, 1);
    STAMP(6);
    v4u rv[3];
    rv[0] = __builtin_amdgcn_raw_buffer_load_b128(rws, R_OFF + dpair * 4096 + aw * 1024 + lane * 16, 0, SC1);
    rv[1] = __builtin_amdgcn_raw_buffer_load_b128(rws, R_OFF + dpair * 4096 + 4096 + aw * 1024 + lane * 16, 0, SC1);
    rv[2] = __builtin_amdgcn_raw_buffer_load_b128(rws, R_OFF + d3 * 4096 + aw * 1024 + lane * 16, 0, SC1);
    const int rot2 = ROT2_EXPR;
    const __amdgpu_buffer_rsrc_t ra = mkrsrc(ws + HP_OFF + mb * HP_MB, HP_MB);
    const __amdgpu_buffer_rsrc_t rb = mkrsrc(ws + W2P_OFF + nb * W2P_NB, W2P_NB);
    const int va = lane * 16 + aw * 4096, vb = lane * 16 + aw * 5120;
    constexpr int WB = WB_N, NKS = 14, POOL = POOL_N;
    v4u fa[NKS][4], fb[NKS][5];
#define KIDX(i) ((i) + rot2 >= NKS ? (i) + rot2 - NKS : (i) + rot2)
#define LOADA(i) do { const int so_ = KIDX(i) * 16384; \
    _Pragma("unroll") for (int m = 0; m < 4; ++m) fa[i][m] = __builtin_amdgcn_raw_buffer_load_b128(ra, va + m * 1024, so_, SC1); } while (0)
#define LOADB(i) do { const int so_ = KIDX(i) * 20480; \
    _Pragma("unroll") for (int n = 0; n < 5; ++n) fb[i][n] = __builtin_amdgcn_raw_buffer_load_b128(rb, vb + (n & 3) * 1024, so_ + (n >> 2) * 4096, SC1); } while (0)
#pragma unroll
    for (int i = 0; i < WB; ++i) LOADB(i);
    SB();
    STAMP(7);
    lds_wait_sleep(L + C_MMA, STAGE_START);
    if (aw == 0 && lane < 32) *(LAS v4u*)(L + CV + (lane >> 4) * CH_BYTES + ((lane >> 3) & 1) * CH_HB + (lane & 7) * 16) = v4u{0, 0, 0, 0};
    if (aw == 3) {
#pragma unroll
      for (int i = 0; i < 4; ++i) {
        const int n = lane + 64 * i;
        if (n < 200) { const int cp = n / 25, sl = n - cp * 25; *(LAS v4u*)(L + CV + (cp >> 2) * CH_BYTES + CH_R + (cp & 3) * CH_RS + 4080 + sl * 16) = v4u{0, 0, 0, 0}; }
      }
    }
    stage_h(L + CV, cu[0], g);
    stage_h(L + CV + CH_BYTES, cu[1], g);
    stage_r(L + CV, rv[0], g);
    stage_r(L + CV + CH_BYTES, rv[1], g);
    if (aw == 0 && lane < 16) *(LAS v4u*)(L + CV3 + (lane >> 3) * CH_HB + (lane & 7) * 16) = v4u{0, 0, 0, 0};
    if (aw == 3) {
#pragma unroll
      for (int i = 0; i < 2; ++i) {
        const int n = lane + 64 * i;
        if (n < 100) { const int cp = n / 25, sl = n - cp * 25; *(LAS v4u*)(L + CV3 + CH_R + cp * CH_RS + 4080 + sl * 16) = v4u{0, 0, 0, 0}; }
      }
    }
    stage_h(L + CV3, cu[2], g);
    stage_r(L + CV3, rv[2], g);
    AUXBAR();
    if (aw == 0) lds_add(L + C_ST, 1u, lane);
    STAMP(8);
    lds_wait_sleep(L + C_H, FH_WAIT);
    for (int i = 0; i < H_DELAY; ++i) __builtin_amdgcn_s_sleep(4);
    STAMP(10);
    if (aw == 1) { poll_flags(ws, FL_H + p * 16 * FSTR, 14, tag, lane); lds_add(L + C_HR, 1u, lane); }
    __builtin_amdgcn_s_setprio(G2_PRIO);
    lds_wait_sleep(L + C_HR, 1);
    STAMP(11);
#define ISSUE(q) do { constexpr int q_ = (q); \
      if constexpr (q_ < 4 * WB) { fa[q_ / 4][q_ % 4] = __builtin_amdgcn_raw_buffer_load_b128(ra, va + (q_ % 4) * 1024, KIDX(q_ / 4) * 16384, SC1); } \
      else if constexpr (q_ < 4 * WB + 9 * (NKS - WB)) { constexpr int k_ = WB + (q_ - 4 * WB) / 9, f_ = (q_ - 4 * WB) % 9; \
        if constexpr (f_ < 4) fa[k_][f_] = __builtin_amdgcn_raw_buffer_load_b128(ra, va + f_ * 1024, KIDX(k_) * 16384, SC1); \
        else fb[k_][f_ - 4] = __builtin_amdgcn_raw_buffer_load_b128(rb, vb + ((f_ - 4) & 3) * 1024, KIDX(k_) * 20480 + ((f_ - 4) >> 2) * 4096, SC1); } \
    } while (0)
    constexpr auto need = [](int k) { k = k > NKS ? NKS : k; return k <= WB ? 4 * k : 4 * WB + 9 * (k - WB); };
    constexpr auto cum = [need](int i) { const int a = need(i + LOOK_N), c = POOL - 5 * WB + (i > 0 ? 9 * i - 6 : 0); return a < c ? a : c; };
    static_for<0, cum(0)>([&](auto q) { ISSUE(decltype(q)::value); });
    SB();
    f32x4 acc2[4][5] = {};
    static_for<0, NKS>([&](auto ii) {
      constexpr int i = decltype(ii)::value;
      constexpr int c0 = cum(i), cn = cum(i + 1) - cum(i);
      static_assert(cn <= 9 && c0 >= need(i + 1), "schedule");
      static_for<0, 20>([&](auto mm) {
        constexpr int mn = decltype(mm)::value, m = mn / 5, n = mn % 5;
        acc2[m][n] = mfma16(fb[i][n], fa[i][m], acc2[m][n]);
        constexpr int slot = (mn & 1) ? (mn >> 1) - (9 - cn) : -1;
        if constexpr (slot >= 0 && mn < 19) { SB(); ISSUE(c0 + slot); SB(); }
      });
      SB();
      if (i == 0) STAMP(12);
    });
    STAMP(13);
    lds_wait(L + C_Y, 4);
    const int ln = __builtin_amdgcn_mbcnt_hi(~0u, __builtin_amdgcn_mbcnt_lo(~0u, opaque0()));
#pragma unroll
    for (int m = 0; m < 4; ++m)
#pragma unroll
      for (int n = 0; n < 5; ++n)
        *(LAS f32x4*)(L + aw * PART + (16 * m + (ln & 15)) * PSTR + n * 64 + (ln >> 4) * 16) = acc2[m][n];
  } else {
    __builtin_amdgcn_s_setprio(MMA_PRIO);
    const __amdgpu_buffer_rsrc_t rw = mkrsrc(w1, D * F * 2);
    unsigned tag;
    if (g1) {
      const __amdgpu_buffer_rsrc_t rx = mkrsrc(x + (size_t)p * 128 * D, 128 * D * 2);
      int vx[2], vw[4];
      {
        const int row = 32 * w + (lane >> 3);
        vx[0] = row * 1280 + (((lane & 7) ^ ((row >> 1) & 7)) << 4);
        vx[1] = (row + 8) * 1280 + (((lane & 7) ^ (((row + 8) >> 1) & 7)) << 4);
#pragma unroll
        for (int ii = 0; ii < 4; ++ii) {
          const int kr = 16 * w + 4 * ii + (lane >> 4);
          vw[ii] = kr * (F * 2) + j * 256 + (((lane & 15) ^ (((kr & 3) << 2) | ((kr >> 2) & 3))) << 4);
        }
      }
      const int rot = ROT_EXPR;
      const unsigned lbase = (unsigned)(uintptr_t)L + w * 4096;
#define KC(s) ((s) + rot >= 10 ? (s) + rot - 10 : (s) + rot)
#define G1LD(s, i) do { \
    if ((i) < 4) glds_fast(rx, lbase + ((s) % NBUF) * 32768 + (i) * 1024, vx[(i) & 1], KC(s) * 128 + ((i) >> 1) * 16 * 1280); \
    else glds_fast(rw, lbase + ((s) % NBUF) * 32768 + 16384 + ((i) - 4) * 1024, vw[((i) - 4) & 3], KC(s) * (64 * F * 2)); \
  } while (0)
#pragma unroll
      for (int i = 0; i < 8; ++i) G1LD(0, i);
#pragma unroll
      for (int i = 0; i < 8; ++i) G1LD(1, i);
      WAITV(16);
      BAR();
      PREP_ITEM(aw, 3); PREP_ITEM(aw, 4);
      WAITL(); lds_add(L + C_AUX, 1u, lane);
      SB();
      STAMP(1);
      const int fh = w & 1, th = w >> 1;
      int xa[4], wa[2][2];
      {
        const int row = 64 * th + (lane & 31);
        const int c0 = hi ^ ((row >> 1) & 7);
        const int blk = (lane >> 4) & 1, q = (lane & 15) >> 2, pp = lane & 3;
#pragma unroll
        for (int s = 0; s < 4; ++s) xa[s] = row * 128 + (((2 * s) ^ c0) << 4);
#pragma unroll
        for (int ft = 0; ft < 2; ++ft)
#pragma unroll
          for (int t = 0; t < 2; ++t) {
            const int kr = 8 * hi + 4 * t + q;
            const int ch = 4 * (2 * fh + ft) + 2 * blk + (pp >> 1);
            wa[ft][t] = 16384 + 256 * kr + ((ch ^ ((q << 2) | ((2 * hi + t) & 3))) << 4) + 8 * (pp & 1);
          }
      }
      f32x16 acc[2][2] = {};
      Frags fA, fB;
#define RDX(f, bo, s, tt) f.x[s][tt] = ldsr128(L + (bo) + xa[s] + (tt) * 4096)
#define RDW(f, bo, s, ft) do { v2u wl_ = trread(L + (bo) + wa[ft][0] + (s) * 4096), wh_ = trread(L + (bo) + wa[ft][1] + (s) * 4096); f.w[s][ft] = v4u{wl_.x, wl_.y, wh_.x, wh_.y}; } while (0)
#define MM(f, s, ft, tt) acc[ft][tt] = mfma32(f.w[s][ft], f.x[s][tt], acc[ft][tt])
#define ITER(s, vm, cur, nxt) do { \
    asm volatile("s_waitcnt vmcnt(" #vm ") lgkmcnt(0)" ::: "memory"); \
    lds_add(L + C_MMA, 1u, lane); SB(); \
    MM(cur, 0, 0, 0); MM(cur, 0, 0, 1); MM(cur, 0, 1, 0); SB(); \
    lds_wait(L + C_MMA, 4 * ((s) + 2)); SB(); \
    constexpr int bo_ = (((s) + 1) % NBUF) * 32768; constexpr bool ld_ = (s) + NBUF < 10; \
    MM(cur, 0, 1, 1); RDX(nxt, bo_, 0, 0); RDX(nxt, bo_, 0, 1); SB(); if (ld_) G1LD((s) + NBUF, 0); SB(); \
    MM(cur, 1, 0, 0); RDW(nxt, bo_, 0, 0); SB(); \
    MM(cur, 1, 0, 1); RDW(nxt, bo_, 0, 1); SB(); if (ld_) G1LD((s) + NBUF, 4); SB(); \
    MM(cur, 1, 1, 0); RDX(nxt, bo_, 1, 0); RDX(nxt, bo_, 1, 1); SB(); if (ld_) G1LD((s) + NBUF, 1); SB(); \
    MM(cur, 1, 1, 1); RDW(nxt, bo_, 1, 0); SB(); \
    MM(cur, 2, 0, 0); RDW(nxt, bo_, 1, 1); SB(); if (ld_) G1LD((s) + NBUF, 5); SB(); \
    MM(cur, 2, 0, 1); RDX(nxt, bo_, 2, 0); RDX(nxt, bo_, 2, 1); SB(); if (ld_) G1LD((s) + NBUF, 2); SB(); \
    MM(cur, 2, 1, 0); RDW(nxt, bo_, 2, 0); SB(); \
    MM(cur, 2, 1, 1); RDW(nxt, bo_, 2, 1); SB(); if (ld_) G1LD((s) + NBUF, 6); SB(); \
    MM(cur, 3, 0, 0); RDX(nxt, bo_, 3, 0); RDX(nxt, bo_, 3, 1); SB(); if (ld_) G1LD((s) + NBUF, 3); SB(); \
    MM(cur, 3, 0, 1); RDW(nxt, bo_, 3, 0); RDW(nxt, bo_, 3, 1); SB(); \
    MM(cur, 3, 1, 0); SB(); if (ld_) G1LD((s) + NBUF, 7); SB(); \
    MM(cur, 3, 1, 1); SB(); \
  } while (0)
      WAITV(8);
      lds_add(L + C_MMA, 1u, lane); lds_wait(L + C_MMA, 4);
      STAMP(2);
#pragma unroll
      for (int s = 0; s < 4; ++s) { RDX(fA, 0, s, 0); RDX(fA, 0, s, 1); RDW(fA, 0, s, 0); RDW(fA, 0, s, 1); }
      SB();
#pragma unroll
      for (int i = 0; i < 8; ++i) G1LD(2, i);
      SB();
      ITER(0, 8, fA, fB);
      ITER(1, 8, fB, fA);
      ITER(2, 8, fA, fB);
      STAMP(3);
      ITER(3, 8, fB, fA);
      ITER(4, 8, fA, fB);
      ITER(5, 8, fB, fA);
      STAMP(4);
      ITER(6, 8, fA, fB);
      ITER(7, 8, fB, fA);
      ITER(8, 0, fA, fB);
#pragma unroll
      for (int s = 0; s < 4; ++s) { MM(fB, s, 0, 0); MM(fB, s, 0, 1); MM(fB, s, 1, 0); MM(fB, s, 1, 1); }
      SB();
      lds_add(L + C_MMA, 1u, lane);
      STAMP(5);
      lds_wait_sleep(L + C_S, 1);
      tag = __builtin_amdgcn_readfirstlane(*(LAS unsigned*)(L + C_TAG));
      STAMP(6);
#pragma unroll
      for (int ft = 0; ft < 2; ++ft) {
        const int fg = 2 * fh + ft;
        const int ks = 4 * j + fg;
        f32x4 c4[4];
#pragma unroll
        for (int g = 0; g < 4; ++g) c4[g] = *(LAS f32x4*)(L + XFC + (32 * fg + 8 * g + 4 * hi) * 4);
#pragma unroll
        for (int tt = 0; tt < 2; ++tt) {
          const int tok = 64 * th + 32 * tt + (lane & 31);
          const f32x2 st = *(LAS f32x2*)(L + XFS + tok * 8);
          const f32x16& a = acc[ft][tt];
          const float zs1 = -1.44269504088896f * st.y, dn1 = __builtin_amdgcn_rcpf(st.y);
          const f32x2 zs = f32x2{zs1, zs1}, dn = f32x2{dn1, dn1};
          const int mb2 = 2 * p + th, mt = 2 * tt + ((lane >> 4) & 1);
          const int off = ks < KS2 ? HP_OFF + mb2 * HP_MB + ks * 4096 + mt * 1024 + ((2 * hi) * 16 + (lane & 15)) * 16 : -1024;
#pragma unroll
          for (int hh = 0; hh < 2; ++hh) {
            unsigned pk[4];
#pragma unroll
            for (int e2 = 0; e2 < 4; ++e2) {
              const int ee = hh * 4 + e2;
              const f32x2 cc = f32x2{c4[ee >> 1][(2 * ee) & 3], c4[ee >> 1][(2 * ee + 1) & 3]};
              const f32x2 dd = __builtin_elementwise_fma(cc, f32x2{-st.x, -st.x}, f32x2{a[2 * ee], a[2 * ee + 1]});
              const f32x2 zz = dd * zs;
              const f32x2 ex = f32x2{__builtin_amdgcn_exp2f(zz.x), __builtin_amdgcn_exp2f(zz.y)};
              const f32x2 ss = __builtin_elementwise_fma(ex, dn, dn);
              const f32x2 hv2 = dd * f32x2{__builtin_amdgcn_rcpf(ss.x), __builtin_amdgcn_rcpf(ss.y)};
              pk[e2] = pack2(hv2.x, hv2.y);
            }
            __builtin_amdgcn_raw_buffer_store_b128(v4u{pk[0], pk[1], pk[2], pk[3]}, rws, off + hh * 256, 0, SC1);
            SB();
          }
        }
      }
      lds_add(L + C_MMA, 1u, lane);
      STAMP(7);
      WAITV(0);
      {
        unsigned old = 0;
        if (lane == 0) old = __hip_atomic_fetch_add((LAS unsigned*)(L + C_H), 1u, RLX_WG);
        if (__builtin_amdgcn_readfirstlane(old) == 3) set_flag(ws, FL_H + (p * 16 + j) * FSTR, tag, lane);
      }
      STAMP(8);
    } else {
      const int idx16 = 4 * rg + pi;
      const int srow = 512 * rg + 64 * (4 * hf + pi) + 16 * w + (lane >> 3);
      const int cg = 14 * hf + idx16;
      const bool csv = idx16 < 14 - hf;
      const __amdgpu_buffer_rsrc_t rx = mkrsrc(x, S * D * 2);
      v4u xv[2][10], wv[20];
#pragma unroll
      for (int h2 = 0; h2 < 2; ++h2)
#pragma unroll
        for (int i = 0; i < 10; ++i) xv[h2][i] = __builtin_amdgcn_raw_buffer_load_b128(rx, (srow + 8 * h2) * 1280 + (lane & 7) * 16 + i * 128, 0, 0);
      SB();
      WAITV(20);
      BAR();
      PREP_ITEM(aw, 3); PREP_ITEM(aw, 4);
      WAITL(); lds_add(L + C_AUX, 1u, lane);
      SB();
      const int wvo = csv ? (160 * w + (lane >> 3)) * (F * 2) + cg * 128 + (lane & 7) * 16 : -16;
#pragma unroll
      for (int i = 0; i < 20; ++i) wv[i] = __builtin_amdgcn_raw_buffer_load_b128(rw, wvo, csv ? i * 8 * F * 2 : 0, 0);
      SB();
      tag = __builtin_amdgcn_readfirstlane(*(LAS unsigned*)(L + C_TAG));
#pragma unroll
      for (int h2 = 0; h2 < 2; ++h2) {
        float s = 0.f;
#pragma unroll
        for (int i = 0; i < 10; ++i)
#pragma unroll
          for (int e2 = 0; e2 < 4; ++e2) s = dot2(xv[h2][i][e2], 0x3f803f80u, s);
        s += __shfl_xor(s, 1); s += __shfl_xor(s, 2); s += __shfl_xor(s, 4);
        const float mean = s * (1.f / D);
        float q0 = 0.f, q1 = 0.f;
#pragma unroll
        for (int i = 0; i < 10; ++i)
#pragma unroll
          for (int e2 = 0; e2 < 4; ++e2) { const float a0 = bflo(xv[h2][i][e2]) - mean, a1 = bfhi(xv[h2][i][e2]) - mean; q0 = fmaf(a0, a0, q0); q1 = fmaf(a1, a1, q1); }
        float q = q0 + q1;
        q += __shfl_xor(q, 1); q += __shfl_xor(q, 2); q += __shfl_xor(q, 4);
        const float den = __builtin_sqrtf(rbf(rbf(q * (1.f / D)) + 1e-5f));
        __builtin_amdgcn_raw_buffer_store_b64(v2u{pack2(mean, den), tag}, rws, (lane & 7) == 0 ? ST_OFF + (srow + 8 * h2) * 8 : -16, 0, SC1);
      }
      float c[8] = {};
#pragma unroll
      for (int i = 0; i < 20; ++i)
#pragma unroll
        for (int e2 = 0; e2 < 4; ++e2) { c[2 * e2] += bflo(wv[i][e2]); c[2 * e2 + 1] += bfhi(wv[i][e2]); }
      *(LAS f32x4*)(L + ((w * 8 + (lane >> 3)) * 64 + (lane & 7) * 8) * 4) = f32x4{c[0], c[1], c[2], c[3]};
      *(LAS f32x4*)(L + ((w * 8 + (lane >> 3)) * 64 + (lane & 7) * 8 + 4) * 4) = f32x4{c[4], c[5], c[6], c[7]};
      WAITL();
      unsigned old = 0;
      if (lane == 0) old = __hip_atomic_fetch_add((LAS unsigned*)(L + C_H), 1u, RLX_WG);
      if (__builtin_amdgcn_readfirstlane(old) == 3) {
        asm volatile("" ::: "memory");
        float t4[4] = {};
#pragma unroll
        for (int i = 0; i < 32; ++i) t4[i & 3] += *(LAS float*)(L + (i * 64 + lane) * 4);
        __builtin_amdgcn_raw_buffer_store_b64(v2u{__float_as_uint((t4[0] + t4[1]) + (t4[2] + t4[3])), tag}, rws, csv ? CS_OFF + (cg * 64 + lane) * 8 : -16, 0, SC1);
      }
      lds_add(L + C_MMA, 12u, lane);
      STAMP(5);
      if (b == 56 && w == 0) {
#pragma unroll 1
        for (unsigned sp = 0; sp < SPIN_CAP; ++sp) {
          bool ok = true;
#pragma unroll
          for (int i = 0; i < 4; ++i) ok = ok && flag_load(ws, FL_P + i * 64 * FSTR, 64, lane) == tag;
          if (__all(ok)) break;
          __builtin_amdgcn_s_sleep(32);
        }
        set_flag(ws, FL_GEN, tag, lane);
      }
    }
    unsigned hb_ = 0;
#define HBAR() do { WAITL(); lds_add(L + C_G2, 1u, lane); hb_ += 4; lds_wait(L + C_G2, hb_); } while (0)
    {
      const __amdgpu_buffer_rsrc_t rxx = mkrsrc(x, S * D * 2);
      lds_wait_sleep(L + C_MMA, 48);
#pragma unroll
      for (int i = 0; i < 3; ++i) {
        const int s = aw + 4 * i;
        if (s < 10) {
          const int n = s * 64 + lane; const int row = n / 10, c = n - row * 10;
          GLDS(rxx, L + EX + s * 1024, (64 * mb + row) * 1280 + 160 * nb + c * 16, 0);
        }
      }
      const float dv = bflo(Dv[dpair + (aw >> 1)]), dv3 = bflo(Dv[d3]);
      lds_wait_sleep(L + C_ST, 1);
      __builtin_amdgcn_s_setprio(C8_PRIO);
      LAS char* Lc = L + CV + (aw >> 1) * CH_BYTES;
      const int cts = aw & 1;
      f32x4 acc[4] = {};
      if (cts == 0) conv8<0>(Lc + aoff, Lc + boff, acc); else conv8<1>(Lc + aoff, Lc + boff, acc);
      __builtin_amdgcn_s_setprio(MMA_PRIO);
      STAMP(9);
#pragma unroll
      for (int ti = 0; ti < 4; ++ti) conv_out(Lc, acc[ti], cts == 0 ? (ti < 2 ? ti : ti + 4) : 2 + ti, dpair + (aw >> 1), dv, e, kq, rws, tag);
      f32x4 ac3[4] = {};
      __builtin_amdgcn_s_setprio(C3_PRIO);
      if (ts3 == 0) conv2<0>(L + CV3 + aoff + aw * 128, L + CV3 + boff + aw * 128, ac3); else conv2<1>(L + CV3 + aoff + aw * 128, L + CV3 + boff + aw * 128, ac3);
      __builtin_amdgcn_s_setprio(MMA_PRIO);
      STAMP(13);
#pragma unroll
      for (int ti = 0; ti < 4; ++ti) *(LAS f32x4*)(L + SC3 + (aw * 4 + ti) * 1024 + lane * 16) = ac3[ti];
      WAITV(4);
      HBAR();
      f32x4 a = {};
#pragma unroll
      for (int ww = 0; ww < 4; ++ww) a += *(LAS f32x4*)(L + SC3 + (ww * 4 + aw) * 1024 + lane * 16);
      conv_out(L + CV3, a, ts3 == 0 ? (aw < 2 ? aw : aw + 4) : 2 + aw, d3, dv3, e, kq, rws, tag);
    }
    STAMP(10);
    {
      const int q = (lane & 15) >> 2, pp = lane & 3, pos = lane & 15;
      const int tok = 16 * aw + 2 * (pos >> 2) + (pos & 1) + 8 * ((pos >> 1) & 1);
      lds_wait_sleep(L + C_S, 1);
      const f32x2 st = *(LAS f32x2*)(L + XFS + ((mb & 1) * 64 + tok) * 8);
      v4u xn[5];
#pragma unroll
      for (int i = 0; i < 5; ++i) {
        const v2u xv2 = *(LAS v2u*)(L + EX + tok * 160 + (4 * i + kq) * 8);
        const unsigned d01 = pack2(bflo(xv2.x) - st.x, bfhi(xv2.x) - st.x), d23 = pack2(bflo(xv2.y) - st.x, bfhi(xv2.y) - st.x);
        xn[i] = v4u{pack2(bflo(d01) * st.y, bfhi(d01) * st.y), pack2(bflo(d23) * st.y, bfhi(d23) * st.y), xv2.x, xv2.y};
      }
      WAITL();
      lds_add(L + C_Y, 1u, lane);
      for (int i = 0; i < Y_DELAY; ++i) __builtin_amdgcn_s_sleep(4);
      STAMP(11);
      {
        v4u yv[5];
        const int yo = Y_OFF + (80 * nb + 4 * aw + kq) * 8192 + mb * 256 + e * 16;
#pragma unroll 1
        for (unsigned sp = 0; sp < SPIN_CAP; ++sp) {
          asm volatile("" ::: "memory");
          bool ok = true;
#pragma unroll
          for (int i = 0; i < 5; ++i) yv[i] = __builtin_amdgcn_raw_buffer_load_b128(rws, yo, i * 16 * 8192, SC1);
#pragma unroll
          for (int i = 0; i < 5; ++i) ok = ok && yv[i].y == tag && yv[i].w == tag;
          if (__all(ok)) break;
          __builtin_amdgcn_s_sleep(POLL_SLEEP);
        }
#pragma unroll
        for (int i = 0; i < 5; ++i) *(LAS v2u*)(L + EY + (16 * i + 4 * aw + kq) * 144 + e * 8) = v2u{yv[i].x, yv[i].z};
      }
      HBAR();
      STAMP(12);
#pragma unroll
      for (int i = 0; i < 5; ++i) {
        const int dq = 4 * i + kq;
        const v2u yv = trread(L + EY + (4 * dq + q) * 144 + aw * 32 + pp * 8);
        const unsigned s01 = pack2(bflo(xn[i].x) * bflo(yv.x), bfhi(xn[i].x) * bfhi(yv.x)), s23 = pack2(bflo(xn[i].y) * bflo(yv.y), bfhi(xn[i].y) * bfhi(yv.y));
        *(LAS v4u*)(L + ES + (tok * 20 + dq) * 16) = v4u{s01, s23, xn[i].z, xn[i].w};
      }
    }
  }
  STAMP(14);
  BAR();
  const int lane2 = __builtin_amdgcn_mbcnt_hi(~0u, __builtin_amdgcn_mbcnt_lo(~0u, opaque0()));
#pragma unroll
  for (int i = 0; i < 3; ++i) {
    const int it = w + 8 * i;
    if (it < 20) {
      const int n = it * 64 + lane2;
      const int tok = n / 20, dq = n - tok * 20;
      f32x4 o = *(LAS f32x4*)(L + tok * PSTR + dq * 16);
#pragma unroll
      for (int ww = 1; ww < 4; ++ww) o += *(LAS f32x4*)(L + ww * PART + tok * PSTR + dq * 16);
      const v4u sx = *(LAS v4u*)(L + ES + n * 16);
      const unsigned o01 = pack2(o[0], o[1]), o23 = pack2(o[2], o[3]);
      const unsigned g01 = pack2(silu_f(bflo(o01)), silu_f(bfhi(o01))), g23 = pack2(silu_f(bflo(o23)), silu_f(bfhi(o23)));
      const unsigned m01 = pack2(bflo(sx.x) * bflo(g01), bfhi(sx.x) * bfhi(g01)), m23 = pack2(bflo(sx.y) * bflo(g23), bfhi(sx.y) * bfhi(g23));
      v2u res;
      res.x = pack2(bflo(sx.z) + bflo(m01), bfhi(sx.z) + bfhi(m01));
      res.y = pack2(bflo(sx.w) + bflo(m23), bfhi(sx.w) + bfhi(m23));
      *(v2u*)(out + (size_t)(64 * mb + tok) * D + 80 * nb + dq * 4) = res;
    }
  }
  STAMP(15);
  STAMP_OUT(0);
}

extern "C" void kernel_launch(void* const* d_in, const int* in_sizes, int n_in,
                              void* d_out, int out_size, void* d_ws, size_t ws_size,
                              hipStream_t stream) {
  fused<<<256, 512, 0, stream>>>((const u16*)d_in[0], (const u16*)d_in[1], (const u16*)d_in[2], (const u16*)d_in[3],
                                 (const u16*)d_in[4], (const u16*)d_in[5], (const u16*)d_in[7], (const u16*)d_in[8],
                                 (char*)d_ws, (u16*)d_out);
}
```

```cpp
#include <hip/hip_runtime.h>
#include <stdint.h>

typedef unsigned short u16;
typedef unsigned v4u __attribute__((ext_vector_type(4)));
typedef unsigned v2u __attribute__((ext_vector_type(2)));
typedef float f32x2 __attribute__((ext_vector_type(2)));
typedef float f32x4 __attribute__((ext_vector_type(4)));
typedef float f32x16 __attribute__((ext_vector_type(16)));
typedef short bf16x8 __attribute__((ext_vector_type(8)));
typedef short s16x4 __attribute__((ext_vector_type(4)));
typedef __bf16 bf16x2 __attribute__((ext_vector_type(2)));
#define LAS __attribute__((address_space(3)))
#define GAS __attribute__((address_space(1)))
#define DI __device__ __forceinline__
#define RLX_AGENT __ATOMIC_RELAXED, __HIP_MEMORY_SCOPE_AGENT
#define RLX_WG __ATOMIC_RELAXED, __HIP_MEMORY_SCOPE_WORKGROUP

constexpr int S = 2048, D = 640, F = 1728;
constexpr int KS2 = 54;
constexpr int FSTR = 128;
constexpr int FL_GEN = 0, FL_P = 131072, FL_H = FL_P + 256 * 256;
constexpr int ST_OFF = 32768;
constexpr int CS_OFF = ST_OFF + S * 8;
constexpr int HP_OFF = 262144;
constexpr int HP_MB = KS2 * 4096;
constexpr int W2P_OFF = HP_OFF + 32 * HP_MB;
constexpr int W2P_NB = KS2 * 5120;
constexpr int R_OFF = W2P_OFF + 8 * W2P_NB;
constexpr int Y_OFF = R_OFF + D * S * 2;
constexpr int WS_BYTES = Y_OFF + D * S * 4;
constexpr unsigned RSRC3 = 0x00020000u;
constexpr int SC1 = 16;
constexpr unsigned SPIN_CAP = 1u << 18;

DI float bflo(unsigned u) { return __uint_as_float(u << 16); }
DI float bfhi(unsigned u) { return __uint_as_float(u & 0xffff0000u); }
DI unsigned pack2(float a, float b) {
  bf16x2 r = __builtin_convertvector(f32x2{a, b}, bf16x2);
  return __builtin_bit_cast(unsigned, r);
}
DI float rbf(float a) { return bflo(pack2(a, a)); }
DI float silu_f(float x) { return x * __builtin_amdgcn_rcpf(1.f + __builtin_amdgcn_exp2f(-1.44269504088896f * x)); }
DI f32x4 mfma16(v4u a, v4u b, f32x4 c) {
  return __builtin_amdgcn_mfma_f32_16x16x32_bf16(__builtin_bit_cast(bf16x8, a), __builtin_bit_cast(bf16x8, b), c, 0, 0, 0);
}
DI f32x16 mfma32(v4u a, v4u b, f32x16 c) {
  return __builtin_amdgcn_mfma_f32_32x32x16_bf16(__builtin_bit_cast(bf16x8, a), __builtin_bit_cast(bf16x8, b), c, 0, 0, 0);
}
DI float dot2(unsigned a, unsigned b, float c) {
  return __builtin_amdgcn_fdot2_f32_bf16(__builtin_bit_cast(bf16x2, a), __builtin_bit_cast(bf16x2, b), c, false);
}
DI v2u trread(LAS char* p) { return __builtin_bit_cast(v2u, __builtin_amdgcn_ds_read_tr16_b64_v4i16((LAS s16x4*)p)); }
DI v4u ldsr128(LAS char* p) { return *(LAS v4u*)p; }
DI __amdgpu_buffer_rsrc_t mkrsrc(const void* p, int bytes) { return __builtin_amdgcn_make_buffer_rsrc((void*)p, 0, bytes, RSRC3); }
DI void glds_asm(__amdgpu_buffer_rsrc_t rs, unsigned ldsa, int voff, int soff) {
  asm volatile("s_nop 4\n\ts_mov_b32 m0, %0\n\ts_nop 0\n\tbuffer_load_dwordx4 %1, %2, %3 offen lds"
               :: "s"(ldsa), "v"(voff), "s"(rs), "s"(soff) : "m0", "memory");
}
DI void glds_fast(__amdgpu_buffer_rsrc_t rs, unsigned ldsa, int voff, int soff) {
  asm volatile("s_mov_b32 m0, %0\n\ts_nop 0\n\tbuffer_load_dwordx4 %1, %2, %3 offen lds"
               :: "s"(ldsa), "v"(voff), "s"(rs), "s"(soff) : "m0", "memory");
}
#define GLDS(rs, ldsp, voff, soff) glds_asm(rs, (unsigned)(uintptr_t)(ldsp), voff, soff)
#define WAITV(n) asm volatile("s_waitcnt vmcnt(" #n ")" ::: "memory")
#define WAITL() asm volatile("s_waitcnt lgkmcnt(0)" ::: "memory")
#define BAR() do { WAITL(); __builtin_amdgcn_s_barrier(); asm volatile("" ::: "memory"); } while (0)
#define SB() __builtin_amdgcn_sched_barrier(0)


constexpr int NBUF = 3;
constexpr int CH_HA = 0, CH_HB = 4224, CH_R = 8448, CH_RS = 4512, CH_BYTES = CH_R + 4 * CH_RS;
constexpr int CV = 98304;
constexpr int CV3 = 32768;
constexpr int SC3 = 65536;
constexpr int PK = CV, PV = PK + 11264, PW = PV + 10240, PS = PW + 10240, PO = PS + 1024;
constexpr int EY = CV, EX = 0, ES = EY + 80 * 144;
constexpr int XFS = CV + 2 * CH_BYTES;
constexpr int XFC = XFS + 1024;
constexpr int CNT = XFC + 512;
constexpr int C_AUX = CNT, C_G2 = CNT + 4, C_Y = CNT + 8, C_H = CNT + 12, C_MMA = CNT + 16, C_S = CNT + 20, C_P = CNT + 24,
              C_TAG = CNT + 60, C_ST = CNT + 40, C_HR = CNT + 32;
constexpr int LDS_BYTES = CNT + 64;
static_assert(PO + 10240 <= XFS && ES + 20480 <= XFS && LDS_BYTES <= 163840, "lds");
constexpr int PSTR = 336, PART = 64 * PSTR;

DI void lds_add(LAS char* c, unsigned v, int lane) {
  if (lane == 0) __hip_atomic_fetch_add((LAS unsigned*)c, v, RLX_WG);
}
DI void lds_wait(LAS char* c, unsigned target) {
  while ((unsigned)__builtin_amdgcn_readfirstlane(__hip_atomic_load((LAS unsigned*)c, RLX_WG)) < target) {}
  asm volatile("" ::: "memory");
}
DI void lds_wait_sleep(LAS char* c, unsigned target) {
  while ((unsigned)__builtin_amdgcn_readfirstlane(__hip_atomic_load((LAS unsigned*)c, RLX_WG)) < target) __builtin_amdgcn_s_sleep(1);
  asm volatile("" ::: "memory");
}
DI unsigned flag_load(char* ws, int off, int n, int lane) {
  return __hip_atomic_load((GAS unsigned*)(ws + off + (lane < n ? lane : 0) * FSTR), RLX_AGENT);
}
DI void poll_flags(char* ws, int off, int n, unsigned tag, int lane) {
#pragma unroll 1
  for (unsigned spins = 0; spins < SPIN_CAP; ++spins) {
    const unsigned v = flag_load(ws, off, n, lane);
    if (__all(v == tag)) break;
    __builtin_amdgcn_s_sleep(8);
  }
  __builtin_amdgcn_fence(__ATOMIC_ACQUIRE, "wavefront");
}
DI void set_flag(char* ws, int off, unsigned tag, int lane) {
  if (lane == 0) __hip_atomic_store((GAS unsigned*)(ws + off), tag, RLX_AGENT);
}

DI unsigned opaque0() { unsigned z; asm volatile("v_mov_b32 %0, 0" : "=v"(z)); return z; }
template <int I> struct IC { static constexpr int value = I; };
template <int B, int E, class Fn> DI void static_for(Fn&& f) { if constexpr (B < E) { f(IC<B>{}); static_for<B + 1, E>(f); } }
struct Frags { v4u x[4][2], w[4][2]; };

template <int TS> DI void conv_ld(LAS char* pa, LAS char* pb, v4u (&A)[8], v4u (&B)[8]) {
  constexpr int NA = TS == 0 ? 8 : 6;
#pragma unroll
  for (int a = 0; a < NA; ++a) { A[a] = ldsr128(pa + a * 512); B[a] = ldsr128(pb + (7 - a) * 512); }
}
template <int TS> DI void conv_mm(const v4u (&A)[8], const v4u (&B)[8], f32x4 (&acc)[4]) {
#pragma unroll
  for (int ti = 0; ti < 4; ++ti) {
    const int n = TS == 0 ? (ti == 0 ? 0 : ti == 1 ? 1 : ti == 2 ? 6 : 7) : 2 + ti;
#pragma unroll
    for (int a = 0; a <= n; ++a) acc[ti] = mfma16(A[a], B[n - a], acc[ti]);
  }
}
DI void conv_sched() {
#pragma unroll
  for (int i = 0; i < 16; ++i) { __builtin_amdgcn_sched_group_barrier(0x8, 1, 0); __builtin_amdgcn_sched_group_barrier(0x100, 1, 0); }
  __builtin_amdgcn_sched_group_barrier(0x8, 2, 0);
}
template <int TS> DI void conv8(LAS char* pa, LAS char* pb, f32x4 (&acc)[4]) {
  v4u A0[8], B0[8], A1[8], B1[8];
  conv_ld<TS>(pa, pb, A0, B0);
  SB();
#pragma unroll 1
  for (int r = 0; r < 8; r += 2) {
    conv_ld<TS>(pa + 64, pb + 64, A1, B1); conv_mm<TS>(A0, B0, acc); conv_sched(); SB();
    pa += 128; pb += 128;
    conv_ld<TS>(pa, pb, A0, B0); conv_mm<TS>(A1, B1, acc); conv_sched(); SB();
  }
}
template <int TS> DI void conv2(LAS char* pa, LAS char* pb, f32x4 (&acc)[4]) {
#pragma unroll 1
  for (int r = 0; r < 2; ++r) {
    v4u A[8], B[8];
    conv_ld<TS>(pa, pb, A, B); conv_mm<TS>(A, B, acc);
    pa += 64; pb += 64;
  }
}
DI unsigned kshift2(float k1, float k2, float k3, float k4, float k5, float c0, float c1, float c2, float c3) {
  unsigned a = pack2(k1 * c3, k2 * c3);
  unsigned t = pack2(k2 * c2, k3 * c2);
  a = pack2(bflo(a) + bflo(t), bfhi(a) + bfhi(t));
  t = pack2(k3 * c1, k4 * c1);
  a = pack2(bflo(a) + bflo(t), bfhi(a) + bfhi(t));
  t = pack2(k4 * c0, k5 * c0);
  return pack2(bflo(a) + bflo(t), bfhi(a) + bfhi(t));
}
DI void stage_h(LAS char* Lc, v4u h, int g) {
  *(LAS v4u*)(Lc + CH_HB + 112 + g * 16) = h;
  LAS char* a = Lc + CH_HA + 114 + g * 16;
  *(LAS u16*)(a) = (u16)(h.x & 0xffff);
  *(LAS unsigned*)(a + 2) = __builtin_amdgcn_alignbit(h.y, h.x, 16);
  *(LAS v2u*)(a + 6) = v2u{__builtin_amdgcn_alignbit(h.z, h.y, 16), __builtin_amdgcn_alignbit(h.w, h.z, 16)};
  *(LAS u16*)(a + 14) = (u16)(h.w >> 16);
}
DI void stage_r(LAS char* Lc, v4u u, int g) {
  LAS char* rb = Lc + CH_R + g * 16;
  *(LAS v4u*)(rb) = u;
  { LAS char* q = rb + CH_RS - 4; *(LAS unsigned*)(q) = u.x; *(LAS v2u*)(q + 4) = v2u{u.y, u.z}; *(LAS unsigned*)(q + 12) = u.w; }
  { LAS char* q = rb + 2 * CH_RS - 8; *(LAS v2u*)(q) = v2u{u.x, u.y}; *(LAS v2u*)(q + 8) = v2u{u.z, u.w}; }
  { LAS char* q = rb + 3 * CH_RS - 12; *(LAS unsigned*)(q) = u.x; *(LAS v2u*)(q + 4) = v2u{u.y, u.z}; *(LAS unsigned*)(q + 12) = u.w; }
}
DI void conv_out(LAS char* Lc, f32x4 a, int n, int d, float dv, int e, int kq, __amdgpu_buffer_rsrc_t rws, unsigned tag) {
  const int t = 256 * n + 64 * (e >> 2) + 2 * (e & 3) + 16 * kq;
  const unsigned ua = *(LAS unsigned*)(Lc + CH_R + 2 * (2046 - t));
  const unsigned ub = *(LAS unsigned*)(Lc + CH_R + 2 * (2038 - t));
  const unsigned c01 = pack2(a[0], a[1]), c23 = pack2(a[2], a[3]);
  const unsigned d01 = pack2(bfhi(ua) * dv, bflo(ua) * dv), d23 = pack2(bfhi(ub) * dv, bflo(ub) * dv);
  v2u o;
  o.x = pack2(bflo(c01) + bflo(d01), bfhi(c01) + bfhi(d01));
  o.y = pack2(bflo(c23) + bflo(d23), bfhi(c23) + bfhi(d23));
  const int off = Y_OFF + d * 8192 + (256 * n + 64 * (e >> 2) + 16 * kq + 4 * (e & 3)) * 4;
  __builtin_amdgcn_raw_buffer_store_b128(v4u{o.x, tag, o.y, tag}, rws, off, 0, SC1);
}

__global__ __launch_bounds__(512, 2) void fused(const u16* x, const u16* __restrict__ kk, const u16* __restrict__ vv,
                                                const u16* __restrict__ sk, const u16* __restrict__ hk, const u16* __restrict__ Dv,
                                                const u16* __restrict__ w1, const u16* __restrict__ w2,
                                                char* ws, u16* out) {
  __shared__ __attribute__((aligned(16))) char smem[LDS_BYTES];
  LAS char* L = (LAS char*)smem;
  if (__builtin_amdgcn_readfirstlane(threadIdx.x >> 6) == 7) {
    unsigned long long pc;
    asm volatile("s_getpc_b64 %0" : "=s"(pc));
    const unsigned co = min(((blockIdx.x >> 3) & 7) * 8192u + (threadIdx.x & 63) * 128u, 39000u);
    asm volatile("s_mov_b32 m0, %0\n\ts_nop 0\n\tglobal_load_lds_dword %1, %2" :: "s"((unsigned)(uintptr_t)(L + XFS)), "v"(co), "s"(pc) : "m0");
  }
  const int b = blockIdx.x, xcd = b & 7, r = b >> 3;
  const int rg = xcd >> 1, hf = xcd & 1, pi = r >> 3, jj = r & 7;
  const int p = 4 * rg + pi, j = 7 * hf + jj;
  const bool g1 = jj < 7;
  const int mbl = 2 * pi + ((r >> 2) & 1), nbl = r & 3;
  const int mb = 8 * rg + mbl, nb = 4 * hf + nbl;
  const int lane = threadIdx.x & 63;
  const int w = __builtin_amdgcn_readfirstlane(threadIdx.x >> 6);
  const int hi = lane >> 5, aw = w & 3;
  const __amdgpu_buffer_rsrc_t rws = mkrsrc(ws, WS_BYTES);
  if (threadIdx.x < 15) *(LAS unsigned*)(L + CNT + threadIdx.x * 4) = 0u;
  const int dpair = 80 * nb + 2 * mb, d3 = 80 * nb + 64 + (mb >> 1), ts3 = mb & 1;
  const int e = lane & 15, kq = lane >> 4;
  const int aoff = ((e & 1) ? CH_HB : CH_HA) + (kq + (e >> 1)) * 16;
  const int boff = CH_R + (3 - (e & 3)) * CH_RS + (248 - 32 * 7 - 8 * (e >> 2) + kq) * 16;

  {
    const __amdgpu_buffer_rsrc_t rk = mkrsrc(kk, S * D * 2), rv_ = mkrsrc(vv, S * D * 2), rsk = mkrsrc(sk, D * 8);
    const int sb = 64 * mb * 1280 + 160 * nb;
    int vo[2];
#pragma unroll
    for (int i = 0; i < 2; ++i) { const int n = (8 * i + w) * 64 + lane; const int row = n / 10; vo[i] = row * 1280 + (n - row * 10) * 16; }
    GLDS(rk, L + PK + w * 1024, vo[0] + sb - 5120, 0);
    GLDS(rv_, L + PV + w * 1024, vo[0], sb);
    {
      const int n = (5 + w) * 64 + lane; const int row = n / 10; const int vo3 = row * 1280 + (n - row * 10) * 16;
      if (w < 3) GLDS(rk, L + PK + (8 + w) * 1024, vo[1] + sb - 5120, 0);
      else if (w < 5) GLDS(rv_, L + PV + (5 + w) * 1024, vo3, sb);
      else if (w == 5) GLDS(rsk, L + PS, lane * 16, nb * 640);
    }
  }
#define PREP_ITEM(tg, cg) do { \
        const int q_ = (lane & 15) >> 2, pp_ = lane & 3, tl = 16 * (tg) + 4 * kq; \
        LAS char* ka_ = L + PK + (tl + q_) * 160 + (cg) * 32 + pp_ * 8; \
        v2u k0 = trread(ka_), k1 = trread(ka_ + 4 * 160); \
        v2u v0 = trread(ka_ + (PV - PK)); \
        v2u sw = *(LAS v2u*)(L + PS + ((cg) * 16 + e) * 8); \
        float kf[8] = {bflo(k0.x), bfhi(k0.x), bflo(k0.y), bfhi(k0.y), bflo(k1.x), bfhi(k1.x), bflo(k1.y), bfhi(k1.y)}; \
        float vf[4] = {bflo(v0.x), bfhi(v0.x), bflo(v0.y), bfhi(v0.y)}; \
        const float c0 = bflo(sw.x), c1 = bfhi(sw.x), c2 = bflo(sw.y), c3 = bfhi(sw.y); \
        float u[4]; \
        _Pragma("unroll") for (int e2 = 0; e2 < 4; e2 += 2) { \
          const unsigned a = kshift2(kf[e2 + 1], kf[e2 + 2], kf[e2 + 3], kf[e2 + 4], kf[e2 + 5], c0, c1, c2, c3); \
          u[e2] = bflo(a) * vf[e2]; u[e2 + 1] = bfhi(a) * vf[e2 + 1]; } \
        v2u o = v2u{pack2(u[3], u[2]), pack2(u[1], u[0])}; \
        const int ch = (cg) * 16 + e; \
        const int pos8 = (60 - tl) >> 2; \
        *(LAS v2u*)(L + PO + ch * 128 + ((((pos8 >> 1) ^ (ch & 7)) << 4) | ((pos8 & 1) << 3))) = o; \
      } while (0)

  if (w >= 4) {
    const unsigned gen = __hip_atomic_load((GAS unsigned*)(ws + FL_GEN), RLX_AGENT);
    WAITV(0);
    const unsigned tag = (unsigned)__builtin_amdgcn_readfirstlane(gen) * 0x9E3779B1u + 0x85EBCA6Bu;
    if (aw == 0 && lane == 0) *(LAS unsigned*)(L + C_TAG) = tag;
#define AUXBAR() do { WAITL(); lds_add(L + C_AUX, 1u, lane); ab += 4; lds_wait(L + C_AUX, ab); } while (0)
    BAR();
    unsigned ab = 4;
    {
      const __amdgpu_buffer_rsrc_t rw2 = mkrsrc(w2, F * D * 2);
#pragma unroll
      for (int i = 0; i < 3; ++i) if (4 * i + aw < 10) {
        const int n = (4 * i + aw) * 64 + lane; const int row = n / 10;
        GLDS(rw2, L + PW + (4 * i + aw) * 1024, (row + (row >= 32 ? 992 : 0)) * 1280 + (n - row * 10) * 16, 32 * mb * 1280 + 160 * nb);
      }
    }
    {
      const int g = lane >> 4, q = (lane & 15) >> 2, pp = lane & 3;
      PREP_ITEM(aw, 0); PREP_ITEM(aw, 1); PREP_ITEM(aw, 2);
      AUXBAR();
#pragma unroll
      for (int i = 0; i < 3; ++i) {
        const int ri = aw + 4 * i;
        if (ri < 10) {
          const int ch = 8 * ri + (lane >> 3), cc = lane & 7;
          v4u val = ldsr128(L + PO + ch * 128 + ((cc ^ (ch & 7)) << 4));
          __builtin_amdgcn_raw_buffer_store_b128(val, rws, R_OFF + (80 * nb + ch) * 4096 + (31 - mb) * 128 + cc * 16, 0, SC1);
        }
      }
      __builtin_amdgcn_s_setprio(0);
      if (aw < 2) WAITV(3); else WAITV(2);
      AUXBAR();
#pragma unroll
      for (int i = 0; i < 3; ++i) {
        const int fi = aw + 4 * i;
        if (fi < 10) {
          const int half = fi / 5, nt = fi - half * 5, ks = mb + 32 * half;
          const int r0 = 16 * (g & 1) + 4 * (g >> 1);
          LAS char* a = L + PW + half * 5120 + (r0 + q) * 160 + nt * 32 + pp * 8;
          v2u lo = trread(a), hi2 = trread(a + 8 * 160);
          __builtin_amdgcn_raw_buffer_store_b128(v4u{lo.x, lo.y, hi2.x, hi2.y}, rws, ks < KS2 ? W2P_OFF + nb * W2P_NB + ks * 5120 + nt * 1024 + lane * 16 : -16, 0, SC1);
        }
      }
    }
    WAITV(0);
    AUXBAR();
    if (aw == 0) set_flag(ws, FL_P + (nb * 32 + mb) * FSTR, tag, lane);
    const __amdgpu_buffer_rsrc_t rh = mkrsrc(hk, D * S * 2);
    v4u cu[3];
    cu[0] = __builtin_amdgcn_raw_buffer_load_b128(rh, dpair * 4096 + aw * 1024 + lane * 16, 0, 0);
    cu[1] = __builtin_amdgcn_raw_buffer_load_b128(rh, dpair * 4096 + 4096 + aw * 1024 + lane * 16, 0, 0);
    cu[2] = __builtin_amdgcn_raw_buffer_load_b128(rh, d3 * 4096 + aw * 1024 + lane * 16, 0, 0);
    const int g = aw * 64 + lane;
    lds_wait_sleep(L + C_MMA, 32);
    if (aw == 1) {
      const int jc = g1 ? j : 0;
      v2u gr[4];
#define GRL(off) __builtin_bit_cast(v2u, __hip_atomic_load((GAS unsigned long long*)(ws + (off)), RLX_AGENT))
#pragma unroll 1
      for (unsigned sp = 0; sp < SPIN_CAP; ++sp) {
        gr[0] = GRL(ST_OFF + (p * 128 + lane) * 8);
        gr[1] = GRL(ST_OFF + (p * 128 + 64 + lane) * 8);
        gr[2] = GRL(CS_OFF + (jc * 128 + lane) * 8);
        gr[3] = GRL(CS_OFF + (jc * 128 + (j == 13 ? 0 : 64) + lane) * 8);
        if (__all(gr[0].y == tag && gr[1].y == tag && gr[2].y == tag && gr[3].y == tag)) break;
        __builtin_amdgcn_s_sleep(8);
      }
      *(LAS f32x2*)(L + XFS + lane * 8) = f32x2{bflo(gr[0].x), 1.f / bfhi(gr[0].x)};
      *(LAS f32x2*)(L + XFS + (64 + lane) * 8) = f32x2{bflo(gr[1].x), 1.f / bfhi(gr[1].x)};
      *(LAS unsigned*)(L + XFC + lane * 4) = gr[2].x;
      *(LAS unsigned*)(L + XFC + (64 + lane) * 4) = gr[3].x;
      WAITL();
      lds_add(L + C_S, 1u, lane);
    }
    if (aw == 2) { poll_flags(ws, FL_P + nb * 32 * FSTR, 32, tag, lane); lds_add(L + C_P, 1u, lane); }
    lds_wait_sleep(L + C_P, 1);
    v4u rv[3];
    rv[0] = __builtin_amdgcn_raw_buffer_load_b128(rws, R_OFF + dpair * 4096 + aw * 1024 + lane * 16, 0, SC1);
    rv[1] = __builtin_amdgcn_raw_buffer_load_b128(rws, R_OFF + dpair * 4096 + 4096 + aw * 1024 + lane * 16, 0, SC1);
    rv[2] = __builtin_amdgcn_raw_buffer_load_b128(rws, R_OFF + d3 * 4096 + aw * 1024 + lane * 16, 0, SC1);
    const __amdgpu_buffer_rsrc_t ra = mkrsrc(ws + HP_OFF + mb * HP_MB, HP_MB);
    const __amdgpu_buffer_rsrc_t rb = mkrsrc(ws + W2P_OFF + nb * W2P_NB, W2P_NB);
    const int va = lane * 16 + aw * 4096, vb = lane * 16 + aw * 5120;
    constexpr int WB = 6, NKS = 14, POOL = 41;
    v4u fa[NKS][4], fb[NKS][5];
#define LOADB(i) do { const int so_ = (i) * 20480; \
    _Pragma("unroll") for (int n = 0; n < 5; ++n) fb[i][n] = __builtin_amdgcn_raw_buffer_load_b128(rb, vb + (n & 3) * 1024, so_ + (n >> 2) * 4096, SC1); } while (0)
#pragma unroll
    for (int i = 0; i < WB; ++i) LOADB(i);
    SB();
    lds_wait_sleep(L + C_MMA, 44);
    if (aw == 0 && lane < 32) *(LAS v4u*)(L + CV + (lane >> 4) * CH_BYTES + ((lane >> 3) & 1) * CH_HB + (lane & 7) * 16) = v4u{0, 0, 0, 0};
    if (aw == 3) {
#pragma unroll
      for (int i = 0; i < 4; ++i) {
        const int n = lane + 64 * i;
        if (n < 200) { const int cp = n / 25, sl = n - cp * 25; *(LAS v4u*)(L + CV + (cp >> 2) * CH_BYTES + CH_R + (cp & 3) * CH_RS + 4080 + sl * 16) = v4u{0, 0, 0, 0}; }
      }
    }
    stage_h(L + CV, cu[0], g);
    stage_h(L + CV + CH_BYTES, cu[1], g);
    stage_r(L + CV, rv[0], g);
    stage_r(L + CV + CH_BYTES, rv[1], g);
    if (aw == 0 && lane < 16) *(LAS v4u*)(L + CV3 + (lane >> 3) * CH_HB + (lane & 7) * 16) = v4u{0, 0, 0, 0};
    if (aw == 3) {
#pragma unroll
      for (int i = 0; i < 2; ++i) {
        const int n = lane + 64 * i;
        if (n < 100) { const int cp = n / 25, sl = n - cp * 25; *(LAS v4u*)(L + CV3 + CH_R + cp * CH_RS + 4080 + sl * 16) = v4u{0, 0, 0, 0}; }
      }
    }
    stage_h(L + CV3, cu[2], g);
    stage_r(L + CV3, rv[2], g);
    AUXBAR();
    if (aw == 0) lds_add(L + C_ST, 1u, lane);
    lds_wait_sleep(L + C_H, 4);
    if (aw == 1) { poll_flags(ws, FL_H + p * 16 * FSTR, 14, tag, lane); lds_add(L + C_HR, 1u, lane); }
    __builtin_amdgcn_s_setprio(0);
    lds_wait_sleep(L + C_HR, 1);
#define ISSUE(q) do { constexpr int q_ = (q); \
      if constexpr (q_ < 4 * WB) { fa[q_ / 4][q_ % 4] = __builtin_amdgcn_raw_buffer_load_b128(ra, va + (q_ % 4) * 1024, (q_ / 4) * 16384, SC1); } \
      else if constexpr (q_ < 4 * WB + 9 * (NKS - WB)) { constexpr int k_ = WB + (q_ - 4 * WB) / 9, f_ = (q_ - 4 * WB) % 9; \
        if constexpr (f_ < 4) fa[k_][f_] = __builtin_amdgcn_raw_buffer_load_b128(ra, va + f_ * 1024, k_ * 16384, SC1); \
        else fb[k_][f_ - 4] = __builtin_amdgcn_raw_buffer_load_b128(rb, vb + ((f_ - 4) & 3) * 1024, k_ * 20480 + ((f_ - 4) >> 2) * 4096, SC1); } \
    } while (0)
    constexpr auto need = [](int k) { k = k > NKS ? NKS : k; return k <= WB ? 4 * k : 4 * WB + 9 * (k - WB); };
    constexpr auto cum = [need](int i) { const int a = need(i + 5), c = POOL - 5 * WB + (i > 0 ? 9 * i - 6 : 0); return a < c ? a : c; };
    static_for<0, cum(0)>([&](auto q) { ISSUE(decltype(q)::value); });
    SB();
#define PIN_ACC() do { _Pragma("unroll") for (int m_ = 0; m_ < 4; ++m_) _Pragma("unroll") for (int n_ = 0; n_ < 5; ++n_) asm volatile("" : "+v"(acc2[m_][n_])); } while (0)
    f32x4 acc2[4][5] = {};
    LAS char* pw = L;
    static_for<0, NKS>([&](auto ii) {
      constexpr int i = decltype(ii)::value;
      constexpr int c0 = cum(i), cn = cum(i + 1) - cum(i);
      static_assert(cn <= 9 && c0 >= need(i + 1), "schedule");
      static_for<0, 20>([&](auto mm) {
        constexpr int mn = decltype(mm)::value, m = mn / 5, n = mn % 5;
        acc2[m][n] = mfma16(fb[i][n], fa[i][m], acc2[m][n]);
        constexpr int slot = (mn & 1) ? (mn >> 1) - (9 - cn) : -1;
        if constexpr (slot >= 0 && mn < 19) { SB(); ISSUE(c0 + slot); SB(); }
        if constexpr (i == NKS - 1 && mn >= 2) { constexpr int m2 = (mn - 2) / 5, n2 = (mn - 2) % 5; SB(); *(LAS f32x4*)(pw + 16 * m2 * PSTR + n2 * 64) = acc2[m2][n2]; SB(); }
      });
      SB();
      if constexpr (i == NKS - 2) {
        PIN_ACC();
        lds_wait(L + C_Y, 4);
        const int ln = __builtin_amdgcn_mbcnt_hi(~0u, __builtin_amdgcn_mbcnt_lo(~0u, opaque0()));
        pw = L + aw * PART + (ln & 15) * PSTR + (ln >> 4) * 16;
        SB();
      }
    });
    *(LAS f32x4*)(pw + 48 * PSTR + 3 * 64) = acc2[3][3];
    *(LAS f32x4*)(pw + 48 * PSTR + 4 * 64) = acc2[3][4];
  } else {
    __builtin_amdgcn_s_setprio(2);
    const __amdgpu_buffer_rsrc_t rw = mkrsrc(w1, D * F * 2);
    unsigned tag;
    if (g1) {
      const __amdgpu_buffer_rsrc_t rx = mkrsrc(x + (size_t)p * 128 * D, 128 * D * 2);
      int vx[2], vw[4];
      {
        const int row = 32 * w + (lane >> 3);
        vx[0] = row * 1280 + (((lane & 7) ^ ((row >> 1) & 7)) << 4);
        vx[1] = (row + 8) * 1280 + (((lane & 7) ^ (((row + 8) >> 1) & 7)) << 4);
#pragma unroll
        for (int ii = 0; ii < 4; ++ii) {
          const int kr = 16 * w + 4 * ii + (lane >> 4);
          vw[ii] = kr * (F * 2) + j * 256 + (((lane & 15) ^ (((kr & 3) << 2) | ((kr >> 2) & 3))) << 4);
        }
      }
      const int rot = (5 * ((pi + jj) & 1) + (5 * xcd) / 8) % 10;
      const unsigned lbase = (unsigned)(uintptr_t)L + w * 4096;
#define KC(s) ((s) + rot >= 10 ? (s) + rot - 10 : (s) + rot)
#define G1LD(s, i) do { \
    if ((i) < 4) glds_fast(rx, lbase + ((s) % NBUF) * 32768 + (i) * 1024, vx[(i) & 1], KC(s) * 128 + ((i) >> 1) * 16 * 1280); \
    else glds_fast(rw, lbase + ((s) % NBUF) * 32768 + 16384 + ((i) - 4) * 1024, vw[((i) - 4) & 3], KC(s) * (64 * F * 2)); \
  } while (0)
#pragma unroll
      for (int i = 0; i < 8; ++i) G1LD(0, i);
#pragma unroll
      for (int i = 0; i < 8; ++i) G1LD(1, i);
      WAITV(16);
      BAR();
      PREP_ITEM(aw, 3); PREP_ITEM(aw, 4);
      WAITL(); lds_add(L + C_AUX, 1u, lane);
      SB();
      const int fh = w & 1, th = w >> 1;
      int xa[4], wa[2][2];
      {
        const int row = 64 * th + (lane & 31);
        const int c0 = hi ^ ((row >> 1) & 7);
        const int blk = (lane >> 4) & 1, q = (lane & 15) >> 2, pp = lane & 3;
#pragma unroll
        for (int s = 0; s < 4; ++s) xa[s] = row * 128 + (((2 * s) ^ c0) << 4);
#pragma unroll
        for (int ft = 0; ft < 2; ++ft)
#pragma unroll
          for (int t = 0; t < 2; ++t) {
            const int kr = 8 * hi + 4 * t + q;
            const int ch = 4 * (2 * fh + ft) + 2 * blk + (pp >> 1);
            wa[ft][t] = 16384 + 256 * kr + ((ch ^ ((q << 2) | ((2 * hi + t) & 3))) << 4) + 8 * (pp & 1);
          }
      }
      f32x16 acc[2][2] = {};
      Frags fA, fB;
#define RDX(f, bo, s, tt) f.x[s][tt] = ldsr128(L + (bo) + xa[s] + (tt) * 4096)
#define RDW(f, bo, s, ft) do { v2u wl_ = trread(L + (bo) + wa[ft][0] + (s) * 4096), wh_ = trread(L + (bo) + wa[ft][1] + (s) * 4096); f.w[s][ft] = v4u{wl_.x, wl_.y, wh_.x, wh_.y}; } while (0)
#define MM(f, s, ft, tt) acc[ft][tt] = mfma32(f.w[s][ft], f.x[s][tt], acc[ft][tt])
#define ITER(s, vm, cur, nxt) do { \
    asm volatile("s_waitcnt vmcnt(" #vm ") lgkmcnt(0)" ::: "memory"); \
    lds_add(L + C_MMA, 1u, lane); SB(); \
    MM(cur, 0, 0, 0); MM(cur, 0, 0, 1); MM(cur, 0, 1, 0); SB(); \
    lds_wait(L + C_MMA, 4 * ((s) + 2)); SB(); \
    constexpr int bo_ = (((s) + 1) % NBUF) * 32768; constexpr bool ld_ = (s) + NBUF < 10; \
    MM(cur, 0, 1, 1); RDX(nxt, bo_, 0, 0); RDX(nxt, bo_, 0, 1); SB(); if (ld_) G1LD((s) + NBUF, 0); SB(); \
    MM(cur, 1, 0, 0); RDW(nxt, bo_, 0, 0); SB(); \
    MM(cur, 1, 0, 1); RDW(nxt, bo_, 0, 1); SB(); if (ld_) G1LD((s) + NBUF, 4); SB(); \
    MM(cur, 1, 1, 0); RDX(nxt, bo_, 1, 0); RDX(nxt, bo_, 1, 1); SB(); if (ld_) G1LD((s) + NBUF, 1); SB(); \
    MM(cur, 1, 1, 1); RDW(nxt, bo_, 1, 0); SB(); \
    MM(cur, 2, 0, 0); RDW(nxt, bo_, 1, 1); SB(); if (ld_) G1LD((s) + NBUF, 5); SB(); \
    MM(cur, 2, 0, 1); RDX(nxt, bo_, 2, 0); RDX(nxt, bo_, 2, 1); SB(); if (ld_) G1LD((s) + NBUF, 2); SB(); \
    MM(cur, 2, 1, 0); RDW(nxt, bo_, 2, 0); SB(); \
    MM(cur, 2, 1, 1); RDW(nxt, bo_, 2, 1); SB(); if (ld_) G1LD((s) + NBUF, 6); SB(); \
    MM(cur, 3, 0, 0); RDX(nxt, bo_, 3, 0); RDX(nxt, bo_, 3, 1); SB(); if (ld_) G1LD((s) + NBUF, 3); SB(); \
    MM(cur, 3, 0, 1); RDW(nxt, bo_, 3, 0); RDW(nxt, bo_, 3, 1); SB(); \
    MM(cur, 3, 1, 0); SB(); if (ld_) G1LD((s) + NBUF, 7); SB(); \
    MM(cur, 3, 1, 1); SB(); \
  } while (0)
      WAITV(8);
      lds_add(L + C_MMA, 1u, lane); lds_wait(L + C_MMA, 4);
#pragma unroll
      for (int s = 0; s < 4; ++s) { RDX(fA, 0, s, 0); RDX(fA, 0, s, 1); RDW(fA, 0, s, 0); RDW(fA, 0, s, 1); }
      SB();
#pragma unroll
      for (int i = 0; i < 8; ++i) G1LD(2, i);
      SB();
      ITER(0, 8, fA, fB);
      ITER(1, 8, fB, fA);
      ITER(2, 8, fA, fB);
      ITER(3, 8, fB, fA);
      ITER(4, 8, fA, fB);
      ITER(5, 8, fB, fA);
      ITER(6, 8, fA, fB);
      ITER(7, 8, fB, fA);
      ITER(8, 0, fA, fB);
#pragma unroll
      for (int s = 0; s < 4; ++s) { MM(fB, s, 0, 0); MM(fB, s, 0, 1); MM(fB, s, 1, 0); MM(fB, s, 1, 1); }
      SB();
      lds_add(L + C_MMA, 1u, lane);
      lds_wait_sleep(L + C_S, 1);
      tag = __builtin_amdgcn_readfirstlane(*(LAS unsigned*)(L + C_TAG));
#pragma unroll
      for (int ft = 0; ft < 2; ++ft) {
        const int fg = 2 * fh + ft;
        const int ks = 4 * j + fg;
        f32x4 c4[4];
#pragma unroll
        for (int g = 0; g < 4; ++g) c4[g] = *(LAS f32x4*)(L + XFC + (32 * fg + 8 * g + 4 * hi) * 4);
#pragma unroll
        for (int tt = 0; tt < 2; ++tt) {
          const int tok = 64 * th + 32 * tt + (lane & 31);
          const f32x2 st = *(LAS f32x2*)(L + XFS + tok * 8);
          const f32x16& a = acc[ft][tt];
          const float zs1 = -1.44269504088896f * st.y, dn1 = __builtin_amdgcn_rcpf(st.y);
          const f32x2 zs = f32x2{zs1, zs1}, dn = f32x2{dn1, dn1};
          const int mb2 = 2 * p + th, mt = 2 * tt + ((lane >> 4) & 1);
          const int off = ks < KS2 ? HP_OFF + mb2 * HP_MB + ks * 4096 + mt * 1024 + ((2 * hi) * 16 + (lane & 15)) * 16 : -1024;
#pragma unroll
          for (int hh = 0; hh < 2; ++hh) {
            unsigned pk[4];
#pragma unroll
            for (int e2 = 0; e2 < 4; ++e2) {
              const int ee = hh * 4 + e2;
              const f32x2 cc = f32x2{c4[ee >> 1][(2 * ee) & 3], c4[ee >> 1][(2 * ee + 1) & 3]};
              const f32x2 dd = __builtin_elementwise_fma(cc, f32x2{-st.x, -st.x}, f32x2{a[2 * ee], a[2 * ee + 1]});
              const f32x2 zz = dd * zs;
              const f32x2 ex = f32x2{__builtin_amdgcn_exp2f(zz.x), __builtin_amdgcn_exp2f(zz.y)};
              const f32x2 ss = __builtin_elementwise_fma(ex, dn, dn);
              const f32x2 hv2 = dd * f32x2{__builtin_amdgcn_rcpf(ss.x), __builtin_amdgcn_rcpf(ss.y)};
              pk[e2] = pack2(hv2.x, hv2.y);
            }
            __builtin_amdgcn_raw_buffer_store_b128(v4u{pk[0], pk[1], pk[2], pk[3]}, rws, off + hh * 256, 0, SC1);
            SB();
          }
        }
      }
      lds_add(L + C_MMA, 1u, lane);
      WAITV(0);
      {
        unsigned old = 0;
        if (lane == 0) old = __hip_atomic_fetch_add((LAS unsigned*)(L + C_H), 1u, RLX_WG);
        if (__builtin_amdgcn_readfirstlane(old) == 3) set_flag(ws, FL_H + (p * 16 + j) * FSTR, tag, lane);
      }
    } else {
      const int idx16 = 4 * rg + pi;
      const int srow = 512 * rg + 64 * (4 * hf + pi) + 16 * w + (lane >> 3);
      const int cg = 14 * hf + idx16;
      const bool csv = idx16 < 14 - hf;
      const __amdgpu_buffer_rsrc_t rx = mkrsrc(x, S * D * 2);
      v4u xv[2][10], wv[20];
#pragma unroll
      for (int h2 = 0; h2 < 2; ++h2)
#pragma unroll
        for (int i = 0; i < 10; ++i) xv[h2][i] = __builtin_amdgcn_raw_buffer_load_b128(rx, (srow + 8 * h2) * 1280 + (lane & 7) * 16 + i * 128, 0, 0);
      SB();
      WAITV(20);
      BAR();
      PREP_ITEM(aw, 3); PREP_ITEM(aw, 4);
      WAITL(); lds_add(L + C_AUX, 1u, lane);
      SB();
      const int wvo = csv ? (160 * w + (lane >> 3)) * (F * 2) + cg * 128 + (lane & 7) * 16 : -16;
#pragma unroll
      for (int i = 0; i < 20; ++i) wv[i] = __builtin_amdgcn_raw_buffer_load_b128(rw, wvo, csv ? i * 8 * F * 2 : 0, 0);
      SB();
      tag = __builtin_amdgcn_readfirstlane(*(LAS unsigned*)(L + C_TAG));
#pragma unroll
      for (int h2 = 0; h2 < 2; ++h2) {
        float s = 0.f;
#pragma unroll
        for (int i = 0; i < 10; ++i)
#pragma unroll
          for (int e2 = 0; e2 < 4; ++e2) s = dot2(xv[h2][i][e2], 0x3f803f80u, s);
        s += __shfl_xor(s, 1); s += __shfl_xor(s, 2); s += __shfl_xor(s, 4);
        const float mean = s * (1.f / D);
        float q0 = 0.f, q1 = 0.f;
#pragma unroll
        for (int i = 0; i < 10; ++i)
#pragma unroll
          for (int e2 = 0; e2 < 4; ++e2) { const float a0 = bflo(xv[h2][i][e2]) - mean, a1 = bfhi(xv[h2][i][e2]) - mean; q0 = fmaf(a0, a0, q0); q1 = fmaf(a1, a1, q1); }
        float q = q0 + q1;
        q += __shfl_xor(q, 1); q += __shfl_xor(q, 2); q += __shfl_xor(q, 4);
        const float den = __builtin_sqrtf(rbf(rbf(q * (1.f / D)) + 1e-5f));
        __builtin_amdgcn_raw_buffer_store_b64(v2u{pack2(mean, den), tag}, rws, (lane & 7) == 0 ? ST_OFF + (srow + 8 * h2) * 8 : -16, 0, SC1);
      }
      float c[8] = {};
#pragma unroll
      for (int i = 0; i < 20; ++i)
#pragma unroll
        for (int e2 = 0; e2 < 4; ++e2) { c[2 * e2] += bflo(wv[i][e2]); c[2 * e2 + 1] += bfhi(wv[i][e2]); }
      *(LAS f32x4*)(L + ((w * 8 + (lane >> 3)) * 64 + (lane & 7) * 8) * 4) = f32x4{c[0], c[1], c[2], c[3]};
      *(LAS f32x4*)(L + ((w * 8 + (lane >> 3)) * 64 + (lane & 7) * 8 + 4) * 4) = f32x4{c[4], c[5], c[6], c[7]};
      WAITL();
      unsigned old = 0;
      if (lane == 0) old = __hip_atomic_fetch_add((LAS unsigned*)(L + C_H), 1u, RLX_WG);
      if (__builtin_amdgcn_readfirstlane(old) == 3) {
        asm volatile("" ::: "memory");
        float t4[4] = {};
#pragma unroll
        for (int i = 0; i < 32; ++i) t4[i & 3] += *(LAS float*)(L + (i * 64 + lane) * 4);
        __builtin_amdgcn_raw_buffer_store_b64(v2u{__float_as_uint((t4[0] + t4[1]) + (t4[2] + t4[3])), tag}, rws, csv ? CS_OFF + (cg * 64 + lane) * 8 : -16, 0, SC1);
      }
      lds_add(L + C_MMA, 12u, lane);
      if (b == 56 && w == 0) {
#pragma unroll 1
        for (unsigned sp = 0; sp < SPIN_CAP; ++sp) {
          bool ok = true;
#pragma unroll
          for (int i = 0; i < 4; ++i) ok = ok && flag_load(ws, FL_P + i * 64 * FSTR, 64, lane) == tag;
          if (__all(ok)) break;
          __builtin_amdgcn_s_sleep(32);
        }
        set_flag(ws, FL_GEN, tag, lane);
      }
    }
    unsigned hb_ = 0;
#define HBAR() do { WAITL(); lds_add(L + C_G2, 1u, lane); hb_ += 4; lds_wait(L + C_G2, hb_); } while (0)
    {
      const __amdgpu_buffer_rsrc_t rxx = mkrsrc(x, S * D * 2);
      lds_wait_sleep(L + C_MMA, 48);
#pragma unroll
      for (int i = 0; i < 3; ++i) {
        const int s = aw + 4 * i;
        if (s < 10) {
          const int n = s * 64 + lane; const int row = n / 10, c = n - row * 10;
          GLDS(rxx, L + EX + s * 1024, (64 * mb + row) * 1280 + 160 * nb + c * 16, 0);
        }
      }
      const float dv = bflo(Dv[dpair + (aw >> 1)]), dv3 = bflo(Dv[d3]);
      lds_wait_sleep(L + C_ST, 1);
      __builtin_amdgcn_s_setprio(1);
      LAS char* Lc = L + CV + (aw >> 1) * CH_BYTES;
      const int cts = aw & 1;
      f32x4 acc[4] = {};
      if (cts == 0) conv8<0>(Lc + aoff, Lc + boff, acc); else conv8<1>(Lc + aoff, Lc + boff, acc);
      __builtin_amdgcn_s_setprio(2);
#pragma unroll
      for (int ti = 0; ti < 4; ++ti) conv_out(Lc, acc[ti], cts == 0 ? (ti < 2 ? ti : ti + 4) : 2 + ti, dpair + (aw >> 1), dv, e, kq, rws, tag);
      f32x4 ac3[4] = {};
      if (ts3 == 0) conv2<0>(L + CV3 + aoff + aw * 128, L + CV3 + boff + aw * 128, ac3); else conv2<1>(L + CV3 + aoff + aw * 128, L + CV3 + boff + aw * 128, ac3);
#pragma unroll
      for (int ti = 0; ti < 4; ++ti) *(LAS f32x4*)(L + SC3 + (aw * 4 + ti) * 1024 + lane * 16) = ac3[ti];
      WAITV(4);
      HBAR();
      f32x4 a = {};
#pragma unroll
      for (int ww = 0; ww < 4; ++ww) a += *(LAS f32x4*)(L + SC3 + (ww * 4 + aw) * 1024 + lane * 16);
      conv_out(L + CV3, a, ts3 == 0 ? (aw < 2 ? aw : aw + 4) : 2 + aw, d3, dv3, e, kq, rws, tag);
    }
    {
      const int q = (lane & 15) >> 2, pp = lane & 3, pos = lane & 15;
      const int tok = 16 * aw + 2 * (pos >> 2) + (pos & 1) + 8 * ((pos >> 1) & 1);
      lds_wait_sleep(L + C_S, 1);
      const f32x2 st = *(LAS f32x2*)(L + XFS + ((mb & 1) * 64 + tok) * 8);
      v4u xn[5];
#pragma unroll
      for (int i = 0; i < 5; ++i) {
        const v2u xv2 = *(LAS v2u*)(L + EX + tok * 160 + (4 * i + kq) * 8);
        const unsigned d01 = pack2(bflo(xv2.x) - st.x, bfhi(xv2.x) - st.x), d23 = pack2(bflo(xv2.y) - st.x, bfhi(xv2.y) - st.x);
        xn[i] = v4u{pack2(bflo(d01) * st.y, bfhi(d01) * st.y), pack2(bflo(d23) * st.y, bfhi(d23) * st.y), xv2.x, xv2.y};
      }
      WAITL();
      lds_add(L + C_Y, 1u, lane);
      for (int i = 0; i < 2; ++i) __builtin_amdgcn_s_sleep(4);
      {
        v4u yv[5];
        const int yo = Y_OFF + (80 * nb + 4 * aw + kq) * 8192 + mb * 256 + e * 16;
#pragma unroll 1
        for (unsigned sp = 0; sp < SPIN_CAP; ++sp) {
          asm volatile("" ::: "memory");
          bool ok = true;
#pragma unroll
          for (int i = 0; i < 5; ++i) yv[i] = __builtin_amdgcn_raw_buffer_load_b128(rws, yo, i * 16 * 8192, SC1);
#pragma unroll
          for (int i = 0; i < 5; ++i) ok = ok && yv[i].y == tag && yv[i].w == tag;
          if (__all(ok)) break;
          __builtin_amdgcn_s_sleep(8);
        }
#pragma unroll
        for (int i = 0; i < 5; ++i) *(LAS v2u*)(L + EY + (16 * i + 4 * aw + kq) * 144 + e * 8) = v2u{yv[i].x, yv[i].z};
      }
      HBAR();
#pragma unroll
      for (int i = 0; i < 5; ++i) {
        const int dq = 4 * i + kq;
        const v2u yv = trread(L + EY + (4 * dq + q) * 144 + aw * 32 + pp * 8);
        const unsigned s01 = pack2(bflo(xn[i].x) * bflo(yv.x), bfhi(xn[i].x) * bfhi(yv.x)), s23 = pack2(bflo(xn[i].y) * bflo(yv.y), bfhi(xn[i].y) * bfhi(yv.y));
        *(LAS v4u*)(L + ES + (tok * 20 + dq) * 16) = v4u{s01, s23, xn[i].z, xn[i].w};
      }
    }
  }
  BAR();
  const int lane2 = __builtin_amdgcn_mbcnt_hi(~0u, __builtin_amdgcn_mbcnt_lo(~0u, opaque0()));
#pragma unroll
  for (int i = 0; i < 3; ++i) {
    const int it = w + 8 * i;
    if (it < 20) {
      const int n = it * 64 + lane2;
      const int tok = n / 20, dq = n - tok * 20;
      f32x4 o = *(LAS f32x4*)(L + tok * PSTR + dq * 16);
#pragma unroll
      for (int ww = 1; ww < 4; ++ww) o += *(LAS f32x4*)(L + ww * PART + tok * PSTR + dq * 16);
      const v4u sx = *(LAS v4u*)(L + ES + n * 16);
      const unsigned o01 = pack2(o[0], o[1]), o23 = pack2(o[2], o[3]);
      const unsigned g01 = pack2(silu_f(bflo(o01)), silu_f(bfhi(o01))), g23 = pack2(silu_f(bflo(o23)), silu_f(bfhi(o23)));
      const unsigned m01 = pack2(bflo(sx.x) * bflo(g01), bfhi(sx.x) * bfhi(g01)), m23 = pack2(bflo(sx.y) * bflo(g23), bfhi(sx.y) * bfhi(g23));
      v2u res;
      res.x = pack2(bflo(sx.z) + bflo(m01), bfhi(sx.z) + bfhi(m01));
      res.y = pack2(bflo(sx.w) + bflo(m23), bfhi(sx.w) + bfhi(m23));
      *(v2u*)(out + (size_t)(64 * mb + tok) * D + 80 * nb + dq * 4) = res;
    }
  }
}

extern "C" void kernel_launch(void* const* d_in, const int* in_sizes, int n_in,
                              void* d_out, int out_size, void* d_ws, size_t ws_size,
                              hipStream_t stream) {
  fused<<<256, 512, 0, stream>>>((const u16*)d_in[0], (const u16*)d_in[1], (const u16*)d_in[2], (const u16*)d_in[3],
                                 (const u16*)d_in[4], (const u16*)d_in[5], (const u16*)d_in[7], (const u16*)d_in[8],
                                 (char*)d_ws, (u16*)d_out);
}
```

```cpp
#include <hip/hip_runtime.h>
#include <stdint.h>

typedef unsigned short u16;
typedef unsigned v4u __attribute__((ext_vector_type(4)));
typedef unsigned v2u __attribute__((ext_vector_type(2)));
typedef float f32x2 __attribute__((ext_vector_type(2)));
typedef float f32x4 __attribute__((ext_vector_type(4)));
typedef float f32x16 __attribute__((ext_vector_type(16)));
typedef short bf16x8 __attribute__((ext_vector_type(8)));
typedef short s16x4 __attribute__((ext_vector_type(4)));
typedef __bf16 bf16x2 __attribute__((ext_vector_type(2)));
#define LAS __attribute__((address_space(3)))
#define GAS __attribute__((address_space(1)))
#define DI __device__ __forceinline__
#define RLX_AGENT __ATOMIC_RELAXED, __HIP_MEMORY_SCOPE_AGENT
#define RLX_WG __ATOMIC_RELAXED, __HIP_MEMORY_SCOPE_WORKGROUP

constexpr int S = 2048, D = 640, F = 1728;
constexpr int KS2 = 54;
constexpr int FSTR = 128;
constexpr int FL_GEN = 0, FL_P = 131072, FL_H = FL_P + 256 * 256;
constexpr int ST_OFF = 32768;
constexpr int CS_OFF = ST_OFF + S * 8;
constexpr int HP_OFF = 262144;
constexpr int HP_MB = KS2 * 4096;
constexpr int W2P_OFF = HP_OFF + 32 * HP_MB;
constexpr int W2P_NB = KS2 * 5120;
constexpr int R_OFF = W2P_OFF + 8 * W2P_NB;
constexpr int Y_OFF = R_OFF + D * S * 2;
constexpr int WS_BYTES = Y_OFF + D * S * 4;
constexpr unsigned RSRC3 = 0x00020000u;
constexpr int SC1 = 16;
constexpr unsigned SPIN_CAP = 1u << 18;

DI float bflo(unsigned u) { return __uint_as_float(u << 16); }
DI float bfhi(unsigned u) { return __uint_as_float(u & 0xffff0000u); }
DI unsigned pack2(float a, float b) {
  bf16x2 r = __builtin_convertvector(f32x2{a, b}, bf16x2);
  return __builtin_bit_cast(unsigned, r);
}
DI float rbf(float a) { return bflo(pack2(a, a)); }
DI float silu_f(float x) { return x * __builtin_amdgcn_rcpf(1.f + __builtin_amdgcn_exp2f(-1.44269504088896f * x)); }
DI f32x4 mfma16(v4u a, v4u b, f32x4 c) {
  return __builtin_amdgcn_mfma_f32_16x16x32_bf16(__builtin_bit_cast(bf16x8, a), __builtin_bit_cast(bf16x8, b), c, 0, 0, 0);
}
DI f32x16 mfma32(v4u a, v4u b, f32x16 c) {
  return __builtin_amdgcn_mfma_f32_32x32x16_bf16(__builtin_bit_cast(bf16x8, a), __builtin_bit_cast(bf16x8, b), c, 0, 0, 0);
}
DI float dot2(unsigned a, unsigned b, float c) {
  return __builtin_amdgcn_fdot2_f32_bf16(__builtin_bit_cast(bf16x2, a), __builtin_bit_cast(bf16x2, b), c, false);
}
DI v2u trread(LAS char* p) { return __builtin_bit_cast(v2u, __builtin_amdgcn_ds_read_tr16_b64_v4i16((LAS s16x4*)p)); }
DI v4u ldsr128(LAS char* p) { return *(LAS v4u*)p; }
DI __amdgpu_buffer_rsrc_t mkrsrc(const void* p, int bytes) { return __builtin_amdgcn_make_buffer_rsrc((void*)p, 0, bytes, RSRC3); }
DI void glds_asm(__amdgpu_buffer_rsrc_t rs, unsigned ldsa, int voff, int soff) {
  asm volatile("s_nop 4\n\ts_mov_b32 m0, %0\n\ts_nop 0\n\tbuffer_load_dwordx4 %1, %2, %3 offen lds"
               :: "s"(ldsa), "v"(voff), "s"(rs), "s"(soff) : "m0", "memory");
}
DI void glds_fast(__amdgpu_buffer_rsrc_t rs, unsigned ldsa, int voff, int soff) {
  asm volatile("s_mov_b32 m0, %0\n\ts_nop 0\n\tbuffer_load_dwordx4 %1, %2, %3 offen lds"
               :: "s"(ldsa), "v"(voff), "s"(rs), "s"(soff) : "m0", "memory");
}
DI void glds_nt(__amdgpu_buffer_rsrc_t rs, unsigned ldsa, int voff, int soff) {
  asm volatile("s_nop 4\n\ts_mov_b32 m0, %0\n\ts_nop 0\n\tbuffer_load_dwordx4 %1, %2, %3 offen nt lds"
               :: "s"(ldsa), "v"(voff), "s"(rs), "s"(soff) : "m0", "memory");
}
#define GLDSN(rs, ldsp, voff, soff) glds_nt(rs, (unsigned)(uintptr_t)(ldsp), voff, soff)
#define GLDS(rs, ldsp, voff, soff) glds_asm(rs, (unsigned)(uintptr_t)(ldsp), voff, soff)
#define WAITV(n) asm volatile("s_waitcnt vmcnt(" #n ")" ::: "memory")
#define WAITL() asm volatile("s_waitcnt lgkmcnt(0)" ::: "memory")
#define BAR() do { WAITL(); __builtin_amdgcn_s_barrier(); asm volatile("" ::: "memory"); } while (0)
#define SB() __builtin_amdgcn_sched_barrier(0)


constexpr int NBUF = 3;
constexpr int CH_HA = 0, CH_HB = 4224, CH_R = 8448, CH_RS = 4512, CH_BYTES = CH_R + 4 * CH_RS;
constexpr int CV = 98304;
constexpr int CV3 = 32768;
constexpr int SC3 = 65536;
constexpr int PK = CV, PV = PK + 11264, PW = PV + 10240, PS = PW + 10240, PO = PS + 1024;
constexpr int EY = CV, EX = 0, ES = EY + 80 * 144;
constexpr int XFS = CV + 2 * CH_BYTES;
constexpr int XFC = XFS + 1024;
constexpr int CNT = XFC + 512;
constexpr int C_AUX = CNT, C_G2 = CNT + 4, C_Y = CNT + 8, C_H = CNT + 12, C_MMA = CNT + 16, C_S = CNT + 20, C_P = CNT + 24,
              C_TAG = CNT + 60, C_ST = CNT + 40, C_HR = CNT + 32;
constexpr int LDS_BYTES = CNT + 64;
static_assert(PO + 10240 <= XFS && ES + 20480 <= XFS && LDS_BYTES <= 163840, "lds");
constexpr int PSTR = 336, PART = 64 * PSTR;

DI void lds_add(LAS char* c, unsigned v, int lane) {
  if (lane == 0) __hip_atomic_fetch_add((LAS unsigned*)c, v, RLX_WG);
}
DI void lds_wait(LAS char* c, unsigned target) {
  while ((unsigned)__builtin_amdgcn_readfirstlane(__hip_atomic_load((LAS unsigned*)c, RLX_WG)) < target) {}
  asm volatile("" ::: "memory");
}
DI void lds_wait_sleep(LAS char* c, unsigned target) {
  while ((unsigned)__builtin_amdgcn_readfirstlane(__hip_atomic_load((LAS unsigned*)c, RLX_WG)) < target) __builtin_amdgcn_s_sleep(1);
  asm volatile("" ::: "memory");
}
DI unsigned flag_load(char* ws, int off, int n, int lane) {
  return __hip_atomic_load((GAS unsigned*)(ws + off + (lane < n ? lane : 0) * FSTR), RLX_AGENT);
}
DI void poll_flags(char* ws, int off, int n, unsigned tag, int lane) {
#pragma unroll 1
  for (unsigned spins = 0; spins < SPIN_CAP; ++spins) {
    const unsigned v = flag_load(ws, off, n, lane);
    if (__all(v == tag)) break;
    __builtin_amdgcn_s_sleep(8);
  }
  __builtin_amdgcn_fence(__ATOMIC_ACQUIRE, "wavefront");
}
DI void set_flag(char* ws, int off, unsigned tag, int lane) {
  if (lane == 0) __hip_atomic_store((GAS unsigned*)(ws + off), tag, RLX_AGENT);
}

DI unsigned opaque0() { unsigned z; asm volatile("v_mov_b32 %0, 0" : "=v"(z)); return z; }
template <int I> struct IC { static constexpr int value = I; };
template <int B, int E, class Fn> DI void static_for(Fn&& f) { if constexpr (B < E) { f(IC<B>{}); static_for<B + 1, E>(f); } }
struct Frags { v4u x[4][2], w[4][2]; };

template <int TS> DI void conv_ld(LAS char* pa, LAS char* pb, v4u (&A)[8], v4u (&B)[8]) {
  constexpr int NA = TS == 0 ? 8 : 6;
#pragma unroll
  for (int a = 0; a < NA; ++a) { A[a] = ldsr128(pa + a * 512); B[a] = ldsr128(pb + (7 - a) * 512); }
}
template <int TS> DI void conv_mm(const v4u (&A)[8], const v4u (&B)[8], f32x4 (&acc)[4]) {
#pragma unroll
  for (int ti = 0; ti < 4; ++ti) {
    const int n = TS == 0 ? (ti == 0 ? 0 : ti == 1 ? 1 : ti == 2 ? 6 : 7) : 2 + ti;
#pragma unroll
    for (int a = 0; a <= n; ++a) acc[ti] = mfma16(A[a], B[n - a], acc[ti]);
  }
}
DI void conv_sched() {
#pragma unroll
  for (int i = 0; i < 16; ++i) { __builtin_amdgcn_sched_group_barrier(0x8, 1, 0); __builtin_amdgcn_sched_group_barrier(0x100, 1, 0); }
  __builtin_amdgcn_sched_group_barrier(0x8, 2, 0);
}
template <int TS> DI void conv8(LAS char* pa, LAS char* pb, f32x4 (&acc)[4]) {
  v4u A0[8], B0[8], A1[8], B1[8];
  conv_ld<TS>(pa, pb, A0, B0);
  SB();
#pragma unroll 1
  for (int r = 0; r < 8; r += 2) {
    conv_ld<TS>(pa + 64, pb + 64, A1, B1); conv_mm<TS>(A0, B0, acc); conv_sched(); SB();
    pa += 128; pb += 128;
    conv_ld<TS>(pa, pb, A0, B0); conv_mm<TS>(A1, B1, acc); conv_sched(); SB();
  }
}
template <int TS> DI void conv2(LAS char* pa, LAS char* pb, f32x4 (&acc)[4]) {
#pragma unroll 1
  for (int r = 0; r < 2; ++r) {
    v4u A[8], B[8];
    conv_ld<TS>(pa, pb, A, B); conv_mm<TS>(A, B, acc);
    pa += 64; pb += 64;
  }
}
DI unsigned kshift2(float k1, float k2, float k3, float k4, float k5, float c0, float c1, float c2, float c3) {
  unsigned a = pack2(k1 * c3, k2 * c3);
  unsigned t = pack2(k2 * c2, k3 * c2);
  a = pack2(bflo(a) + bflo(t), bfhi(a) + bfhi(t));
  t = pack2(k3 * c1, k4 * c1);
  a = pack2(bflo(a) + bflo(t), bfhi(a) + bfhi(t));
  t = pack2(k4 * c0, k5 * c0);
  return pack2(bflo(a) + bflo(t), bfhi(a) + bfhi(t));
}
DI void stage_h(LAS char* Lc, v4u h, int g) {
  *(LAS v4u*)(Lc + CH_HB + 112 + g * 16) = h;
  LAS char* a = Lc + CH_HA + 114 + g * 16;
  *(LAS u16*)(a) = (u16)(h.x & 0xffff);
  *(LAS unsigned*)(a + 2) = __builtin_amdgcn_alignbit(h.y, h.x, 16);
  *(LAS v2u*)(a + 6) = v2u{__builtin_amdgcn_alignbit(h.z, h.y, 16), __builtin_amdgcn_alignbit(h.w, h.z, 16)};
  *(LAS u16*)(a + 14) = (u16)(h.w >> 16);
}
DI void stage_r(LAS char* Lc, v4u u, int g) {
  LAS char* rb = Lc + CH_R + g * 16;
  *(LAS v4u*)(rb) = u;
  { LAS char* q = rb + CH_RS - 4; *(LAS unsigned*)(q) = u.x; *(LAS v2u*)(q + 4) = v2u{u.y, u.z}; *(LAS unsigned*)(q + 12) = u.w; }
  { LAS char* q = rb + 2 * CH_RS - 8; *(LAS v2u*)(q) = v2u{u.x, u.y}; *(LAS v2u*)(q + 8) = v2u{u.z, u.w}; }
  { LAS char* q = rb + 3 * CH_RS - 12; *(LAS unsigned*)(q) = u.x; *(LAS v2u*)(q + 4) = v2u{u.y, u.z}; *(LAS unsigned*)(q + 12) = u.w; }
}
DI void conv_out(LAS char* Lc, f32x4 a, int n, int d, float dv, int e, int kq, __amdgpu_buffer_rsrc_t rws, unsigned tag) {
  const int t = 256 * n + 64 * (e >> 2) + 2 * (e & 3) + 16 * kq;
  const unsigned ua = *(LAS unsigned*)(Lc + CH_R + 2 * (2046 - t));
  const unsigned ub = *(LAS unsigned*)(Lc + CH_R + 2 * (2038 - t));
  const unsigned c01 = pack2(a[0], a[1]), c23 = pack2(a[2], a[3]);
  const unsigned d01 = pack2(bfhi(ua) * dv, bflo(ua) * dv), d23 = pack2(bfhi(ub) * dv, bflo(ub) * dv);
  v2u o;
  o.x = pack2(bflo(c01) + bflo(d01), bfhi(c01) + bfhi(d01));
  o.y = pack2(bflo(c23) + bflo(d23), bfhi(c23) + bfhi(d23));
  const int off = Y_OFF + d * 8192 + (256 * n + 64 * (e >> 2) + 16 * kq + 4 * (e & 3)) * 4;
  __builtin_amdgcn_raw_buffer_store_b128(v4u{o.x, tag, o.y, tag}, rws, off, 0, SC1);
}

__global__ __launch_bounds__(512, 2) void fused(const u16* x, const u16* __restrict__ kk, const u16* __restrict__ vv,
                                                const u16* __restrict__ sk, const u16* __restrict__ hk, const u16* __restrict__ Dv,
                                                const u16* __restrict__ w1, const u16* __restrict__ w2,
                                                char* ws, u16* out) {
  __shared__ __attribute__((aligned(16))) char smem[LDS_BYTES];
  LAS char* L = (LAS char*)smem;
  if (__builtin_amdgcn_readfirstlane(threadIdx.x >> 6) == 7) {
    unsigned long long pc;
    asm volatile("s_getpc_b64 %0" : "=s"(pc));
    const unsigned co = min(((blockIdx.x >> 3) & 7) * 8192u + (threadIdx.x & 63) * 128u, 39000u);
    asm volatile("s_mov_b32 m0, %0\n\ts_nop 0\n\tglobal_load_lds_dword %1, %2" :: "s"((unsigned)(uintptr_t)(L + XFS)), "v"(co), "s"(pc) : "m0");
  }
  const int b = blockIdx.x, xcd = b & 7, r = b >> 3;
  const int rg = xcd >> 1, hf = xcd & 1, pi = r >> 3, jj = r & 7;
  const int p = 4 * rg + pi, j = 7 * hf + jj;
  const bool g1 = jj < 7;
  const int mbl = 2 * pi + ((r >> 2) & 1), nbl = r & 3;
  const int mb = 8 * rg + mbl, nb = 4 * hf + nbl;
  const int lane = threadIdx.x & 63;
  const int w = __builtin_amdgcn_readfirstlane(threadIdx.x >> 6);
  const int hi = lane >> 5, aw = w & 3;
  const __amdgpu_buffer_rsrc_t rws = mkrsrc(ws, WS_BYTES);
  if (threadIdx.x < 15) *(LAS unsigned*)(L + CNT + threadIdx.x * 4) = 0u;
  const int dpair = 80 * nb + 2 * mb, d3 = 80 * nb + 64 + (mb >> 1), ts3 = mb & 1;
  const int e = lane & 15, kq = lane >> 4;
  const int aoff = ((e & 1) ? CH_HB : CH_HA) + (kq + (e >> 1)) * 16;
  const int boff = CH_R + (3 - (e & 3)) * CH_RS + (248 - 32 * 7 - 8 * (e >> 2) + kq) * 16;

  {
    const __amdgpu_buffer_rsrc_t rk = mkrsrc(kk, S * D * 2), rv_ = mkrsrc(vv, S * D * 2), rsk = mkrsrc(sk, D * 8);
    const int sb = 64 * mb * 1280 + 160 * nb;
    int vo[2];
#pragma unroll
    for (int i = 0; i < 2; ++i) { const int n = (8 * i + w) * 64 + lane; const int row = n / 10; vo[i] = row * 1280 + (n - row * 10) * 16; }
    GLDSN(rk, L + PK + w * 1024, vo[0] + sb - 5120, 0);
    GLDSN(rv_, L + PV + w * 1024, vo[0], sb);
    {
      const int n = (5 + w) * 64 + lane; const int row = n / 10; const int vo3 = row * 1280 + (n - row * 10) * 16;
      if (w < 3) GLDSN(rk, L + PK + (8 + w) * 1024, vo[1] + sb - 5120, 0);
      else if (w < 5) GLDSN(rv_, L + PV + (5 + w) * 1024, vo3, sb);
      else if (w == 5) GLDS(rsk, L + PS, lane * 16, nb * 640);
    }
  }
#define PREP_ITEM(tg, cg) do { \
        const int q_ = (lane & 15) >> 2, pp_ = lane & 3, tl = 16 * (tg) + 4 * kq; \
        LAS char* ka_ = L + PK + (tl + q_) * 160 + (cg) * 32 + pp_ * 8; \
        v2u k0 = trread(ka_), k1 = trread(ka_ + 4 * 160); \
        v2u v0 = trread(ka_ + (PV - PK)); \
        v2u sw = *(LAS v2u*)(L + PS + ((cg) * 16 + e) * 8); \
        float kf[8] = {bflo(k0.x), bfhi(k0.x), bflo(k0.y), bfhi(k0.y), bflo(k1.x), bfhi(k1.x), bflo(k1.y), bfhi(k1.y)}; \
        float vf[4] = {bflo(v0.x), bfhi(v0.x), bflo(v0.y), bfhi(v0.y)}; \
        const float c0 = bflo(sw.x), c1 = bfhi(sw.x), c2 = bflo(sw.y), c3 = bfhi(sw.y); \
        float u[4]; \
        _Pragma("unroll") for (int e2 = 0; e2 < 4; e2 += 2) { \
          const unsigned a = kshift2(kf[e2 + 1], kf[e2 + 2], kf[e2 + 3], kf[e2 + 4], kf[e2 + 5], c0, c1, c2, c3); \
          u[e2] = bflo(a) * vf[e2]; u[e2 + 1] = bfhi(a) * vf[e2 + 1]; } \
        v2u o = v2u{pack2(u[3], u[2]), pack2(u[1], u[0])}; \
        const int ch = (cg) * 16 + e; \
        const int pos8 = (60 - tl) >> 2; \
        *(LAS v2u*)(L + PO + ch * 128 + ((((pos8 >> 1) ^ (ch & 7)) << 4) | ((pos8 & 1) << 3))) = o; \
      } while (0)

  if (w >= 4) {
    const unsigned gen = __hip_atomic_load((GAS unsigned*)(ws + FL_GEN), RLX_AGENT);
    WAITV(0);
    const unsigned tag = (unsigned)__builtin_amdgcn_readfirstlane(gen) * 0x9E3779B1u + 0x85EBCA6Bu;
    if (aw == 0 && lane == 0) *(LAS unsigned*)(L + C_TAG) = tag;
#define AUXBAR() do { WAITL(); lds_add(L + C_AUX, 1u, lane); ab += 4; lds_wait(L + C_AUX, ab); } while (0)
    BAR();
    unsigned ab = 4;
    {
      const __amdgpu_buffer_rsrc_t rw2 = mkrsrc(w2, F * D * 2);
#pragma unroll
      for (int i = 0; i < 3; ++i) if (4 * i + aw < 10) {
        const int n = (4 * i + aw) * 64 + lane; const int row = n / 10;
        GLDSN(rw2, L + PW + (4 * i + aw) * 1024, (row + (row >= 32 ? 992 : 0)) * 1280 + (n - row * 10) * 16, 32 * mb * 1280 + 160 * nb);
      }
    }
    {
      const int g = lane >> 4, q = (lane & 15) >> 2, pp = lane & 3;
      PREP_ITEM(aw, 0); PREP_ITEM(aw, 1); PREP_ITEM(aw, 2);
      AUXBAR();
#pragma unroll
      for (int i = 0; i < 3; ++i) {
        const int ri = aw + 4 * i;
        if (ri < 10) {
          const int ch = 8 * ri + (lane >> 3), cc = lane & 7;
          v4u val = ldsr128(L + PO + ch * 128 + ((cc ^ (ch & 7)) << 4));
          __builtin_amdgcn_raw_buffer_store_b128(val, rws, R_OFF + (80 * nb + ch) * 4096 + (31 - mb) * 128 + cc * 16, 0, SC1);
        }
      }
      __builtin_amdgcn_s_setprio(0);
      if (aw < 2) WAITV(3); else WAITV(2);
      AUXBAR();
#pragma unroll
      for (int i = 0; i < 3; ++i) {
        const int fi = aw + 4 * i;
        if (fi < 10) {
          const int half = fi / 5, nt = fi - half * 5, ks = mb + 32 * half;
          const int r0 = 16 * (g & 1) + 4 * (g >> 1);
          LAS char* a = L + PW + half * 5120 + (r0 + q) * 160 + nt * 32 + pp * 8;
          v2u lo = trread(a), hi2 = trread(a + 8 * 160);
          __builtin_amdgcn_raw_buffer_store_b128(v4u{lo.x, lo.y, hi2.x, hi2.y}, rws, ks < KS2 ? W2P_OFF + nb * W2P_NB + ks * 5120 + nt * 1024 + lane * 16 : -16, 0, SC1);
        }
      }
    }
    WAITV(0);
    AUXBAR();
    if (aw == 0) set_flag(ws, FL_P + (nb * 32 + mb) * FSTR, tag, lane);
    const __amdgpu_buffer_rsrc_t rh = mkrsrc(hk, D * S * 2);
    v4u cu[3];
    cu[0] = __builtin_amdgcn_raw_buffer_load_b128(rh, dpair * 4096 + aw * 1024 + lane * 16, 0, 2);
    cu[1] = __builtin_amdgcn_raw_buffer_load_b128(rh, dpair * 4096 + 4096 + aw * 1024 + lane * 16, 0, 2);
    cu[2] = __builtin_amdgcn_raw_buffer_load_b128(rh, d3 * 4096 + aw * 1024 + lane * 16, 0, 0);
    const int g = aw * 64 + lane;
    lds_wait_sleep(L + C_MMA, 32);
    if (aw == 1) {
      const int jc = g1 ? j : 0;
      v2u gr[4];
#define GRL(off) __builtin_bit_cast(v2u, __hip_atomic_load((GAS unsigned long long*)(ws + (off)), RLX_AGENT))
#pragma unroll 1
      for (unsigned sp = 0; sp < SPIN_CAP; ++sp) {
        gr[0] = GRL(ST_OFF + (p * 128 + lane) * 8);
        gr[1] = GRL(ST_OFF + (p * 128 + 64 + lane) * 8);
        gr[2] = GRL(CS_OFF + (jc * 128 + lane) * 8);
        gr[3] = GRL(CS_OFF + (jc * 128 + (j == 13 ? 0 : 64) + lane) * 8);
        if (__all(gr[0].y == tag && gr[1].y == tag && gr[2].y == tag && gr[3].y == tag)) break;
        __builtin_amdgcn_s_sleep(8);
      }
      *(LAS f32x2*)(L + XFS + lane * 8) = f32x2{bflo(gr[0].x), 1.f / bfhi(gr[0].x)};
      *(LAS f32x2*)(L + XFS + (64 + lane) * 8) = f32x2{bflo(gr[1].x), 1.f / bfhi(gr[1].x)};
      *(LAS unsigned*)(L + XFC + lane * 4) = gr[2].x;
      *(LAS unsigned*)(L + XFC + (64 + lane) * 4) = gr[3].x;
      WAITL();
      lds_add(L + C_S, 1u, lane);
    }
    if (aw == 2) { poll_flags(ws, FL_P + nb * 32 * FSTR, 32, tag, lane); lds_add(L + C_P, 1u, lane); }
    lds_wait_sleep(L + C_P, 1);
    v4u rv[3];
    rv[0] = __builtin_amdgcn_raw_buffer_load_b128(rws, R_OFF + dpair * 4096 + aw * 1024 + lane * 16, 0, SC1);
    rv[1] = __builtin_amdgcn_raw_buffer_load_b128(rws, R_OFF + dpair * 4096 + 4096 + aw * 1024 + lane * 16, 0, SC1);
    rv[2] = __builtin_amdgcn_raw_buffer_load_b128(rws, R_OFF + d3 * 4096 + aw * 1024 + lane * 16, 0, SC1);
    const __amdgpu_buffer_rsrc_t ra = mkrsrc(ws + HP_OFF + mb * HP_MB, HP_MB);
    const __amdgpu_buffer_rsrc_t rb = mkrsrc(ws + W2P_OFF + nb * W2P_NB, W2P_NB);
    const int va = lane * 16 + aw * 4096, vb = lane * 16 + aw * 5120;
    constexpr int WB = 6, NKS = 14, POOL = 41;
    v4u fa[NKS][4], fb[NKS][5];
#define LOADB(i) do { const int so_ = (i) * 20480; \
    _Pragma("unroll") for (int n = 0; n < 5; ++n) fb[i][n] = __builtin_amdgcn_raw_buffer_load_b128(rb, vb + (n & 3) * 1024, so_ + (n >> 2) * 4096, SC1); } while (0)
#pragma unroll
    for (int i = 0; i < WB; ++i) LOADB(i);
    SB();
    lds_wait_sleep(L + C_MMA, 44);
    if (aw == 0 && lane < 32) *(LAS v4u*)(L + CV + (lane >> 4) * CH_BYTES + ((lane >> 3) & 1) * CH_HB + (lane & 7) * 16) = v4u{0, 0, 0, 0};
    if (aw == 3) {
#pragma unroll
      for (int i = 0; i < 4; ++i) {
        const int n = lane + 64 * i;
        if (n < 200) { const int cp = n / 25, sl = n - cp * 25; *(LAS v4u*)(L + CV + (cp >> 2) * CH_BYTES + CH_R + (cp & 3) * CH_RS + 4080 + sl * 16) = v4u{0, 0, 0, 0}; }
      }
    }
    stage_h(L + CV, cu[0], g);
    stage_h(L + CV + CH_BYTES, cu[1], g);
    stage_r(L + CV, rv[0], g);
    stage_r(L + CV + CH_BYTES, rv[1], g);
    if (aw == 0 && lane < 16) *(LAS v4u*)(L + CV3 + (lane >> 3) * CH_HB + (lane & 7) * 16) = v4u{0, 0, 0, 0};
    if (aw == 3) {
#pragma unroll
      for (int i = 0; i < 2; ++i) {
        const int n = lane + 64 * i;
        if (n < 100) { const int cp = n / 25, sl = n - cp * 25; *(LAS v4u*)(L + CV3 + CH_R + cp * CH_RS + 4080 + sl * 16) = v4u{0, 0, 0, 0}; }
      }
    }
    stage_h(L + CV3, cu[2], g);
    stage_r(L + CV3, rv[2], g);
    AUXBAR();
    if (aw == 0) lds_add(L + C_ST, 1u, lane);
    lds_wait_sleep(L + C_H, 4);
    if (aw == 1) { poll_flags(ws, FL_H + p * 16 * FSTR, 14, tag, lane); lds_add(L + C_HR, 1u, lane); }
    __builtin_amdgcn_s_setprio(0);
    lds_wait_sleep(L + C_HR, 1);
#define ISSUE(q) do { constexpr int q_ = (q); \
      if constexpr (q_ < 4 * WB) { fa[q_ / 4][q_ % 4] = __builtin_amdgcn_raw_buffer_load_b128(ra, va + (q_ % 4) * 1024, (q_ / 4) * 16384, SC1); } \
      else if constexpr (q_ < 4 * WB + 9 * (NKS - WB)) { constexpr int k_ = WB + (q_ - 4 * WB) / 9, f_ = (q_ - 4 * WB) % 9; \
        if constexpr (f_ < 4) fa[k_][f_] = __builtin_amdgcn_raw_buffer_load_b128(ra, va + f_ * 1024, k_ * 16384, SC1); \
        else fb[k_][f_ - 4] = __builtin_amdgcn_raw_buffer_load_b128(rb, vb + ((f_ - 4) & 3) * 1024, k_ * 20480 + ((f_ - 4) >> 2) * 4096, SC1); } \
    } while (0)
    constexpr auto need = [](int k) { k = k > NKS ? NKS : k; return k <= WB ? 4 * k : 4 * WB + 9 * (k - WB); };
    constexpr auto cum = [need](int i) { const int a = need(i + 5), c = POOL - 5 * WB + (i > 0 ? 9 * i - 6 : 0); return a < c ? a : c; };
    static_for<0, cum(0)>([&](auto q) { ISSUE(decltype(q)::value); });
    SB();
#define PIN_ACC() do { _Pragma("unroll") for (int m_ = 0; m_ < 4; ++m_) _Pragma("unroll") for (int n_ = 0; n_ < 5; ++n_) asm volatile("" : "+v"(acc2[m_][n_])); } while (0)
    f32x4 acc2[4][5] = {};
    LAS char* pw = L;
    static_for<0, NKS>([&](auto ii) {
      constexpr int i = decltype(ii)::value;
      constexpr int c0 = cum(i), cn = cum(i + 1) - cum(i);
      static_assert(cn <= 9 && c0 >= need(i + 1), "schedule");
      static_for<0, 20>([&](auto mm) {
        constexpr int mn = decltype(mm)::value, m = mn / 5, n = mn % 5;
        acc2[m][n] = mfma16(fb[i][n], fa[i][m], acc2[m][n]);
        constexpr int slot = (mn & 1) ? (mn >> 1) - (9 - cn) : -1;
        if constexpr (slot >= 0 && mn < 19) { SB(); ISSUE(c0 + slot); SB(); }
        if constexpr (i == NKS - 1 && mn >= 2) { constexpr int m2 = (mn - 2) / 5, n2 = (mn - 2) % 5; SB(); *(LAS f32x4*)(pw + 16 * m2 * PSTR + n2 * 64) = acc2[m2][n2]; SB(); }
      });
      SB();
      if constexpr (i == NKS - 2) {
        PIN_ACC();
        lds_wait(L + C_Y, 4);
        const int ln = __builtin_amdgcn_mbcnt_hi(~0u, __builtin_amdgcn_mbcnt_lo(~0u, opaque0()));
        pw = L + aw * PART + (ln & 15) * PSTR + (ln >> 4) * 16;
        SB();
      }
    });
    *(LAS f32x4*)(pw + 48 * PSTR + 3 * 64) = acc2[3][3];
    *(LAS f32x4*)(pw + 48 * PSTR + 4 * 64) = acc2[3][4];
  } else {
    __builtin_amdgcn_s_setprio(2);
    const __amdgpu_buffer_rsrc_t rw = mkrsrc(w1, D * F * 2);
    unsigned tag;
    if (g1) {
      const __amdgpu_buffer_rsrc_t rx = mkrsrc(x + (size_t)p * 128 * D, 128 * D * 2);
      int vx[2], vw[4];
      {
        const int row = 32 * w + (lane >> 3);
        vx[0] = row * 1280 + (((lane & 7) ^ ((row >> 1) & 7)) << 4);
        vx[1] = (row + 8) * 1280 + (((lane & 7) ^ (((row + 8) >> 1) & 7)) << 4);
#pragma unroll
        for (int ii = 0; ii < 4; ++ii) {
          const int kr = 16 * w + 4 * ii + (lane >> 4);
          vw[ii] = kr * (F * 2) + j * 256 + (((lane & 15) ^ (((kr & 3) << 2) | ((kr >> 2) & 3))) << 4);
        }
      }
      const int rot = (5 * ((pi + jj) & 1) + (5 * xcd) / 8) % 10;
      const unsigned lbase = (unsigned)(uintptr_t)L + w * 4096;
#define KC(s) ((s) + rot >= 10 ? (s) + rot - 10 : (s) + rot)
#define G1LD(s, i) do { \
    if ((i) < 4) glds_fast(rx, lbase + ((s) % NBUF) * 32768 + (i) * 1024, vx[(i) & 1], KC(s) * 128 + ((i) >> 1) * 16 * 1280); \
    else glds_fast(rw, lbase + ((s) % NBUF) * 32768 + 16384 + ((i) - 4) * 1024, vw[((i) - 4) & 3], KC(s) * (64 * F * 2)); \
  } while (0)
#pragma unroll
      for (int i = 0; i < 8; ++i) G1LD(0, i);
#pragma unroll
      for (int i = 0; i < 8; ++i) G1LD(1, i);
      WAITV(16);
      BAR();
      PREP_ITEM(aw, 3); PREP_ITEM(aw, 4);
      WAITL(); lds_add(L + C_AUX, 1u, lane);
      SB();
      const int fh = w & 1, th = w >> 1;
      int xa[4], wa[2][2];
      {
        const int row = 64 * th + (lane & 31);
        const int c0 = hi ^ ((row >> 1) & 7);
        const int blk = (lane >> 4) & 1, q = (lane & 15) >> 2, pp = lane & 3;
#pragma unroll
        for (int s = 0; s < 4; ++s) xa[s] = row * 128 + (((2 * s) ^ c0) << 4);
#pragma unroll
        for (int ft = 0; ft < 2; ++ft)
#pragma unroll
          for (int t = 0; t < 2; ++t) {
            const int kr = 8 * hi + 4 * t + q;
            const int ch = 4 * (2 * fh + ft) + 2 * blk + (pp >> 1);
            wa[ft][t] = 16384 + 256 * kr + ((ch ^ ((q << 2) | ((2 * hi + t) & 3))) << 4) + 8 * (pp & 1);
          }
      }
      f32x16 acc[2][2] = {};
      Frags fA, fB;
#define RDX(f, bo, s, tt) f.x[s][tt] = ldsr128(L + (bo) + xa[s] + (tt) * 4096)
#define RDW(f, bo, s, ft) do { v2u wl_ = trread(L + (bo) + wa[ft][0] + (s) * 4096), wh_ = trread(L + (bo) + wa[ft][1] + (s) * 4096); f.w[s][ft] = v4u{wl_.x, wl_.y, wh_.x, wh_.y}; } while (0)
#define MM(f, s, ft, tt) acc[ft][tt] = mfma32(f.w[s][ft], f.x[s][tt], acc[ft][tt])
#define ITER(s, vm, cur, nxt) do { \
    asm volatile("s_waitcnt vmcnt(" #vm ") lgkmcnt(0)" ::: "memory"); \
    lds_add(L + C_MMA, 1u, lane); SB(); \
    MM(cur, 0, 0, 0); MM(cur, 0, 0, 1); MM(cur, 0, 1, 0); SB(); \
    lds_wait(L + C_MMA, 4 * ((s) + 2)); SB(); \
    constexpr int bo_ = (((s) + 1) % NBUF) * 32768; constexpr bool ld_ = (s) + NBUF < 10; \
    MM(cur, 0, 1, 1); RDX(nxt, bo_, 0, 0); RDX(nxt, bo_, 0, 1); SB(); if (ld_) G1LD((s) + NBUF, 0); SB(); \
    MM(cur, 1, 0, 0); RDW(nxt, bo_, 0, 0); SB(); \
    MM(cur, 1, 0, 1); RDW(nxt, bo_, 0, 1); SB(); if (ld_) G1LD((s) + NBUF, 4); SB(); \
    MM(cur, 1, 1, 0); RDX(nxt, bo_, 1, 0); RDX(nxt, bo_, 1, 1); SB(); if (ld_) G1LD((s) + NBUF, 1); SB(); \
    MM(cur, 1, 1, 1); RDW(nxt, bo_, 1, 0); SB(); \
    MM(cur, 2, 0, 0); RDW(nxt, bo_, 1, 1); SB(); if (ld_) G1LD((s) + NBUF, 5); SB(); \
    MM(cur, 2, 0, 1); RDX(nxt, bo_, 2, 0); RDX(nxt, bo_, 2, 1); SB(); if (ld_) G1LD((s) + NBUF, 2); SB(); \
    MM(cur, 2, 1, 0); RDW(nxt, bo_, 2, 0); SB(); \
    MM(cur, 2, 1, 1); RDW(nxt, bo_, 2, 1); SB(); if (ld_) G1LD((s) + NBUF, 6); SB(); \
    MM(cur, 3, 0, 0); RDX(nxt, bo_, 3, 0); RDX(nxt, bo_, 3, 1); SB(); if (ld_) G1LD((s) + NBUF, 3); SB(); \
    MM(cur, 3, 0, 1); RDW(nxt, bo_, 3, 0); RDW(nxt, bo_, 3, 1); SB(); \
    MM(cur, 3, 1, 0); SB(); if (ld_) G1LD((s) + NBUF, 7); SB(); \
    MM(cur, 3, 1, 1); SB(); \
  } while (0)
      WAITV(8);
      lds_add(L + C_MMA, 1u, lane); lds_wait(L + C_MMA, 4);
#pragma unroll
      for (int s = 0; s < 4; ++s) { RDX(fA, 0, s, 0); RDX(fA, 0, s, 1); RDW(fA, 0, s, 0); RDW(fA, 0, s, 1); }
      SB();
#pragma unroll
      for (int i = 0; i < 8; ++i) G1LD(2, i);
      SB();
      ITER(0, 8, fA, fB);
      ITER(1, 8, fB, fA);
      ITER(2, 8, fA, fB);
      ITER(3, 8, fB, fA);
      ITER(4, 8, fA, fB);
      ITER(5, 8, fB, fA);
      ITER(6, 8, fA, fB);
      ITER(7, 8, fB, fA);
      ITER(8, 0, fA, fB);
#pragma unroll
      for (int s = 0; s < 4; ++s) { MM(fB, s, 0, 0); MM(fB, s, 0, 1); MM(fB, s, 1, 0); MM(fB, s, 1, 1); }
      SB();
      lds_add(L + C_MMA, 1u, lane);
      lds_wait_sleep(L + C_S, 1);
      tag = __builtin_amdgcn_readfirstlane(*(LAS unsigned*)(L + C_TAG));
#pragma unroll
      for (int ft = 0; ft < 2; ++ft) {
        const int fg = 2 * fh + ft;
        const int ks = 4 * j + fg;
        f32x4 c4[4];
#pragma unroll
        for (int g = 0; g < 4; ++g) c4[g] = *(LAS f32x4*)(L + XFC + (32 * fg + 8 * g + 4 * hi) * 4);
#pragma unroll
        for (int tt = 0; tt < 2; ++tt) {
          const int tok = 64 * th + 32 * tt + (lane & 31);
          const f32x2 st = *(LAS f32x2*)(L + XFS + tok * 8);
          const f32x16& a = acc[ft][tt];
          const float zs1 = -1.44269504088896f * st.y, dn1 = __builtin_amdgcn_rcpf(st.y);
          const f32x2 zs = f32x2{zs1, zs1}, dn = f32x2{dn1, dn1};
          const int mb2 = 2 * p + th, mt = 2 * tt + ((lane >> 4) & 1);
          const int off = ks < KS2 ? HP_OFF + mb2 * HP_MB + ks * 4096 + mt * 1024 + ((2 * hi) * 16 + (lane & 15)) * 16 : -1024;
#pragma unroll
          for (int hh = 0; hh < 2; ++hh) {
            unsigned pk[4];
#pragma unroll
            for (int e2 = 0; e2 < 4; ++e2) {
              const int ee = hh * 4 + e2;
              const f32x2 cc = f32x2{c4[ee >> 1][(2 * ee) & 3], c4[ee >> 1][(2 * ee + 1) & 3]};
              const f32x2 dd = __builtin_elementwise_fma(cc, f32x2{-st.x, -st.x}, f32x2{a[2 * ee], a[2 * ee + 1]});
              const f32x2 zz = dd * zs;
              const f32x2 ex = f32x2{__builtin_amdgcn_exp2f(zz.x), __builtin_amdgcn_exp2f(zz.y)};
              const f32x2 ss = __builtin_elementwise_fma(ex, dn, dn);
              const f32x2 hv2 = dd * f32x2{__builtin_amdgcn_rcpf(ss.x), __builtin_amdgcn_rcpf(ss.y)};
              pk[e2] = pack2(hv2.x, hv2.y);
            }
            __builtin_amdgcn_raw_buffer_store_b128(v4u{pk[0], pk[1], pk[2], pk[3]}, rws, off + hh * 256, 0, SC1);
            SB();
          }
        }
      }
      lds_add(L + C_MMA, 1u, lane);
      WAITV(0);
      {
        unsigned old = 0;
        if (lane == 0) old = __hip_atomic_fetch_add((LAS unsigned*)(L + C_H), 1u, RLX_WG);
        if (__builtin_amdgcn_readfirstlane(old) == 3) set_flag(ws, FL_H + (p * 16 + j) * FSTR, tag, lane);
      }
    } else {
      const int idx16 = 4 * rg + pi;
      const int srow = 512 * rg + 64 * (4 * hf + pi) + 16 * w + (lane >> 3);
      const int cg = 14 * hf + idx16;
      const bool csv = idx16 < 14 - hf;
      const __amdgpu_buffer_rsrc_t rx = mkrsrc(x, S * D * 2);
      v4u xv[2][10], wv[20];
#pragma unroll
      for (int h2 = 0; h2 < 2; ++h2)
#pragma unroll
        for (int i = 0; i < 10; ++i) xv[h2][i] = __builtin_amdgcn_raw_buffer_load_b128(rx, (srow + 8 * h2) * 1280 + (lane & 7) * 16 + i * 128, 0, 0);
      SB();
      WAITV(20);
      BAR();
      PREP_ITEM(aw, 3); PREP_ITEM(aw, 4);
      WAITL(); lds_add(L + C_AUX, 1u, lane);
      SB();
      const int wvo = csv ? (160 * w + (lane >> 3)) * (F * 2) + cg * 128 + (lane & 7) * 16 : -16;
#pragma unroll
      for (int i = 0; i < 20; ++i) wv[i] = __builtin_amdgcn_raw_buffer_load_b128(rw, wvo, csv ? i * 8 * F * 2 : 0, 0);
      SB();
      tag = __builtin_amdgcn_readfirstlane(*(LAS unsigned*)(L + C_TAG));
#pragma unroll
      for (int h2 = 0; h2 < 2; ++h2) {
        float s = 0.f;
#pragma unroll
        for (int i = 0; i < 10; ++i)
#pragma unroll
          for (int e2 = 0; e2 < 4; ++e2) s = dot2(xv[h2][i][e2], 0x3f803f80u, s);
        s += __shfl_xor(s, 1); s += __shfl_xor(s, 2); s += __shfl_xor(s, 4);
        const float mean = s * (1.f / D);
        float q0 = 0.f, q1 = 0.f;
#pragma unroll
        for (int i = 0; i < 10; ++i)
#pragma unroll
          for (int e2 = 0; e2 < 4; ++e2) { const float a0 = bflo(xv[h2][i][e2]) - mean, a1 = bfhi(xv[h2][i][e2]) - mean; q0 = fmaf(a0, a0, q0); q1 = fmaf(a1, a1, q1); }
        float q = q0 + q1;
        q += __shfl_xor(q, 1); q += __shfl_xor(q, 2); q += __shfl_xor(q, 4);
        const float den = __builtin_sqrtf(rbf(rbf(q * (1.f / D)) + 1e-5f));
        __builtin_amdgcn_raw_buffer_store_b64(v2u{pack2(mean, den), tag}, rws, (lane & 7) == 0 ? ST_OFF + (srow + 8 * h2) * 8 : -16, 0, SC1);
      }
      float c[8] = {};
#pragma unroll
      for (int i = 0; i < 20; ++i)
#pragma unroll
        for (int e2 = 0; e2 < 4; ++e2) { c[2 * e2] += bflo(wv[i][e2]); c[2 * e2 + 1] += bfhi(wv[i][e2]); }
      *(LAS f32x4*)(L + ((w * 8 + (lane >> 3)) * 64 + (lane & 7) * 8) * 4) = f32x4{c[0], c[1], c[2], c[3]};
      *(LAS f32x4*)(L + ((w * 8 + (lane >> 3)) * 64 + (lane & 7) * 8 + 4) * 4) = f32x4{c[4], c[5], c[6], c[7]};
      WAITL();
      unsigned old = 0;
      if (lane == 0) old = __hip_atomic_fetch_add((LAS unsigned*)(L + C_H), 1u, RLX_WG);
      if (__builtin_amdgcn_readfirstlane(old) == 3) {
        asm volatile("" ::: "memory");
        float t4[4] = {};
#pragma unroll
        for (int i = 0; i < 32; ++i) t4[i & 3] += *(LAS float*)(L + (i * 64 + lane) * 4);
        __builtin_amdgcn_raw_buffer_store_b64(v2u{__float_as_uint((t4[0] + t4[1]) + (t4[2] + t4[3])), tag}, rws, csv ? CS_OFF + (cg * 64 + lane) * 8 : -16, 0, SC1);
      }
      lds_add(L + C_MMA, 12u, lane);
      if (b == 56 && w == 0) {
#pragma unroll 1
        for (unsigned sp = 0; sp < SPIN_CAP; ++sp) {
          bool ok = true;
#pragma unroll
          for (int i = 0; i < 4; ++i) ok = ok && flag_load(ws, FL_P + i * 64 * FSTR, 64, lane) == tag;
          if (__all(ok)) break;
          __builtin_amdgcn_s_sleep(32);
        }
        set_flag(ws, FL_GEN, tag, lane);
      }
    }
    unsigned hb_ = 0;
#define HBAR() do { WAITL(); lds_add(L + C_G2, 1u, lane); hb_ += 4; lds_wait(L + C_G2, hb_); } while (0)
    {
      const __amdgpu_buffer_rsrc_t rxx = mkrsrc(x, S * D * 2);
      lds_wait_sleep(L + C_MMA, 48);
#pragma unroll
      for (int i = 0; i < 3; ++i) {
        const int s = aw + 4 * i;
        if (s < 10) {
          const int n = s * 64 + lane; const int row = n / 10, c = n - row * 10;
          GLDS(rxx, L + EX + s * 1024, (64 * mb + row) * 1280 + 160 * nb + c * 16, 0);
        }
      }
      const float dv = bflo(Dv[dpair + (aw >> 1)]), dv3 = bflo(Dv[d3]);
      lds_wait_sleep(L + C_ST, 1);
      __builtin_amdgcn_s_setprio(1);
      LAS char* Lc = L + CV + (aw >> 1) * CH_BYTES;
      const int cts = aw & 1;
      f32x4 acc[4] = {};
      if (cts == 0) conv8<0>(Lc + aoff, Lc + boff, acc); else conv8<1>(Lc + aoff, Lc + boff, acc);
      __builtin_amdgcn_s_setprio(2);
#pragma unroll
      for (int ti = 0; ti < 4; ++ti) conv_out(Lc, acc[ti], cts == 0 ? (ti < 2 ? ti : ti + 4) : 2 + ti, dpair + (aw >> 1), dv, e, kq, rws, tag);
      f32x4 ac3[4] = {};
      if (ts3 == 0) conv2<0>(L + CV3 + aoff + aw * 128, L + CV3 + boff + aw * 128, ac3); else conv2<1>(L + CV3 + aoff + aw * 128, L + CV3 + boff + aw * 128, ac3);
#pragma unroll
      for (int ti = 0; ti < 4; ++ti) *(LAS f32x4*)(L + SC3 + (aw * 4 + ti) * 1024 + lane * 16) = ac3[ti];
      WAITV(4);
      HBAR();
      f32x4 a = {};
#pragma unroll
      for (int ww = 0; ww < 4; ++ww) a += *(LAS f32x4*)(L + SC3 + (ww * 4 + aw) * 1024 + lane * 16);
      conv_out(L + CV3, a, ts3 == 0 ? (aw < 2 ? aw : aw + 4) : 2 + aw, d3, dv3, e, kq, rws, tag);
    }
    {
      const int q = (lane & 15) >> 2, pp = lane & 3, pos = lane & 15;
      const int tok = 16 * aw + 2 * (pos >> 2) + (pos & 1) + 8 * ((pos >> 1) & 1);
      lds_wait_sleep(L + C_S, 1);
      const f32x2 st = *(LAS f32x2*)(L + XFS + ((mb & 1) * 64 + tok) * 8);
      v4u xn[5];
#pragma unroll
      for (int i = 0; i < 5; ++i) {
        const v2u xv2 = *(LAS v2u*)(L + EX + tok * 160 + (4 * i + kq) * 8);
        const unsigned d01 = pack2(bflo(xv2.x) - st.x, bfhi(xv2.x) - st.x), d23 = pack2(bflo(xv2.y) - st.x, bfhi(xv2.y) - st.x);
        xn[i] = v4u{pack2(bflo(d01) * st.y, bfhi(d01) * st.y), pack2(bflo(d23) * st.y, bfhi(d23) * st.y), xv2.x, xv2.y};
      }
      WAITL();
      lds_add(L + C_Y, 1u, lane);
      for (int i = 0; i < 2; ++i) __builtin_amdgcn_s_sleep(4);
      {
        v4u yv[5];
        const int yo = Y_OFF + (80 * nb + 4 * aw + kq) * 8192 + mb * 256 + e * 16;
#pragma unroll 1
        for (unsigned sp = 0; sp < SPIN_CAP; ++sp) {
          asm volatile("" ::: "memory");
          bool ok = true;
#pragma unroll
          for (int i = 0; i < 5; ++i) yv[i] = __builtin_amdgcn_raw_buffer_load_b128(rws, yo, i * 16 * 8192, SC1);
#pragma unroll
          for (int i = 0; i < 5; ++i) ok = ok && yv[i].y == tag && yv[i].w == tag;
          if (__all(ok)) break;
          __builtin_amdgcn_s_sleep(8);
        }
#pragma unroll
        for (int i = 0; i < 5; ++i) *(LAS v2u*)(L + EY + (16 * i + 4 * aw + kq) * 144 + e * 8) = v2u{yv[i].x, yv[i].z};
      }
      HBAR();
#pragma unroll
      for (int i = 0; i < 5; ++i) {
        const int dq = 4 * i + kq;
        const v2u yv = trread(L + EY + (4 * dq + q) * 144 + aw * 32 + pp * 8);
        const unsigned s01 = pack2(bflo(xn[i].x) * bflo(yv.x), bfhi(xn[i].x) * bfhi(yv.x)), s23 = pack2(bflo(xn[i].y) * bflo(yv.y), bfhi(xn[i].y) * bfhi(yv.y));
        *(LAS v4u*)(L + ES + (tok * 20 + dq) * 16) = v4u{s01, s23, xn[i].z, xn[i].w};
      }
    }
  }
  BAR();
  const int lane2 = __builtin_amdgcn_mbcnt_hi(~0u, __builtin_amdgcn_mbcnt_lo(~0u, opaque0()));
#pragma unroll
  for (int i = 0; i < 3; ++i) {
    const int it = w + 8 * i;
    if (it < 20) {
      const int n = it * 64 + lane2;
      const int tok = n / 20, dq = n - tok * 20;
      f32x4 o = *(LAS f32x4*)(L + tok * PSTR + dq * 16);
#pragma unroll
      for (int ww = 1; ww < 4; ++ww) o += *(LAS f32x4*)(L + ww * PART + tok * PSTR + dq * 16);
      const v4u sx = *(LAS v4u*)(L + ES + n * 16);
      const unsigned o01 = pack2(o[0], o[1]), o23 = pack2(o[2], o[3]);
      const unsigned g01 = pack2(silu_f(bflo(o01)), silu_f(bfhi(o01))), g23 = pack2(silu_f(bflo(o23)), silu_f(bfhi(o23)));
      const unsigned m01 = pack2(bflo(sx.x) * bflo(g01), bfhi(sx.x) * bfhi(g01)), m23 = pack2(bflo(sx.y) * bflo(g23), bfhi(sx.y) * bfhi(g23));
      v2u res;
      res.x = pack2(bflo(sx.z) + bflo(m01), bfhi(sx.z) + bfhi(m01));
      res.y = pack2(bflo(sx.w) + bflo(m23), bfhi(sx.w) + bfhi(m23));
      __builtin_amdgcn_raw_buffer_store_b64(res, mkrsrc(out, S * D * 2), ((64 * mb + tok) * D + 80 * nb + dq * 4) * 2, 0, 16);
    }
  }
}

extern "C" void kernel_launch(void* const* d_in, const int* in_sizes, int n_in,
                              void* d_out, int out_size, void* d_ws, size_t ws_size,
                              hipStream_t stream) {
  fused<<<256, 512, 0, stream>>>((const u16*)d_in[0], (const u16*)d_in[1], (const u16*)d_in[2], (const u16*)d_in[3],
                                 (const u16*)d_in[4], (const u16*)d_in[5], (const u16*)d_in[7], (const u16*)d_in[8],
                                 (char*)d_ws, (u16*)d_out);
}
```

```cpp
#define EXF_STAMP 1
#define EXF_WAVE 0
#include <hip/hip_runtime.h>
#include <stdint.h>

typedef unsigned short u16;
typedef unsigned v4u __attribute__((ext_vector_type(4)));
typedef unsigned v2u __attribute__((ext_vector_type(2)));
typedef float f32x2 __attribute__((ext_vector_type(2)));
typedef float f32x4 __attribute__((ext_vector_type(4)));
typedef float f32x16 __attribute__((ext_vector_type(16)));
typedef short bf16x8 __attribute__((ext_vector_type(8)));
typedef short s16x4 __attribute__((ext_vector_type(4)));
typedef __bf16 bf16x2 __attribute__((ext_vector_type(2)));
#define LAS __attribute__((address_space(3)))
#define GAS __attribute__((address_space(1)))
#define DI __device__ __forceinline__
#define RLX_AGENT __ATOMIC_RELAXED, __HIP_MEMORY_SCOPE_AGENT
#define RLX_WG __ATOMIC_RELAXED, __HIP_MEMORY_SCOPE_WORKGROUP

#define FSTR_N 128
constexpr int S = 2048, D = 640, F = 1728;
constexpr int KS2 = 54;
constexpr int FSTR = FSTR_N;
constexpr int FL_GEN = 0, FL_P = 131072, FL_H = FL_P + 256 * 256;
constexpr int ST_OFF = 32768;
constexpr int CS_OFF = ST_OFF + S * 8;
constexpr int HP_OFF = 262144;
constexpr int HP_MB = KS2 * 4096;
constexpr int W2P_OFF = HP_OFF + 32 * HP_MB;
constexpr int W2P_NB = KS2 * 5120;
constexpr int R_OFF = W2P_OFF + 8 * W2P_NB;
constexpr int Y_OFF = R_OFF + D * S * 2;
constexpr int WS_BYTES = Y_OFF + D * S * 4;
constexpr unsigned RSRC3 = 0x00020000u;
constexpr int SC1 = 16;
constexpr unsigned SPIN_CAP = 1u << 18;

DI float bflo(unsigned u) { return __uint_as_float(u << 16); }
DI float bfhi(unsigned u) { return __uint_as_float(u & 0xffff0000u); }
DI unsigned pack2(float a, float b) {
  bf16x2 r = __builtin_convertvector(f32x2{a, b}, bf16x2);
  return __builtin_bit_cast(unsigned, r);
}
DI float rbf(float a) { return bflo(pack2(a, a)); }
DI float silu_f(float x) { return x * __builtin_amdgcn_rcpf(1.f + __builtin_amdgcn_exp2f(-1.44269504088896f * x)); }
DI f32x4 mfma16(v4u a, v4u b, f32x4 c) {
  return __builtin_amdgcn_mfma_f32_16x16x32_bf16(__builtin_bit_cast(bf16x8, a), __builtin_bit_cast(bf16x8, b), c, 0, 0, 0);
}
DI f32x16 mfma32(v4u a, v4u b, f32x16 c) {
  return __builtin_amdgcn_mfma_f32_32x32x16_bf16(__builtin_bit_cast(bf16x8, a), __builtin_bit_cast(bf16x8, b), c, 0, 0, 0);
}
DI float dot2(unsigned a, unsigned b, float c) {
  return __builtin_amdgcn_fdot2_f32_bf16(__builtin_bit_cast(bf16x2, a), __builtin_bit_cast(bf16x2, b), c, false);
}
DI v2u trread(LAS char* p) { return __builtin_bit_cast(v2u, __builtin_amdgcn_ds_read_tr16_b64_v4i16((LAS s16x4*)p)); }
DI v4u ldsr128(LAS char* p) { return *(LAS v4u*)p; }
DI __amdgpu_buffer_rsrc_t mkrsrc(const void* p, int bytes) { return __builtin_amdgcn_make_buffer_rsrc((void*)p, 0, bytes, RSRC3); }
DI void glds_asm(__amdgpu_buffer_rsrc_t rs, unsigned ldsa, int voff, int soff) {
  asm volatile("s_nop 4\n\ts_mov_b32 m0, %0\n\ts_nop 0\n\tbuffer_load_dwordx4 %1, %2, %3 offen lds"
               :: "s"(ldsa), "v"(voff), "s"(rs), "s"(soff) : "m0", "memory");
}
DI void glds_fast(__amdgpu_buffer_rsrc_t rs, unsigned ldsa, int voff, int soff) {
  asm volatile("s_mov_b32 m0, %0\n\ts_nop 0\n\tbuffer_load_dwordx4 %1, %2, %3 offen lds"
               :: "s"(ldsa), "v"(voff), "s"(rs), "s"(soff) : "m0", "memory");
}
#define GLDS(rs, ldsp, voff, soff) glds_asm(rs, (unsigned)(uintptr_t)(ldsp), voff, soff)
#define WAITV(n) asm volatile("s_waitcnt vmcnt(" #n ")" ::: "memory")
#define WAITL() asm volatile("s_waitcnt lgkmcnt(0)" ::: "memory")
#define BAR() do { WAITL(); __builtin_amdgcn_s_barrier(); asm volatile("" ::: "memory"); } while (0)
#define SB() __builtin_amdgcn_sched_barrier(0)

#define STAMP_DECL unsigned long long xt0 = 0, xt1 = 0
#define STAMP(i) do { if ((i) == 0) asm volatile("s_memrealtime %0\n\ts_waitcnt lgkmcnt(0)" : "=s"(xt0) :: "memory"); if ((i) == EXF_STAMP) asm volatile("s_memrealtime %0\n\ts_waitcnt lgkmcnt(0)" : "=s"(xt1) :: "memory"); } while (0)
#define STAMP_OUT(kid) do { if (blockIdx.x == 73 && w == EXF_WAVE) { unsigned long long t2 = __builtin_amdgcn_s_memrealtime(); const unsigned long long tgt = t2 + 10 * (xt1 - xt0); while (__builtin_amdgcn_s_memrealtime() < tgt) __builtin_amdgcn_s_sleep(1); } } while (0)

#define ROT_EXPR ((5 * ((pi + jj) & 1) + (5 * xcd) / 8) % 10)
#define WA_N 3
#define POLL_SLEEP 8
#define CODE_PF 1
#define CODE_BYTES 39000u
#define EXACT_PREP 1
#define PREP_PRIO 3
#define CONV_PRIO 1
#define WB_N 6
#define ROT2_EXPR 0
#define DELAY_POLL 32
#define ABL 0
#define FH_AUX 1
#define Y_DELAY 2
#define C8_PRIO 1
#define G2_PRIO 0
#define POOL_N 40
#define S1_EARLY 1
#define STAGE_START 44
#define LOOK_N 5
#define FH_WAIT 4
#define H_DELAY 0
#define C3_PRIO 2
#define FAST_EPI 1
#define MMA_PREP 1
#define KV_DELAY 0
#define EARLY_PART 1
#define SEL_RETRY 0
#define Y_SLEEP 4
#define AUX_PRIO 0
#define MMA_PRIO 2
#define CONV_START 44
constexpr int NBUF = 3;
constexpr int CH_HA = 0, CH_HB = 4224, CH_R = 8448, CH_RS = 4512, CH_BYTES = CH_R + 4 * CH_RS;
constexpr int CV = 98304;
constexpr int CV3 = 32768;
constexpr int SC3 = 65536;
constexpr int PK = CV, PV = PK + 11264, PW = PV + 10240, PS = PW + 10240, PO = PS + 1024;
constexpr int EY = CV, EX = 0, ES = EY + 80 * 144;
constexpr int XFS = CV + 2 * CH_BYTES;
constexpr int XFC = XFS + 1024;
constexpr int CNT = XFC + 512;
constexpr int C_AUX = CNT, C_G2 = CNT + 4, C_Y = CNT + 8, C_H = CNT + 12, C_MMA = CNT + 16, C_S = CNT + 20, C_P = CNT + 24,
              C_TAG = CNT + 60, C_ST = CNT + 40, C_HR = CNT + 32, C_YR = CNT + 36;
constexpr int LDS_BYTES = CNT + 64;
static_assert(PO + 10240 <= XFS && ES + 20480 <= XFS && LDS_BYTES <= 163840, "lds");
constexpr int PSTR = 336, PART = 64 * PSTR;

DI void lds_add(LAS char* c, unsigned v, int lane) {
  if (lane == 0) __hip_atomic_fetch_add((LAS unsigned*)c, v, RLX_WG);
}
DI void lds_wait(LAS char* c, unsigned target) {
  while ((unsigned)__builtin_amdgcn_readfirstlane(__hip_atomic_load((LAS unsigned*)c, RLX_WG)) < target) {}
  asm volatile("" ::: "memory");
}
DI void lds_wait_sleep(LAS char* c, unsigned target) {
  while ((unsigned)__builtin_amdgcn_readfirstlane(__hip_atomic_load((LAS unsigned*)c, RLX_WG)) < target) __builtin_amdgcn_s_sleep(1);
  asm volatile("" ::: "memory");
}
DI unsigned flag_load(char* ws, int off, int n, int lane) {
  return __hip_atomic_load((GAS unsigned*)(ws + off + (lane < n ? lane : 0) * FSTR), RLX_AGENT);
}
DI void poll_flags(char* ws, int off, int n, unsigned tag, int lane) {
#pragma unroll 1
  for (unsigned spins = 0; spins < SPIN_CAP; ++spins) {
    const unsigned v = flag_load(ws, off, n, lane);
    if (__all(v == tag)) break;
    __builtin_amdgcn_s_sleep(POLL_SLEEP);
  }
  __builtin_amdgcn_fence(__ATOMIC_ACQUIRE, "wavefront");
}
DI void set_flag(char* ws, int off, unsigned tag, int lane) {
  if (lane == 0) __hip_atomic_store((GAS unsigned*)(ws + off), tag, RLX_AGENT);
}

DI unsigned opaque0() { unsigned z; asm volatile("v_mov_b32 %0, 0" : "=v"(z)); return z; }
template <int I> struct IC { static constexpr int value = I; };
template <int B, int E, class Fn> DI void static_for(Fn&& f) { if constexpr (B < E) { f(IC<B>{}); static_for<B + 1, E>(f); } }
struct Frags { v4u x[4][2], w[4][2]; };

template <int TS> DI void conv_ld(LAS char* pa, LAS char* pb, v4u (&A)[8], v4u (&B)[8]) {
  constexpr int NA = TS == 0 ? 8 : 6;
#pragma unroll
  for (int a = 0; a < NA; ++a) { A[a] = ldsr128(pa + a * 512); B[a] = ldsr128(pb + (7 - a) * 512); }
}
template <int TS> DI void conv_mm(const v4u (&A)[8], const v4u (&B)[8], f32x4 (&acc)[4]) {
#pragma unroll
  for (int ti = 0; ti < 4; ++ti) {
    const int n = TS == 0 ? (ti == 0 ? 0 : ti == 1 ? 1 : ti == 2 ? 6 : 7) : 2 + ti;
#pragma unroll
    for (int a = 0; a <= n; ++a) acc[ti] = mfma16(A[a], B[n - a], acc[ti]);
  }
}
DI void conv_sched() {
#pragma unroll
  for (int i = 0; i < 16; ++i) { __builtin_amdgcn_sched_group_barrier(0x8, 1, 0); __builtin_amdgcn_sched_group_barrier(0x100, 1, 0); }
  __builtin_amdgcn_sched_group_barrier(0x8, 2, 0);
}
template <int TS> DI void conv8(LAS char* pa, LAS char* pb, f32x4 (&acc)[4]) {
  v4u A0[8], B0[8], A1[8], B1[8];
  conv_ld<TS>(pa, pb, A0, B0);
  SB();
#pragma unroll 1
  for (int r = 0; r < 8; r += 2) {
    conv_ld<TS>(pa + 64, pb + 64, A1, B1); conv_mm<TS>(A0, B0, acc); conv_sched(); SB();
    pa += 128; pb += 128;
    conv_ld<TS>(pa, pb, A0, B0); conv_mm<TS>(A1, B1, acc); conv_sched(); SB();
  }
}
template <int TS> DI void conv2(LAS char* pa, LAS char* pb, f32x4 (&acc)[4]) {
#pragma unroll 1
  for (int r = 0; r < 2; ++r) {
    v4u A[8], B[8];
    conv_ld<TS>(pa, pb, A, B); conv_mm<TS>(A, B, acc);
    pa += 64; pb += 64;
  }
}
DI unsigned kshift2(float k1, float k2, float k3, float k4, float k5, float c0, float c1, float c2, float c3) {
  unsigned a = pack2(k1 * c3, k2 * c3);
  unsigned t = pack2(k2 * c2, k3 * c2);
  a = pack2(bflo(a) + bflo(t), bfhi(a) + bfhi(t));
  t = pack2(k3 * c1, k4 * c1);
  a = pack2(bflo(a) + bflo(t), bfhi(a) + bfhi(t));
  t = pack2(k4 * c0, k5 * c0);
  return pack2(bflo(a) + bflo(t), bfhi(a) + bfhi(t));
}
DI void stage_h(LAS char* Lc, v4u h, int g) {
  *(LAS v4u*)(Lc + CH_HB + 112 + g * 16) = h;
  LAS char* a = Lc + CH_HA + 114 + g * 16;
  *(LAS u16*)(a) = (u16)(h.x & 0xffff);
  *(LAS unsigned*)(a + 2) = __builtin_amdgcn_alignbit(h.y, h.x, 16);
  *(LAS v2u*)(a + 6) = v2u{__builtin_amdgcn_alignbit(h.z, h.y, 16), __builtin_amdgcn_alignbit(h.w, h.z, 16)};
  *(LAS u16*)(a + 14) = (u16)(h.w >> 16);
}
DI void stage_r(LAS char* Lc, v4u u, int g) {
  LAS char* rb = Lc + CH_R + g * 16;
  *(LAS v4u*)(rb) = u;
  { LAS char* q = rb + CH_RS - 4; *(LAS unsigned*)(q) = u.x; *(LAS v2u*)(q + 4) = v2u{u.y, u.z}; *(LAS unsigned*)(q + 12) = u.w; }
  { LAS char* q = rb + 2 * CH_RS - 8; *(LAS v2u*)(q) = v2u{u.x, u.y}; *(LAS v2u*)(q + 8) = v2u{u.z, u.w}; }
  { LAS char* q = rb + 3 * CH_RS - 12; *(LAS unsigned*)(q) = u.x; *(LAS v2u*)(q + 4) = v2u{u.y, u.z}; *(LAS unsigned*)(q + 12) = u.w; }
}
DI void conv_out(LAS char* Lc, f32x4 a, int n, int d, float dv, int e, int kq, __amdgpu_buffer_rsrc_t rws, unsigned tag) {
  const int t = 256 * n + 64 * (e >> 2) + 2 * (e & 3) + 16 * kq;
  const unsigned ua = *(LAS unsigned*)(Lc + CH_R + 2 * (2046 - t));
  const unsigned ub = *(LAS unsigned*)(Lc + CH_R + 2 * (2038 - t));
  const unsigned c01 = pack2(a[0], a[1]), c23 = pack2(a[2], a[3]);
  const unsigned d01 = pack2(bfhi(ua) * dv, bflo(ua) * dv), d23 = pack2(bfhi(ub) * dv, bflo(ub) * dv);
  v2u o;
  o.x = pack2(bflo(c01) + bflo(d01), bfhi(c01) + bfhi(d01));
  o.y = pack2(bflo(c23) + bflo(d23), bfhi(c23) + bfhi(d23));
  const int off = Y_OFF + d * 8192 + (256 * n + 64 * (e >> 2) + 16 * kq + 4 * (e & 3)) * 4;
  __builtin_amdgcn_raw_buffer_store_b128(v4u{o.x, tag, o.y, tag}, rws, off, 0, SC1);
}

__global__ __launch_bounds__(512, 2) void fused(const u16* __restrict__ x, const u16* __restrict__ kk, const u16* __restrict__ vv,
                                                const u16* __restrict__ sk, const u16* __restrict__ hk, const u16* __restrict__ Dv,
                                                const u16* __restrict__ w1, const u16* __restrict__ w2,
                                                char* ws, u16* out) {
  __shared__ __attribute__((aligned(16))) char smem[LDS_BYTES];
  LAS char* L = (LAS char*)smem;
  if (__builtin_amdgcn_readfirstlane(threadIdx.x >> 6) == 7) {
    unsigned long long pc;
    asm volatile("s_getpc_b64 %0" : "=s"(pc));
    const unsigned co = min(((blockIdx.x >> 3) & 7) * 8192u + (threadIdx.x & 63) * 128u, CODE_BYTES);
    asm volatile("s_mov_b32 m0, %0\n\ts_nop 0\n\tglobal_load_lds_dword %1, %2" :: "s"((unsigned)(uintptr_t)(L + XFS)), "v"(co), "s"(pc) : "m0");
  }
  const int b = blockIdx.x, xcd = b & 7, r = b >> 3;
  const int rg = xcd >> 1, hf = xcd & 1, pi = r >> 3, jj = r & 7;
  const int p = 4 * rg + pi, j = 7 * hf + jj;
  const bool g1 = jj < 7;
  const int mbl = 2 * pi + ((r >> 2) & 1), nbl = r & 3;
  const int mb = 8 * rg + mbl, nb = 4 * hf + nbl;
  const int lane = threadIdx.x & 63;
  const int w = __builtin_amdgcn_readfirstlane(threadIdx.x >> 6);
  const int hi = lane >> 5, aw = w & 3;
  STAMP_DECL; STAMP(0);
  const __amdgpu_buffer_rsrc_t rws = mkrsrc(ws, WS_BYTES);
  if (threadIdx.x < 15) *(LAS unsigned*)(L + CNT + threadIdx.x * 4) = 0u;
  const int dpair = 80 * nb + 2 * mb, d3 = 80 * nb + 64 + (mb >> 1), ts3 = mb & 1;
  const int e = lane & 15, kq = lane >> 4;
  const int aoff = ((e & 1) ? CH_HB : CH_HA) + (kq + (e >> 1)) * 16;
  const int boff = CH_R + (3 - (e & 3)) * CH_RS + (248 - 32 * 7 - 8 * (e >> 2) + kq) * 16;

  {
    const __amdgpu_buffer_rsrc_t rk = mkrsrc(kk, S * D * 2), rv_ = mkrsrc(vv, S * D * 2), rsk = mkrsrc(sk, D * 8);
    const int sb = 64 * mb * 1280 + 160 * nb;
    int vo[2];
#pragma unroll
    for (int i = 0; i < 2; ++i) { const int n = (8 * i + w) * 64 + lane; const int row = n / 10; vo[i] = row * 1280 + (n - row * 10) * 16; }
    GLDS(rk, L + PK + w * 1024, vo[0] + sb - 5120, 0);
    GLDS(rv_, L + PV + w * 1024, vo[0], sb);
    {
      const int n = (5 + w) * 64 + lane; const int row = n / 10; const int vo3 = row * 1280 + (n - row * 10) * 16;
      if (w < 3) GLDS(rk, L + PK + (8 + w) * 1024, vo[1] + sb - 5120, 0);
      else if (w < 5) GLDS(rv_, L + PV + (5 + w) * 1024, vo3, sb);
      else if (w == 5) GLDS(rsk, L + PS, lane * 16, nb * 640);
    }
  }
#define PREP_ITEM(tg, cg) do { \
        const int q_ = (lane & 15) >> 2, pp_ = lane & 3, tl = 16 * (tg) + 4 * kq; \
        LAS char* ka_ = L + PK + (tl + q_) * 160 + (cg) * 32 + pp_ * 8; \
        v2u k0 = trread(ka_), k1 = trread(ka_ + 4 * 160); \
        v2u v0 = trread(ka_ + (PV - PK)); \
        v2u sw = *(LAS v2u*)(L + PS + ((cg) * 16 + e) * 8); \
        float kf[8] = {bflo(k0.x), bfhi(k0.x), bflo(k0.y), bfhi(k0.y), bflo(k1.x), bfhi(k1.x), bflo(k1.y), bfhi(k1.y)}; \
        float vf[4] = {bflo(v0.x), bfhi(v0.x), bflo(v0.y), bfhi(v0.y)}; \
        const float c0 = bflo(sw.x), c1 = bfhi(sw.x), c2 = bflo(sw.y), c3 = bfhi(sw.y); \
        float u[4]; \
        _Pragma("unroll") for (int e2 = 0; e2 < 4; e2 += 2) { \
          const unsigned a = kshift2(kf[e2 + 1], kf[e2 + 2], kf[e2 + 3], kf[e2 + 4], kf[e2 + 5], c0, c1, c2, c3); \
          u[e2] = bflo(a) * vf[e2]; u[e2 + 1] = bfhi(a) * vf[e2 + 1]; } \
        v2u o = v2u{pack2(u[3], u[2]), pack2(u[1], u[0])}; \
        const int ch = (cg) * 16 + e; \
        const int pos8 = (60 - tl) >> 2; \
        *(LAS v2u*)(L + PO + ch * 128 + ((((pos8 >> 1) ^ (ch & 7)) << 4) | ((pos8 & 1) << 3))) = o; \
      } while (0)

  if (w >= 4) {
    unsigned gen;
    asm volatile("s_load_dword %0, %1, 0x0\n\ts_waitcnt lgkmcnt(0)" : "=s"(gen) : "s"(ws) : "memory");
    const unsigned tag = gen * 0x9E3779B1u + 0x85EBCA6Bu;
    STAMP(1);
    if (aw == 0 && lane == 0) *(LAS unsigned*)(L + C_TAG) = tag;
#define AUXBAR() do { WAITL(); lds_add(L + C_AUX, 1u, lane); ab += 4; lds_wait(L + C_AUX, ab); } while (0)
    WAITV(0);
    BAR();
    unsigned ab = 4;
    STAMP(2);
    if (ABL & 4) lds_wait_sleep(L + C_MMA, 44);
    {
      const __amdgpu_buffer_rsrc_t rw2 = mkrsrc(w2, F * D * 2);
#pragma unroll
      for (int i = 0; i < 3; ++i) if (4 * i + aw < 10) {
        const int n = (4 * i + aw) * 64 + lane; const int row = n / 10;
        GLDS(rw2, L + PW + (4 * i + aw) * 1024, (row + (row >= 32 ? 992 : 0)) * 1280 + (n - row * 10) * 16, 32 * mb * 1280 + 160 * nb);
      }
    }
    {
      const int g = lane >> 4, q = (lane & 15) >> 2, pp = lane & 3;
      PREP_ITEM(aw, 0); PREP_ITEM(aw, 1); PREP_ITEM(aw, 2);
      AUXBAR();
#pragma unroll
      for (int i = 0; i < 3; ++i) {
        const int ri = aw + 4 * i;
        if (ri < 10) {
          const int ch = 8 * ri + (lane >> 3), cc = lane & 7;
          v4u val = ldsr128(L + PO + ch * 128 + ((cc ^ (ch & 7)) << 4));
          __builtin_amdgcn_raw_buffer_store_b128(val, rws, (ABL & 1) ? -16 : R_OFF + (80 * nb + ch) * 4096 + (31 - mb) * 128 + cc * 16, 0, SC1);
        }
      }
      __builtin_amdgcn_s_setprio(AUX_PRIO);
      if (aw < 2) WAITV(3); else WAITV(2);
      AUXBAR();
      STAMP(3);
#pragma unroll
      for (int i = 0; i < 3; ++i) {
        const int fi = aw + 4 * i;
        if (fi < 10) {
          const int half = fi / 5, nt = fi - half * 5, ks = mb + 32 * half;
          const int r0 = 16 * (g & 1) + 4 * (g >> 1);
          LAS char* a = L + PW + half * 5120 + (r0 + q) * 160 + nt * 32 + pp * 8;
          v2u lo = trread(a), hi2 = trread(a + 8 * 160);
          __builtin_amdgcn_raw_buffer_store_b128(v4u{lo.x, lo.y, hi2.x, hi2.y}, rws, (ks < KS2 && !(ABL & 1)) ? W2P_OFF + nb * W2P_NB + ks * 5120 + nt * 1024 + lane * 16 : -16, 0, SC1);
        }
      }
    }
    STAMP(4);
    WAITV(0);
    AUXBAR();
    if (aw == 0) set_flag(ws, FL_P + (nb * 32 + mb) * FSTR, tag, lane);
    STAMP(5);
    if (ABL & 2) lds_wait_sleep(L + C_MMA, 44);
    const __amdgpu_buffer_rsrc_t rh = mkrsrc(hk, D * S * 2);
    v4u cu[3];
    cu[0] = __builtin_amdgcn_raw_buffer_load_b128(rh, dpair * 4096 + aw * 1024 + lane * 16, 0, 0);
    cu[1] = __builtin_amdgcn_raw_buffer_load_b128(rh, dpair * 4096 + 4096 + aw * 1024 + lane * 16, 0, 0);
    cu[2] = __builtin_amdgcn_raw_buffer_load_b128(rh, d3 * 4096 + aw * 1024 + lane * 16, 0, 0);
    const int g = aw * 64 + lane;
    lds_wait_sleep(L + C_MMA, DELAY_POLL);
    if (aw == 1) {
      const int jc = g1 ? j : 0;
      v2u gr[4];
#define GRL(off) __builtin_bit_cast(v2u, __hip_atomic_load((GAS unsigned long long*)(ws + (off)), RLX_AGENT))
#pragma unroll 1
      for (unsigned sp = 0; sp < SPIN_CAP; ++sp) {
        gr[0] = GRL(ST_OFF + (p * 128 + lane) * 8);
        gr[1] = GRL(ST_OFF + (p * 128 + 64 + lane) * 8);
        gr[2] = GRL(CS_OFF + (jc * 128 + lane) * 8);
        gr[3] = GRL(CS_OFF + (jc * 128 + (j == 13 ? 0 : 64) + lane) * 8);
        if (__all(gr[0].y == tag && gr[1].y == tag && gr[2].y == tag && gr[3].y == tag)) break;
        __builtin_amdgcn_s_sleep(POLL_SLEEP);
      }
      *(LAS f32x2*)(L + XFS + lane * 8) = f32x2{bflo(gr[0].x), 1.f / bfhi(gr[0].x)};
      *(LAS f32x2*)(L + XFS + (64 + lane) * 8) = f32x2{bflo(gr[1].x), 1.f / bfhi(gr[1].x)};
      *(LAS unsigned*)(L + XFC + lane * 4) = gr[2].x;
      *(LAS unsigned*)(L + XFC + (64 + lane) * 4) = gr[3].x;
      WAITL();
      lds_add(L + C_S, 1u, lane);
    }
    if (aw == 2) { poll_flags(ws, FL_P + nb * 32 * FSTR, 32, tag, lane); lds_add(L + C_P, 1u, lane); }
    lds_wait_sleep(L + C_P, 1);
    STAMP(6);
    v4u rv[3];
    rv[0] = __builtin_amdgcn_raw_buffer_load_b128(rws, R_OFF + dpair * 4096 + aw * 1024 + lane * 16, 0, SC1);
    rv[1] = __builtin_amdgcn_raw_buffer_load_b128(rws, R_OFF + dpair * 4096 + 4096 + aw * 1024 + lane * 16, 0, SC1);
    rv[2] = __builtin_amdgcn_raw_buffer_load_b128(rws, R_OFF + d3 * 4096 + aw * 1024 + lane * 16, 0, SC1);
    const int rot2 = ROT2_EXPR;
    const __amdgpu_buffer_rsrc_t ra = mkrsrc(ws + HP_OFF + mb * HP_MB, HP_MB);
    const __amdgpu_buffer_rsrc_t rb = mkrsrc(ws + W2P_OFF + nb * W2P_NB, W2P_NB);
    const int va = lane * 16 + aw * 4096, vb = lane * 16 + aw * 5120;
    constexpr int WB = WB_N, NKS = 14, POOL = POOL_N;
    v4u fa[NKS][4], fb[NKS][5];
#define KIDX(i) ((i) + rot2 >= NKS ? (i) + rot2 - NKS : (i) + rot2)
#define LOADA(i) do { const int so_ = KIDX(i) * 16384; \
    _Pragma("unroll") for (int m = 0; m < 4; ++m) fa[i][m] = __builtin_amdgcn_raw_buffer_load_b128(ra, va + m * 1024, so_, SC1); } while (0)
#define LOADB(i) do { const int so_ = KIDX(i) * 20480; \
    _Pragma("unroll") for (int n = 0; n < 5; ++n) fb[i][n] = __builtin_amdgcn_raw_buffer_load_b128(rb, vb + (n & 3) * 1024, so_ + (n >> 2) * 4096, SC1); } while (0)
#pragma unroll
    for (int i = 0; i < WB; ++i) LOADB(i);
    SB();
    STAMP(7);
    lds_wait_sleep(L + C_MMA, STAGE_START);
    if (aw == 0 && lane < 32) *(LAS v4u*)(L + CV + (lane >> 4) * CH_BYTES + ((lane >> 3) & 1) * CH_HB + (lane & 7) * 16) = v4u{0, 0, 0, 0};
    if (aw == 3) {
#pragma unroll
      for (int i = 0; i < 4; ++i) {
        const int n = lane + 64 * i;
        if (n < 200) { const int cp = n / 25, sl = n - cp * 25; *(LAS v4u*)(L + CV + (cp >> 2) * CH_BYTES + CH_R + (cp & 3) * CH_RS + 4080 + sl * 16) = v4u{0, 0, 0, 0}; }
      }
    }
    stage_h(L + CV, cu[0], g);
    stage_h(L + CV + CH_BYTES, cu[1], g);
    stage_r(L + CV, rv[0], g);
    stage_r(L + CV + CH_BYTES, rv[1], g);
    if (aw == 0 && lane < 16) *(LAS v4u*)(L + CV3 + (lane >> 3) * CH_HB + (lane & 7) * 16) = v4u{0, 0, 0, 0};
    if (aw == 3) {
#pragma unroll
      for (int i = 0; i < 2; ++i) {
        const int n = lane + 64 * i;
        if (n < 100) { const int cp = n / 25, sl = n - cp * 25; *(LAS v4u*)(L + CV3 + CH_R + cp * CH_RS + 4080 + sl * 16) = v4u{0, 0, 0, 0}; }
      }
    }
    stage_h(L + CV3, cu[2], g);
    stage_r(L + CV3, rv[2], g);
    AUXBAR();
    if (aw == 0) lds_add(L + C_ST, 1u, lane);
    STAMP(8);
    lds_wait_sleep(L + C_H, FH_WAIT);
    for (int i = 0; i < H_DELAY; ++i) __builtin_amdgcn_s_sleep(4);
    STAMP(10);
    if (aw == 1) { poll_flags(ws, FL_H + p * 16 * FSTR, 14, tag, lane); lds_add(L + C_HR, 1u, lane); }
    __builtin_amdgcn_s_setprio(G2_PRIO);
    lds_wait_sleep(L + C_HR, 1);
    STAMP(11);
#define ISSUE(q) do { constexpr int q_ = (q); \
      if constexpr (q_ < 4 * WB) { fa[q_ / 4][q_ % 4] = __builtin_amdgcn_raw_buffer_load_b128(ra, va + (q_ % 4) * 1024, KIDX(q_ / 4) * 16384, SC1); } \
      else if constexpr (q_ < 4 * WB + 9 * (NKS - WB)) { constexpr int k_ = WB + (q_ - 4 * WB) / 9, f_ = (q_ - 4 * WB) % 9; \
        if constexpr (f_ < 4) fa[k_][f_] = __builtin_amdgcn_raw_buffer_load_b128(ra, va + f_ * 1024, KIDX(k_) * 16384, SC1); \
        else fb[k_][f_ - 4] = __builtin_amdgcn_raw_buffer_load_b128(rb, vb + ((f_ - 4) & 3) * 1024, KIDX(k_) * 20480 + ((f_ - 4) >> 2) * 4096, SC1); } \
    } while (0)
    constexpr auto need = [](int k) { k = k > NKS ? NKS : k; return k <= WB ? 4 * k : 4 * WB + 9 * (k - WB); };
    constexpr auto cum = [need](int i) { const int a = need(i + LOOK_N), c = POOL - 5 * WB + (i > 0 ? 9 * i - 6 : 0); return a < c ? a : c; };
    static_for<0, cum(0)>([&](auto q) { ISSUE(decltype(q)::value); });
    SB();
#define PIN_ACC() do { _Pragma("unroll") for (int m_ = 0; m_ < 4; ++m_) _Pragma("unroll") for (int n_ = 0; n_ < 5; ++n_) asm volatile("" : "+v"(acc2[m_][n_])); } while (0)
    f32x4 acc2[4][5] = {};
    LAS char* pw = L;
    static_for<0, NKS>([&](auto ii) {
      constexpr int i = decltype(ii)::value;
      constexpr int c0 = cum(i), cn = cum(i + 1) - cum(i);
      static_assert(cn <= 9 && c0 >= need(i + 1), "schedule");
      static_for<0, 20>([&](auto mm) {
        constexpr int mn = decltype(mm)::value, m = mn / 5, n = mn % 5;
        acc2[m][n] = mfma16(fb[i][n], fa[i][m], acc2[m][n]);
        constexpr int slot = (mn & 1) ? (mn >> 1) - (9 - cn) : -1;
        if constexpr (slot >= 0 && mn < 19) { SB(); ISSUE(c0 + slot); SB(); }
        if constexpr (i == NKS - 1 && mn >= 2) { constexpr int m2 = (mn - 2) / 5, n2 = (mn - 2) % 5; SB(); *(LAS f32x4*)(pw + 16 * m2 * PSTR + n2 * 64) = acc2[m2][n2]; SB(); }
      });
      SB();
      if constexpr (i == NKS - 2) {
        PIN_ACC();
        lds_wait(L + C_Y, 4);
        const int ln = __builtin_amdgcn_mbcnt_hi(~0u, __builtin_amdgcn_mbcnt_lo(~0u, opaque0()));
        pw = L + aw * PART + (ln & 15) * PSTR + (ln >> 4) * 16;
        SB();
      }
      if (i == 0) STAMP(12);
    });
    *(LAS f32x4*)(pw + 48 * PSTR + 3 * 64) = acc2[3][3];
    *(LAS f32x4*)(pw + 48 * PSTR + 4 * 64) = acc2[3][4];
  } else {
    __builtin_amdgcn_s_setprio(MMA_PRIO);
    const __amdgpu_buffer_rsrc_t rw = mkrsrc(w1, D * F * 2);
    unsigned tag;
    if (g1) {
      const __amdgpu_buffer_rsrc_t rx = mkrsrc(x + (size_t)p * 128 * D, 128 * D * 2);
      int vx[2], vw[4];
      {
        const int row = 32 * w + (lane >> 3);
        vx[0] = row * 1280 + (((lane & 7) ^ ((row >> 1) & 7)) << 4);
        vx[1] = (row + 8) * 1280 + (((lane & 7) ^ (((row + 8) >> 1) & 7)) << 4);
#pragma unroll
        for (int ii = 0; ii < 4; ++ii) {
          const int kr = 16 * w + 4 * ii + (lane >> 4);
          vw[ii] = kr * (F * 2) + j * 256 + (((lane & 15) ^ (((kr & 3) << 2) | ((kr >> 2) & 3))) << 4);
        }
      }
      const int rot = ROT_EXPR;
      const unsigned lbase = (unsigned)(uintptr_t)L + w * 4096;
#define KC(s) ((s) + rot >= 10 ? (s) + rot - 10 : (s) + rot)
#define G1LD(s, i) do { \
    if ((i) < 4) glds_fast(rx, lbase + ((s) % NBUF) * 32768 + (i) * 1024, vx[(i) & 1], KC(s) * 128 + ((i) >> 1) * 16 * 1280); \
    else glds_fast(rw, lbase + ((s) % NBUF) * 32768 + 16384 + ((i) - 4) * 1024, vw[((i) - 4) & 3], KC(s) * (64 * F * 2)); \
  } while (0)
#pragma unroll
      for (int i = 0; i < 8; ++i) G1LD(0, i);
#pragma unroll
      for (int i = 0; i < 8; ++i) G1LD(1, i);
      WAITV(16);
      BAR();
      PREP_ITEM(aw, 3); PREP_ITEM(aw, 4);
      WAITL(); lds_add(L + C_AUX, 1u, lane);
      SB();
      STAMP(1);
      const int fh = w & 1, th = w >> 1;
      int xa[4], wa[2][2];
      {
        const int row = 64 * th + (lane & 31);
        const int c0 = hi ^ ((row >> 1) & 7);
        const int blk = (lane >> 4) & 1, q = (lane & 15) >> 2, pp = lane & 3;
#pragma unroll
        for (int s = 0; s < 4; ++s) xa[s] = row * 128 + (((2 * s) ^ c0) << 4);
#pragma unroll
        for (int ft = 0; ft < 2; ++ft)
#pragma unroll
          for (int t = 0; t < 2; ++t) {
            const int kr = 8 * hi + 4 * t + q;
            const int ch = 4 * (2 * fh + ft) + 2 * blk + (pp >> 1);
            wa[ft][t] = 16384 + 256 * kr + ((ch ^ ((q << 2) | ((2 * hi + t) & 3))) << 4) + 8 * (pp & 1);
          }
      }
      f32x16 acc[2][2] = {};
      Frags fA, fB;
#define RDX(f, bo, s, tt) f.x[s][tt] = ldsr128(L + (bo) + xa[s] + (tt) * 4096)
#define RDW(f, bo, s, ft) do { v2u wl_ = trread(L + (bo) + wa[ft][0] + (s) * 4096), wh_ = trread(L + (bo) + wa[ft][1] + (s) * 4096); f.w[s][ft] = v4u{wl_.x, wl_.y, wh_.x, wh_.y}; } while (0)
#define MM(f, s, ft, tt) acc[ft][tt] = mfma32(f.w[s][ft], f.x[s][tt], acc[ft][tt])
#define ITER(s, vm, cur, nxt) do { \
    asm volatile("s_waitcnt vmcnt(" #vm ") lgkmcnt(0)" ::: "memory"); \
    lds_add(L + C_MMA, 1u, lane); SB(); \
    MM(cur, 0, 0, 0); MM(cur, 0, 0, 1); MM(cur, 0, 1, 0); SB(); \
    lds_wait(L + C_MMA, 4 * ((s) + 2)); SB(); \
    constexpr int bo_ = (((s) + 1) % NBUF) * 32768; constexpr bool ld_ = (s) + NBUF < 10; \
    MM(cur, 0, 1, 1); RDX(nxt, bo_, 0, 0); RDX(nxt, bo_, 0, 1); SB(); if (ld_) G1LD((s) + NBUF, 0); SB(); \
    MM(cur, 1, 0, 0); RDW(nxt, bo_, 0, 0); SB(); \
    MM(cur, 1, 0, 1); RDW(nxt, bo_, 0, 1); SB(); if (ld_) G1LD((s) + NBUF, 4); SB(); \
    MM(cur, 1, 1, 0); RDX(nxt, bo_, 1, 0); RDX(nxt, bo_, 1, 1); SB(); if (ld_) G1LD((s) + NBUF, 1); SB(); \
    MM(cur, 1, 1, 1); RDW(nxt, bo_, 1, 0); SB(); \
    MM(cur, 2, 0, 0); RDW(nxt, bo_, 1, 1); SB(); if (ld_) G1LD((s) + NBUF, 5); SB(); \
    MM(cur, 2, 0, 1); RDX(nxt, bo_, 2, 0); RDX(nxt, bo_, 2, 1); SB(); if (ld_) G1LD((s) + NBUF, 2); SB(); \
    MM(cur, 2, 1, 0); RDW(nxt, bo_, 2, 0); SB(); \
    MM(cur, 2, 1, 1); RDW(nxt, bo_, 2, 1); SB(); if (ld_) G1LD((s) + NBUF, 6); SB(); \
    MM(cur, 3, 0, 0); RDX(nxt, bo_, 3, 0); RDX(nxt, bo_, 3, 1); SB(); if (ld_) G1LD((s) + NBUF, 3); SB(); \
    MM(cur, 3, 0, 1); RDW(nxt, bo_, 3, 0); RDW(nxt, bo_, 3, 1); SB(); \
    MM(cur, 3, 1, 0); SB(); if (ld_) G1LD((s) + NBUF, 7); SB(); \
    MM(cur, 3, 1, 1); SB(); \
  } while (0)
      WAITV(8);
      lds_add(L + C_MMA, 1u, lane); lds_wait(L + C_MMA, 4);
      STAMP(2);
#pragma unroll
      for (int s = 0; s < 4; ++s) { RDX(fA, 0, s, 0); RDX(fA, 0, s, 1); RDW(fA, 0, s, 0); RDW(fA, 0, s, 1); }
      SB();
#pragma unroll
      for (int i = 0; i < 8; ++i) G1LD(2, i);
      SB();
      ITER(0, 8, fA, fB);
      ITER(1, 8, fB, fA);
      ITER(2, 8, fA, fB);
      STAMP(3);
      ITER(3, 8, fB, fA);
      ITER(4, 8, fA, fB);
      ITER(5, 8, fB, fA);
      STAMP(4);
      ITER(6, 8, fA, fB);
      ITER(7, 8, fB, fA);
      ITER(8, 0, fA, fB);
#pragma unroll
      for (int s = 0; s < 4; ++s) { MM(fB, s, 0, 0); MM(fB, s, 0, 1); MM(fB, s, 1, 0); MM(fB, s, 1, 1); }
      SB();
      lds_add(L + C_MMA, 1u, lane);
      STAMP(5);
      lds_wait_sleep(L + C_S, 1);
      tag = __builtin_amdgcn_readfirstlane(*(LAS unsigned*)(L + C_TAG));
      STAMP(6);
#pragma unroll
      for (int ft = 0; ft < 2; ++ft) {
        const int fg = 2 * fh + ft;
        const int ks = 4 * j + fg;
        f32x4 c4[4];
#pragma unroll
        for (int g = 0; g < 4; ++g) c4[g] = *(LAS f32x4*)(L + XFC + (32 * fg + 8 * g + 4 * hi) * 4);
#pragma unroll
        for (int tt = 0; tt < 2; ++tt) {
          const int tok = 64 * th + 32 * tt + (lane & 31);
          const f32x2 st = *(LAS f32x2*)(L + XFS + tok * 8);
          const f32x16& a = acc[ft][tt];
          const float zs1 = -1.44269504088896f * st.y, dn1 = __builtin_amdgcn_rcpf(st.y);
          const f32x2 zs = f32x2{zs1, zs1}, dn = f32x2{dn1, dn1};
          const int mb2 = 2 * p + th, mt = 2 * tt + ((lane >> 4) & 1);
          const int off = ks < KS2 ? HP_OFF + mb2 * HP_MB + ks * 4096 + mt * 1024 + ((2 * hi) * 16 + (lane & 15)) * 16 : -1024;
#pragma unroll
          for (int hh = 0; hh < 2; ++hh) {
            unsigned pk[4];
#pragma unroll
            for (int e2 = 0; e2 < 4; ++e2) {
              const int ee = hh * 4 + e2;
              const f32x2 cc = f32x2{c4[ee >> 1][(2 * ee) & 3], c4[ee >> 1][(2 * ee + 1) & 3]};
              const f32x2 dd = __builtin_elementwise_fma(cc, f32x2{-st.x, -st.x}, f32x2{a[2 * ee], a[2 * ee + 1]});
              const f32x2 zz = dd * zs;
              const f32x2 ex = f32x2{__builtin_amdgcn_exp2f(zz.x), __builtin_amdgcn_exp2f(zz.y)};
              const f32x2 ss = __builtin_elementwise_fma(ex, dn, dn);
              const f32x2 hv2 = dd * f32x2{__builtin_amdgcn_rcpf(ss.x), __builtin_amdgcn_rcpf(ss.y)};
              pk[e2] = pack2(hv2.x, hv2.y);
            }
            __builtin_amdgcn_raw_buffer_store_b128(v4u{pk[0], pk[1], pk[2], pk[3]}, rws, off + hh * 256, 0, SC1);
            SB();
          }
        }
      }
      lds_add(L + C_MMA, 1u, lane);
      STAMP(7);
      WAITV(0);
      {
        unsigned old = 0;
        if (lane == 0) old = __hip_atomic_fetch_add((LAS unsigned*)(L + C_H), 1u, RLX_WG);
        if (__builtin_amdgcn_readfirstlane(old) == 3) set_flag(ws, FL_H + (p * 16 + j) * FSTR, tag, lane);
      }
      STAMP(8);
    } else {
      const int idx16 = 4 * rg + pi;
      const int srow = 512 * rg + 64 * (4 * hf + pi) + 16 * w + (lane >> 3);
      const int cg = 14 * hf + idx16;
      const bool csv = idx16 < 14 - hf;
      const __amdgpu_buffer_rsrc_t rx = mkrsrc(x, S * D * 2);
      v4u xv[2][10], wv[20];
#pragma unroll
      for (int h2 = 0; h2 < 2; ++h2)
#pragma unroll
        for (int i = 0; i < 10; ++i) xv[h2][i] = __builtin_amdgcn_raw_buffer_load_b128(rx, (srow + 8 * h2) * 1280 + (lane & 7) * 16 + i * 128, 0, 0);
      SB();
      WAITV(20);
      BAR();
      PREP_ITEM(aw, 3); PREP_ITEM(aw, 4);
      WAITL(); lds_add(L + C_AUX, 1u, lane);
      SB();
      const int wvo = csv ? (160 * w + (lane >> 3)) * (F * 2) + cg * 128 + (lane & 7) * 16 : -16;
#pragma unroll
      for (int i = 0; i < 20; ++i) wv[i] = __builtin_amdgcn_raw_buffer_load_b128(rw, wvo, csv ? i * 8 * F * 2 : 0, 0);
      SB();
      tag = __builtin_amdgcn_readfirstlane(*(LAS unsigned*)(L + C_TAG));
#pragma unroll
      for (int h2 = 0; h2 < 2; ++h2) {
        float s = 0.f;
#pragma unroll
        for (int i = 0; i < 10; ++i)
#pragma unroll
          for (int e2 = 0; e2 < 4; ++e2) s = dot2(xv[h2][i][e2], 0x3f803f80u, s);
        s += __shfl_xor(s, 1); s += __shfl_xor(s, 2); s += __shfl_xor(s, 4);
        const float mean = s * (1.f / D);
        float q0 = 0.f, q1 = 0.f;
#pragma unroll
        for (int i = 0; i < 10; ++i)
#pragma unroll
          for (int e2 = 0; e2 < 4; ++e2) { const float a0 = bflo(xv[h2][i][e2]) - mean, a1 = bfhi(xv[h2][i][e2]) - mean; q0 = fmaf(a0, a0, q0); q1 = fmaf(a1, a1, q1); }
        float q = q0 + q1;
        q += __shfl_xor(q, 1); q += __shfl_xor(q, 2); q += __shfl_xor(q, 4);
        const float den = __builtin_sqrtf(rbf(rbf(q * (1.f / D)) + 1e-5f));
        __builtin_amdgcn_raw_buffer_store_b64(v2u{pack2(mean, den), tag}, rws, (lane & 7) == 0 ? ST_OFF + (srow + 8 * h2) * 8 : -16, 0, SC1);
      }
      float c[8] = {};
#pragma unroll
      for (int i = 0; i < 20; ++i)
#pragma unroll
        for (int e2 = 0; e2 < 4; ++e2) { c[2 * e2] += bflo(wv[i][e2]); c[2 * e2 + 1] += bfhi(wv[i][e2]); }
      *(LAS f32x4*)(L + ((w * 8 + (lane >> 3)) * 64 + (lane & 7) * 8) * 4) = f32x4{c[0], c[1], c[2], c[3]};
      *(LAS f32x4*)(L + ((w * 8 + (lane >> 3)) * 64 + (lane & 7) * 8 + 4) * 4) = f32x4{c[4], c[5], c[6], c[7]};
      WAITL();
      unsigned old = 0;
      if (lane == 0) old = __hip_atomic_fetch_add((LAS unsigned*)(L + C_H), 1u, RLX_WG);
      if (__builtin_amdgcn_readfirstlane(old) == 3) {
        asm volatile("" ::: "memory");
        float t4[4] = {};
#pragma unroll
        for (int i = 0; i < 32; ++i) t4[i & 3] += *(LAS float*)(L + (i * 64 + lane) * 4);
        __builtin_amdgcn_raw_buffer_store_b64(v2u{__float_as_uint((t4[0] + t4[1]) + (t4[2] + t4[3])), tag}, rws, csv ? CS_OFF + (cg * 64 + lane) * 8 : -16, 0, SC1);
      }
      lds_add(L + C_MMA, 12u, lane);
      STAMP(5);
      if (b == 56 && w == 0) {
#pragma unroll 1
        for (unsigned sp = 0; sp < SPIN_CAP; ++sp) {
          bool ok = true;
#pragma unroll
          for (int i = 0; i < 4; ++i) ok = ok && flag_load(ws, FL_P + i * 64 * FSTR, 64, lane) == tag;
          if (__all(ok)) break;
          __builtin_amdgcn_s_sleep(32);
        }
        set_flag(ws, FL_GEN, tag, lane);
      }
    }
    unsigned hb_ = 0;
#define HBAR() do { WAITL(); lds_add(L + C_G2, 1u, lane); hb_ += 4; lds_wait(L + C_G2, hb_); } while (0)
    {
      const __amdgpu_buffer_rsrc_t rxx = mkrsrc(x, S * D * 2);
      lds_wait_sleep(L + C_MMA, 48);
#pragma unroll
      for (int i = 0; i < 3; ++i) {
        const int s = aw + 4 * i;
        if (s < 10) {
          const int n = s * 64 + lane; const int row = n / 10, c = n - row * 10;
          GLDS(rxx, L + EX + s * 1024, (64 * mb + row) * 1280 + 160 * nb + c * 16, 0);
        }
      }
      const float dv = bflo(Dv[dpair + (aw >> 1)]), dv3 = bflo(Dv[d3]);
      lds_wait_sleep(L + C_ST, 1);
      __builtin_amdgcn_s_setprio(C8_PRIO);
      LAS char* Lc = L + CV + (aw >> 1) * CH_BYTES;
      const int cts = aw & 1;
      f32x4 acc[4] = {};
      if (cts == 0) conv8<0>(Lc + aoff, Lc + boff, acc); else conv8<1>(Lc + aoff, Lc + boff, acc);
      __builtin_amdgcn_s_setprio(MMA_PRIO);
      STAMP(9);
#pragma unroll
      for (int ti = 0; ti < 4; ++ti) conv_out(Lc, acc[ti], cts == 0 ? (ti < 2 ? ti : ti + 4) : 2 + ti, dpair + (aw >> 1), dv, e, kq, rws, tag);
      f32x4 ac3[4] = {};
      __builtin_amdgcn_s_setprio(C3_PRIO);
      if (ts3 == 0) conv2<0>(L + CV3 + aoff + aw * 128, L + CV3 + boff + aw * 128, ac3); else conv2<1>(L + CV3 + aoff + aw * 128, L + CV3 + boff + aw * 128, ac3);
      __builtin_amdgcn_s_setprio(MMA_PRIO);
      STAMP(13);
#pragma unroll
      for (int ti = 0; ti < 4; ++ti) *(LAS f32x4*)(L + SC3 + (aw * 4 + ti) * 1024 + lane * 16) = ac3[ti];
      WAITV(4);
      HBAR();
      f32x4 a = {};
#pragma unroll
      for (int ww = 0; ww < 4; ++ww) a += *(LAS f32x4*)(L + SC3 + (ww * 4 + aw) * 1024 + lane * 16);
      conv_out(L + CV3, a, ts3 == 0 ? (aw < 2 ? aw : aw + 4) : 2 + aw, d3, dv3, e, kq, rws, tag);
    }
    STAMP(10);
    {
      const int q = (lane & 15) >> 2, pp = lane & 3, pos = lane & 15;
      const int tok = 16 * aw + 2 * (pos >> 2) + (pos & 1) + 8 * ((pos >> 1) & 1);
      lds_wait_sleep(L + C_S, 1);
      const f32x2 st = *(LAS f32x2*)(L + XFS + ((mb & 1) * 64 + tok) * 8);
      v4u xn[5];
#pragma unroll
      for (int i = 0; i < 5; ++i) {
        const v2u xv2 = *(LAS v2u*)(L + EX + tok * 160 + (4 * i + kq) * 8);
        const unsigned d01 = pack2(bflo(xv2.x) - st.x, bfhi(xv2.x) - st.x), d23 = pack2(bflo(xv2.y) - st.x, bfhi(xv2.y) - st.x);
        xn[i] = v4u{pack2(bflo(d01) * st.y, bfhi(d01) * st.y), pack2(bflo(d23) * st.y, bfhi(d23) * st.y), xv2.x, xv2.y};
      }
      WAITL();
      lds_add(L + C_Y, 1u, lane);
      for (int i = 0; i < Y_DELAY; ++i) __builtin_amdgcn_s_sleep(4);
      STAMP(11);
      {
        v4u yv[5];
        const int yo = Y_OFF + (80 * nb + 4 * aw + kq) * 8192 + mb * 256 + e * 16;
#pragma unroll 1
        for (unsigned sp = 0; sp < SPIN_CAP; ++sp) {
          asm volatile("" ::: "memory");
          bool ok = true;
#pragma unroll
          for (int i = 0; i < 5; ++i) yv[i] = __builtin_amdgcn_raw_buffer_load_b128(rws, yo, i * 16 * 8192, SC1);
#pragma unroll
          for (int i = 0; i < 5; ++i) ok = ok && yv[i].y == tag && yv[i].w == tag;
          if (__all(ok)) break;
          __builtin_amdgcn_s_sleep(POLL_SLEEP);
        }
#pragma unroll
        for (int i = 0; i < 5; ++i) *(LAS v2u*)(L + EY + (16 * i + 4 * aw + kq) * 144 + e * 8) = v2u{yv[i].x, yv[i].z};
      }
      HBAR();
      STAMP(12);
#pragma unroll
      for (int i = 0; i < 5; ++i) {
        const int dq = 4 * i + kq;
        const v2u yv = trread(L + EY + (4 * dq + q) * 144 + aw * 32 + pp * 8);
        const unsigned s01 = pack2(bflo(xn[i].x) * bflo(yv.x), bfhi(xn[i].x) * bfhi(yv.x)), s23 = pack2(bflo(xn[i].y) * bflo(yv.y), bfhi(xn[i].y) * bfhi(yv.y));
        *(LAS v4u*)(L + ES + (tok * 20 + dq) * 16) = v4u{s01, s23, xn[i].z, xn[i].w};
      }
    }
  }
  STAMP(14);
  BAR();
  const int lane2 = __builtin_amdgcn_mbcnt_hi(~0u, __builtin_amdgcn_mbcnt_lo(~0u, opaque0()));
#pragma unroll
  for (int i = 0; i < 3; ++i) {
    const int it = w + 8 * i;
    if (it < 20) {
      const int n = it * 64 + lane2;
      const int tok = n / 20, dq = n - tok * 20;
      f32x4 o = *(LAS f32x4*)(L + tok * PSTR + dq * 16);
#pragma unroll
      for (int ww = 1; ww < 4; ++ww) o += *(LAS f32x4*)(L + ww * PART + tok * PSTR + dq * 16);
      const v4u sx = *(LAS v4u*)(L + ES + n * 16);
      const unsigned o01 = pack2(o[0], o[1]), o23 = pack2(o[2], o[3]);
      const unsigned g01 = pack2(silu_f(bflo(o01)), silu_f(bfhi(o01))), g23 = pack2(silu_f(bflo(o23)), silu_f(bfhi(o23)));
      const unsigned m01 = pack2(bflo(sx.x) * bflo(g01), bfhi(sx.x) * bfhi(g01)), m23 = pack2(bflo(sx.y) * bflo(g23), bfhi(sx.y) * bfhi(g23));
      v2u res;
      res.x = pack2(bflo(sx.z) + bflo(m01), bfhi(sx.z) + bfhi(m01));
      res.y = pack2(bflo(sx.w) + bflo(m23), bfhi(sx.w) + bfhi(m23));
      *(v2u*)(out + (size_t)(64 * mb + tok) * D + 80 * nb + dq * 4) = res;
    }
  }
  STAMP(15);
  STAMP_OUT(0);
}

extern "C" void kernel_launch(void* const* d_in, const int* in_sizes, int n_in,
                              void* d_out, int out_size, void* d_ws, size_t ws_size,
                              hipStream_t stream) {
  fused<<<256, 512, 0, stream>>>((const u16*)d_in[0], (const u16*)d_in[1], (const u16*)d_in[2], (const u16*)d_in[3],
                                 (const u16*)d_in[4], (const u16*)d_in[5], (const u16*)d_in[7], (const u16*)d_in[8],
                                 (char*)d_ws, (u16*)d_out);
}
```

```cpp
#include <hip/hip_runtime.h>
#include <stdint.h>

typedef unsigned short u16;
typedef unsigned v4u __attribute__((ext_vector_type(4)));
typedef unsigned v2u __attribute__((ext_vector_type(2)));
typedef float f32x2 __attribute__((ext_vector_type(2)));
typedef float f32x4 __attribute__((ext_vector_type(4)));
typedef float f32x16 __attribute__((ext_vector_type(16)));
typedef short bf16x8 __attribute__((ext_vector_type(8)));
typedef short s16x4 __attribute__((ext_vector_type(4)));
typedef __bf16 bf16x2 __attribute__((ext_vector_type(2)));
#define LAS __attribute__((address_space(3)))
#define GAS __attribute__((address_space(1)))
#define DI __device__ __forceinline__
#define RLX_AGENT __ATOMIC_RELAXED, __HIP_MEMORY_SCOPE_AGENT
#define RLX_WG __ATOMIC_RELAXED, __HIP_MEMORY_SCOPE_WORKGROUP

constexpr int S = 2048, D = 640, F = 1728;
constexpr int KS2 = 54;
constexpr int FSTR = 128;
constexpr int FL_GEN = 0, FL_P = 131072, FL_H = FL_P + 256 * 256;
constexpr int ST_OFF = 32768;
constexpr int CS_OFF = ST_OFF + S * 8;
constexpr int HP_OFF = 262144;
constexpr int HP_MB = KS2 * 4096;
constexpr int W2P_OFF = HP_OFF + 32 * HP_MB;
constexpr int W2P_NB = KS2 * 5120;
constexpr int R_OFF = W2P_OFF + 8 * W2P_NB;
constexpr int Y_OFF = R_OFF + D * S * 2;
constexpr int WS_BYTES = Y_OFF + D * S * 4;
constexpr unsigned RSRC3 = 0x00020000u;
constexpr int SC1 = 16;
constexpr unsigned SPIN_CAP = 1u << 18;

DI float bflo(unsigned u) { return __uint_as_float(u << 16); }
DI float bfhi(unsigned u) { return __uint_as_float(u & 0xffff0000u); }
DI unsigned pack2(float a, float b) {
  bf16x2 r = __builtin_convertvector(f32x2{a, b}, bf16x2);
  return __builtin_bit_cast(unsigned, r);
}
DI float rbf(float a) { return bflo(pack2(a, a)); }
DI float silu_f(float x) { return x * __builtin_amdgcn_rcpf(1.f + __builtin_amdgcn_exp2f(-1.44269504088896f * x)); }
DI f32x4 mfma16(v4u a, v4u b, f32x4 c) {
  return __builtin_amdgcn_mfma_f32_16x16x32_bf16(__builtin_bit_cast(bf16x8, a), __builtin_bit_cast(bf16x8, b), c, 0, 0, 0);
}
DI f32x16 mfma32(v4u a, v4u b, f32x16 c) {
  return __builtin_amdgcn_mfma_f32_32x32x16_bf16(__builtin_bit_cast(bf16x8, a), __builtin_bit_cast(bf16x8, b), c, 0, 0, 0);
}
DI float dot2(unsigned a, unsigned b, float c) {
  return __builtin_amdgcn_fdot2_f32_bf16(__builtin_bit_cast(bf16x2, a), __builtin_bit_cast(bf16x2, b), c, false);
}
DI v2u trread(LAS char* p) { return __builtin_bit_cast(v2u, __builtin_amdgcn_ds_read_tr16_b64_v4i16((LAS s16x4*)p)); }
DI v4u ldsr128(LAS char* p) { return *(LAS v4u*)p; }
DI __amdgpu_buffer_rsrc_t mkrsrc(const void* p, int bytes) { return __builtin_amdgcn_make_buffer_rsrc((void*)p, 0, bytes, RSRC3); }
DI void glds_asm(__amdgpu_buffer_rsrc_t rs, unsigned ldsa, int voff, int soff) {
  asm volatile("s_nop 4\n\ts_mov_b32 m0, %0\n\ts_nop 0\n\tbuffer_load_dwordx4 %1, %2, %3 offen lds"
               :: "s"(ldsa), "v"(voff), "s"(rs), "s"(soff) : "m0", "memory");
}
DI void glds_fast(__amdgpu_buffer_rsrc_t rs, unsigned ldsa, int voff, int soff) {
  asm volatile("s_mov_b32 m0, %0\n\ts_nop 0\n\tbuffer_load_dwordx4 %1, %2, %3 offen lds"
               :: "s"(ldsa), "v"(voff), "s"(rs), "s"(soff) : "m0", "memory");
}
#define GLDS(rs, ldsp, voff, soff) glds_asm(rs, (unsigned)(uintptr_t)(ldsp), voff, soff)
#define WAITV(n) asm volatile("s_waitcnt vmcnt(" #n ")" ::: "memory")
#define WAITL() asm volatile("s_waitcnt lgkmcnt(0)" ::: "memory")
#define BAR() do { WAITL(); __builtin_amdgcn_s_barrier(); asm volatile("" ::: "memory"); } while (0)
#define SB() __builtin_amdgcn_sched_barrier(0)


constexpr int NBUF = 3;
constexpr int CH_HA = 0, CH_HB = 4224, CH_R = 8448, CH_RS = 4512, CH_BYTES = CH_R + 4 * CH_RS;
constexpr int CV = 98304;
constexpr int CV3 = 32768;
constexpr int SC3 = 65536;
constexpr int PK = CV, PV = PK + 11264, PW = PV + 10240, PS = PW + 10240, PO = PS + 1024;
constexpr int EY = CV, EX = 0, ES = EY + 80 * 144;
constexpr int XFS = CV + 2 * CH_BYTES;
constexpr int XFC = XFS + 1024;
constexpr int CNT = XFC + 512;
constexpr int C_AUX = CNT, C_G2 = CNT + 4, C_Y = CNT + 8, C_H = CNT + 12, C_MMA = CNT + 16, C_S = CNT + 20, C_P = CNT + 24,
              C_TAG = CNT + 60, C_ST = CNT + 40, C_HR = CNT + 32;
constexpr int LDS_BYTES = CNT + 64;
static_assert(PO + 10240 <= XFS && ES + 20480 <= XFS && LDS_BYTES <= 163840, "lds");
constexpr int PSTR = 336, PART = 64 * PSTR;

DI void lds_add(LAS char* c, unsigned v, int lane) {
  if (lane == 0) __hip_atomic_fetch_add((LAS unsigned*)c, v, RLX_WG);
}
DI void lds_wait(LAS char* c, unsigned target) {
  while ((unsigned)__builtin_amdgcn_readfirstlane(__hip_atomic_load((LAS unsigned*)c, RLX_WG)) < target) {}
  asm volatile("" ::: "memory");
}
DI void lds_wait_sleep(LAS char* c, unsigned target) {
  while ((unsigned)__builtin_amdgcn_readfirstlane(__hip_atomic_load((LAS unsigned*)c, RLX_WG)) < target) __builtin_amdgcn_s_sleep(1);
  asm volatile("" ::: "memory");
}
DI unsigned flag_load(char* ws, int off, int n, int lane) {
  return __hip_atomic_load((GAS unsigned*)(ws + off + (lane < n ? lane : 0) * FSTR), RLX_AGENT);
}
DI void poll_flags(char* ws, int off, int n, unsigned tag, int lane) {
#pragma unroll 1
  for (unsigned spins = 0; spins < SPIN_CAP; ++spins) {
    const unsigned v = flag_load(ws, off, n, lane);
    if (__all(v == tag)) break;
    __builtin_amdgcn_s_sleep(8);
  }
  __builtin_amdgcn_fence(__ATOMIC_ACQUIRE, "wavefront");
}
DI void set_flag(char* ws, int off, unsigned tag, int lane) {
  if (lane == 0) __hip_atomic_store((GAS unsigned*)(ws + off), tag, RLX_AGENT);
}

DI unsigned opaque0() { unsigned z; asm volatile("v_mov_b32 %0, 0" : "=v"(z)); return z; }
template <int I> struct IC { static constexpr int value = I; };
template <int B, int E, class Fn> DI void static_for(Fn&& f) { if constexpr (B < E) { f(IC<B>{}); static_for<B + 1, E>(f); } }
struct Frags { v4u x[4][2], w[4][2]; };

template <int TS> DI void conv_ld(LAS char* pa, LAS char* pb, v4u (&A)[8], v4u (&B)[8]) {
  constexpr int NA = TS == 0 ? 8 : 6;
#pragma unroll
  for (int a = 0; a < NA; ++a) { A[a] = ldsr128(pa + a * 512); B[a] = ldsr128(pb + (7 - a) * 512); }
}
template <int TS> DI void conv_mm(const v4u (&A)[8], const v4u (&B)[8], f32x4 (&acc)[4]) {
#pragma unroll
  for (int ti = 0; ti < 4; ++ti) {
    const int n = TS == 0 ? (ti == 0 ? 0 : ti == 1 ? 1 : ti == 2 ? 6 : 7) : 2 + ti;
#pragma unroll
    for (int a = 0; a <= n; ++a) acc[ti] = mfma16(A[a], B[n - a], acc[ti]);
  }
}
DI void conv_sched() {
#pragma unroll
  for (int i = 0; i < 16; ++i) { __builtin_amdgcn_sched_group_barrier(0x8, 1, 0); __builtin_amdgcn_sched_group_barrier(0x100, 1, 0); }
  __builtin_amdgcn_sched_group_barrier(0x8, 2, 0);
}
template <int TS> DI void conv8(LAS char* pa, LAS char* pb, f32x4 (&acc)[4]) {
  v4u A0[8], B0[8], A1[8], B1[8];
  conv_ld<TS>(pa, pb, A0, B0);
  SB();
#pragma unroll 1
  for (int r = 0; r < 8; r += 2) {
    conv_ld<TS>(pa + 64, pb + 64, A1, B1); conv_mm<TS>(A0, B0, acc); conv_sched(); SB();
    pa += 128; pb += 128;
    conv_ld<TS>(pa, pb, A0, B0); conv_mm<TS>(A1, B1, acc); conv_sched(); SB();
  }
}
template <int TS> DI void conv2(LAS char* pa, LAS char* pb, f32x4 (&acc)[4]) {
#pragma unroll 1
  for (int r = 0; r < 2; ++r) {
    v4u A[8], B[8];
    conv_ld<TS>(pa, pb, A, B); conv_mm<TS>(A, B, acc);
    pa += 64; pb += 64;
  }
}
DI unsigned kshift2(float k1, float k2, float k3, float k4, float k5, float c0, float c1, float c2, float c3) {
  unsigned a = pack2(k1 * c3, k2 * c3);
  unsigned t = pack2(k2 * c2, k3 * c2);
  a = pack2(bflo(a) + bflo(t), bfhi(a) + bfhi(t));
  t = pack2(k3 * c1, k4 * c1);
  a = pack2(bflo(a) + bflo(t), bfhi(a) + bfhi(t));
  t = pack2(k4 * c0, k5 * c0);
  return pack2(bflo(a) + bflo(t), bfhi(a) + bfhi(t));
}
DI void stage_h(LAS char* Lc, v4u h, int g) {
  *(LAS v4u*)(Lc + CH_HB + 112 + g * 16) = h;
  LAS char* a = Lc + CH_HA + 114 + g * 16;
  *(LAS u16*)(a) = (u16)(h.x & 0xffff);
  *(LAS unsigned*)(a + 2) = __builtin_amdgcn_alignbit(h.y, h.x, 16);
  *(LAS v2u*)(a + 6) = v2u{__builtin_amdgcn_alignbit(h.z, h.y, 16), __builtin_amdgcn_alignbit(h.w, h.z, 16)};
  *(LAS u16*)(a + 14) = (u16)(h.w >> 16);
}
DI void stage_r(LAS char* Lc, v4u u, int g) {
  LAS char* rb = Lc + CH_R + g * 16;
  *(LAS v4u*)(rb) = u;
  { LAS char* q = rb + CH_RS - 4; *(LAS unsigned*)(q) = u.x; *(LAS v2u*)(q + 4) = v2u{u.y, u.z}; *(LAS unsigned*)(q + 12) = u.w; }
  { LAS char* q = rb + 2 * CH_RS - 8; *(LAS v2u*)(q) = v2u{u.x, u.y}; *(LAS v2u*)(q + 8) = v2u{u.z, u.w}; }
  { LAS char* q = rb + 3 * CH_RS - 12; *(LAS unsigned*)(q) = u.x; *(LAS v2u*)(q + 4) = v2u{u.y, u.z}; *(LAS unsigned*)(q + 12) = u.w; }
}
DI void conv_out(LAS char* Lc, f32x4 a, int n, int d, float dv, int e, int kq, __amdgpu_buffer_rsrc_t rws, unsigned tag) {
  const int t = 256 * n + 64 * (e >> 2) + 2 * (e & 3) + 16 * kq;
  const unsigned ua = *(LAS unsigned*)(Lc + CH_R + 2 * (2046 - t));
  const unsigned ub = *(LAS unsigned*)(Lc + CH_R + 2 * (2038 - t));
  const unsigned c01 = pack2(a[0], a[1]), c23 = pack2(a[2], a[3]);
  const unsigned d01 = pack2(bfhi(ua) * dv, bflo(ua) * dv), d23 = pack2(bfhi(ub) * dv, bflo(ub) * dv);
  v2u o;
  o.x = pack2(bflo(c01) + bflo(d01), bfhi(c01) + bfhi(d01));
  o.y = pack2(bflo(c23) + bflo(d23), bfhi(c23) + bfhi(d23));
  const int off = Y_OFF + d * 8192 + (256 * n + 64 * (e >> 2) + 16 * kq + 4 * (e & 3)) * 4;
  __builtin_amdgcn_raw_buffer_store_b128(v4u{o.x, tag, o.y, tag}, rws, off, 0, SC1);
}

__global__ __launch_bounds__(512, 2) void fused(const u16* x, const u16* __restrict__ kk, const u16* __restrict__ vv,
                                                const u16* __restrict__ sk, const u16* __restrict__ hk, const u16* __restrict__ Dv,
                                                const u16* __restrict__ w1, const u16* __restrict__ w2,
                                                char* ws, u16* out) {
  __shared__ __attribute__((aligned(16))) char smem[LDS_BYTES];
  LAS char* L = (LAS char*)smem;
  if (__builtin_amdgcn_readfirstlane(threadIdx.x >> 6) == 7) {
    unsigned long long pc;
    asm volatile("s_getpc_b64 %0" : "=s"(pc));
    const unsigned co = min(((blockIdx.x >> 3) & 7) * 8192u + (threadIdx.x & 63) * 128u, 39000u);
    asm volatile("s_mov_b32 m0, %0\n\ts_nop 0\n\tglobal_load_lds_dword %1, %2" :: "s"((unsigned)(uintptr_t)(L + XFS)), "v"(co), "s"(pc) : "m0");
  }
  const int b = blockIdx.x, xcd = b & 7, r = b >> 3;
  const int rg = xcd >> 1, hf = xcd & 1, pi = r >> 3, jj = r & 7;
  const int p = 4 * rg + pi, j = 7 * hf + jj;
  const bool g1 = jj < 7;
  const int mbl = 2 * pi + ((r >> 2) & 1), nbl = r & 3;
  const int mb = 8 * rg + mbl, nb = 4 * hf + nbl;
  const int lane = threadIdx.x & 63;
  const int w = __builtin_amdgcn_readfirstlane(threadIdx.x >> 6);
  const int hi = lane >> 5, aw = w & 3;
  const __amdgpu_buffer_rsrc_t rws = mkrsrc(ws, WS_BYTES);
  if (threadIdx.x < 15) *(LAS unsigned*)(L + CNT + threadIdx.x * 4) = 0u;
  const int dpair = 80 * nb + 2 * mb, d3 = 80 * nb + 64 + (mb >> 1), ts3 = mb & 1;
  const int e = lane & 15, kq = lane >> 4;
  const int aoff = ((e & 1) ? CH_HB : CH_HA) + (kq + (e >> 1)) * 16;
  const int boff = CH_R + (3 - (e & 3)) * CH_RS + (248 - 32 * 7 - 8 * (e >> 2) + kq) * 16;

  {
    const __amdgpu_buffer_rsrc_t rk = mkrsrc(kk, S * D * 2), rv_ = mkrsrc(vv, S * D * 2), rsk = mkrsrc(sk, D * 8);
    const int sb = 64 * mb * 1280 + 160 * nb;
    int vo[2];
#pragma unroll
    for (int i = 0; i < 2; ++i) { const int n = (8 * i + w) * 64 + lane; const int row = n / 10; vo[i] = row * 1280 + (n - row * 10) * 16; }
    GLDS(rk, L + PK + w * 1024, vo[0] + sb - 5120, 0);
    GLDS(rv_, L + PV + w * 1024, vo[0], sb);
    {
      const int n = (5 + w) * 64 + lane; const int row = n / 10; const int vo3 = row * 1280 + (n - row * 10) * 16;
      if (w < 3) GLDS(rk, L + PK + (8 + w) * 1024, vo[1] + sb - 5120, 0);
      else if (w < 5) GLDS(rv_, L + PV + (5 + w) * 1024, vo3, sb);
      else if (w == 5) GLDS(rsk, L + PS, lane * 16, nb * 640);
    }
  }
#define PREP_ITEM(tg, cg) do { \
        const int q_ = (lane & 15) >> 2, pp_ = lane & 3, tl = 16 * (tg) + 4 * kq; \
        LAS char* ka_ = L + PK + (tl + q_) * 160 + (cg) * 32 + pp_ * 8; \
        v2u k0 = trread(ka_), k1 = trread(ka_ + 4 * 160); \
        v2u v0 = trread(ka_ + (PV - PK)); \
        v2u sw = *(LAS v2u*)(L + PS + ((cg) * 16 + e) * 8); \
        float kf[8] = {bflo(k0.x), bfhi(k0.x), bflo(k0.y), bfhi(k0.y), bflo(k1.x), bfhi(k1.x), bflo(k1.y), bfhi(k1.y)}; \
        float vf[4] = {bflo(v0.x), bfhi(v0.x), bflo(v0.y), bfhi(v0.y)}; \
        const float c0 = bflo(sw.x), c1 = bfhi(sw.x), c2 = bflo(sw.y), c3 = bfhi(sw.y); \
        float u[4]; \
        _Pragma("unroll") for (int e2 = 0; e2 < 4; e2 += 2) { \
          const unsigned a = kshift2(kf[e2 + 1], kf[e2 + 2], kf[e2 + 3], kf[e2 + 4], kf[e2 + 5], c0, c1, c2, c3); \
          u[e2] = bflo(a) * vf[e2]; u[e2 + 1] = bfhi(a) * vf[e2 + 1]; } \
        v2u o = v2u{pack2(u[3], u[2]), pack2(u[1], u[0])}; \
        const int ch = (cg) * 16 + e; \
        const int pos8 = (60 - tl) >> 2; \
        *(LAS v2u*)(L + PO + ch * 128 + ((((pos8 >> 1) ^ (ch & 7)) << 4) | ((pos8 & 1) << 3))) = o; \
      } while (0)

  if (w >= 4) {
    const unsigned gen = __hip_atomic_load((GAS unsigned*)(ws + FL_GEN), RLX_AGENT);
    WAITV(0);
    const unsigned tag = (unsigned)__builtin_amdgcn_readfirstlane(gen) * 0x9E3779B1u + 0x85EBCA6Bu;
    if (aw == 0 && lane == 0) *(LAS unsigned*)(L + C_TAG) = tag;
#define AUXBAR() do { WAITL(); lds_add(L + C_AUX, 1u, lane); ab += 4; lds_wait(L + C_AUX, ab); } while (0)
    BAR();
    unsigned ab = 4;
    {
      const __amdgpu_buffer_rsrc_t rw2 = mkrsrc(w2, F * D * 2);
#pragma unroll
      for (int i = 0; i < 3; ++i) if (4 * i + aw < 10) {
        const int n = (4 * i + aw) * 64 + lane; const int row = n / 10;
        GLDS(rw2, L + PW + (4 * i + aw) * 1024, (row + (row >= 32 ? 992 : 0)) * 1280 + (n - row * 10) * 16, 32 * mb * 1280 + 160 * nb);
      }
    }
    {
      const int g = lane >> 4, q = (lane & 15) >> 2, pp = lane & 3;
      PREP_ITEM(aw, 0); PREP_ITEM(aw, 1); PREP_ITEM(aw, 2);
      AUXBAR();
#pragma unroll
      for (int i = 0; i < 3; ++i) {
        const int ri = aw + 4 * i;
        if (ri < 10) {
          const int ch = 8 * ri + (lane >> 3), cc = lane & 7;
          v4u val = ldsr128(L + PO + ch * 128 + ((cc ^ (ch & 7)) << 4));
          __builtin_amdgcn_raw_buffer_store_b128(val, rws, R_OFF + (80 * nb + ch) * 4096 + (31 - mb) * 128 + cc * 16, 0, SC1);
        }
      }
      __builtin_amdgcn_s_setprio(0);
      if (aw < 2) WAITV(3); else WAITV(2);
      AUXBAR();
#pragma unroll
      for (int i = 0; i < 3; ++i) {
        const int fi = aw + 4 * i;
        if (fi < 10) {
          const int half = fi / 5, nt = fi - half * 5, ks = mb + 32 * half;
          const int r0 = 16 * (g & 1) + 4 * (g >> 1);
          LAS char* a = L + PW + half * 5120 + (r0 + q) * 160 + nt * 32 + pp * 8;
          v2u lo = trread(a), hi2 = trread(a + 8 * 160);
          __builtin_amdgcn_raw_buffer_store_b128(v4u{lo.x, lo.y, hi2.x, hi2.y}, rws, ks < KS2 ? W2P_OFF + nb * W2P_NB + ks * 5120 + nt * 1024 + lane * 16 : -16, 0, SC1);
        }
      }
    }
    WAITV(0);
    AUXBAR();
    if (aw == 0) set_flag(ws, FL_P + (nb * 32 + mb) * FSTR, tag, lane);
    const __amdgpu_buffer_rsrc_t rh = mkrsrc(hk, D * S * 2);
    v4u cu[3];
    cu[0] = __builtin_amdgcn_raw_buffer_load_b128(rh, dpair * 4096 + aw * 1024 + lane * 16, 0, 0);
    cu[1] = __builtin_amdgcn_raw_buffer_load_b128(rh, dpair * 4096 + 4096 + aw * 1024 + lane * 16, 0, 0);
    cu[2] = __builtin_amdgcn_raw_buffer_load_b128(rh, d3 * 4096 + aw * 1024 + lane * 16, 0, 0);
    const int g = aw * 64 + lane;
    lds_wait_sleep(L + C_MMA, 32);
    if (aw == 1) {
      const int jc = g1 ? j : 0;
      v2u gr[4];
#define GRL(off) __builtin_bit_cast(v2u, __hip_atomic_load((GAS unsigned long long*)(ws + (off)), RLX_AGENT))
#pragma unroll 1
      for (unsigned sp = 0; sp < SPIN_CAP; ++sp) {
        gr[0] = GRL(ST_OFF + (p * 128 + lane) * 8);
        gr[1] = GRL(ST_OFF + (p * 128 + 64 + lane) * 8);
        gr[2] = GRL(CS_OFF + (jc * 128 + lane) * 8);
        gr[3] = GRL(CS_OFF + (jc * 128 + (j == 13 ? 0 : 64) + lane) * 8);
        if (__all(gr[0].y == tag && gr[1].y == tag && gr[2].y == tag && gr[3].y == tag)) break;
        __builtin_amdgcn_s_sleep(8);
      }
      *(LAS f32x2*)(L + XFS + lane * 8) = f32x2{bflo(gr[0].x), 1.f / bfhi(gr[0].x)};
      *(LAS f32x2*)(L + XFS + (64 + lane) * 8) = f32x2{bflo(gr[1].x), 1.f / bfhi(gr[1].x)};
      *(LAS unsigned*)(L + XFC + lane * 4) = gr[2].x;
      *(LAS unsigned*)(L + XFC + (64 + lane) * 4) = gr[3].x;
      WAITL();
      lds_add(L + C_S, 1u, lane);
    }
    if (aw == 2) { poll_flags(ws, FL_P + nb * 32 * FSTR, 32, tag, lane); lds_add(L + C_P, 1u, lane); }
    lds_wait_sleep(L + C_P, 1);
    v4u rv[3];
    rv[0] = __builtin_amdgcn_raw_buffer_load_b128(rws, R_OFF + dpair * 4096 + aw * 1024 + lane * 16, 0, SC1);
    rv[1] = __builtin_amdgcn_raw_buffer_load_b128(rws, R_OFF + dpair * 4096 + 4096 + aw * 1024 + lane * 16, 0, SC1);
    rv[2] = __builtin_amdgcn_raw_buffer_load_b128(rws, R_OFF + d3 * 4096 + aw * 1024 + lane * 16, 0, SC1);
    const __amdgpu_buffer_rsrc_t ra = mkrsrc(ws + HP_OFF + mb * HP_MB, HP_MB);
    const __amdgpu_buffer_rsrc_t rb = mkrsrc(ws + W2P_OFF + nb * W2P_NB, W2P_NB);
    const int va = lane * 16 + aw * 4096, vb = lane * 16 + aw * 5120;
    constexpr int WB = 6, NKS = 14, POOL = 41;
    v4u fa[NKS][4], fb[NKS][5];
#define LOADB(i) do { const int so_ = (i) * 20480; \
    _Pragma("unroll") for (int n = 0; n < 5; ++n) fb[i][n] = __builtin_amdgcn_raw_buffer_load_b128(rb, vb + (n & 3) * 1024, so_ + (n >> 2) * 4096, SC1); } while (0)
#pragma unroll
    for (int i = 0; i < WB; ++i) LOADB(i);
    SB();
    lds_wait_sleep(L + C_MMA, 44);
    if (aw == 0 && lane < 32) *(LAS v4u*)(L + CV + (lane >> 4) * CH_BYTES + ((lane >> 3) & 1) * CH_HB + (lane & 7) * 16) = v4u{0, 0, 0, 0};
    if (aw == 3) {
#pragma unroll
      for (int i = 0; i < 4; ++i) {
        const int n = lane + 64 * i;
        if (n < 200) { const int cp = n / 25, sl = n - cp * 25; *(LAS v4u*)(L + CV + (cp >> 2) * CH_BYTES + CH_R + (cp & 3) * CH_RS + 4080 + sl * 16) = v4u{0, 0, 0, 0}; }
      }
    }
    stage_h(L + CV, cu[0], g);
    stage_h(L + CV + CH_BYTES, cu[1], g);
    stage_r(L + CV, rv[0], g);
    stage_r(L + CV + CH_BYTES, rv[1], g);
    if (aw == 0 && lane < 16) *(LAS v4u*)(L + CV3 + (lane >> 3) * CH_HB + (lane & 7) * 16) = v4u{0, 0, 0, 0};
    if (aw == 3) {
#pragma unroll
      for (int i = 0; i < 2; ++i) {
        const int n = lane + 64 * i;
        if (n < 100) { const int cp = n / 25, sl = n - cp * 25; *(LAS v4u*)(L + CV3 + CH_R + cp * CH_RS + 4080 + sl * 16) = v4u{0, 0, 0, 0}; }
      }
    }
    stage_h(L + CV3, cu[2], g);
    stage_r(L + CV3, rv[2], g);
    AUXBAR();
    if (aw == 0) lds_add(L + C_ST, 1u, lane);
    lds_wait_sleep(L + C_H, 4);
    if (aw == 1) { poll_flags(ws, FL_H + p * 16 * FSTR, 14, tag, lane); lds_add(L + C_HR, 1u, lane); }
    __builtin_amdgcn_s_setprio(0);
    lds_wait_sleep(L + C_HR, 1);
#define ISSUE(q) do { constexpr int q_ = (q); \
      if constexpr (q_ < 4 * WB) { fa[q_ / 4][q_ % 4] = __builtin_amdgcn_raw_buffer_load_b128(ra, va + (q_ % 4) * 1024, (q_ / 4) * 16384, SC1); } \
      else if constexpr (q_ < 4 * WB + 9 * (NKS - WB)) { constexpr int k_ = WB + (q_ - 4 * WB) / 9, f_ = (q_ - 4 * WB) % 9; \
        if constexpr (f_ < 4) fa[k_][f_] = __builtin_amdgcn_raw_buffer_load_b128(ra, va + f_ * 1024, k_ * 16384, SC1); \
        else fb[k_][f_ - 4] = __builtin_amdgcn_raw_buffer_load_b128(rb, vb + ((f_ - 4) & 3) * 1024, k_ * 20480 + ((f_ - 4) >> 2) * 4096, SC1); } \
    } while (0)
    constexpr auto need = [](int k) { k = k > NKS ? NKS : k; return k <= WB ? 4 * k : 4 * WB + 9 * (k - WB); };
    constexpr auto cum = [need](int i) { const int a = need(i + 5), c = POOL - 5 * WB + (i > 0 ? 9 * i - 6 : 0); return a < c ? a : c; };
    static_for<0, cum(0)>([&](auto q) { ISSUE(decltype(q)::value); });
    SB();
#define PIN_ACC() do { _Pragma("unroll") for (int m_ = 0; m_ < 4; ++m_) _Pragma("unroll") for (int n_ = 0; n_ < 5; ++n_) asm volatile("" : "+v"(acc2[m_][n_])); } while (0)
    f32x4 acc2[4][5] = {};
    LAS char* pw = L;
    static_for<0, NKS>([&](auto ii) {
      constexpr int i = decltype(ii)::value;
      constexpr int c0 = cum(i), cn = cum(i + 1) - cum(i);
      static_assert(cn <= 9 && c0 >= need(i + 1), "schedule");
      static_for<0, 20>([&](auto mm) {
        constexpr int mn = decltype(mm)::value, m = mn / 5, n = mn % 5;
        acc2[m][n] = mfma16(fb[i][n], fa[i][m], acc2[m][n]);
        constexpr int slot = (mn & 1) ? (mn >> 1) - (9 - cn) : -1;
        if constexpr (slot >= 0 && mn < 19) { SB(); ISSUE(c0 + slot); SB(); }
        if constexpr (i == NKS - 1 && mn >= 2) { constexpr int m2 = (mn - 2) / 5, n2 = (mn - 2) % 5; SB(); *(LAS f32x4*)(pw + 16 * m2 * PSTR + n2 * 64) = acc2[m2][n2]; SB(); }
      });
      SB();
      if constexpr (i == NKS - 2) {
        PIN_ACC();
        lds_wait(L + C_Y, 4);
        const int ln = __builtin_amdgcn_mbcnt_hi(~0u, __builtin_amdgcn_mbcnt_lo(~0u, opaque0()));
        pw = L + aw * PART + (ln & 15) * PSTR + (ln >> 4) * 16;
        SB();
      }
    });
    *(LAS f32x4*)(pw + 48 * PSTR + 3 * 64) = acc2[3][3];
    *(LAS f32x4*)(pw + 48 * PSTR + 4 * 64) = acc2[3][4];
  } else {
    __builtin_amdgcn_s_setprio(2);
    const __amdgpu_buffer_rsrc_t rw = mkrsrc(w1, D * F * 2);
    unsigned tag;
    if (g1) {
      const __amdgpu_buffer_rsrc_t rx = mkrsrc(x + (size_t)p * 128 * D, 128 * D * 2);
      int vx[2], vw[4];
      {
        const int row = 32 * w + (lane >> 3);
        vx[0] = row * 1280 + (((lane & 7) ^ ((row >> 1) & 7)) << 4);
        vx[1] = (row + 8) * 1280 + (((lane & 7) ^ (((row + 8) >> 1) & 7)) << 4);
#pragma unroll
        for (int ii = 0; ii < 4; ++ii) {
          const int kr = 16 * w + 4 * ii + (lane >> 4);
          vw[ii] = kr * (F * 2) + j * 256 + (((lane & 15) ^ (((kr & 3) << 2) | ((kr >> 2) & 3))) << 4);
        }
      }
      const int rot = (5 * ((pi + jj) & 1) + (5 * xcd) / 8) % 10;
      const unsigned lbase = (unsigned)(uintptr_t)L + w * 4096;
#define KC(s) ((s) + rot >= 10 ? (s) + rot - 10 : (s) + rot)
#define G1LD(s, i) do { \
    if ((i) < 4) glds_fast(rx, lbase + ((s) % NBUF) * 32768 + (i) * 1024, vx[(i) & 1], KC(s) * 128 + ((i) >> 1) * 16 * 1280); \
    else glds_fast(rw, lbase + ((s) % NBUF) * 32768 + 16384 + ((i) - 4) * 1024, vw[((i) - 4) & 3], KC(s) * (64 * F * 2)); \
  } while (0)
#pragma unroll
      for (int i = 0; i < 8; ++i) G1LD(0, i);
#pragma unroll
      for (int i = 0; i < 8; ++i) G1LD(1, i);
      {
        v4u da = v4u{0x3f9d3e41u + lane * 0x01030507u, 0xbe773f11u ^ (lane * 0x00c100b3u), 0x3e2bbf53u + lane * 0x00050301u, 0x3fc1be99u ^ (lane * 0x01110171u)};
        da &= 0xbfffbfffu;
        f32x16 dz = {};
#pragma unroll 1
        for (int i = 0; i < 12; ++i) { dz = mfma32(da, da, dz); da.x ^= 0x00550033u; dz = mfma32(da, da, dz); da.y ^= 0x00330055u; }
        asm volatile("" :: "v"(dz));
      }
      WAITV(16);
      BAR();
      PREP_ITEM(aw, 3); PREP_ITEM(aw, 4);
      WAITL(); lds_add(L + C_AUX, 1u, lane);
      SB();
      const int fh = w & 1, th = w >> 1;
      int xa[4], wa[2][2];
      {
        const int row = 64 * th + (lane & 31);
        const int c0 = hi ^ ((row >> 1) & 7);
        const int blk = (lane >> 4) & 1, q = (lane & 15) >> 2, pp = lane & 3;
#pragma unroll
        for (int s = 0; s < 4; ++s) xa[s] = row * 128 + (((2 * s) ^ c0) << 4);
#pragma unroll
        for (int ft = 0; ft < 2; ++ft)
#pragma unroll
          for (int t = 0; t < 2; ++t) {
            const int kr = 8 * hi + 4 * t + q;
            const int ch = 4 * (2 * fh + ft) + 2 * blk + (pp >> 1);
            wa[ft][t] = 16384 + 256 * kr + ((ch ^ ((q << 2) | ((2 * hi + t) & 3))) << 4) + 8 * (pp & 1);
          }
      }
      f32x16 acc[2][2] = {};
      Frags fA, fB;
#define RDX(f, bo, s, tt) f.x[s][tt] = ldsr128(L + (bo) + xa[s] + (tt) * 4096)
#define RDW(f, bo, s, ft) do { v2u wl_ = trread(L + (bo) + wa[ft][0] + (s) * 4096), wh_ = trread(L + (bo) + wa[ft][1] + (s) * 4096); f.w[s][ft] = v4u{wl_.x, wl_.y, wh_.x, wh_.y}; } while (0)
#define MM(f, s, ft, tt) acc[ft][tt] = mfma32(f.w[s][ft], f.x[s][tt], acc[ft][tt])
#define ITER(s, vm, cur, nxt) do { \
    asm volatile("s_waitcnt vmcnt(" #vm ") lgkmcnt(0)" ::: "memory"); \
    lds_add(L + C_MMA, 1u, lane); SB(); \
    MM(cur, 0, 0, 0); MM(cur, 0, 0, 1); MM(cur, 0, 1, 0); SB(); \
    lds_wait(L + C_MMA, 4 * ((s) + 2)); SB(); \
    constexpr int bo_ = (((s) + 1) % NBUF) * 32768; constexpr bool ld_ = (s) + NBUF < 10; \
    MM(cur, 0, 1, 1); RDX(nxt, bo_, 0, 0); RDX(nxt, bo_, 0, 1); SB(); if (ld_) G1LD((s) + NBUF, 0); SB(); \
    MM(cur, 1, 0, 0); RDW(nxt, bo_, 0, 0); SB(); \
    MM(cur, 1, 0, 1); RDW(nxt, bo_, 0, 1); SB(); if (ld_) G1LD((s) + NBUF, 4); SB(); \
    MM(cur, 1, 1, 0); RDX(nxt, bo_, 1, 0); RDX(nxt, bo_, 1, 1); SB(); if (ld_) G1LD((s) + NBUF, 1); SB(); \
    MM(cur, 1, 1, 1); RDW(nxt, bo_, 1, 0); SB(); \
    MM(cur, 2, 0, 0); RDW(nxt, bo_, 1, 1); SB(); if (ld_) G1LD((s) + NBUF, 5); SB(); \
    MM(cur, 2, 0, 1); RDX(nxt, bo_, 2, 0); RDX(nxt, bo_, 2, 1); SB(); if (ld_) G1LD((s) + NBUF, 2); SB(); \
    MM(cur, 2, 1, 0); RDW(nxt, bo_, 2, 0); SB(); \
    MM(cur, 2, 1, 1); RDW(nxt, bo_, 2, 1); SB(); if (ld_) G1LD((s) + NBUF, 6); SB(); \
    MM(cur, 3, 0, 0); RDX(nxt, bo_, 3, 0); RDX(nxt, bo_, 3, 1); SB(); if (ld_) G1LD((s) + NBUF, 3); SB(); \
    MM(cur, 3, 0, 1); RDW(nxt, bo_, 3, 0); RDW(nxt, bo_, 3, 1); SB(); \
    MM(cur, 3, 1, 0); SB(); if (ld_) G1LD((s) + NBUF, 7); SB(); \
    MM(cur, 3, 1, 1); SB(); \
  } while (0)
      WAITV(8);
      lds_add(L + C_MMA, 1u, lane); lds_wait(L + C_MMA, 4);
#pragma unroll
      for (int s = 0; s < 4; ++s) { RDX(fA, 0, s, 0); RDX(fA, 0, s, 1); RDW(fA, 0, s, 0); RDW(fA, 0, s, 1); }
      SB();
#pragma unroll
      for (int i = 0; i < 8; ++i) G1LD(2, i);
      SB();
      ITER(0, 8, fA, fB);
      ITER(1, 8, fB, fA);
      ITER(2, 8, fA, fB);
      ITER(3, 8, fB, fA);
      ITER(4, 8, fA, fB);
      ITER(5, 8, fB, fA);
      ITER(6, 8, fA, fB);
      ITER(7, 8, fB, fA);
      ITER(8, 0, fA, fB);
#pragma unroll
      for (int s = 0; s < 4; ++s) { MM(fB, s, 0, 0); MM(fB, s, 0, 1); MM(fB, s, 1, 0); MM(fB, s, 1, 1); }
      SB();
      lds_add(L + C_MMA, 1u, lane);
      lds_wait_sleep(L + C_S, 1);
      tag = __builtin_amdgcn_readfirstlane(*(LAS unsigned*)(L + C_TAG));
#pragma unroll
      for (int ft = 0; ft < 2; ++ft) {
        const int fg = 2 * fh + ft;
        const int ks = 4 * j + fg;
        f32x4 c4[4];
#pragma unroll
        for (int g = 0; g < 4; ++g) c4[g] = *(LAS f32x4*)(L + XFC + (32 * fg + 8 * g + 4 * hi) * 4);
#pragma unroll
        for (int tt = 0; tt < 2; ++tt) {
          const int tok = 64 * th + 32 * tt + (lane & 31);
          const f32x2 st = *(LAS f32x2*)(L + XFS + tok * 8);
          const f32x16& a = acc[ft][tt];
          const float zs1 = -1.44269504088896f * st.y, dn1 = __builtin_amdgcn_rcpf(st.y);
          const f32x2 zs = f32x2{zs1, zs1}, dn = f32x2{dn1, dn1};
          const int mb2 = 2 * p + th, mt = 2 * tt + ((lane >> 4) & 1);
          const int off = ks < KS2 ? HP_OFF + mb2 * HP_MB + ks * 4096 + mt * 1024 + ((2 * hi) * 16 + (lane & 15)) * 16 : -1024;
#pragma unroll
          for (int hh = 0; hh < 2; ++hh) {
            unsigned pk[4];
#pragma unroll
            for (int e2 = 0; e2 < 4; ++e2) {
              const int ee = hh * 4 + e2;
              const f32x2 cc = f32x2{c4[ee >> 1][(2 * ee) & 3], c4[ee >> 1][(2 * ee + 1) & 3]};
              const f32x2 dd = __builtin_elementwise_fma(cc, f32x2{-st.x, -st.x}, f32x2{a[2 * ee], a[2 * ee + 1]});
              const f32x2 zz = dd * zs;
              const f32x2 ex = f32x2{__builtin_amdgcn_exp2f(zz.x), __builtin_amdgcn_exp2f(zz.y)};
              const f32x2 ss = __builtin_elementwise_fma(ex, dn, dn);
              const f32x2 hv2 = dd * f32x2{__builtin_amdgcn_rcpf(ss.x), __builtin_amdgcn_rcpf(ss.y)};
              pk[e2] = pack2(hv2.x, hv2.y);
            }
            __builtin_amdgcn_raw_buffer_store_b128(v4u{pk[0], pk[1], pk[2], pk[3]}, rws, off + hh * 256, 0, SC1);
            SB();
          }
        }
      }
      lds_add(L + C_MMA, 1u, lane);
      WAITV(0);
      {
        unsigned old = 0;
        if (lane == 0) old = __hip_atomic_fetch_add((LAS unsigned*)(L + C_H), 1u, RLX_WG);
        if (__builtin_amdgcn_readfirstlane(old) == 3) set_flag(ws, FL_H + (p * 16 + j) * FSTR, tag, lane);
      }
    } else {
      const int idx16 = 4 * rg + pi;
      const int srow = 512 * rg + 64 * (4 * hf + pi) + 16 * w + (lane >> 3);
      const int cg = 14 * hf + idx16;
      const bool csv = idx16 < 14 - hf;
      const __amdgpu_buffer_rsrc_t rx = mkrsrc(x, S * D * 2);
      v4u xv[2][10], wv[20];
#pragma unroll
      for (int h2 = 0; h2 < 2; ++h2)
#pragma unroll
        for (int i = 0; i < 10; ++i) xv[h2][i] = __builtin_amdgcn_raw_buffer_load_b128(rx, (srow + 8 * h2) * 1280 + (lane & 7) * 16 + i * 128, 0, 0);
      SB();
      WAITV(20);
      BAR();
      PREP_ITEM(aw, 3); PREP_ITEM(aw, 4);
      WAITL(); lds_add(L + C_AUX, 1u, lane);
      SB();
      const int wvo = csv ? (160 * w + (lane >> 3)) * (F * 2) + cg * 128 + (lane & 7) * 16 : -16;
#pragma unroll
      for (int i = 0; i < 20; ++i) wv[i] = __builtin_amdgcn_raw_buffer_load_b128(rw, wvo, csv ? i * 8 * F * 2 : 0, 0);
      SB();
      tag = __builtin_amdgcn_readfirstlane(*(LAS unsigned*)(L + C_TAG));
#pragma unroll
      for (int h2 = 0; h2 < 2; ++h2) {
        float s = 0.f;
#pragma unroll
        for (int i = 0; i < 10; ++i)
#pragma unroll
          for (int e2 = 0; e2 < 4; ++e2) s = dot2(xv[h2][i][e2], 0x3f803f80u, s);
        s += __shfl_xor(s, 1); s += __shfl_xor(s, 2); s += __shfl_xor(s, 4);
        const float mean = s * (1.f / D);
        float q0 = 0.f, q1 = 0.f;
#pragma unroll
        for (int i = 0; i < 10; ++i)
#pragma unroll
          for (int e2 = 0; e2 < 4; ++e2) { const float a0 = bflo(xv[h2][i][e2]) - mean, a1 = bfhi(xv[h2][i][e2]) - mean; q0 = fmaf(a0, a0, q0); q1 = fmaf(a1, a1, q1); }
        float q = q0 + q1;
        q += __shfl_xor(q, 1); q += __shfl_xor(q, 2); q += __shfl_xor(q, 4);
        const float den = __builtin_sqrtf(rbf(rbf(q * (1.f / D)) + 1e-5f));
        __builtin_amdgcn_raw_buffer_store_b64(v2u{pack2(mean, den), tag}, rws, (lane & 7) == 0 ? ST_OFF + (srow + 8 * h2) * 8 : -16, 0, SC1);
      }
      float c[8] = {};
#pragma unroll
      for (int i = 0; i < 20; ++i)
#pragma unroll
        for (int e2 = 0; e2 < 4; ++e2) { c[2 * e2] += bflo(wv[i][e2]); c[2 * e2 + 1] += bfhi(wv[i][e2]); }
      *(LAS f32x4*)(L + ((w * 8 + (lane >> 3)) * 64 + (lane & 7) * 8) * 4) = f32x4{c[0], c[1], c[2], c[3]};
      *(LAS f32x4*)(L + ((w * 8 + (lane >> 3)) * 64 + (lane & 7) * 8 + 4) * 4) = f32x4{c[4], c[5], c[6], c[7]};
      WAITL();
      unsigned old = 0;
      if (lane == 0) old = __hip_atomic_fetch_add((LAS unsigned*)(L + C_H), 1u, RLX_WG);
      if (__builtin_amdgcn_readfirstlane(old) == 3) {
        asm volatile("" ::: "memory");
        float t4[4] = {};
#pragma unroll
        for (int i = 0; i < 32; ++i) t4[i & 3] += *(LAS float*)(L + (i * 64 + lane) * 4);
        __builtin_amdgcn_raw_buffer_store_b64(v2u{__float_as_uint((t4[0] + t4[1]) + (t4[2] + t4[3])), tag}, rws, csv ? CS_OFF + (cg * 64 + lane) * 8 : -16, 0, SC1);
      }
      lds_add(L + C_MMA, 12u, lane);
      if (b == 56 && w == 0) {
#pragma unroll 1
        for (unsigned sp = 0; sp < SPIN_CAP; ++sp) {
          bool ok = true;
#pragma unroll
          for (int i = 0; i < 4; ++i) ok = ok && flag_load(ws, FL_P + i * 64 * FSTR, 64, lane) == tag;
          if (__all(ok)) break;
          __builtin_amdgcn_s_sleep(32);
        }
        set_flag(ws, FL_GEN, tag, lane);
      }
    }
    unsigned hb_ = 0;
#define HBAR() do { WAITL(); lds_add(L + C_G2, 1u, lane); hb_ += 4; lds_wait(L + C_G2, hb_); } while (0)
    {
      const __amdgpu_buffer_rsrc_t rxx = mkrsrc(x, S * D * 2);
      lds_wait_sleep(L + C_MMA, 48);
#pragma unroll
      for (int i = 0; i < 3; ++i) {
        const int s = aw + 4 * i;
        if (s < 10) {
          const int n = s * 64 + lane; const int row = n / 10, c = n - row * 10;
          GLDS(rxx, L + EX + s * 1024, (64 * mb + row) * 1280 + 160 * nb + c * 16, 0);
        }
      }
      const float dv = bflo(Dv[dpair + (aw >> 1)]), dv3 = bflo(Dv[d3]);
      lds_wait_sleep(L + C_ST, 1);
      __builtin_amdgcn_s_setprio(1);
      LAS char* Lc = L + CV + (aw >> 1) * CH_BYTES;
      const int cts = aw & 1;
      f32x4 acc[4] = {};
      if (cts == 0) conv8<0>(Lc + aoff, Lc + boff, acc); else conv8<1>(Lc + aoff, Lc + boff, acc);
      __builtin_amdgcn_s_setprio(2);
#pragma unroll
      for (int ti = 0; ti < 4; ++ti) conv_out(Lc, acc[ti], cts == 0 ? (ti < 2 ? ti : ti + 4) : 2 + ti, dpair + (aw >> 1), dv, e, kq, rws, tag);
      f32x4 ac3[4] = {};
      if (ts3 == 0) conv2<0>(L + CV3 + aoff + aw * 128, L + CV3 + boff + aw * 128, ac3); else conv2<1>(L + CV3 + aoff + aw * 128, L + CV3 + boff + aw * 128, ac3);
#pragma unroll
      for (int ti = 0; ti < 4; ++ti) *(LAS f32x4*)(L + SC3 + (aw * 4 + ti) * 1024 + lane * 16) = ac3[ti];
      WAITV(4);
      HBAR();
      f32x4 a = {};
#pragma unroll
      for (int ww = 0; ww < 4; ++ww) a += *(LAS f32x4*)(L + SC3 + (ww * 4 + aw) * 1024 + lane * 16);
      conv_out(L + CV3, a, ts3 == 0 ? (aw < 2 ? aw : aw + 4) : 2 + aw, d3, dv3, e, kq, rws, tag);
    }
    {
      const int q = (lane & 15) >> 2, pp = lane & 3, pos = lane & 15;
      const int tok = 16 * aw + 2 * (pos >> 2) + (pos & 1) + 8 * ((pos >> 1) & 1);
      lds_wait_sleep(L + C_S, 1);
      const f32x2 st = *(LAS f32x2*)(L + XFS + ((mb & 1) * 64 + tok) * 8);
      v4u xn[5];
#pragma unroll
      for (int i = 0; i < 5; ++i) {
        const v2u xv2 = *(LAS v2u*)(L + EX + tok * 160 + (4 * i + kq) * 8);
        const unsigned d01 = pack2(bflo(xv2.x) - st.x, bfhi(xv2.x) - st.x), d23 = pack2(bflo(xv2.y) - st.x, bfhi(xv2.y) - st.x);
        xn[i] = v4u{pack2(bflo(d01) * st.y, bfhi(d01) * st.y), pack2(bflo(d23) * st.y, bfhi(d23) * st.y), xv2.x, xv2.y};
      }
      WAITL();
      lds_add(L + C_Y, 1u, lane);
      for (int i = 0; i < 2; ++i) __builtin_amdgcn_s_sleep(4);
      {
        v4u yv[5];
        const int yo = Y_OFF + (80 * nb + 4 * aw + kq) * 8192 + mb * 256 + e * 16;
#pragma unroll 1
        for (unsigned sp = 0; sp < SPIN_CAP; ++sp) {
          asm volatile("" ::: "memory");
          bool ok = true;
#pragma unroll
          for (int i = 0; i < 5; ++i) yv[i] = __builtin_amdgcn_raw_buffer_load_b128(rws, yo, i * 16 * 8192, SC1);
#pragma unroll
          for (int i = 0; i < 5; ++i) ok = ok && yv[i].y == tag && yv[i].w == tag;
          if (__all(ok)) break;
          __builtin_amdgcn_s_sleep(8);
        }
#pragma unroll
        for (int i = 0; i < 5; ++i) *(LAS v2u*)(L + EY + (16 * i + 4 * aw + kq) * 144 + e * 8) = v2u{yv[i].x, yv[i].z};
      }
      HBAR();
#pragma unroll
      for (int i = 0; i < 5; ++i) {
        const int dq = 4 * i + kq;
        const v2u yv = trread(L + EY + (4 * dq + q) * 144 + aw * 32 + pp * 8);
        const unsigned s01 = pack2(bflo(xn[i].x) * bflo(yv.x), bfhi(xn[i].x) * bfhi(yv.x)), s23 = pack2(bflo(xn[i].y) * bflo(yv.y), bfhi(xn[i].y) * bfhi(yv.y));
        *(LAS v4u*)(L + ES + (tok * 20 + dq) * 16) = v4u{s01, s23, xn[i].z, xn[i].w};
      }
    }
  }
  BAR();
  const int lane2 = __builtin_amdgcn_mbcnt_hi(~0u, __builtin_amdgcn_mbcnt_lo(~0u, opaque0()));
#pragma unroll
  for (int i = 0; i < 3; ++i) {
    const int it = w + 8 * i;
    if (it < 20) {
      const int n = it * 64 + lane2;
      const int tok = n / 20, dq = n - tok * 20;
      f32x4 o = *(LAS f32x4*)(L + tok * PSTR + dq * 16);
#pragma unroll
      for (int ww = 1; ww < 4; ++ww) o += *(LAS f32x4*)(L + ww * PART + tok * PSTR + dq * 16);
      const v4u sx = *(LAS v4u*)(L + ES + n * 16);
      const unsigned o01 = pack2(o[0], o[1]), o23 = pack2(o[2], o[3]);
      const unsigned g01 = pack2(silu_f(bflo(o01)), silu_f(bfhi(o01))), g23 = pack2(silu_f(bflo(o23)), silu_f(bfhi(o23)));
      const unsigned m01 = pack2(bflo(sx.x) * bflo(g01), bfhi(sx.x) * bfhi(g01)), m23 = pack2(bflo(sx.y) * bflo(g23), bfhi(sx.y) * bfhi(g23));
      v2u res;
      res.x = pack2(bflo(sx.z) + bflo(m01), bfhi(sx.z) + bfhi(m01));
      res.y = pack2(bflo(sx.w) + bflo(m23), bfhi(sx.w) + bfhi(m23));
      __builtin_amdgcn_raw_buffer_store_b64(res, mkrsrc(out, S * D * 2), ((64 * mb + tok) * D + 80 * nb + dq * 4) * 2, 0, 16);
    }
  }
}

extern "C" void kernel_launch(void* const* d_in, const int* in_sizes, int n_in,
                              void* d_out, int out_size, void* d_ws, size_t ws_size,
                              hipStream_t stream) {
  fused<<<256, 512, 0, stream>>>((const u16*)d_in[0], (const u16*)d_in[1], (const u16*)d_in[2], (const u16*)d_in[3],
                                 (const u16*)d_in[4], (const u16*)d_in[5], (const u16*)d_in[7], (const u16*)d_in[8],
                                 (char*)d_ws, (u16*)d_out);
}
```

```cpp
#include <hip/hip_runtime.h>
#include <stdint.h>

typedef unsigned short u16;
typedef unsigned v4u __attribute__((ext_vector_type(4)));
typedef unsigned v2u __attribute__((ext_vector_type(2)));
typedef float f32x2 __attribute__((ext_vector_type(2)));
typedef float f32x4 __attribute__((ext_vector_type(4)));
typedef float f32x16 __attribute__((ext_vector_type(16)));
typedef short bf16x8 __attribute__((ext_vector_type(8)));
typedef short s16x4 __attribute__((ext_vector_type(4)));
typedef __bf16 bf16x2 __attribute__((ext_vector_type(2)));
#define LAS __attribute__((address_space(3)))
#define GAS __attribute__((address_space(1)))
#define DI __device__ __forceinline__
#define RLX_AGENT __ATOMIC_RELAXED, __HIP_MEMORY_SCOPE_AGENT
#define RLX_WG __ATOMIC_RELAXED, __HIP_MEMORY_SCOPE_WORKGROUP

constexpr int S = 2048, D = 640, F = 1728;
constexpr int KS2 = 54;
constexpr int FSTR = 128;
constexpr int FL_GEN = 0, FL_P = 131072, FL_H = FL_P + 256 * 256;
constexpr int ST_OFF = 32768;
constexpr int CS_OFF = ST_OFF + S * 8;
constexpr int HP_OFF = 262144;
constexpr int HP_MB = KS2 * 4096;
constexpr int W2P_OFF = HP_OFF + 32 * HP_MB;
constexpr int W2P_NB = KS2 * 5120;
constexpr int R_OFF = W2P_OFF + 8 * W2P_NB;
constexpr int Y_OFF = R_OFF + D * S * 2;
constexpr int WS_BYTES = Y_OFF + D * S * 4;
constexpr unsigned RSRC3 = 0x00020000u;
constexpr int SC1 = 16;
constexpr unsigned SPIN_CAP = 1u << 18;

DI float bflo(unsigned u) { return __uint_as_float(u << 16); }
DI float bfhi(unsigned u) { return __uint_as_float(u & 0xffff0000u); }
DI unsigned pack2(float a, float b) {
  bf16x2 r = __builtin_convertvector(f32x2{a, b}, bf16x2);
  return __builtin_bit_cast(unsigned, r);
}
DI float rbf(float a) { return bflo(pack2(a, a)); }
DI float silu_f(float x) { return x * __builtin_amdgcn_rcpf(1.f + __builtin_amdgcn_exp2f(-1.44269504088896f * x)); }
DI f32x4 mfma16(v4u a, v4u b, f32x4 c) {
  return __builtin_amdgcn_mfma_f32_16x16x32_bf16(__builtin_bit_cast(bf16x8, a), __builtin_bit_cast(bf16x8, b), c, 0, 0, 0);
}
DI f32x16 mfma32(v4u a, v4u b, f32x16 c) {
  return __builtin_amdgcn_mfma_f32_32x32x16_bf16(__builtin_bit_cast(bf16x8, a), __builtin_bit_cast(bf16x8, b), c, 0, 0, 0);
}
DI float dot2(unsigned a, unsigned b, float c) {
  return __builtin_amdgcn_fdot2_f32_bf16(__builtin_bit_cast(bf16x2, a), __builtin_bit_cast(bf16x2, b), c, false);
}
DI v2u trread(LAS char* p) { return __builtin_bit_cast(v2u, __builtin_amdgcn_ds_read_tr16_b64_v4i16((LAS s16x4*)p)); }
DI v4u ldsr128(LAS char* p) { return *(LAS v4u*)p; }
DI __amdgpu_buffer_rsrc_t mkrsrc(const void* p, int bytes) { return __builtin_amdgcn_make_buffer_rsrc((void*)p, 0, bytes, RSRC3); }
DI void glds_asm(__amdgpu_buffer_rsrc_t rs, unsigned ldsa, int voff, int soff) {
  asm volatile("s_nop 4\n\ts_mov_b32 m0, %0\n\ts_nop 0\n\tbuffer_load_dwordx4 %1, %2, %3 offen lds"
               :: "s"(ldsa), "v"(voff), "s"(rs), "s"(soff) : "m0", "memory");
}
DI void glds_fast(__amdgpu_buffer_rsrc_t rs, unsigned ldsa, int voff, int soff) {
  asm volatile("s_mov_b32 m0, %0\n\ts_nop 0\n\tbuffer_load_dwordx4 %1, %2, %3 offen lds"
               :: "s"(ldsa), "v"(voff), "s"(rs), "s"(soff) : "m0", "memory");
}
#define GLDS(rs, ldsp, voff, soff) glds_asm(rs, (unsigned)(uintptr_t)(ldsp), voff, soff)
#define WAITV(n) asm volatile("s_waitcnt vmcnt(" #n ")" ::: "memory")
#define WAITL() asm volatile("s_waitcnt lgkmcnt(0)" ::: "memory")
#define BAR() do { WAITL(); __builtin_amdgcn_s_barrier(); asm volatile("" ::: "memory"); } while (0)
#define SB() __builtin_amdgcn_sched_barrier(0)


constexpr int NBUF = 3;
constexpr int CH_HA = 0, CH_HB = 4224, CH_R = 8448, CH_RS = 4512, CH_BYTES = CH_R + 4 * CH_RS;
constexpr int CV = 98304;
constexpr int CV3 = 32768;
constexpr int SC3 = 65536;
constexpr int PK = CV, PV = PK + 11264, PW = PV + 10240, PS = PW + 10240, PO = PS + 1024;
constexpr int EY = CV, EX = 0, ES = EY + 80 * 144;
constexpr int XFS = CV + 2 * CH_BYTES;
constexpr int XFC = XFS + 1024;
constexpr int CNT = XFC + 512;
constexpr int C_AUX = CNT, C_G2 = CNT + 4, C_Y = CNT + 8, C_H = CNT + 12, C_MMA = CNT + 16, C_S = CNT + 20, C_P = CNT + 24,
              C_TAG = CNT + 60, C_ST = CNT + 40, C_HR = CNT + 32;
constexpr int LDS_BYTES = CNT + 64;
static_assert(PO + 10240 <= XFS && ES + 20480 <= XFS && LDS_BYTES <= 163840, "lds");
constexpr int PSTR = 336, PART = 64 * PSTR;

DI void lds_add(LAS char* c, unsigned v, int lane) {
  if (lane == 0) __hip_atomic_fetch_add((LAS unsigned*)c, v, RLX_WG);
}
DI void lds_wait(LAS char* c, unsigned target) {
  while ((unsigned)__builtin_amdgcn_readfirstlane(__hip_atomic_load((LAS unsigned*)c, RLX_WG)) < target) {}
  asm volatile("" ::: "memory");
}
DI void lds_wait_sleep(LAS char* c, unsigned target) {
  while ((unsigned)__builtin_amdgcn_readfirstlane(__hip_atomic_load((LAS unsigned*)c, RLX_WG)) < target) __builtin_amdgcn_s_sleep(1);
  asm volatile("" ::: "memory");
}
DI unsigned flag_load(char* ws, int off, int n, int lane) {
  return __hip_atomic_load((GAS unsigned*)(ws + off + (lane < n ? lane : 0) * FSTR), RLX_AGENT);
}
DI void poll_flags(char* ws, int off, int n, unsigned tag, int lane) {
#pragma unroll 1
  for (unsigned spins = 0; spins < SPIN_CAP; ++spins) {
    const unsigned v = flag_load(ws, off, n, lane);
    if (__all(v == tag)) break;
    __builtin_amdgcn_s_sleep(8);
  }
  __builtin_amdgcn_fence(__ATOMIC_ACQUIRE, "wavefront");
}
DI void set_flag(char* ws, int off, unsigned tag, int lane) {
  if (lane == 0) __hip_atomic_store((GAS unsigned*)(ws + off), tag, RLX_AGENT);
}

DI unsigned opaque0() { unsigned z; asm volatile("v_mov_b32 %0, 0" : "=v"(z)); return z; }
template <int I> struct IC { static constexpr int value = I; };
template <int B, int E, class Fn> DI void static_for(Fn&& f) { if constexpr (B < E) { f(IC<B>{}); static_for<B + 1, E>(f); } }
struct Frags { v4u x[4][2], w[4][2]; };

template <int TS> DI void conv_ld(LAS char* pa, LAS char* pb, v4u (&A)[8], v4u (&B)[8]) {
  constexpr int NA = TS == 0 ? 8 : 6;
#pragma unroll
  for (int a = 0; a < NA; ++a) { A[a] = ldsr128(pa + a * 512); B[a] = ldsr128(pb + (7 - a) * 512); }
}
template <int TS> DI void conv_mm(const v4u (&A)[8], const v4u (&B)[8], f32x4 (&acc)[4]) {
#pragma unroll
  for (int ti = 0; ti < 4; ++ti) {
    const int n = TS == 0 ? (ti == 0 ? 0 : ti == 1 ? 1 : ti == 2 ? 6 : 7) : 2 + ti;
#pragma unroll
    for (int a = 0; a <= n; ++a) acc[ti] = mfma16(A[a], B[n - a], acc[ti]);
  }
}
DI void conv_sched() {
#pragma unroll
  for (int i = 0; i < 16; ++i) { __builtin_amdgcn_sched_group_barrier(0x8, 1, 0); __builtin_amdgcn_sched_group_barrier(0x100, 1, 0); }
  __builtin_amdgcn_sched_group_barrier(0x8, 2, 0);
}
template <int TS> DI void conv8(LAS char* pa, LAS char* pb, f32x4 (&acc)[4]) {
  v4u A0[8], B0[8], A1[8], B1[8];
  conv_ld<TS>(pa, pb, A0, B0);
  SB();
#pragma unroll 1
  for (int r = 0; r < 8; r += 2) {
    conv_ld<TS>(pa + 64, pb + 64, A1, B1); conv_mm<TS>(A0, B0, acc); conv_sched(); SB();
    pa += 128; pb += 128;
    conv_ld<TS>(pa, pb, A0, B0); conv_mm<TS>(A1, B1, acc); conv_sched(); SB();
  }
}
template <int TS> DI void conv2(LAS char* pa, LAS char* pb, f32x4 (&acc)[4]) {
#pragma unroll 1
  for (int r = 0; r < 2; ++r) {
    v4u A[8], B[8];
    conv_ld<TS>(pa, pb, A, B); conv_mm<TS>(A, B, acc);
    pa += 64; pb += 64;
  }
}
DI unsigned kshift2(float k1, float k2, float k3, float k4, float k5, float c0, float c1, float c2, float c3) {
  unsigned a = pack2(k1 * c3, k2 * c3);
  unsigned t = pack2(k2 * c2, k3 * c2);
  a = pack2(bflo(a) + bflo(t), bfhi(a) + bfhi(t));
  t = pack2(k3 * c1, k4 * c1);
  a = pack2(bflo(a) + bflo(t), bfhi(a) + bfhi(t));
  t = pack2(k4 * c0, k5 * c0);
  return pack2(bflo(a) + bflo(t), bfhi(a) + bfhi(t));
}
DI void stage_h(LAS char* Lc, v4u h, int g) {
  *(LAS v4u*)(Lc + CH_HB + 112 + g * 16) = h;
  LAS char* a = Lc + CH_HA + 114 + g * 16;
  *(LAS u16*)(a) = (u16)(h.x & 0xffff);
  *(LAS unsigned*)(a + 2) = __builtin_amdgcn_alignbit(h.y, h.x, 16);
  *(LAS v2u*)(a + 6) = v2u{__builtin_amdgcn_alignbit(h.z, h.y, 16), __builtin_amdgcn_alignbit(h.w, h.z, 16)};
  *(LAS u16*)(a + 14) = (u16)(h.w >> 16);
}
DI void stage_r(LAS char* Lc, v4u u, int g) {
  LAS char* rb = Lc + CH_R + g * 16;
  *(LAS v4u*)(rb) = u;
  { LAS char* q = rb + CH_RS - 4; *(LAS unsigned*)(q) = u.x; *(LAS v2u*)(q + 4) = v2u{u.y, u.z}; *(LAS unsigned*)(q + 12) = u.w; }
  { LAS char* q = rb + 2 * CH_RS - 8; *(LAS v2u*)(q) = v2u{u.x, u.y}; *(LAS v2u*)(q + 8) = v2u{u.z, u.w}; }
  { LAS char* q = rb + 3 * CH_RS - 12; *(LAS unsigned*)(q) = u.x; *(LAS v2u*)(q + 4) = v2u{u.y, u.z}; *(LAS unsigned*)(q + 12) = u.w; }
}
DI void conv_out(LAS char* Lc, f32x4 a, int n, int d, float dv, int e, int kq, __amdgpu_buffer_rsrc_t rws, unsigned tag) {
  const int t = 256 * n + 64 * (e >> 2) + 2 * (e & 3) + 16 * kq;
  const unsigned ua = *(LAS unsigned*)(Lc + CH_R + 2 * (2046 - t));
  const unsigned ub = *(LAS unsigned*)(Lc + CH_R + 2 * (2038 - t));
  const unsigned c01 = pack2(a[0], a[1]), c23 = pack2(a[2], a[3]);
  const unsigned d01 = pack2(bfhi(ua) * dv, bflo(ua) * dv), d23 = pack2(bfhi(ub) * dv, bflo(ub) * dv);
  v2u o;
  o.x = pack2(bflo(c01) + bflo(d01), bfhi(c01) + bfhi(d01));
  o.y = pack2(bflo(c23) + bflo(d23), bfhi(c23) + bfhi(d23));
  const int off = Y_OFF + d * 8192 + (256 * n + 64 * (e >> 2) + 16 * kq + 4 * (e & 3)) * 4;
  __builtin_amdgcn_raw_buffer_store_b128(v4u{o.x, tag, o.y, tag}, rws, off, 0, SC1);
}

__global__ __launch_bounds__(512, 2) void fused(const u16* x, const u16* __restrict__ kk, const u16* __restrict__ vv,
                                                const u16* __restrict__ sk, const u16* __restrict__ hk, const u16* __restrict__ Dv,
                                                const u16* __restrict__ w1, const u16* __restrict__ w2,
                                                char* ws, u16* out) {
  __shared__ __attribute__((aligned(16))) char smem[LDS_BYTES];
  LAS char* L = (LAS char*)smem;
  if (__builtin_amdgcn_readfirstlane(threadIdx.x >> 6) == 7) {
    unsigned long long pc;
    asm volatile("s_getpc_b64 %0" : "=s"(pc));
    const unsigned co = min(((blockIdx.x >> 3) & 7) * 8192u + (threadIdx.x & 63) * 128u, 39000u);
    asm volatile("s_mov_b32 m0, %0\n\ts_nop 0\n\tglobal_load_lds_dword %1, %2" :: "s"((unsigned)(uintptr_t)(L + XFS)), "v"(co), "s"(pc) : "m0");
  }
  const int b = blockIdx.x, xcd = b & 7, r = b >> 3;
  const int rg = xcd >> 1, hf = xcd & 1, pi = r >> 3, jj = r & 7;
  const int p = 4 * rg + pi, j = 7 * hf + jj;
  const bool g1 = jj < 7;
  const int mbl = 2 * pi + ((r >> 2) & 1), nbl = r & 3;
  const int mb = 8 * rg + mbl, nb = 4 * hf + nbl;
  const int lane = threadIdx.x & 63;
  const int w = __builtin_amdgcn_readfirstlane(threadIdx.x >> 6);
  const int hi = lane >> 5, aw = w & 3;
  const __amdgpu_buffer_rsrc_t rws = mkrsrc(ws, WS_BYTES);
  if (threadIdx.x < 15) *(LAS unsigned*)(L + CNT + threadIdx.x * 4) = 0u;
  const int dpair = 80 * nb + 2 * mb, d3 = 80 * nb + 64 + (mb >> 1), ts3 = mb & 1;
  const int e = lane & 15, kq = lane >> 4;
  const int aoff = ((e & 1) ? CH_HB : CH_HA) + (kq + (e >> 1)) * 16;
  const int boff = CH_R + (3 - (e & 3)) * CH_RS + (248 - 32 * 7 - 8 * (e >> 2) + kq) * 16;

  {
    const __amdgpu_buffer_rsrc_t rk = mkrsrc(kk, S * D * 2), rv_ = mkrsrc(vv, S * D * 2), rsk = mkrsrc(sk, D * 8);
    const int sb = 64 * mb * 1280 + 160 * nb;
    int vo[2];
#pragma unroll
    for (int i = 0; i < 2; ++i) { const int n = (8 * i + w) * 64 + lane; const int row = n / 10; vo[i] = row * 1280 + (n - row * 10) * 16; }
    GLDS(rk, L + PK + w * 1024, vo[0] + sb - 5120, 0);
    GLDS(rv_, L + PV + w * 1024, vo[0], sb);
    {
      const int n = (5 + w) * 64 + lane; const int row = n / 10; const int vo3 = row * 1280 + (n - row * 10) * 16;
      if (w < 3) GLDS(rk, L + PK + (8 + w) * 1024, vo[1] + sb - 5120, 0);
      else if (w < 5) GLDS(rv_, L + PV + (5 + w) * 1024, vo3, sb);
      else if (w == 5) GLDS(rsk, L + PS, lane * 16, nb * 640);
    }
  }
#define PREP_ITEM(tg, cg) do { \
        const int q_ = (lane & 15) >> 2, pp_ = lane & 3, tl = 16 * (tg) + 4 * kq; \
        LAS char* ka_ = L + PK + (tl + q_) * 160 + (cg) * 32 + pp_ * 8; \
        v2u k0 = trread(ka_), k1 = trread(ka_ + 4 * 160); \
        v2u v0 = trread(ka_ + (PV - PK)); \
        v2u sw = *(LAS v2u*)(L + PS + ((cg) * 16 + e) * 8); \
        float kf[8] = {bflo(k0.x), bfhi(k0.x), bflo(k0.y), bfhi(k0.y), bflo(k1.x), bfhi(k1.x), bflo(k1.y), bfhi(k1.y)}; \
        float vf[4] = {bflo(v0.x), bfhi(v0.x), bflo(v0.y), bfhi(v0.y)}; \
        const float c0 = bflo(sw.x), c1 = bfhi(sw.x), c2 = bflo(sw.y), c3 = bfhi(sw.y); \
        float u[4]; \
        _Pragma("unroll") for (int e2 = 0; e2 < 4; e2 += 2) { \
          const unsigned a = kshift2(kf[e2 + 1], kf[e2 + 2], kf[e2 + 3], kf[e2 + 4], kf[e2 + 5], c0, c1, c2, c3); \
          u[e2] = bflo(a) * vf[e2]; u[e2 + 1] = bfhi(a) * vf[e2 + 1]; } \
        v2u o = v2u{pack2(u[3], u[2]), pack2(u[1], u[0])}; \
        const int ch = (cg) * 16 + e; \
        const int pos8 = (60 - tl) >> 2; \
        *(LAS v2u*)(L + PO + ch * 128 + ((((pos8 >> 1) ^ (ch & 7)) << 4) | ((pos8 & 1) << 3))) = o; \
      } while (0)

  if (w >= 4) {
    const unsigned gen = __hip_atomic_load((GAS unsigned*)(ws + FL_GEN), RLX_AGENT);
    WAITV(0);
    const unsigned tag = (unsigned)__builtin_amdgcn_readfirstlane(gen) * 0x9E3779B1u + 0x85EBCA6Bu;
    if (aw == 0 && lane == 0) *(LAS unsigned*)(L + C_TAG) = tag;
#define AUXBAR() do { WAITL(); lds_add(L + C_AUX, 1u, lane); ab += 4; lds_wait(L + C_AUX, ab); } while (0)
    BAR();
    unsigned ab = 4;
    {
      const __amdgpu_buffer_rsrc_t rw2 = mkrsrc(w2, F * D * 2);
#pragma unroll
      for (int i = 0; i < 3; ++i) if (4 * i + aw < 10) {
        const int n = (4 * i + aw) * 64 + lane; const int row = n / 10;
        GLDS(rw2, L + PW + (4 * i + aw) * 1024, (row + (row >= 32 ? 992 : 0)) * 1280 + (n - row * 10) * 16, 32 * mb * 1280 + 160 * nb);
      }
    }
    {
      const int g = lane >> 4, q = (lane & 15) >> 2, pp = lane & 3;
      PREP_ITEM(aw, 0); PREP_ITEM(aw, 1); PREP_ITEM(aw, 2);
      AUXBAR();
#pragma unroll
      for (int i = 0; i < 3; ++i) {
        const int ri = aw + 4 * i;
        if (ri < 10) {
          const int ch = 8 * ri + (lane >> 3), cc = lane & 7;
          v4u val = ldsr128(L + PO + ch * 128 + ((cc ^ (ch & 7)) << 4));
          __builtin_amdgcn_raw_buffer_store_b128(val, rws, R_OFF + (80 * nb + ch) * 4096 + (31 - mb) * 128 + cc * 16, 0, SC1);
        }
      }
      __builtin_amdgcn_s_setprio(0);
      if (aw < 2) WAITV(3); else WAITV(2);
      AUXBAR();
#pragma unroll
      for (int i = 0; i < 3; ++i) {
        const int fi = aw + 4 * i;
        if (fi < 10) {
          const int half = fi / 5, nt = fi - half * 5, ks = mb + 32 * half;
          const int r0 = 16 * (g & 1) + 4 * (g >> 1);
          LAS char* a = L + PW + half * 5120 + (r0 + q) * 160 + nt * 32 + pp * 8;
          v2u lo = trread(a), hi2 = trread(a + 8 * 160);
          __builtin_amdgcn_raw_buffer_store_b128(v4u{lo.x, lo.y, hi2.x, hi2.y}, rws, ks < KS2 ? W2P_OFF + nb * W2P_NB + ks * 5120 + nt * 1024 + lane * 16 : -16, 0, SC1);
        }
      }
    }
    WAITV(0);
    AUXBAR();
    if (aw == 0) set_flag(ws, FL_P + (nb * 32 + mb) * FSTR, tag, lane);
    const __amdgpu_buffer_rsrc_t rh = mkrsrc(hk, D * S * 2);
    v4u cu[3];
    cu[0] = __builtin_amdgcn_raw_buffer_load_b128(rh, dpair * 4096 + aw * 1024 + lane * 16, 0, 0);
    cu[1] = __builtin_amdgcn_raw_buffer_load_b128(rh, dpair * 4096 + 4096 + aw * 1024 + lane * 16, 0, 0);
    cu[2] = __builtin_amdgcn_raw_buffer_load_b128(rh, d3 * 4096 + aw * 1024 + lane * 16, 0, 0);
    const int g = aw * 64 + lane;
    lds_wait_sleep(L + C_MMA, 32);
    if (aw == 1) {
      const int jc = g1 ? j : 0;
      v2u gr[4];
#define GRL(off) __builtin_bit_cast(v2u, __hip_atomic_load((GAS unsigned long long*)(ws + (off)), RLX_AGENT))
#pragma unroll 1
      for (unsigned sp = 0; sp < SPIN_CAP; ++sp) {
        gr[0] = GRL(ST_OFF + (p * 128 + lane) * 8);
        gr[1] = GRL(ST_OFF + (p * 128 + 64 + lane) * 8);
        gr[2] = GRL(CS_OFF + (jc * 128 + lane) * 8);
        gr[3] = GRL(CS_OFF + (jc * 128 + (j == 13 ? 0 : 64) + lane) * 8);
        if (__all(gr[0].y == tag && gr[1].y == tag && gr[2].y == tag && gr[3].y == tag)) break;
        __builtin_amdgcn_s_sleep(8);
      }
      *(LAS f32x2*)(L + XFS + lane * 8) = f32x2{bflo(gr[0].x), 1.f / bfhi(gr[0].x)};
      *(LAS f32x2*)(L + XFS + (64 + lane) * 8) = f32x2{bflo(gr[1].x), 1.f / bfhi(gr[1].x)};
      *(LAS unsigned*)(L + XFC + lane * 4) = gr[2].x;
      *(LAS unsigned*)(L + XFC + (64 + lane) * 4) = gr[3].x;
      WAITL();
      lds_add(L + C_S, 1u, lane);
    }
    if (aw == 2) { poll_flags(ws, FL_P + nb * 32 * FSTR, 32, tag, lane); lds_add(L + C_P, 1u, lane); }
    lds_wait_sleep(L + C_P, 1);
    v4u rv[3];
    rv[0] = __builtin_amdgcn_raw_buffer_load_b128(rws, R_OFF + dpair * 4096 + aw * 1024 + lane * 16, 0, SC1);
    rv[1] = __builtin_amdgcn_raw_buffer_load_b128(rws, R_OFF + dpair * 4096 + 4096 + aw * 1024 + lane * 16, 0, SC1);
    rv[2] = __builtin_amdgcn_raw_buffer_load_b128(rws, R_OFF + d3 * 4096 + aw * 1024 + lane * 16, 0, SC1);
    const __amdgpu_buffer_rsrc_t ra = mkrsrc(ws + HP_OFF + mb * HP_MB, HP_MB);
    const __amdgpu_buffer_rsrc_t rb = mkrsrc(ws + W2P_OFF + nb * W2P_NB, W2P_NB);
    const int va = lane * 16 + aw * 4096, vb = lane * 16 + aw * 5120;
    constexpr int WB = 6, NKS = 14, POOL = 41;
    v4u fa[NKS][4], fb[NKS][5];
#define LOADB(i) do { const int so_ = (i) * 20480; \
    _Pragma("unroll") for (int n = 0; n < 5; ++n) fb[i][n] = __builtin_amdgcn_raw_buffer_load_b128(rb, vb + (n & 3) * 1024, so_ + (n >> 2) * 4096, SC1); } while (0)
#pragma unroll
    for (int i = 0; i < WB; ++i) LOADB(i);
    SB();
    lds_wait_sleep(L + C_MMA, 44);
    if (aw == 0 && lane < 32) *(LAS v4u*)(L + CV + (lane >> 4) * CH_BYTES + ((lane >> 3) & 1) * CH_HB + (lane & 7) * 16) = v4u{0, 0, 0, 0};
    if (aw == 3) {
#pragma unroll
      for (int i = 0; i < 4; ++i) {
        const int n = lane + 64 * i;
        if (n < 200) { const int cp = n / 25, sl = n - cp * 25; *(LAS v4u*)(L + CV + (cp >> 2) * CH_BYTES + CH_R + (cp & 3) * CH_RS + 4080 + sl * 16) = v4u{0, 0, 0, 0}; }
      }
    }
    stage_h(L + CV, cu[0], g);
    stage_h(L + CV + CH_BYTES, cu[1], g);
    stage_r(L + CV, rv[0], g);
    stage_r(L + CV + CH_BYTES, rv[1], g);
    if (aw == 0 && lane < 16) *(LAS v4u*)(L + CV3 + (lane >> 3) * CH_HB + (lane & 7) * 16) = v4u{0, 0, 0, 0};
    if (aw == 3) {
#pragma unroll
      for (int i = 0; i < 2; ++i) {
        const int n = lane + 64 * i;
        if (n < 100) { const int cp = n / 25, sl = n - cp * 25; *(LAS v4u*)(L + CV3 + CH_R + cp * CH_RS + 4080 + sl * 16) = v4u{0, 0, 0, 0}; }
      }
    }
    stage_h(L + CV3, cu[2], g);
    stage_r(L + CV3, rv[2], g);
    AUXBAR();
    if (aw == 0) lds_add(L + C_ST, 1u, lane);
    lds_wait_sleep(L + C_H, 4);
    if (aw == 1) { poll_flags(ws, FL_H + p * 16 * FSTR, 14, tag, lane); lds_add(L + C_HR, 1u, lane); }
    __builtin_amdgcn_s_setprio(0);
    lds_wait_sleep(L + C_HR, 1);
#define ISSUE(q) do { constexpr int q_ = (q); \
      if constexpr (q_ < 4 * WB) { fa[q_ / 4][q_ % 4] = __builtin_amdgcn_raw_buffer_load_b128(ra, va + (q_ % 4) * 1024, (q_ / 4) * 16384, SC1); } \
      else if constexpr (q_ < 4 * WB + 9 * (NKS - WB)) { constexpr int k_ = WB + (q_ - 4 * WB) / 9, f_ = (q_ - 4 * WB) % 9; \
        if constexpr (f_ < 4) fa[k_][f_] = __builtin_amdgcn_raw_buffer_load_b128(ra, va + f_ * 1024, k_ * 16384, SC1); \
        else fb[k_][f_ - 4] = __builtin_amdgcn_raw_buffer_load_b128(rb, vb + ((f_ - 4) & 3) * 1024, k_ * 20480 + ((f_ - 4) >> 2) * 4096, SC1); } \
    } while (0)
    constexpr auto need = [](int k) { k = k > NKS ? NKS : k; return k <= WB ? 4 * k : 4 * WB + 9 * (k - WB); };
    constexpr auto cum = [need](int i) { const int a = need(i + 5), c = POOL - 5 * WB + (i > 0 ? 9 * i - 6 : 0); return a < c ? a : c; };
    static_for<0, cum(0)>([&](auto q) { ISSUE(decltype(q)::value); });
    SB();
#define PIN_ACC() do { _Pragma("unroll") for (int m_ = 0; m_ < 4; ++m_) _Pragma("unroll") for (int n_ = 0; n_ < 5; ++n_) asm volatile("" : "+v"(acc2[m_][n_])); } while (0)
    f32x4 acc2[4][5] = {};
    LAS char* pw = L;
    static_for<0, NKS>([&](auto ii) {
      constexpr int i = decltype(ii)::value;
      constexpr int c0 = cum(i), cn = cum(i + 1) - cum(i);
      static_assert(cn <= 9 && c0 >= need(i + 1), "schedule");
      static_for<0, 20>([&](auto mm) {
        constexpr int mn = decltype(mm)::value, m = mn / 5, n = mn % 5;
        acc2[m][n] = mfma16(fb[i][n], fa[i][m], acc2[m][n]);
        constexpr int slot = (mn & 1) ? (mn >> 1) - (9 - cn) : -1;
        if constexpr (slot >= 0 && mn < 19) { SB(); ISSUE(c0 + slot); SB(); }
        if constexpr (i == NKS - 1 && mn >= 2) { constexpr int m2 = (mn - 2) / 5, n2 = (mn - 2) % 5; SB(); *(LAS f32x4*)(pw + 16 * m2 * PSTR + n2 * 64) = acc2[m2][n2]; SB(); }
      });
      SB();
      if constexpr (i == NKS - 2) {
        PIN_ACC();
        lds_wait(L + C_Y, 4);
        const int ln = __builtin_amdgcn_mbcnt_hi(~0u, __builtin_amdgcn_mbcnt_lo(~0u, opaque0()));
        pw = L + aw * PART + (ln & 15) * PSTR + (ln >> 4) * 16;
        SB();
      }
    });
    *(LAS f32x4*)(pw + 48 * PSTR + 3 * 64) = acc2[3][3];
    *(LAS f32x4*)(pw + 48 * PSTR + 4 * 64) = acc2[3][4];
  } else {
    __builtin_amdgcn_s_setprio(2);
    const __amdgpu_buffer_rsrc_t rw = mkrsrc(w1, D * F * 2);
    unsigned tag;
    if (g1) {
      const __amdgpu_buffer_rsrc_t rx = mkrsrc(x + (size_t)p * 128 * D, 128 * D * 2);
      int vx[2], vw[4];
      {
        const int row = 32 * w + (lane >> 3);
        vx[0] = row * 1280 + (((lane & 7) ^ ((row >> 1) & 7)) << 4);
        vx[1] = (row + 8) * 1280 + (((lane & 7) ^ (((row + 8) >> 1) & 7)) << 4);
#pragma unroll
        for (int ii = 0; ii < 4; ++ii) {
          const int kr = 16 * w + 4 * ii + (lane >> 4);
          vw[ii] = kr * (F * 2) + j * 256 + (((lane & 15) ^ (((kr & 3) << 2) | ((kr >> 2) & 3))) << 4);
        }
      }
      const int rot = (5 * ((pi + jj) & 1) + (5 * xcd) / 8) % 10;
      const unsigned lbase = (unsigned)(uintptr_t)L + w * 4096;
#define KC(s) ((s) + rot >= 10 ? (s) + rot - 10 : (s) + rot)
#define G1LD(s, i) do { \
    if ((i) < 4) glds_fast(rx, lbase + ((s) % NBUF) * 32768 + (i) * 1024, vx[(i) & 1], KC(s) * 128 + ((i) >> 1) * 16 * 1280); \
    else glds_fast(rw, lbase + ((s) % NBUF) * 32768 + 16384 + ((i) - 4) * 1024, vw[((i) - 4) & 3], KC(s) * (64 * F * 2)); \
  } while (0)
#pragma unroll
      for (int i = 0; i < 8; ++i) G1LD(0, i);
#pragma unroll
      for (int i = 0; i < 8; ++i) G1LD(1, i);
      WAITV(16);
      BAR();
      PREP_ITEM(aw, 3); PREP_ITEM(aw, 4);
      WAITL(); lds_add(L + C_AUX, 1u, lane);
      SB();
      const int fh = w & 1, th = w >> 1;
      int xa[4], wa[2][2];
      {
        const int row = 64 * th + (lane & 31);
        const int c0 = hi ^ ((row >> 1) & 7);
        const int blk = (lane >> 4) & 1, q = (lane & 15) >> 2, pp = lane & 3;
#pragma unroll
        for (int s = 0; s < 4; ++s) xa[s] = row * 128 + (((2 * s) ^ c0) << 4);
#pragma unroll
        for (int ft = 0; ft < 2; ++ft)
#pragma unroll
          for (int t = 0; t < 2; ++t) {
            const int kr = 8 * hi + 4 * t + q;
            const int ch = 4 * (2 * fh + ft) + 2 * blk + (pp >> 1);
            wa[ft][t] = 16384 + 256 * kr + ((ch ^ ((q << 2) | ((2 * hi + t) & 3))) << 4) + 8 * (pp & 1);
          }
      }
      f32x16 acc[2][2] = {};
      Frags fA, fB;
#define RDX(f, bo, s, tt) f.x[s][tt] = ldsr128(L + (bo) + xa[s] + (tt) * 4096)
#define RDW(f, bo, s, ft) do { v2u wl_ = trread(L + (bo) + wa[ft][0] + (s) * 4096), wh_ = trread(L + (bo) + wa[ft][1] + (s) * 4096); f.w[s][ft] = v4u{wl_.x, wl_.y, wh_.x, wh_.y}; } while (0)
#define MM(f, s, ft, tt) acc[ft][tt] = mfma32(f.w[s][ft], f.x[s][tt], acc[ft][tt])
#define ITER(s, vm, cur, nxt) do { \
    asm volatile("s_waitcnt vmcnt(" #vm ") lgkmcnt(0)" ::: "memory"); \
    lds_add(L + C_MMA, 1u, lane); SB(); \
    MM(cur, 0, 0, 0); MM(cur, 0, 0, 1); MM(cur, 0, 1, 0); SB(); \
    lds_wait(L + C_MMA, 4 * ((s) + 2)); SB(); \
    constexpr int bo_ = (((s) + 1) % NBUF) * 32768; constexpr bool ld_ = (s) + NBUF < 10; \
    MM(cur, 0, 1, 1); RDX(nxt, bo_, 0, 0); RDX(nxt, bo_, 0, 1); SB(); if (ld_) G1LD((s) + NBUF, 0); SB(); \
    MM(cur, 1, 0, 0); RDW(nxt, bo_, 0, 0); SB(); \
    MM(cur, 1, 0, 1); RDW(nxt, bo_, 0, 1); SB(); if (ld_) G1LD((s) + NBUF, 4); SB(); \
    MM(cur, 1, 1, 0); RDX(nxt, bo_, 1, 0); RDX(nxt, bo_, 1, 1); SB(); if (ld_) G1LD((s) + NBUF, 1); SB(); \
    MM(cur, 1, 1, 1); RDW(nxt, bo_, 1, 0); SB(); \
    MM(cur, 2, 0, 0); RDW(nxt, bo_, 1, 1); SB(); if (ld_) G1LD((s) + NBUF, 5); SB(); \
    MM(cur, 2, 0, 1); RDX(nxt, bo_, 2, 0); RDX(nxt, bo_, 2, 1); SB(); if (ld_) G1LD((s) + NBUF, 2); SB(); \
    MM(cur, 2, 1, 0); RDW(nxt, bo_, 2, 0); SB(); \
    MM(cur, 2, 1, 1); RDW(nxt, bo_, 2, 1); SB(); if (ld_) G1LD((s) + NBUF, 6); SB(); \
    MM(cur, 3, 0, 0); RDX(nxt, bo_, 3, 0); RDX(nxt, bo_, 3, 1); SB(); if (ld_) G1LD((s) + NBUF, 3); SB(); \
    MM(cur, 3, 0, 1); RDW(nxt, bo_, 3, 0); RDW(nxt, bo_, 3, 1); SB(); \
    MM(cur, 3, 1, 0); SB(); if (ld_) G1LD((s) + NBUF, 7); SB(); \
    MM(cur, 3, 1, 1); SB(); \
  } while (0)
      WAITV(8);
      lds_add(L + C_MMA, 1u, lane); lds_wait(L + C_MMA, 4);
#pragma unroll
      for (int s = 0; s < 4; ++s) { RDX(fA, 0, s, 0); RDX(fA, 0, s, 1); RDW(fA, 0, s, 0); RDW(fA, 0, s, 1); }
      SB();
#pragma unroll
      for (int i = 0; i < 8; ++i) G1LD(2, i);
      SB();
      ITER(0, 8, fA, fB);
      ITER(1, 8, fB, fA);
      ITER(2, 8, fA, fB);
      ITER(3, 8, fB, fA);
      ITER(4, 8, fA, fB);
      ITER(5, 8, fB, fA);
      ITER(6, 8, fA, fB);
      ITER(7, 8, fB, fA);
      ITER(8, 0, fA, fB);
#pragma unroll
      for (int s = 0; s < 4; ++s) { MM(fB, s, 0, 0); MM(fB, s, 0, 1); MM(fB, s, 1, 0); MM(fB, s, 1, 1); }
      SB();
      lds_add(L + C_MMA, 1u, lane);
      lds_wait_sleep(L + C_S, 1);
      tag = __builtin_amdgcn_readfirstlane(*(LAS unsigned*)(L + C_TAG));
#pragma unroll
      for (int ft = 0; ft < 2; ++ft) {
        const int fg = 2 * fh + ft;
        const int ks = 4 * j + fg;
        f32x4 c4[4];
#pragma unroll
        for (int g = 0; g < 4; ++g) c4[g] = *(LAS f32x4*)(L + XFC + (32 * fg + 8 * g + 4 * hi) * 4);
#pragma unroll
        for (int tt = 0; tt < 2; ++tt) {
          const int tok = 64 * th + 32 * tt + (lane & 31);
          const f32x2 st = *(LAS f32x2*)(L + XFS + tok * 8);
          const f32x16& a = acc[ft][tt];
          const float zs1 = -1.44269504088896f * st.y, dn1 = __builtin_amdgcn_rcpf(st.y);
          const f32x2 zs = f32x2{zs1, zs1}, dn = f32x2{dn1, dn1};
          const int mb2 = 2 * p + th, mt = 2 * tt + ((lane >> 4) & 1);
          const int off = ks < KS2 ? HP_OFF + mb2 * HP_MB + ks * 4096 + mt * 1024 + ((2 * hi) * 16 + (lane & 15)) * 16 : -1024;
#pragma unroll
          for (int hh = 0; hh < 2; ++hh) {
            unsigned pk[4];
#pragma unroll
            for (int e2 = 0; e2 < 4; ++e2) {
              const int ee = hh * 4 + e2;
              const f32x2 cc = f32x2{c4[ee >> 1][(2 * ee) & 3], c4[ee >> 1][(2 * ee + 1) & 3]};
              const f32x2 dd = __builtin_elementwise_fma(cc, f32x2{-st.x, -st.x}, f32x2{a[2 * ee], a[2 * ee + 1]});
              const f32x2 zz = dd * zs;
              const f32x2 ex = f32x2{__builtin_amdgcn_exp2f(zz.x), __builtin_amdgcn_exp2f(zz.y)};
              const f32x2 ss = __builtin_elementwise_fma(ex, dn, dn);
              const f32x2 hv2 = dd * f32x2{__builtin_amdgcn_rcpf(ss.x), __builtin_amdgcn_rcpf(ss.y)};
              pk[e2] = pack2(hv2.x, hv2.y);
            }
            __builtin_amdgcn_raw_buffer_store_b128(v4u{pk[0], pk[1], pk[2], pk[3]}, rws, off + hh * 256, 0, SC1);
            SB();
          }
        }
      }
      lds_add(L + C_MMA, 1u, lane);
      WAITV(0);
      {
        unsigned old = 0;
        if (lane == 0) old = __hip_atomic_fetch_add((LAS unsigned*)(L + C_H), 1u, RLX_WG);
        if (__builtin_amdgcn_readfirstlane(old) == 3) set_flag(ws, FL_H + (p * 16 + j) * FSTR, tag, lane);
      }
    } else {
      const int idx16 = 4 * rg + pi;
      const int srow = 512 * rg + 64 * (4 * hf + pi) + 16 * w + (lane >> 3);
      const int cg = 14 * hf + idx16;
      const bool csv = idx16 < 14 - hf;
      const __amdgpu_buffer_rsrc_t rx = mkrsrc(x, S * D * 2);
      v4u xv[2][10], wv[20];
#pragma unroll
      for (int h2 = 0; h2 < 2; ++h2)
#pragma unroll
        for (int i = 0; i < 10; ++i) xv[h2][i] = __builtin_amdgcn_raw_buffer_load_b128(rx, (srow + 8 * h2) * 1280 + (lane & 7) * 16 + i * 128, 0, 0);
      SB();
      WAITV(20);
      BAR();
      PREP_ITEM(aw, 3); PREP_ITEM(aw, 4);
      WAITL(); lds_add(L + C_AUX, 1u, lane);
      SB();
      const int wvo = csv ? (160 * w + (lane >> 3)) * (F * 2) + cg * 128 + (lane & 7) * 16 : -16;
#pragma unroll
      for (int i = 0; i < 20; ++i) wv[i] = __builtin_amdgcn_raw_buffer_load_b128(rw, wvo, csv ? i * 8 * F * 2 : 0, 0);
      SB();
      tag = __builtin_amdgcn_readfirstlane(*(LAS unsigned*)(L + C_TAG));
#pragma unroll
      for (int h2 = 0; h2 < 2; ++h2) {
        float s = 0.f;
#pragma unroll
        for (int i = 0; i < 10; ++i)
#pragma unroll
          for (int e2 = 0; e2 < 4; ++e2) s = dot2(xv[h2][i][e2], 0x3f803f80u, s);
        s += __shfl_xor(s, 1); s += __shfl_xor(s, 2); s += __shfl_xor(s, 4);
        const float mean = s * (1.f / D);
        float q0 = 0.f, q1 = 0.f;
#pragma unroll
        for (int i = 0; i < 10; ++i)
#pragma unroll
          for (int e2 = 0; e2 < 4; ++e2) { const float a0 = bflo(xv[h2][i][e2]) - mean, a1 = bfhi(xv[h2][i][e2]) - mean; q0 = fmaf(a0, a0, q0); q1 = fmaf(a1, a1, q1); }
        float q = q0 + q1;
        q += __shfl_xor(q, 1); q += __shfl_xor(q, 2); q += __shfl_xor(q, 4);
        const float den = __builtin_sqrtf(rbf(rbf(q * (1.f / D)) + 1e-5f));
        __builtin_amdgcn_raw_buffer_store_b64(v2u{pack2(mean, den), tag}, rws, (lane & 7) == 0 ? ST_OFF + (srow + 8 * h2) * 8 : -16, 0, SC1);
      }
      float c[8] = {};
#pragma unroll
      for (int i = 0; i < 20; ++i)
#pragma unroll
        for (int e2 = 0; e2 < 4; ++e2) { c[2 * e2] += bflo(wv[i][e2]); c[2 * e2 + 1] += bfhi(wv[i][e2]); }
      *(LAS f32x4*)(L + ((w * 8 + (lane >> 3)) * 64 + (lane & 7) * 8) * 4) = f32x4{c[0], c[1], c[2], c[3]};
      *(LAS f32x4*)(L + ((w * 8 + (lane >> 3)) * 64 + (lane & 7) * 8 + 4) * 4) = f32x4{c[4], c[5], c[6], c[7]};
      WAITL();
      unsigned old = 0;
      if (lane == 0) old = __hip_atomic_fetch_add((LAS unsigned*)(L + C_H), 1u, RLX_WG);
      if (__builtin_amdgcn_readfirstlane(old) == 3) {
        asm volatile("" ::: "memory");
        float t4[4] = {};
#pragma unroll
        for (int i = 0; i < 32; ++i) t4[i & 3] += *(LAS float*)(L + (i * 64 + lane) * 4);
        __builtin_amdgcn_raw_buffer_store_b64(v2u{__float_as_uint((t4[0] + t4[1]) + (t4[2] + t4[3])), tag}, rws, csv ? CS_OFF + (cg * 64 + lane) * 8 : -16, 0, SC1);
      }
      lds_add(L + C_MMA, 12u, lane);
      if (b == 56 && w == 0) {
#pragma unroll 1
        for (unsigned sp = 0; sp < SPIN_CAP; ++sp) {
          bool ok = true;
#pragma unroll
          for (int i = 0; i < 4; ++i) ok = ok && flag_load(ws, FL_P + i * 64 * FSTR, 64, lane) == tag;
          if (__all(ok)) break;
          __builtin_amdgcn_s_sleep(32);
        }
        set_flag(ws, FL_GEN, tag, lane);
      }
    }
    unsigned hb_ = 0;
#define HBAR() do { WAITL(); lds_add(L + C_G2, 1u, lane); hb_ += 4; lds_wait(L + C_G2, hb_); } while (0)
    {
      const __amdgpu_buffer_rsrc_t rxx = mkrsrc(x, S * D * 2);
      lds_wait_sleep(L + C_MMA, 48);
#pragma unroll
      for (int i = 0; i < 3; ++i) {
        const int s = aw + 4 * i;
        if (s < 10) {
          const int n = s * 64 + lane; const int row = n / 10, c = n - row * 10;
          GLDS(rxx, L + EX + s * 1024, (64 * mb + row) * 1280 + 160 * nb + c * 16, 0);
        }
      }
      const float dv = bflo(Dv[dpair + (aw >> 1)]), dv3 = bflo(Dv[d3]);
      lds_wait_sleep(L + C_ST, 1);
      __builtin_amdgcn_s_setprio(1);
      LAS char* Lc = L + CV + (aw >> 1) * CH_BYTES;
      const int cts = aw & 1;
      f32x4 acc[4] = {};
      if (cts == 0) conv8<0>(Lc + aoff, Lc + boff, acc); else conv8<1>(Lc + aoff, Lc + boff, acc);
      __builtin_amdgcn_s_setprio(2);
#pragma unroll
      for (int ti = 0; ti < 4; ++ti) conv_out(Lc, acc[ti], cts == 0 ? (ti < 2 ? ti : ti + 4) : 2 + ti, dpair + (aw >> 1), dv, e, kq, rws, tag);
      f32x4 ac3[4] = {};
      if (ts3 == 0) conv2<0>(L + CV3 + aoff + aw * 128, L + CV3 + boff + aw * 128, ac3); else conv2<1>(L + CV3 + aoff + aw * 128, L + CV3 + boff + aw * 128, ac3);
#pragma unroll
      for (int ti = 0; ti < 4; ++ti) *(LAS f32x4*)(L + SC3 + (aw * 4 + ti) * 1024 + lane * 16) = ac3[ti];
      WAITV(4);
      HBAR();
      f32x4 a = {};
#pragma unroll
      for (int ww = 0; ww < 4; ++ww) a += *(LAS f32x4*)(L + SC3 + (ww * 4 + aw) * 1024 + lane * 16);
      conv_out(L + CV3, a, ts3 == 0 ? (aw < 2 ? aw : aw + 4) : 2 + aw, d3, dv3, e, kq, rws, tag);
    }
    {
      const int q = (lane & 15) >> 2, pp = lane & 3, pos = lane & 15;
      const int tok = 16 * aw + 2 * (pos >> 2) + (pos & 1) + 8 * ((pos >> 1) & 1);
      lds_wait_sleep(L + C_S, 1);
      const f32x2 st = *(LAS f32x2*)(L + XFS + ((mb & 1) * 64 + tok) * 8);
      v4u xn[5];
#pragma unroll
      for (int i = 0; i < 5; ++i) {
        const v2u xv2 = *(LAS v2u*)(L + EX + tok * 160 + (4 * i + kq) * 8);
        const unsigned d01 = pack2(bflo(xv2.x) - st.x, bfhi(xv2.x) - st.x), d23 = pack2(bflo(xv2.y) - st.x, bfhi(xv2.y) - st.x);
        xn[i] = v4u{pack2(bflo(d01) * st.y, bfhi(d01) * st.y), pack2(bflo(d23) * st.y, bfhi(d23) * st.y), xv2.x, xv2.y};
      }
      WAITL();
      lds_add(L + C_Y, 1u, lane);
      for (int i = 0; i < 2; ++i) __builtin_amdgcn_s_sleep(4);
      {
        v4u yv[5];
        const int yo = Y_OFF + (80 * nb + 4 * aw + kq) * 8192 + mb * 256 + e * 16;
#pragma unroll 1
        for (unsigned sp = 0; sp < SPIN_CAP; ++sp) {
          asm volatile("" ::: "memory");
          bool ok = true;
#pragma unroll
          for (int i = 0; i < 5; ++i) yv[i] = __builtin_amdgcn_raw_buffer_load_b128(rws, yo, i * 16 * 8192, SC1);
#pragma unroll
          for (int i = 0; i < 5; ++i) ok = ok && yv[i].y == tag && yv[i].w == tag;
          if (__all(ok)) break;
          __builtin_amdgcn_s_sleep(8);
        }
#pragma unroll
        for (int i = 0; i < 5; ++i) *(LAS v2u*)(L + EY + (16 * i + 4 * aw + kq) * 144 + e * 8) = v2u{yv[i].x, yv[i].z};
      }
      HBAR();
#pragma unroll
      for (int i = 0; i < 5; ++i) {
        const int dq = 4 * i + kq;
        const v2u yv = trread(L + EY + (4 * dq + q) * 144 + aw * 32 + pp * 8);
        const unsigned s01 = pack2(bflo(xn[i].x) * bflo(yv.x), bfhi(xn[i].x) * bfhi(yv.x)), s23 = pack2(bflo(xn[i].y) * bflo(yv.y), bfhi(xn[i].y) * bfhi(yv.y));
        *(LAS v4u*)(L + ES + (tok * 20 + dq) * 16) = v4u{s01, s23, xn[i].z, xn[i].w};
      }
    }
  }
  BAR();
  const int lane2 = __builtin_amdgcn_mbcnt_hi(~0u, __builtin_amdgcn_mbcnt_lo(~0u, opaque0()));
#pragma unroll
  for (int i = 0; i < 3; ++i) {
    const int it = w + 8 * i;
    if (it < 20) {
      const int n = it * 64 + lane2;
      const int tok = n / 20, dq = n - tok * 20;
      f32x4 o = *(LAS f32x4*)(L + tok * PSTR + dq * 16);
#pragma unroll
      for (int ww = 1; ww < 4; ++ww) o += *(LAS f32x4*)(L + ww * PART + tok * PSTR + dq * 16);
      const v4u sx = *(LAS v4u*)(L + ES + n * 16);
      const unsigned o01 = pack2(o[0], o[1]), o23 = pack2(o[2], o[3]);
      const unsigned g01 = pack2(silu_f(bflo(o01)), silu_f(bfhi(o01))), g23 = pack2(silu_f(bflo(o23)), silu_f(bfhi(o23)));
      const unsigned m01 = pack2(bflo(sx.x) * bflo(g01), bfhi(sx.x) * bfhi(g01)), m23 = pack2(bflo(sx.y) * bflo(g23), bfhi(sx.y) * bfhi(g23));
      v2u res;
      res.x = pack2(bflo(sx.z) + bflo(m01), bfhi(sx.z) + bfhi(m01));
      res.y = pack2(bflo(sx.w) + bflo(m23), bfhi(sx.w) + bfhi(m23));
      __builtin_amdgcn_raw_buffer_store_b64(res, mkrsrc(out, S * D * 2), ((64 * mb + tok) * D + 80 * nb + dq * 4) * 2, 0, 16);
    }
  }
}

extern "C" void kernel_launch(void* const* d_in, const int* in_sizes, int n_in,
                              void* d_out, int out_size, void* d_ws, size_t ws_size,
                              hipStream_t stream) {
  fused<<<256, 512, 0, stream>>>((const u16*)d_in[0], (const u16*)d_in[1], (const u16*)d_in[2], (const u16*)d_in[3],
                                 (const u16*)d_in[4], (const u16*)d_in[5], (const u16*)d_in[7], (const u16*)d_in[8],
                                 (char*)d_ws, (u16*)d_out);
}
```
